```python
import torch

def _softmax(x, axis=-1):
    return torch.softmax(x, dim=axis)

def _gelu(x):
    return torch.nn.functional.gelu(x, approximate='tanh')

def _silu(x):
    return torch.nn.functional.silu(x)

def _sigmoid(x):
    return torch.sigmoid(x)

def _layernorm(x, axis=-1, eps=1e-05):
    m = x.mean(dim=axis, keepdim=True)
    v = x.var(dim=axis, keepdim=True, unbiased=False)
    return (x - m) / torch.sqrt(v + eps)

def _scatter_add(tgt, idx, src, axis=0):
    return tgt.clone().index_add_(axis, idx, src)

def _segment_sum(src, seg, n):
    out = torch.zeros((n,) + src.shape[1:], dtype=src.dtype)
    return out.index_add_(0, seg, src)

def _pad(x, pw):
    flat = tuple((v for pair in reversed(pw) for v in pair))
    return torch.nn.functional.pad(x, flat)
kernel_name = 'sequential_preLN__memory_aug_recurrent_mem_tr__swiglu_ffn__prompt'

def setup_inputs():

    _w = lambda a: (lambda t: t.to(torch.bfloat16) if t.is_floating_point() else t)(torch.as_tensor(a))

    def _A_setup():
        B, S, Dm = (1, 2048, 576)
        H, Dh = (32, 128)
        M, seg = (8, 32)
        seg = min(seg, S)
        S = S // seg * seg
        return {'x': _w((lambda *s: torch.randn(*s, dtype=torch.bfloat16) * 0.05)(B, S, Dm)), 'mem0': _w((lambda *s: torch.randn(*s, dtype=torch.bfloat16) * 0.2)(B, M, Dm)), 'Wq': _w((lambda *s: torch.randn(*s, dtype=torch.bfloat16) * 1.6)(Dm, H, Dh) * Dm ** (-0.5)), 'Wk': _w((lambda *s: torch.randn(*s, dtype=torch.bfloat16) * 1.6)(Dm, H, Dh) * Dm ** (-0.5)), 'Wv': _w((lambda *s: torch.randn(*s, dtype=torch.bfloat16) * 0.2)(Dm, H, Dh) * Dm ** (-0.5)), 'Wo': _w((lambda *s: torch.randn(*s, dtype=torch.bfloat16) * 0.2)(H, Dh, Dm) * (H * Dh) ** (-0.5)), 'seg_len': seg}
    _A_in = _A_setup()

    def _B_setup():
        B, S, D, F = (1, 2048, 576, 4608)
        return {'x': _w((lambda *s: torch.randn(*s, dtype=torch.bfloat16) * 0.05)(B, S, D)), 'w_gate': _w((lambda *s: torch.randn(*s, dtype=torch.bfloat16) * 0.05)(D, F)), 'w_up': _w((lambda *s: torch.randn(*s, dtype=torch.bfloat16) * 0.05)(D, F)), 'w_down': _w((lambda *s: torch.randn(*s, dtype=torch.bfloat16) * 0.05)(F, D))}
    _B_in = _B_setup()
    return {'A_x': _A_in['x'], 'A_mem0': _A_in['mem0'], 'A_Wq': _A_in['Wq'], 'A_Wk': _A_in['Wk'], 'A_Wv': _A_in['Wv'], 'A_Wo': _A_in['Wo'], 'A_seg_len': _A_in['seg_len'], 'B_x': _B_in['x'], 'B_w_gate': _B_in['w_gate'], 'B_w_up': _B_in['w_up'], 'B_w_down': _B_in['w_down']}

def reference(A_x, A_mem0, A_Wq, A_Wk, A_Wv, A_Wo, A_seg_len, B_x, B_w_gate, B_w_up, B_w_down):

    def _A_ref(x, mem0, Wq, Wk, Wv, Wo, seg_len):
        B, S, Dm = x.shape
        H, Dh = (Wq.shape[1], Wq.shape[2])
        M = mem0.shape[1]
        scale = Dh ** (-0.5)
        n_seg = S // seg_len
        L = M + seg_len
        idx = torch.arange(L)
        mask = (idx[:, None] < M) | (idx[:, None] >= idx[None, :])
        mem = mem0
        outs = []
        for s in range(n_seg):
            xs = x[:, s * seg_len:(s + 1) * seg_len, :].clone()
            h = torch.concatenate([mem, xs], axis=1)
            q = torch.einsum('btd,dhe->bhte', h, Wq)
            k = torch.einsum('btd,dhe->bhte', h, Wk)
            v = torch.einsum('btd,dhe->bhte', h, Wv)
            scores = torch.einsum('bhqe,bhke->bhqk', q, k) * scale
            scores = torch.where(mask[None, None], scores, -1000000000.0)
            p = _softmax(scores, axis=-1)
            o = torch.einsum('bhqk,bhke->bhqe', p, v)
            o = torch.einsum('bhte,hed->btd', o, Wo)
            mem = o[:, :M, :].clone()
            outs.append(o[:, M:, :])
        return torch.concatenate(outs, axis=1)

    def _B_ref(x, w_gate, w_up, w_down):
        g = _silu(x @ w_gate)
        u = x @ w_up
        return g * u @ w_down
    x = A_x
    h = x + _A_ref(_layernorm(x, axis=-1), A_mem0, A_Wq, A_Wk, A_Wv, A_Wo, A_seg_len)
    h2 = h + _B_ref(_layernorm(h, axis=-1), B_w_gate, B_w_up, B_w_down)
    return h2
```

```cpp
#include <hip/hip_runtime.h>
#include <hip/hip_bf16.h>
#include <utility>

typedef __bf16 bf8 __attribute__((ext_vector_type(8)));
typedef __bf16 bf4 __attribute__((ext_vector_type(4)));
typedef __bf16 bf2 __attribute__((ext_vector_type(2)));
typedef short s4 __attribute__((ext_vector_type(4)));
typedef float f4 __attribute__((ext_vector_type(4)));
typedef float f2 __attribute__((ext_vector_type(2)));
typedef unsigned u4 __attribute__((ext_vector_type(4)));
typedef unsigned u2 __attribute__((ext_vector_type(2)));
#define LAS __attribute__((address_space(3)))
#define DI __device__ __forceinline__
typedef LAS char* lptr;
typedef __amdgpu_buffer_rsrc_t rsrc_t;
typedef const unsigned short* cw16;

constexpr int D = 576, H = 32, NSEG = 64, NRB = 128, F = 4608;
constexpr unsigned MB = 1u << 20;
constexpr unsigned OFF_X = 0;
constexpr unsigned OFF_ST = 2 * MB;
constexpr unsigned OFF_PG = OFF_ST + 32768;
constexpr unsigned PG_M = OFF_PG, PG_O = PG_M + 64 * 128, PG_H = PG_O + 32 * 128, PG_A = PG_H + 48 * 128;
constexpr unsigned OFF_CTL = PG_A + 72 * 128;
constexpr unsigned OFF_CP = OFF_CTL + 1024;
constexpr unsigned OFF_DBG = OFF_CP + 4096;
constexpr unsigned OFF_HD = OFF_ST - 32768 + 5 * MB / 8, OFF_HA = OFF_HD + 32768;
constexpr unsigned OFF_XN = 3 * MB, OFF_H = 6 * MB, OFF_HS = 9 * MB, OFF_Q = 10 * MB, OFF_KM = 58 * MB, OFF_VM = 66 * MB, OFF_O = 74 * MB, OFF_A = 90 * MB, WS_END = 108 * MB;
constexpr unsigned S_VM = 0, S_KR = 128, S_QR = 256, S_SC = 384;
constexpr int LDS_BYTES = 163840;
constexpr int TSM = 62;
constexpr int TS0 = 56;
#define AH 2
#define FPACE 16

constexpr unsigned SPIN_CAP = 1u << 19;
#define SLEEP 2
#define ROLES 15

DI unsigned rfl(unsigned x) { return __builtin_amdgcn_readfirstlane(x); }
DI int lane_id() { int l; asm volatile("v_mbcnt_lo_u32_b32 %0, -1, 0\n\tv_mbcnt_hi_u32_b32 %0, -1, %0" : "=v"(l)); return l; }
DI unsigned cvtpk(float lo, float hi) { f2 v = {lo, hi}; return __builtin_bit_cast(unsigned, __builtin_convertvector(v, bf2)); }
DI float blo(unsigned x) { return __uint_as_float(x << 16); }
DI float bhi(unsigned x) { return __uint_as_float(x & 0xffff0000u); }
DI float sum_g(float x) {
  auto r = __builtin_amdgcn_permlane16_swap(__float_as_uint(x), __float_as_uint(x), false, false);
  x = __uint_as_float(r[0]) + __uint_as_float(r[1]);
  auto q = __builtin_amdgcn_permlane32_swap(__float_as_uint(x), __float_as_uint(x), false, false);
  return __uint_as_float(q[0]) + __uint_as_float(q[1]);
}
DI float max_g(float x) {
  auto r = __builtin_amdgcn_permlane16_swap(__float_as_uint(x), __float_as_uint(x), false, false);
  x = fmaxf(__uint_as_float(r[0]), __uint_as_float(r[1]));
  auto q = __builtin_amdgcn_permlane32_swap(__float_as_uint(x), __float_as_uint(x), false, false);
  return fmaxf(__uint_as_float(q[0]), __uint_as_float(q[1]));
}
#define BAR() asm volatile("s_waitcnt lgkmcnt(0)\n\ts_barrier" ::: "memory")
#define FENCE() asm volatile("" ::: "memory")
template <int N> DI void waitv() { asm volatile("s_waitcnt vmcnt(%0)" ::"i"(N) : "memory"); }
template <int OFF> DI void dma16p(const char* src, unsigned ldsbase) {
  unsigned keep;
  asm volatile("s_mov_b32 %0, m0\n\ts_add_u32 m0, %2, %3\n\ts_nop 0\n\tglobal_load_lds_dwordx4 %1, off\n\ts_mov_b32 m0, %0" : "=&s"(keep) : "v"(src), "s"(ldsbase), "i"(OFF) : "memory", "scc");
}
DI bf8 asbf(u4 x) { return __builtin_bit_cast(bf8, x); }
DI f4 mf32(bf8 a, bf8 b, f4 c) { return __builtin_amdgcn_mfma_f32_16x16x32_bf16(a, b, c, 0, 0, 0); }
DI u4 ld16(rsrc_t r, unsigned v, unsigned s) { return __builtin_amdgcn_raw_buffer_load_b128(r, v, s, 16); }
DI u2 ld8(rsrc_t r, unsigned v, unsigned s) { return __builtin_amdgcn_raw_buffer_load_b64(r, v, s, 16); }
DI unsigned ld4(rsrc_t r, unsigned v, unsigned s) { return __builtin_amdgcn_raw_buffer_load_b32(r, v, s, 16); }
template <int AUX = 16> DI void st16(u4 x, rsrc_t r, unsigned v, unsigned s) { __builtin_amdgcn_raw_buffer_store_b128(x, r, v, s, AUX); asm volatile("s_nop 1" ::"v"(x) : "memory"); }
DI void st8(u2 x, rsrc_t r, unsigned v, unsigned s) { __builtin_amdgcn_raw_buffer_store_b64(x, r, v, s, 16); }
DI void st4(unsigned x, rsrc_t r, unsigned v, unsigned s) { __builtin_amdgcn_raw_buffer_store_b32(x, r, v, s, 16); }
DI bool spin_gt(rsrc_t ws, unsigned voff, unsigned soff, unsigned thr) {
  for (unsigned n = 0;; ++n) {
    FENCE();
    if (__all(ld4(ws, voff, soff) > thr)) return true;
    if (n > SPIN_CAP) { st4(1u, ws, OFF_CTL, 0); return false; }
    if ((n & 255u) == 255u && rfl(ld4(ws, OFF_CTL, 0))) return false;
    __builtin_amdgcn_s_sleep(SLEEP);
  }
}
template <int... Is, class Fn> DI void sfor_(std::integer_sequence<int, Is...>, Fn&& f) { (f(std::integral_constant<int, Is>{}), ...); }
template <int N, class Fn> DI void sfor(Fn&& f) { sfor_(std::make_integer_sequence<int, N>{}, f); }

#define T2(ev)
#define TS(i)
#define TR(role, on, i)
struct Params { cw16 x, mem0, Wq, Wk, Wv, Wo, Wg, Wu, Wd; unsigned short* out; char* ws; };
typedef const __attribute__((address_space(4))) Params* kargs_t;
DI kargs_t kargs() { kargs_t k = (kargs_t)__builtin_amdgcn_kernarg_segment_ptr(); asm volatile("" : "+s"(k)); return k; }
typedef const __attribute__((address_space(1))) u4* gu4;

DI float dpp_ror(float x, int n) {
  const int i = __float_as_int(x);
  return __int_as_float(n == 1 ? __builtin_amdgcn_update_dpp(i, i, 0x121, 0xf, 0xf, false) : n == 2 ? __builtin_amdgcn_update_dpp(i, i, 0x122, 0xf, 0xf, false)
                      : n == 4 ? __builtin_amdgcn_update_dpp(i, i, 0x124, 0xf, 0xf, false) : __builtin_amdgcn_update_dpp(i, i, 0x128, 0xf, 0xf, false));
}
DI float max64(float x) { x = fmaxf(x, dpp_ror(x, 1)); x = fmaxf(x, dpp_ror(x, 2)); x = fmaxf(x, dpp_ror(x, 4)); x = fmaxf(x, dpp_ror(x, 8)); return max_g(x); }

DI void hs_begin(rsrc_t ws, int w, int l) { if (w == 0 && l == 0) st4(0u, ws, OFF_HA + blockIdx.x * 128, 0); }
DI bool hs_wait(rsrc_t ws, int w, int l) {
  for (unsigned n = 0;; ++n) {
    FENCE();
    if (rfl(ld4(ws, OFF_HA + blockIdx.x * 128, 0))) break;
    if (n > SPIN_CAP) return false;
    __builtin_amdgcn_s_sleep(2);
  }
  if (w == 0 && l == 0) st4(1u, ws, OFF_HD + blockIdx.x * 128, 0);
  return true;
}
DI void hs_open(rsrc_t ws, int t) {
#pragma unroll
  for (int i = 0; i < 16; ++i) if (i * 4096 + t * 16 < (int)(OFF_CP + 2560 - OFF_ST)) st16((u4){0, 0, 0, 0}, ws, t * 16, OFF_ST + i * 4096);
  st4(0u, ws, OFF_HD + t * 128, 0);
  waitv<0>(); BAR();
  st4(1u, ws, OFF_HA + t * 128, 0);
}
DI bool hs_round(rsrc_t ws, int t) {
  FENCE();
  const bool open = ld4(ws, OFF_HD + t * 128, 0) == 0u && t != (int)blockIdx.x;
  if (open) st4(1u, ws, OFF_HA + t * 128, 0);
  return !__any(open);
}

DI float sum8(float x) {
  int i = __float_as_int(x); x += __int_as_float(__builtin_amdgcn_update_dpp(i, i, 0xb1, 0xf, 0xf, false));
  i = __float_as_int(x); x += __int_as_float(__builtin_amdgcn_update_dpp(i, i, 0x4e, 0xf, 0xf, false));
  i = __float_as_int(x); return x + __int_as_float(__builtin_amdgcn_update_dpp(i, i, 0x141, 0xf, 0xf, false));
}
DI void ln_rows(kargs_t p, rsrc_t ws, int seg, int w, int l) {
  const int row = 32 * seg + 8 * w + (l >> 3);
  gu4 xr = (gu4)(p->x + row * D) + (l & 7);
  u4 xv[9];
#pragma unroll
  for (int i = 0; i < 9; ++i) xv[i] = xr[8 * i];
  float s = 0.f;
#pragma unroll
  for (int i = 0; i < 9; ++i)
#pragma unroll
    for (int j = 0; j < 4; ++j) s += blo(xv[i][j]) + bhi(xv[i][j]);
  const float mu = sum8(s) * (1.f / D);
  float q = 0.f;
#pragma unroll
  for (int i = 0; i < 9; ++i)
#pragma unroll
    for (int j = 0; j < 4; ++j) { const float a = blo(xv[i][j]) - mu, c = bhi(xv[i][j]) - mu; q += a * a + c * c; }
  const float rs = 1.f / sqrtf(sum8(q) * (1.f / D) + 1e-5f);
#pragma unroll
  for (int i = 0; i < 9; ++i) {
    u4 o;
#pragma unroll
    for (int j = 0; j < 4; ++j) o[j] = cvtpk((blo(xv[i][j]) - mu) * rs, (bhi(xv[i][j]) - mu) * rs);
    st16(o, ws, (l & 7) * 256 + (row & 15) * 16 + i * 2048, OFF_XN + (2 * seg + (w >> 1)) * 18432);
  }
  waitv<0>(); BAR();
}
DI void bound_part(kargs_t p, rsrc_t ws, lptr lds, int seg, int w, int l) {
  const int t = 64 * w + l;
  const unsigned x0 = (1 && (t & 8)) ? 0x1f0u : 0x3e0u, x1 = (1 && (t & 8)) ? 0xfffffff8u : 0xfffffff0u;
#pragma unroll
  for (int i = 0; i < 8; ++i) st16((u4){x0, x1, x0, x1}, ws, t * 16 + (i & 1) * 4096, OFF_X + seg * 32768 + (i >> 1) * 8192);
  gu4 wo = (gu4)p->Wo + seg * 4608;
  f4 a0 = {0, 0, 0, 0}, a1 = a0;
  u4 av[22];
#pragma unroll
  for (int i = 0; i < 22; ++i) av[i] = wo[(t < 216 && t + 216 * i < 4608) ? t + 216 * i : 0];
#pragma unroll
  for (int i = 0; i < 22; ++i)
    if (i < 21 || t < 72) {
      a0 += (f4){fabsf(blo(av[i][0])), fabsf(bhi(av[i][0])), fabsf(blo(av[i][1])), fabsf(bhi(av[i][1]))};
      a1 += (f4){fabsf(blo(av[i][2])), fabsf(bhi(av[i][2])), fabsf(blo(av[i][3])), fabsf(bhi(av[i][3]))};
    }
  LAS f4* red = (LAS f4*)lds;
  if (t >= 72 && t < 216) { red[2 * (t - 72)] = a0; red[2 * (t - 72) + 1] = a1; }
  waitv<0>(); BAR();
  float am = 0.f;
  if (t < 72) { a0 += red[2 * t] + red[2 * t + 144]; a1 += red[2 * t + 1] + red[2 * t + 145];
    am = fmaxf(fmaxf(fmaxf(a0[0], a0[1]), fmaxf(a0[2], a0[3])), fmaxf(fmaxf(a1[0], a1[1]), fmaxf(a1[2], a1[3]))); }
  am = max64(am);
  if (t == 64) *(LAS float*)(lds + 8192) = am;
  BAR();
  if (t == 0) st4(__float_as_uint(fmaxf(am, *(LAS float*)(lds + 8192))) | 1u, ws, OFF_CP + seg * 4, 0);
  BAR();
}

DI void stage_slab(const char* b0, const char* b1, unsigned pitch, int w, int l) {
  const int g = l >> 4, cp = l & 15;
  const unsigned lb = rfl(w * 36864);
  sfor<2>([&](auto PAR) {
    constexpr int par = PAR;
    const int ch = cp ^ ((g | (par << 2)) << 1);
    const char* src = (ch >= 8 ? b1 : b0) + (size_t)(144 * w + 8 * par + g) * pitch + (ch & 7) * 16;
    sfor<18>([&](auto I) { constexpr int i = I; dma16p<(4 * (i >> 1) + 2 * par + (i & 1)) * 1024>(src + (size_t)(16 * (i >> 1) + 4 * (i & 1)) * pitch, lb); });
  });
}

DI void fill_part(rsrc_t ws, int k, int w) {
  const int l = lane_id(), t = 64 * w + l;
  const u4 fill = {~0u, ~0u, ~0u, ~0u};
  constexpr int NS = NSEG - TS0;
  for (int c = k; c < (NSEG - TSM) * 32; c += 24) { st16(fill, ws, t * 16, OFF_KM + TSM * 131072 + c * 4096); st16(fill, ws, t * 16, OFF_VM + TSM * 131072 + c * 4096); __builtin_amdgcn_s_sleep(2 * FPACE); }
  for (int c = k; c < NS * 64; c += 24) { st16(fill, ws, t * 16, OFF_O + TS0 * 262144 + c * 4096); __builtin_amdgcn_s_sleep(FPACE); }
  for (int c = k; c < NS * 72; c += 24) { st16(fill, ws, t * 16, OFF_A + TS0 * 294912 + c * 4096); __builtin_amdgcn_s_sleep(FPACE); }
  for (int c = k; c < NS * 9; c += 24) st16(fill, ws, t * 16, OFF_H + TS0 * 36864 + c * 4096);
  if (k < NS && t < 192) st16(fill, ws, t * 16, OFF_HS + (TS0 + k) * 3072);
  waitv<0>(); BAR();
  if (t == 0) st4(1u, ws, OFF_CTL + 256 + k * 4, 0);
}

template <int NA> DI void att_role(rsrc_t ws, int h, int cls) {
  const int l = lane_id();
  const int g = l >> 4, r = l & 15;
  const unsigned fw = OFF_ST + (l % 3) * 128 + h * 4, pm = PG_M + (2 * h + (l & 1)) * 128;
  constexpr float CS = 0.08838834764831845f * 1.4426950408889634f;
  u4 Q[8], K[8], V[8], km[4], vm[4];
  unsigned scb, nf, np;
  if (!spin_gt(ws, fw, cls * 512, 0)) return;
  auto loadqkv = [&](int s) {
    const unsigned so = (s * 32 + h) * 8192;
#pragma unroll
    for (int i = 0; i < 8; ++i) { Q[i] = ld16(ws, l * 16 + i * 1024, OFF_Q + so); K[i] = ld16(ws, l * 16 + i * 1024, OFF_Q + 16 * MB + so); }
#pragma unroll
    for (int i = 0; i < 8; ++i) V[i] = ld16(ws, l * 16 + i * 1024, OFF_Q + 32 * MB + so);
  };
  auto loadmem = [&](int s) {
#pragma unroll
    for (int i = 0; i < 4; ++i) km[i] = ld16(ws, l * 16 + i * 1024, OFF_KM + (s * 32 + h) * 4096);
#pragma unroll
    for (int i = 0; i < 4; ++i) vm[i] = ld16(ws, l * 16 + i * 1024, OFF_VM + (s * 32 + h) * 4096);
    scb = ld4(ws, OFF_ST + S_SC + h * 4, s * 512);
    nf = ld4(ws, fw, (s + NA < NSEG ? s + NA : s) * 512);
    np = ld4(ws, pm, 0);
  };
  loadqkv(cls);
  bool have = false;
  for (int s = cls; s < NSEG; s += NA) {
    f4 t00 = {0, 0, 0, 0}, t01 = t00, t11 = t00;
#pragma unroll
    for (int ks = 0; ks < 4; ++ks) { t00 = mf32(asbf(K[ks]), asbf(Q[ks]), t00); t01 = mf32(asbf(K[ks]), asbf(Q[4 + ks]), t01); t11 = mf32(asbf(K[4 + ks]), asbf(Q[4 + ks]), t11); }
    float m0 = -3.0e38f, m1;
#pragma unroll
    for (int j = 0; j < 4; ++j) if (4 * g + j > r) { t00[j] = -__builtin_inff(); t11[j] = -__builtin_inff(); }
    m0 = fmaxf(fmaxf(t00[0], t00[1]), fmaxf(t00[2], t00[3]));
    m1 = fmaxf(fmaxf(fmaxf(t01[0], t01[1]), fmaxf(t01[2], t01[3])), fmaxf(fmaxf(t11[0], t11[1]), fmaxf(t11[2], t11[3])));
    if (!have) {
      if (!spin_gt(ws, pm, 0, s)) return;
      TR(9, h == 0, s); T2(0);
      for (unsigned n = 0;; ++n) {
        FENCE();
        loadmem(s);
        if (s < TSM) break;
        unsigned mx = 0;
#pragma unroll
        for (int i = 0; i < 4; ++i)
#pragma unroll
          for (int j = 0; j < 4; ++j) mx = max(mx, max(km[i][j], vm[i][j]));
        if (__all(mx != ~0u && scb != 0u) || n > 4096) break;
      }
    }
    const bool ann = s >= TS0;
    asm volatile("" ::"v"(V[0]), "v"(V[7]), "v"(km[3]), "v"(vm[3]), "v"(scb), "v"(nf), "v"(np));
    auto announce = [&]() { FENCE(); if (ann && (h & 3) == 0 && l == 0) st4(s + NA, ws, PG_O + ((h >> 2) * NA + cls) * 128, 0); FENCE(); };
    if (AH == 0) announce();
    T2(1);
    const float sInv = __uint_as_float(scb);
    f4 u0 = {0, 0, 0, 0}, u1 = u0;
#pragma unroll
    for (int ks = 0; ks < 4; ++ks) { u0 = mf32(asbf(km[ks]), asbf(Q[ks]), u0); u1 = mf32(asbf(km[ks]), asbf(Q[4 + ks]), u1); }
    const float fm = CS * sInv;
    m0 = max_g(fmaxf(m0 * CS, fmaxf(fmaxf(u0[0], u0[1]), fmaxf(u0[2], u0[3])) * fm));
    m1 = max_g(fmaxf(m1 * CS, fmaxf(fmaxf(u1[0], u1[1]), fmaxf(u1[2], u1[3])) * fm));
    float l0 = 0.f, l1 = 0.f;
#pragma unroll
    for (int j = 0; j < 4; ++j) {
      t00[j] = __builtin_amdgcn_exp2f(t00[j] * CS - m0); u0[j] = __builtin_amdgcn_exp2f(u0[j] * fm - m0);
      t01[j] = __builtin_amdgcn_exp2f(t01[j] * CS - m1); t11[j] = __builtin_amdgcn_exp2f(t11[j] * CS - m1); u1[j] = __builtin_amdgcn_exp2f(u1[j] * fm - m1);
      l0 += t00[j] + 0.5f * u0[j]; l1 += t01[j] + t11[j] + 0.5f * u1[j];
    }
    const float n0 = 1.f / sum_g(l0), n1 = 1.f / sum_g(l1), n0m = n0 * sInv, n1m = n1 * sInv;
    const bf8 P0 = asbf((u4){cvtpk(t00[0] * n0, t00[1] * n0), cvtpk(t00[2] * n0, t00[3] * n0), cvtpk(u0[0] * n0m, u0[1] * n0m), cvtpk(u0[2] * n0m, u0[3] * n0m)});
    const bf8 P1 = asbf((u4){cvtpk(t01[0] * n1, t01[1] * n1), cvtpk(t01[2] * n1, t01[3] * n1), cvtpk(t11[0] * n1, t11[1] * n1), cvtpk(t11[2] * n1, t11[3] * n1)});
    const bf8 M1 = asbf((u4){0u, 0u, cvtpk(u1[0] * n1m, u1[1] * n1m), cvtpk(u1[2] * n1m, u1[3] * n1m)});
    if (AH == 1) { asm volatile("" ::"v"(P0), "v"(P1), "v"(M1)); announce(); }
    u4 o[2][4];
#pragma unroll
    for (int u = 0; u < 4; ++u) {
      const f4 z = {0, 0, 0, 0};
      const bf8 va = asbf((u4){V[2 * u].x, V[2 * u].y, vm[u].x, vm[u].y}), vb = asbf((u4){V[2 * u + 1].x, V[2 * u + 1].y, vm[u].z, vm[u].w});
      const f4 a0 = mf32(va, P0, z), b0 = mf32(vb, P0, z);
      const f4 a1 = mf32(va, M1, mf32(asbf(V[2 * u]), P1, z)), b1 = mf32(vb, M1, mf32(asbf(V[2 * u + 1]), P1, z));
      o[0][u] = (u4){cvtpk(a0[0], a0[1]), cvtpk(a0[2], a0[3]), cvtpk(b0[0], b0[1]), cvtpk(b0[2], b0[3])};
      o[1][u] = (u4){cvtpk(a1[0], a1[1]), cvtpk(a1[2], a1[3]), cvtpk(b1[0], b1[1]), cvtpk(b1[2], b1[3])};
    }
#pragma unroll
    for (int tb = 0; tb < 2; ++tb)
#pragma unroll
      for (int u = 0; u < 4; ++u) st16(o[tb][u], ws, l * 16 + u * 1024, OFF_O + ((2 * s + tb) * 128 + 4 * h) * 1024);
    if (AH == 2) announce();
    FENCE();
    T2(2);
    const bool more = s + NA < NSEG;
    const bool pre = more && __all(nf != 0u);
    have = pre && s + NA < TSM && __all(np > (unsigned)(s + NA));
    if (ann) { if (have) { loadqkv(s + NA); loadmem(s + NA); } }
    else {
      if (have) { loadqkv(s + NA); loadmem(s + NA); waitv<35>(); } else waitv<0>();
      BAR();
      if ((h & 3) == 0 && l == 0) st4(s + NA, ws, PG_O + ((h >> 2) * NA + cls) * 128, 0);
    }
    TR(0, h == 0, s); T2(3);
    FENCE();
    if (pre && !have) loadqkv(s + NA);
    if (more && !pre) { if (!spin_gt(ws, fw, (s + NA) * 512, 0)) return; loadqkv(s + NA); }
  }
}

#define REC_ASM \
  "s_load_dwordx2 s[98:99], %0, 8\n\t" \
  "s_load_dwordx8 s[88:95], %0, 16\n\t" \
  "s_load_dwordx2 s[96:97], %0, 80\n\t" \
  "s_bfe_u32 s68, %1, 0x50001\n\t" \
  "s_and_b32 s69, %1, 1\n\t" \
  "s_bfe_u32 s70, %1, 0x20008\n\t" \
  "s_lshr_b32 s61, %1, 16\n\t" \
  "s_lshl_b32 s61, s61, 7\n\t" \
  "s_mov_b64 exec, -1\n\t" \
  "s_waitcnt lgkmcnt(0)\n\t" \
  "v_mbcnt_lo_u32_b32 v16, -1, 0\n\t" \
  "v_mbcnt_hi_u32_b32 v16, -1, v16\n\t" \
  "v_lshrrev_b32 v17, 4, v16\n\t" \
  "v_and_b32 v18, 15, v16\n\t" \
  "v_lshrrev_b32 v19, 2, v18\n\t" \
  "v_and_b32 v20, 3, v16\n\t" \
  "s_add_u32 s20, s96, 0\n\t" \
  "s_addc_u32 s21, s97, 0\n\t" \
  "s_and_b32 s21, s21, 0xffff\n\t" \
  "s_mov_b32 s22, 2097152\n\t" \
  "s_mov_b32 s23, 0x20000\n\t" \
  "s_add_u32 s24, s96, 27131904\n\t" \
  "s_addc_u32 s25, s97, 0\n\t" \
  "s_and_b32 s25, s25, 0xffff\n\t" \
  "s_mov_b32 s26, 16908288\n\t" \
  "s_mov_b32 s27, 0x20000\n\t" \
  "s_add_u32 s28, s96, 43909120\n\t" \
  "s_addc_u32 s29, s97, 0\n\t" \
  "s_and_b32 s29, s29, 0xffff\n\t" \
  "s_mov_b32 s30, 16908288\n\t" \
  "s_mov_b32 s31, 0x20000\n\t" \
  "s_add_u32 s36, s96, 2097152\n\t" \
  "s_addc_u32 s37, s97, 0\n\t" \
  "s_and_b32 s37, s37, 0xffff\n\t" \
  "s_mov_b32 s38, 688128\n\t" \
  "s_mov_b32 s39, 0x20000\n\t" \
  "s_mul_i32 s55, s69, 8388608\n\t" \
  "s_add_u32 s55, s55, 60817408\n\t" \
  "s_add_u32 s40, s96, s55\n\t" \
  "s_addc_u32 s41, s97, 0\n\t" \
  "s_and_b32 s41, s41, 0xffff\n\t" \
  "s_mov_b32 s42, 8388608\n\t" \
  "s_mov_b32 s43, 0x20000\n\t" \
  "s_add_u32 s61, s61, 655360\n\t" \
  "s_cmp_eq_u32 s70, 0\n\t" \
  "s_cselect_b64 exec, 1, 0\n\t" \
  "v_mov_b32 v69, 0\n\t" \
  "buffer_store_dword v69, off, s[36:39], s61 sc1\n\t" \
  "s_mov_b64 exec, -1\n\t" \
  "s_mov_b32 s80, s98\n\t" \
  "s_and_b32 s81, s99, 0xffff\n\t" \
  "s_mov_b32 s82, 9216\n\t" \
  "s_mov_b32 s83, 0x20000\n\t" \
  "s_lshl_b32 s55, s70, 6\n\t" \
  "v_add_u32 v96, s55, v16\n\t" \
  "v_lshlrev_b32 v26, 4, v96\n\t" \
  "v_add_u32 v27, 4096, v26\n\t" \
  "v_add_u32 v28, 8192, v26\n\t" \
  "buffer_load_dwordx4 v[84:87], v26, s[80:83], 0 offen\n\t" \
  "buffer_load_dwordx4 v[88:91], v27, s[80:83], 0 offen\n\t" \
  "buffer_load_dwordx4 v[92:95], v28, s[80:83], 0 offen\n\t" \
  "s_lshl_b32 s55, s70, 17\n\t" \
  "s_lshl_b32 s56, s68, 8\n\t" \
  "s_add_u32 s55, s55, s56\n\t" \
  "v_lshlrev_b32 v26, 13, v17\n\t" \
  "v_add_u32 v26, s55, v26\n\t" \
  "v_and_b32 v27, 1, v17\n\t" \
  "v_lshlrev_b32 v27, 1, v27\n\t" \
  "v_xor_b32 v27, v27, v18\n\t" \
  "v_xor_b32 v28, 0, v27\n\t" \
  "v_lshl_add_u32 v21, v28, 4, v26\n\t" \
  "v_add_u32 v21, 0, v21\n\t" \
  "v_xor_b32 v28, 4, v27\n\t" \
  "v_lshl_add_u32 v22, v28, 4, v26\n\t" \
  "v_add_u32 v22, 32768, v22\n\t" \
  "v_xor_b32 v28, 8, v27\n\t" \
  "v_lshl_add_u32 v23, v28, 4, v26\n\t" \
  "v_add_u32 v23, 65536, v23\n\t" \
  "v_xor_b32 v28, 12, v27\n\t" \
  "v_lshl_add_u32 v24, v28, 4, v26\n\t" \
  "v_add_u32 v24, 98304, v24\n\t" \
  "s_mul_i32 s55, s68, 147456\n\t" \
  "s_mul_i32 s56, s69, 576\n\t" \
  "s_add_u32 s55, s55, s56\n\t" \
  "s_add_u32 s66, s94, s55\n\t" \
  "s_addc_u32 s67, s95, 0\n\t" \
  "s_mul_i32 s56, s70, 1152\n\t" \
  "s_mul_i32 s57, s70, 18432\n\t" \
  "s_mov_b32 s72, 58255\n\t" \
  "v_add_u32 v26, s56, v16\n\t" \
  "v_add_u32 v26, 0, v26\n\t" \
  "v_mul_u32_u24 v27, s72, v26\n\t" \
  "v_lshrrev_b32 v27, 21, v27\n\t" \
  "v_mul_u32_u24 v27, 576, v27\n\t" \
  "v_lshl_add_u32 v30, v26, 4, v27\n\t" \
  "s_add_u32 m0, s57, 0\n\t" \
  "s_nop 0\n\t" \
  "global_load_lds_dwordx4 v30, s[66:67]\n\t" \
  "v_add_u32 v26, s56, v16\n\t" \
  "v_add_u32 v26, 64, v26\n\t" \
  "v_mul_u32_u24 v27, s72, v26\n\t" \
  "v_lshrrev_b32 v27, 21, v27\n\t" \
  "v_mul_u32_u24 v27, 576, v27\n\t" \
  "v_lshl_add_u32 v31, v26, 4, v27\n\t" \
  "s_add_u32 m0, s57, 1024\n\t" \
  "s_nop 0\n\t" \
  "global_load_lds_dwordx4 v31, s[66:67]\n\t" \
  "v_add_u32 v26, s56, v16\n\t" \
  "v_add_u32 v26, 128, v26\n\t" \
  "v_mul_u32_u24 v27, s72, v26\n\t" \
  "v_lshrrev_b32 v27, 21, v27\n\t" \
  "v_mul_u32_u24 v27, 576, v27\n\t" \
  "v_lshl_add_u32 v32, v26, 4, v27\n\t" \
  "s_add_u32 m0, s57, 2048\n\t" \
  "s_nop 0\n\t" \
  "global_load_lds_dwordx4 v32, s[66:67]\n\t" \
  "v_add_u32 v26, s56, v16\n\t" \
  "v_add_u32 v26, 192, v26\n\t" \
  "v_mul_u32_u24 v27, s72, v26\n\t" \
  "v_lshrrev_b32 v27, 21, v27\n\t" \
  "v_mul_u32_u24 v27, 576, v27\n\t" \
  "v_lshl_add_u32 v33, v26, 4, v27\n\t" \
  "s_add_u32 m0, s57, 3072\n\t" \
  "s_nop 0\n\t" \
  "global_load_lds_dwordx4 v33, s[66:67]\n\t" \
  "v_add_u32 v26, s56, v16\n\t" \
  "v_add_u32 v26, 256, v26\n\t" \
  "v_mul_u32_u24 v27, s72, v26\n\t" \
  "v_lshrrev_b32 v27, 21, v27\n\t" \
  "v_mul_u32_u24 v27, 576, v27\n\t" \
  "v_lshl_add_u32 v34, v26, 4, v27\n\t" \
  "s_add_u32 m0, s57, 4096\n\t" \
  "s_nop 0\n\t" \
  "global_load_lds_dwordx4 v34, s[66:67]\n\t" \
  "v_add_u32 v26, s56, v16\n\t" \
  "v_add_u32 v26, 320, v26\n\t" \
  "v_mul_u32_u24 v27, s72, v26\n\t" \
  "v_lshrrev_b32 v27, 21, v27\n\t" \
  "v_mul_u32_u24 v27, 576, v27\n\t" \
  "v_lshl_add_u32 v35, v26, 4, v27\n\t" \
  "s_add_u32 m0, s57, 5120\n\t" \
  "s_nop 0\n\t" \
  "global_load_lds_dwordx4 v35, s[66:67]\n\t" \
  "v_add_u32 v26, s56, v16\n\t" \
  "v_add_u32 v26, 384, v26\n\t" \
  "v_mul_u32_u24 v27, s72, v26\n\t" \
  "v_lshrrev_b32 v27, 21, v27\n\t" \
  "v_mul_u32_u24 v27, 576, v27\n\t" \
  "v_lshl_add_u32 v36, v26, 4, v27\n\t" \
  "s_add_u32 m0, s57, 6144\n\t" \
  "s_nop 0\n\t" \
  "global_load_lds_dwordx4 v36, s[66:67]\n\t" \
  "v_add_u32 v26, s56, v16\n\t" \
  "v_add_u32 v26, 448, v26\n\t" \
  "v_mul_u32_u24 v27, s72, v26\n\t" \
  "v_lshrrev_b32 v27, 21, v27\n\t" \
  "v_mul_u32_u24 v27, 576, v27\n\t" \
  "v_lshl_add_u32 v37, v26, 4, v27\n\t" \
  "s_add_u32 m0, s57, 7168\n\t" \
  "s_nop 0\n\t" \
  "global_load_lds_dwordx4 v37, s[66:67]\n\t" \
  "v_add_u32 v26, s56, v16\n\t" \
  "v_add_u32 v26, 512, v26\n\t" \
  "v_mul_u32_u24 v27, s72, v26\n\t" \
  "v_lshrrev_b32 v27, 21, v27\n\t" \
  "v_mul_u32_u24 v27, 576, v27\n\t" \
  "v_lshl_add_u32 v38, v26, 4, v27\n\t" \
  "s_add_u32 m0, s57, 8192\n\t" \
  "s_nop 0\n\t" \
  "global_load_lds_dwordx4 v38, s[66:67]\n\t" \
  "v_add_u32 v26, s56, v16\n\t" \
  "v_add_u32 v26, 576, v26\n\t" \
  "v_mul_u32_u24 v27, s72, v26\n\t" \
  "v_lshrrev_b32 v27, 21, v27\n\t" \
  "v_mul_u32_u24 v27, 576, v27\n\t" \
  "v_lshl_add_u32 v39, v26, 4, v27\n\t" \
  "s_add_u32 m0, s57, 9216\n\t" \
  "s_nop 0\n\t" \
  "global_load_lds_dwordx4 v39, s[66:67]\n\t" \
  "v_add_u32 v26, s56, v16\n\t" \
  "v_add_u32 v26, 640, v26\n\t" \
  "v_mul_u32_u24 v27, s72, v26\n\t" \
  "v_lshrrev_b32 v27, 21, v27\n\t" \
  "v_mul_u32_u24 v27, 576, v27\n\t" \
  "v_lshl_add_u32 v40, v26, 4, v27\n\t" \
  "s_add_u32 m0, s57, 10240\n\t" \
  "s_nop 0\n\t" \
  "global_load_lds_dwordx4 v40, s[66:67]\n\t" \
  "v_add_u32 v26, s56, v16\n\t" \
  "v_add_u32 v26, 704, v26\n\t" \
  "v_mul_u32_u24 v27, s72, v26\n\t" \
  "v_lshrrev_b32 v27, 21, v27\n\t" \
  "v_mul_u32_u24 v27, 576, v27\n\t" \
  "v_lshl_add_u32 v41, v26, 4, v27\n\t" \
  "s_add_u32 m0, s57, 11264\n\t" \
  "s_nop 0\n\t" \
  "global_load_lds_dwordx4 v41, s[66:67]\n\t" \
  "v_add_u32 v26, s56, v16\n\t" \
  "v_add_u32 v26, 768, v26\n\t" \
  "v_mul_u32_u24 v27, s72, v26\n\t" \
  "v_lshrrev_b32 v27, 21, v27\n\t" \
  "v_mul_u32_u24 v27, 576, v27\n\t" \
  "v_lshl_add_u32 v42, v26, 4, v27\n\t" \
  "s_add_u32 m0, s57, 12288\n\t" \
  "s_nop 0\n\t" \
  "global_load_lds_dwordx4 v42, s[66:67]\n\t" \
  "v_add_u32 v26, s56, v16\n\t" \
  "v_add_u32 v26, 832, v26\n\t" \
  "v_mul_u32_u24 v27, s72, v26\n\t" \
  "v_lshrrev_b32 v27, 21, v27\n\t" \
  "v_mul_u32_u24 v27, 576, v27\n\t" \
  "v_lshl_add_u32 v43, v26, 4, v27\n\t" \
  "s_add_u32 m0, s57, 13312\n\t" \
  "s_nop 0\n\t" \
  "global_load_lds_dwordx4 v43, s[66:67]\n\t" \
  "v_add_u32 v26, s56, v16\n\t" \
  "v_add_u32 v26, 896, v26\n\t" \
  "v_mul_u32_u24 v27, s72, v26\n\t" \
  "v_lshrrev_b32 v27, 21, v27\n\t" \
  "v_mul_u32_u24 v27, 576, v27\n\t" \
  "v_lshl_add_u32 v44, v26, 4, v27\n\t" \
  "s_add_u32 m0, s57, 14336\n\t" \
  "s_nop 0\n\t" \
  "global_load_lds_dwordx4 v44, s[66:67]\n\t" \
  "v_add_u32 v26, s56, v16\n\t" \
  "v_add_u32 v26, 960, v26\n\t" \
  "v_mul_u32_u24 v27, s72, v26\n\t" \
  "v_lshrrev_b32 v27, 21, v27\n\t" \
  "v_mul_u32_u24 v27, 576, v27\n\t" \
  "v_lshl_add_u32 v45, v26, 4, v27\n\t" \
  "s_add_u32 m0, s57, 15360\n\t" \
  "s_nop 0\n\t" \
  "global_load_lds_dwordx4 v45, s[66:67]\n\t" \
  "v_add_u32 v26, s56, v16\n\t" \
  "v_add_u32 v26, 1024, v26\n\t" \
  "v_mul_u32_u24 v27, s72, v26\n\t" \
  "v_lshrrev_b32 v27, 21, v27\n\t" \
  "v_mul_u32_u24 v27, 576, v27\n\t" \
  "v_lshl_add_u32 v46, v26, 4, v27\n\t" \
  "s_add_u32 m0, s57, 16384\n\t" \
  "s_nop 0\n\t" \
  "global_load_lds_dwordx4 v46, s[66:67]\n\t" \
  "v_add_u32 v26, s56, v16\n\t" \
  "v_add_u32 v26, 1088, v26\n\t" \
  "v_mul_u32_u24 v27, s72, v26\n\t" \
  "v_lshrrev_b32 v27, 21, v27\n\t" \
  "v_mul_u32_u24 v27, 576, v27\n\t" \
  "v_lshl_add_u32 v47, v26, 4, v27\n\t" \
  "s_add_u32 m0, s57, 17408\n\t" \
  "s_nop 0\n\t" \
  "global_load_lds_dwordx4 v47, s[66:67]\n\t" \
  "s_cmp_eq_u32 s69, 0\n\t" \
  "s_cbranch_scc0 Lrec_oa_%=\n\t" \
  "s_add_u32 s66, s88, 1572864\n\t" \
  "s_addc_u32 s67, s89, 0\n\t" \
  "s_lshl_b32 s55, s70, 12\n\t" \
  "s_add_u32 m0, s55, 81920\n\t" \
  "s_nop 0\n\t" \
  "global_load_lds_dwordx4 v21, s[66:67]\n\t" \
  "s_add_u32 m0, s55, 82944\n\t" \
  "s_nop 0\n\t" \
  "global_load_lds_dwordx4 v22, s[66:67]\n\t" \
  "s_add_u32 m0, s55, 83968\n\t" \
  "s_nop 0\n\t" \
  "global_load_lds_dwordx4 v23, s[66:67]\n\t" \
  "s_add_u32 m0, s55, 84992\n\t" \
  "s_nop 0\n\t" \
  "global_load_lds_dwordx4 v24, s[66:67]\n\t" \
  "s_add_u32 s66, s88, 2097152\n\t" \
  "s_addc_u32 s67, s89, 0\n\t" \
  "s_lshl_b32 s55, s70, 12\n\t" \
  "s_add_u32 m0, s55, 98304\n\t" \
  "s_nop 0\n\t" \
  "global_load_lds_dwordx4 v21, s[66:67]\n\t" \
  "s_add_u32 m0, s55, 99328\n\t" \
  "s_nop 0\n\t" \
  "global_load_lds_dwordx4 v22, s[66:67]\n\t" \
  "s_add_u32 m0, s55, 100352\n\t" \
  "s_nop 0\n\t" \
  "global_load_lds_dwordx4 v23, s[66:67]\n\t" \
  "s_add_u32 m0, s55, 101376\n\t" \
  "s_nop 0\n\t" \
  "global_load_lds_dwordx4 v24, s[66:67]\n\t" \
  "s_add_u32 s66, s88, 2621440\n\t" \
  "s_addc_u32 s67, s89, 0\n\t" \
  "s_lshl_b32 s55, s70, 12\n\t" \
  "s_add_u32 m0, s55, 114688\n\t" \
  "s_nop 0\n\t" \
  "global_load_lds_dwordx4 v21, s[66:67]\n\t" \
  "s_add_u32 m0, s55, 115712\n\t" \
  "s_nop 0\n\t" \
  "global_load_lds_dwordx4 v22, s[66:67]\n\t" \
  "s_add_u32 m0, s55, 116736\n\t" \
  "s_nop 0\n\t" \
  "global_load_lds_dwordx4 v23, s[66:67]\n\t" \
  "s_add_u32 m0, s55, 117760\n\t" \
  "s_nop 0\n\t" \
  "global_load_lds_dwordx4 v24, s[66:67]\n\t" \
  "s_branch Lrec_pa_%=\n\t" \
  "Lrec_oa_%=:\n\t" \
  "s_add_u32 s66, s90, 2621440\n\t" \
  "s_addc_u32 s67, s91, 0\n\t" \
  "s_lshl_b32 s55, s70, 12\n\t" \
  "s_add_u32 m0, s55, 98304\n\t" \
  "s_nop 0\n\t" \
  "global_load_lds_dwordx4 v21, s[66:67]\n\t" \
  "s_add_u32 m0, s55, 99328\n\t" \
  "s_nop 0\n\t" \
  "global_load_lds_dwordx4 v22, s[66:67]\n\t" \
  "s_add_u32 m0, s55, 100352\n\t" \
  "s_nop 0\n\t" \
  "global_load_lds_dwordx4 v23, s[66:67]\n\t" \
  "s_add_u32 m0, s55, 101376\n\t" \
  "s_nop 0\n\t" \
  "global_load_lds_dwordx4 v24, s[66:67]\n\t" \
  "s_add_u32 s66, s90, 3145728\n\t" \
  "s_addc_u32 s67, s91, 0\n\t" \
  "s_lshl_b32 s55, s70, 12\n\t" \
  "s_add_u32 m0, s55, 114688\n\t" \
  "s_nop 0\n\t" \
  "global_load_lds_dwordx4 v21, s[66:67]\n\t" \
  "s_add_u32 m0, s55, 115712\n\t" \
  "s_nop 0\n\t" \
  "global_load_lds_dwordx4 v22, s[66:67]\n\t" \
  "s_add_u32 m0, s55, 116736\n\t" \
  "s_nop 0\n\t" \
  "global_load_lds_dwordx4 v23, s[66:67]\n\t" \
  "s_add_u32 m0, s55, 117760\n\t" \
  "s_nop 0\n\t" \
  "global_load_lds_dwordx4 v24, s[66:67]\n\t" \
  "s_add_u32 s66, s90, 3670016\n\t" \
  "s_addc_u32 s67, s91, 0\n\t" \
  "s_lshl_b32 s55, s70, 12\n\t" \
  "s_add_u32 m0, s55, 81920\n\t" \
  "s_nop 0\n\t" \
  "global_load_lds_dwordx4 v21, s[66:67]\n\t" \
  "s_add_u32 m0, s55, 82944\n\t" \
  "s_nop 0\n\t" \
  "global_load_lds_dwordx4 v22, s[66:67]\n\t" \
  "s_add_u32 m0, s55, 83968\n\t" \
  "s_nop 0\n\t" \
  "global_load_lds_dwordx4 v23, s[66:67]\n\t" \
  "s_add_u32 m0, s55, 84992\n\t" \
  "s_nop 0\n\t" \
  "global_load_lds_dwordx4 v24, s[66:67]\n\t" \
  "Lrec_pa_%=:\n\t" \
  "s_add_u32 s66, s92, 2621440\n\t" \
  "s_addc_u32 s67, s93, 0\n\t" \
  "s_lshl_b32 s55, s70, 12\n\t" \
  "s_add_u32 m0, s55, 131072\n\t" \
  "s_nop 0\n\t" \
  "global_load_lds_dwordx4 v21, s[66:67]\n\t" \
  "s_add_u32 m0, s55, 132096\n\t" \
  "s_nop 0\n\t" \
  "global_load_lds_dwordx4 v22, s[66:67]\n\t" \
  "s_add_u32 m0, s55, 133120\n\t" \
  "s_nop 0\n\t" \
  "global_load_lds_dwordx4 v23, s[66:67]\n\t" \
  "s_add_u32 m0, s55, 134144\n\t" \
  "s_nop 0\n\t" \
  "global_load_lds_dwordx4 v24, s[66:67]\n\t" \
  "v_and_b32 v50, 1, v17\n\t" \
  "v_lshrrev_b32 v51, 1, v17\n\t" \
  "v_and_b32 v52, 1, v19\n\t" \
  "v_lshrrev_b32 v53, 1, v19\n\t" \
  "v_lshl_add_u32 v26, v50, 4, v52\n\t" \
  "v_lshl_add_u32 v54, v51, 3, v26\n\t" \
  "v_lshl_add_u32 v54, v53, 2, v54\n\t" \
  "v_mul_u32_u24 v54, 576, v54\n\t" \
  "v_lshl_add_u32 v54, v20, 3, v54\n\t" \
  "s_and_b32 s55, s70, 1\n\t" \
  "s_add_u32 s55, s55, 16\n\t" \
  "s_lshl_b32 s55, s55, 5\n\t" \
  "v_add_u32 v54, s55, v54\n\t" \
  "v_lshl_add_u32 v55, v51, 2, v26\n\t" \
  "v_mul_u32_u24 v55, 576, v55\n\t" \
  "v_lshl_add_u32 v55, v20, 3, v55\n\t" \
  "v_lshl_add_u32 v55, v53, 5, v55\n\t" \
  "s_lshl_b32 s55, s70, 7\n\t" \
  "v_add_u32 v55, s55, v55\n\t" \
  "v_lshlrev_b32 v26, 4, v51\n\t" \
  "v_lshl_add_u32 v26, v53, 3, v26\n\t" \
  "v_lshl_add_u32 v26, v50, 2, v26\n\t" \
  "v_add_u32 v26, v26, v52\n\t" \
  "v_lshl_add_u32 v29, v50, 1, v52\n\t" \
  "v_lshl_add_u32 v29, v53, 2, v29\n\t" \
  "v_lshlrev_b32 v29, 1, v29\n\t" \
  "s_lshl_b32 s55, s70, 2\n\t" \
  "v_lshrrev_b32 v27, 1, v20\n\t" \
  "v_add_u32 v27, s55, v27\n\t" \
  "v_xor_b32 v27, v27, v29\n\t" \
  "v_lshlrev_b32 v26, 8, v26\n\t" \
  "v_lshl_add_u32 v26, v27, 4, v26\n\t" \
  "v_and_b32 v27, 1, v20\n\t" \
  "v_lshl_add_u32 v25, v27, 3, v26\n\t" \
  "v_lshlrev_b32 v26, 4, v16\n\t" \
  "s_lshl_b32 s55, s70, 12\n\t" \
  "v_add_u32 v122, s55, v26\n\t" \
  "v_add_u32 v123, 0x10000, v122\n\t" \
  "v_add_u32 v124, 139264, v26\n\t" \
  "s_lshl_b32 s55, s70, 11\n\t" \
  "s_add_u32 s55, s55, 131072\n\t" \
  "v_add_u32 v125, s55, v26\n\t" \
  "v_and_b32 v30, 7, v16\n\t" \
  "v_lshrrev_b32 v27, 5, v16\n\t" \
  "s_lshl_b32 s55, s70, 9\n\t" \
  "s_add_u32 s55, s55, 156672\n\t" \
  "v_lshlrev_b32 v27, 8, v27\n\t" \
  "v_lshl_add_u32 v27, v18, 4, v27\n\t" \
  "v_lshl_add_u32 v27, v50, 3, v27\n\t" \
  "v_add_u32 v126, s55, v27\n\t" \
  "v_lshl_add_u32 v27, v17, 3, v19\n\t" \
  "v_lshlrev_b32 v27, 4, v27\n\t" \
  "v_and_b32 v26, 1, v16\n\t" \
  "v_lshl_add_u32 v27, v26, 3, v27\n\t" \
  "v_add_u32 v127, 156672, v27\n\t" \
  "v_lshlrev_b32 v28, 7, v17\n\t" \
  "v_lshl_add_u32 v28, v30, 4, v28\n\t" \
  "v_add_u32 v129, 160768, v28\n\t" \
  "v_cmp_gt_u32 s[64:65], 8, v18\n\t" \
  "v_mov_b32 v26, 156672\n\t" \
  "v_mov_b32 v27, 160768\n\t" \
  "s_nop 1\n\t" \
  "v_cndmask_b32 v26, v26, v27, s[64:65]\n\t" \
  "v_add_u32 v26, v26, v28\n\t" \
  "s_lshl_b32 s55, s70, 9\n\t" \
  "v_add_u32 v128, s55, v26\n\t" \
  "v_mov_b32 v26, 0xeeee\n\t" \
  "v_mov_b32 v27, 0x4444\n\t" \
  "v_cndmask_b32 v132, v26, v27, s[64:65]\n\t" \
  "s_lshl_b32 s56, s69, 1\n\t" \
  "s_and_b32 s55, s70, 1\n\t" \
  "s_add_u32 s55, s55, s56\n\t" \
  "s_lshl_b32 s55, s55, 13\n\t" \
  "s_add_u32 s55, s55, 128\n\t" \
  "s_and_b32 s56, s68, 1\n\t" \
  "s_lshl_b32 s56, s56, 9\n\t" \
  "s_add_u32 s55, s55, s56\n\t" \
  "v_lshl_add_u32 v26, v50, 1, v51\n\t" \
  "v_lshrrev_b32 v27, 3, v18\n\t" \
  "v_lshl_add_u32 v26, v26, 1, v27\n\t" \
  "v_lshlrev_b32 v26, 10, v26\n\t" \
  "v_lshl_add_u32 v26, v30, 3, v26\n\t" \
  "v_add_u32 v130, s55, v26\n\t" \
  "s_lshl_b32 s55, s70, 2\n\t" \
  "s_lshl_b32 s56, s69, 4\n\t" \
  "s_add_u32 s55, s55, s56\n\t" \
  "s_lshl_b32 s55, s55, 10\n\t" \
  "v_lshlrev_b32 v26, 9, v50\n\t" \
  "v_lshl_add_u32 v26, v18, 3, v26\n\t" \
  "v_lshl_add_u32 v27, v51, 10, v26\n\t" \
  "v_add_u32 v131, s55, v27\n\t" \
  "v_mov_b32 v116, 0x3f803f80\n\t" \
  "v_mov_b32 v117, 0x3f803f80\n\t" \
  "v_mov_b32 v118, 0x3f803f80\n\t" \
  "v_mov_b32 v119, 0x3f803f80\n\t" \
  "s_mov_b32 s73, 0xffffffc0\n\t" \
  "s_mov_b32 s44, 0\n\t" \
  "s_mov_b32 s48, 0\n\t" \
  "s_lshl_b32 s46, s68, 13\n\t" \
  "s_lshl_b32 s47, s68, 12\n\t" \
  "s_mov_b32 s45, 0\n\t" \
  "s_add_u32 s34, s96, 32768\n\t" \
  "s_addc_u32 s35, s97, 0\n\t" \
  "s_cmp_lt_u32 s70, 2\n\t" \
  "s_cselect_b64 s[98:99], -1, 0\n\t" \
  "s_lshl_b32 s96, s68, 1\n\t" \
  "s_add_u32 s96, s96, s69\n\t" \
  "s_lshl_b32 s96, s96, 7\n\t" \
  "s_add_u32 s96, s96, 32768\n\t" \
  "s_lshl_b32 s97, s68, 2\n\t" \
  "s_add_u32 s97, s97, 384\n\t" \
  "s_cmp_eq_u32 s70, 0\n\t" \
  "s_cselect_b64 s[94:95], 1, 0\n\t" \
  "s_waitcnt vmcnt(16)\n\t" \
  "s_barrier\n\t" \
  "ds_read_b64_tr_b16 v[240:241], v54 offset:0\n\t" \
  "ds_read_b64_tr_b16 v[242:243], v54 offset:1152\n\t" \
  "ds_read_b64_tr_b16 v[244:245], v54 offset:18432\n\t" \
  "ds_read_b64_tr_b16 v[246:247], v54 offset:19584\n\t" \
  "ds_read_b64_tr_b16 v[248:249], v54 offset:36864\n\t" \
  "ds_read_b64_tr_b16 v[250:251], v54 offset:38016\n\t" \
  "ds_read_b64_tr_b16 v[252:253], v54 offset:55296\n\t" \
  "ds_read_b64_tr_b16 v[254:255], v54 offset:56448\n\t" \
  "s_waitcnt lgkmcnt(0)\n\t" \
  "ds_read_b64_tr_b16 v[176:177], v55 offset:0\n\t" \
  "ds_read_b64_tr_b16 v[178:179], v55 offset:1152\n\t" \
  "ds_read_b64_tr_b16 v[180:181], v55 offset:4608\n\t" \
  "ds_read_b64_tr_b16 v[182:183], v55 offset:5760\n\t" \
  "ds_read_b64_tr_b16 v[184:185], v55 offset:18432\n\t" \
  "ds_read_b64_tr_b16 v[186:187], v55 offset:19584\n\t" \
  "ds_read_b64_tr_b16 v[188:189], v55 offset:23040\n\t" \
  "ds_read_b64_tr_b16 v[190:191], v55 offset:24192\n\t" \
  "s_waitcnt lgkmcnt(0)\n\t" \
  "ds_read_b64_tr_b16 v[192:193], v55 offset:36864\n\t" \
  "ds_read_b64_tr_b16 v[194:195], v55 offset:38016\n\t" \
  "ds_read_b64_tr_b16 v[196:197], v55 offset:41472\n\t" \
  "ds_read_b64_tr_b16 v[198:199], v55 offset:42624\n\t" \
  "ds_read_b64_tr_b16 v[200:201], v55 offset:55296\n\t" \
  "ds_read_b64_tr_b16 v[202:203], v55 offset:56448\n\t" \
  "ds_read_b64_tr_b16 v[204:205], v55 offset:59904\n\t" \
  "ds_read_b64_tr_b16 v[206:207], v55 offset:61056\n\t" \
  "s_waitcnt lgkmcnt(0)\n\t" \
  "ds_read_b64_tr_b16 v[208:209], v55 offset:64\n\t" \
  "ds_read_b64_tr_b16 v[210:211], v55 offset:1216\n\t" \
  "ds_read_b64_tr_b16 v[212:213], v55 offset:4672\n\t" \
  "ds_read_b64_tr_b16 v[214:215], v55 offset:5824\n\t" \
  "ds_read_b64_tr_b16 v[216:217], v55 offset:18496\n\t" \
  "ds_read_b64_tr_b16 v[218:219], v55 offset:19648\n\t" \
  "ds_read_b64_tr_b16 v[220:221], v55 offset:23104\n\t" \
  "ds_read_b64_tr_b16 v[222:223], v55 offset:24256\n\t" \
  "s_waitcnt lgkmcnt(0)\n\t" \
  "ds_read_b64_tr_b16 v[224:225], v55 offset:36928\n\t" \
  "ds_read_b64_tr_b16 v[226:227], v55 offset:38080\n\t" \
  "ds_read_b64_tr_b16 v[228:229], v55 offset:41536\n\t" \
  "ds_read_b64_tr_b16 v[230:231], v55 offset:42688\n\t" \
  "ds_read_b64_tr_b16 v[232:233], v55 offset:55360\n\t" \
  "ds_read_b64_tr_b16 v[234:235], v55 offset:56512\n\t" \
  "ds_read_b64_tr_b16 v[236:237], v55 offset:59968\n\t" \
  "ds_read_b64_tr_b16 v[238:239], v55 offset:61120\n\t" \
  "s_waitcnt lgkmcnt(0)\n\t" \
  "s_barrier\n\t" \
  "s_cmp_eq_u32 s69, 0\n\t" \
  "s_cbranch_scc0 Lrec_ob_%=\n\t" \
  "s_add_u32 s66, s88, 3145728\n\t" \
  "s_addc_u32 s67, s89, 0\n\t" \
  "s_lshl_b32 s55, s70, 12\n\t" \
  "s_add_u32 m0, s55, 0\n\t" \
  "s_nop 0\n\t" \
  "global_load_lds_dwordx4 v21, s[66:67]\n\t" \
  "s_add_u32 m0, s55, 1024\n\t" \
  "s_nop 0\n\t" \
  "global_load_lds_dwordx4 v22, s[66:67]\n\t" \
  "s_add_u32 m0, s55, 2048\n\t" \
  "s_nop 0\n\t" \
  "global_load_lds_dwordx4 v23, s[66:67]\n\t" \
  "s_add_u32 m0, s55, 3072\n\t" \
  "s_nop 0\n\t" \
  "global_load_lds_dwordx4 v24, s[66:67]\n\t" \
  "s_add_u32 s66, s88, 3670016\n\t" \
  "s_addc_u32 s67, s89, 0\n\t" \
  "s_lshl_b32 s55, s70, 12\n\t" \
  "s_add_u32 m0, s55, 16384\n\t" \
  "s_nop 0\n\t" \
  "global_load_lds_dwordx4 v21, s[66:67]\n\t" \
  "s_add_u32 m0, s55, 17408\n\t" \
  "s_nop 0\n\t" \
  "global_load_lds_dwordx4 v22, s[66:67]\n\t" \
  "s_add_u32 m0, s55, 18432\n\t" \
  "s_nop 0\n\t" \
  "global_load_lds_dwordx4 v23, s[66:67]\n\t" \
  "s_add_u32 m0, s55, 19456\n\t" \
  "s_nop 0\n\t" \
  "global_load_lds_dwordx4 v24, s[66:67]\n\t" \
  "s_add_u32 s66, s88, 4194304\n\t" \
  "s_addc_u32 s67, s89, 0\n\t" \
  "s_lshl_b32 s55, s70, 12\n\t" \
  "s_add_u32 m0, s55, 32768\n\t" \
  "s_nop 0\n\t" \
  "global_load_lds_dwordx4 v21, s[66:67]\n\t" \
  "s_add_u32 m0, s55, 33792\n\t" \
  "s_nop 0\n\t" \
  "global_load_lds_dwordx4 v22, s[66:67]\n\t" \
  "s_add_u32 m0, s55, 34816\n\t" \
  "s_nop 0\n\t" \
  "global_load_lds_dwordx4 v23, s[66:67]\n\t" \
  "s_add_u32 m0, s55, 35840\n\t" \
  "s_nop 0\n\t" \
  "global_load_lds_dwordx4 v24, s[66:67]\n\t" \
  "s_add_u32 s66, s90, 1048576\n\t" \
  "s_addc_u32 s67, s91, 0\n\t" \
  "s_lshl_b32 s55, s70, 12\n\t" \
  "s_add_u32 m0, s55, 49152\n\t" \
  "s_nop 0\n\t" \
  "global_load_lds_dwordx4 v21, s[66:67]\n\t" \
  "s_add_u32 m0, s55, 50176\n\t" \
  "s_nop 0\n\t" \
  "global_load_lds_dwordx4 v22, s[66:67]\n\t" \
  "s_add_u32 m0, s55, 51200\n\t" \
  "s_nop 0\n\t" \
  "global_load_lds_dwordx4 v23, s[66:67]\n\t" \
  "s_add_u32 m0, s55, 52224\n\t" \
  "s_nop 0\n\t" \
  "global_load_lds_dwordx4 v24, s[66:67]\n\t" \
  "s_add_u32 s66, s90, 1572864\n\t" \
  "s_addc_u32 s67, s91, 0\n\t" \
  "s_lshl_b32 s55, s70, 12\n\t" \
  "s_add_u32 m0, s55, 65536\n\t" \
  "s_nop 0\n\t" \
  "global_load_lds_dwordx4 v21, s[66:67]\n\t" \
  "s_add_u32 m0, s55, 66560\n\t" \
  "s_nop 0\n\t" \
  "global_load_lds_dwordx4 v22, s[66:67]\n\t" \
  "s_add_u32 m0, s55, 67584\n\t" \
  "s_nop 0\n\t" \
  "global_load_lds_dwordx4 v23, s[66:67]\n\t" \
  "s_add_u32 m0, s55, 68608\n\t" \
  "s_nop 0\n\t" \
  "global_load_lds_dwordx4 v24, s[66:67]\n\t" \
  "s_waitcnt vmcnt(32)\n\t" \
  "s_barrier\n\t" \
  "v_xor_b32 v26, 0, v25\n\t" \
  "v_add_u32 v26, 81920, v26\n\t" \
  "ds_read_b64_tr_b16 a[0:1], v26 offset:0\n\t" \
  "ds_read_b64_tr_b16 a[2:3], v26 offset:512\n\t" \
  "ds_read_b64_tr_b16 a[4:5], v26 offset:8192\n\t" \
  "ds_read_b64_tr_b16 a[6:7], v26 offset:8704\n\t" \
  "v_xor_b32 v26, 32, v25\n\t" \
  "v_add_u32 v26, 81920, v26\n\t" \
  "ds_read_b64_tr_b16 a[8:9], v26 offset:0\n\t" \
  "ds_read_b64_tr_b16 a[10:11], v26 offset:512\n\t" \
  "ds_read_b64_tr_b16 a[12:13], v26 offset:8192\n\t" \
  "ds_read_b64_tr_b16 a[14:15], v26 offset:8704\n\t" \
  "s_waitcnt lgkmcnt(0)\n\t" \
  "s_barrier\n\t" \
  "s_add_u32 s66, s90, 2097152\n\t" \
  "s_addc_u32 s67, s91, 0\n\t" \
  "s_lshl_b32 s55, s70, 12\n\t" \
  "s_add_u32 m0, s55, 81920\n\t" \
  "s_nop 0\n\t" \
  "global_load_lds_dwordx4 v21, s[66:67]\n\t" \
  "s_add_u32 m0, s55, 82944\n\t" \
  "s_nop 0\n\t" \
  "global_load_lds_dwordx4 v22, s[66:67]\n\t" \
  "s_add_u32 m0, s55, 83968\n\t" \
  "s_nop 0\n\t" \
  "global_load_lds_dwordx4 v23, s[66:67]\n\t" \
  "s_add_u32 m0, s55, 84992\n\t" \
  "s_nop 0\n\t" \
  "global_load_lds_dwordx4 v24, s[66:67]\n\t" \
  "s_waitcnt vmcnt(32)\n\t" \
  "s_barrier\n\t" \
  "v_xor_b32 v26, 0, v25\n\t" \
  "v_add_u32 v26, 98304, v26\n\t" \
  "ds_read_b64_tr_b16 a[16:17], v26 offset:0\n\t" \
  "ds_read_b64_tr_b16 a[18:19], v26 offset:512\n\t" \
  "ds_read_b64_tr_b16 a[20:21], v26 offset:8192\n\t" \
  "ds_read_b64_tr_b16 a[22:23], v26 offset:8704\n\t" \
  "v_xor_b32 v26, 32, v25\n\t" \
  "v_add_u32 v26, 98304, v26\n\t" \
  "ds_read_b64_tr_b16 a[24:25], v26 offset:0\n\t" \
  "ds_read_b64_tr_b16 a[26:27], v26 offset:512\n\t" \
  "ds_read_b64_tr_b16 a[28:29], v26 offset:8192\n\t" \
  "ds_read_b64_tr_b16 a[30:31], v26 offset:8704\n\t" \
  "s_waitcnt lgkmcnt(0)\n\t" \
  "s_barrier\n\t" \
  "s_add_u32 s66, s90, 2621440\n\t" \
  "s_addc_u32 s67, s91, 0\n\t" \
  "s_lshl_b32 s55, s70, 12\n\t" \
  "s_add_u32 m0, s55, 98304\n\t" \
  "s_nop 0\n\t" \
  "global_load_lds_dwordx4 v21, s[66:67]\n\t" \
  "s_add_u32 m0, s55, 99328\n\t" \
  "s_nop 0\n\t" \
  "global_load_lds_dwordx4 v22, s[66:67]\n\t" \
  "s_add_u32 m0, s55, 100352\n\t" \
  "s_nop 0\n\t" \
  "global_load_lds_dwordx4 v23, s[66:67]\n\t" \
  "s_add_u32 m0, s55, 101376\n\t" \
  "s_nop 0\n\t" \
  "global_load_lds_dwordx4 v24, s[66:67]\n\t" \
  "s_waitcnt vmcnt(32)\n\t" \
  "s_barrier\n\t" \
  "v_xor_b32 v26, 0, v25\n\t" \
  "v_add_u32 v26, 114688, v26\n\t" \
  "ds_read_b64_tr_b16 a[32:33], v26 offset:0\n\t" \
  "ds_read_b64_tr_b16 a[34:35], v26 offset:512\n\t" \
  "ds_read_b64_tr_b16 a[36:37], v26 offset:8192\n\t" \
  "ds_read_b64_tr_b16 a[38:39], v26 offset:8704\n\t" \
  "v_xor_b32 v26, 32, v25\n\t" \
  "v_add_u32 v26, 114688, v26\n\t" \
  "ds_read_b64_tr_b16 a[40:41], v26 offset:0\n\t" \
  "ds_read_b64_tr_b16 a[42:43], v26 offset:512\n\t" \
  "ds_read_b64_tr_b16 a[44:45], v26 offset:8192\n\t" \
  "ds_read_b64_tr_b16 a[46:47], v26 offset:8704\n\t" \
  "s_waitcnt lgkmcnt(0)\n\t" \
  "s_barrier\n\t" \
  "s_add_u32 s66, s90, 3145728\n\t" \
  "s_addc_u32 s67, s91, 0\n\t" \
  "s_lshl_b32 s55, s70, 12\n\t" \
  "s_add_u32 m0, s55, 114688\n\t" \
  "s_nop 0\n\t" \
  "global_load_lds_dwordx4 v21, s[66:67]\n\t" \
  "s_add_u32 m0, s55, 115712\n\t" \
  "s_nop 0\n\t" \
  "global_load_lds_dwordx4 v22, s[66:67]\n\t" \
  "s_add_u32 m0, s55, 116736\n\t" \
  "s_nop 0\n\t" \
  "global_load_lds_dwordx4 v23, s[66:67]\n\t" \
  "s_add_u32 m0, s55, 117760\n\t" \
  "s_nop 0\n\t" \
  "global_load_lds_dwordx4 v24, s[66:67]\n\t" \
  "s_waitcnt vmcnt(28)\n\t" \
  "s_barrier\n\t" \
  "v_xor_b32 v26, 0, v25\n\t" \
  "v_add_u32 v26, 0, v26\n\t" \
  "ds_read_b64_tr_b16 a[48:49], v26 offset:0\n\t" \
  "ds_read_b64_tr_b16 a[50:51], v26 offset:512\n\t" \
  "ds_read_b64_tr_b16 a[52:53], v26 offset:8192\n\t" \
  "ds_read_b64_tr_b16 a[54:55], v26 offset:8704\n\t" \
  "v_xor_b32 v26, 32, v25\n\t" \
  "v_add_u32 v26, 0, v26\n\t" \
  "ds_read_b64_tr_b16 a[56:57], v26 offset:0\n\t" \
  "ds_read_b64_tr_b16 a[58:59], v26 offset:512\n\t" \
  "ds_read_b64_tr_b16 a[60:61], v26 offset:8192\n\t" \
  "ds_read_b64_tr_b16 a[62:63], v26 offset:8704\n\t" \
  "s_waitcnt lgkmcnt(0)\n\t" \
  "s_barrier\n\t" \
  "s_add_u32 s66, s90, 3670016\n\t" \
  "s_addc_u32 s67, s91, 0\n\t" \
  "s_lshl_b32 s55, s70, 12\n\t" \
  "s_add_u32 m0, s55, 0\n\t" \
  "s_nop 0\n\t" \
  "global_load_lds_dwordx4 v21, s[66:67]\n\t" \
  "s_add_u32 m0, s55, 1024\n\t" \
  "s_nop 0\n\t" \
  "global_load_lds_dwordx4 v22, s[66:67]\n\t" \
  "s_add_u32 m0, s55, 2048\n\t" \
  "s_nop 0\n\t" \
  "global_load_lds_dwordx4 v23, s[66:67]\n\t" \
  "s_add_u32 m0, s55, 3072\n\t" \
  "s_nop 0\n\t" \
  "global_load_lds_dwordx4 v24, s[66:67]\n\t" \
  "v_xor_b32 v26, 0, v25\n\t" \
  "v_add_u32 v26, 131072, v26\n\t" \
  "ds_read_b64_tr_b16 v[0:1], v26 offset:0\n\t" \
  "ds_read_b64_tr_b16 v[2:3], v26 offset:512\n\t" \
  "ds_read_b64_tr_b16 v[4:5], v26 offset:8192\n\t" \
  "ds_read_b64_tr_b16 v[6:7], v26 offset:8704\n\t" \
  "v_xor_b32 v26, 32, v25\n\t" \
  "v_add_u32 v26, 131072, v26\n\t" \
  "ds_read_b64_tr_b16 v[168:169], v26 offset:0\n\t" \
  "ds_read_b64_tr_b16 v[170:171], v26 offset:512\n\t" \
  "ds_read_b64_tr_b16 v[172:173], v26 offset:8192\n\t" \
  "ds_read_b64_tr_b16 v[174:175], v26 offset:8704\n\t" \
  "s_waitcnt lgkmcnt(0)\n\t" \
  "s_barrier\n\t" \
  "ds_write_b128 v125, v[0:3] offset:0\n\t" \
  "ds_write_b128 v125, v[4:7] offset:1024\n\t" \
  "s_waitcnt lgkmcnt(0)\n\t" \
  "v_mov_b32 v134, 0\n\t" \
  "s_mov_b32 s72, 58255\n\t" \
  "v_add_u32 v33, 0, v96\n\t" \
  "v_mul_u32_u24 v56, s72, v33\n\t" \
  "v_lshrrev_b32 v56, 22, v56\n\t" \
  "v_mul_u32_u24 v57, 72, v56\n\t" \
  "v_sub_u32 v57, v33, v57\n\t" \
  "v_lshrrev_b32 v58, 1, v57\n\t" \
  "v_lshlrev_b32 v58, 8, v58\n\t" \
  "v_and_b32 v57, 1, v57\n\t" \
  "v_lshl_add_u32 v58, v57, 7, v58\n\t" \
  "v_lshl_add_u32 v58, v56, 4, v58\n\t" \
  "v_add_u32 v58, 139264, v58\n\t" \
  "v_cmp_gt_u32 vcc, 576, v33\n\t" \
  "s_nop 0\n\t" \
  "s_mov_b64 exec, vcc\n\t" \
  "ds_write_b128 v58, v[84:87]\n\t" \
  "s_mov_b64 exec, -1\n\t" \
  "v_lshlrev_b32 v56, 16, v84\n\t" \
  "v_and_b32 v57, 0xffff0000, v84\n\t" \
  "v_max3_f32 v134, v134, |v56|, |v57|\n\t" \
  "v_lshlrev_b32 v56, 16, v85\n\t" \
  "v_and_b32 v57, 0xffff0000, v85\n\t" \
  "v_max3_f32 v134, v134, |v56|, |v57|\n\t" \
  "v_lshlrev_b32 v56, 16, v86\n\t" \
  "v_and_b32 v57, 0xffff0000, v86\n\t" \
  "v_max3_f32 v134, v134, |v56|, |v57|\n\t" \
  "v_lshlrev_b32 v56, 16, v87\n\t" \
  "v_and_b32 v57, 0xffff0000, v87\n\t" \
  "v_max3_f32 v134, v134, |v56|, |v57|\n\t" \
  "v_add_u32 v33, 256, v96\n\t" \
  "v_mul_u32_u24 v56, s72, v33\n\t" \
  "v_lshrrev_b32 v56, 22, v56\n\t" \
  "v_mul_u32_u24 v57, 72, v56\n\t" \
  "v_sub_u32 v57, v33, v57\n\t" \
  "v_lshrrev_b32 v58, 1, v57\n\t" \
  "v_lshlrev_b32 v58, 8, v58\n\t" \
  "v_and_b32 v57, 1, v57\n\t" \
  "v_lshl_add_u32 v58, v57, 7, v58\n\t" \
  "v_lshl_add_u32 v58, v56, 4, v58\n\t" \
  "v_add_u32 v58, 139264, v58\n\t" \
  "v_cmp_gt_u32 vcc, 576, v33\n\t" \
  "s_nop 0\n\t" \
  "s_mov_b64 exec, vcc\n\t" \
  "ds_write_b128 v58, v[88:91]\n\t" \
  "s_mov_b64 exec, -1\n\t" \
  "v_lshlrev_b32 v56, 16, v88\n\t" \
  "v_and_b32 v57, 0xffff0000, v88\n\t" \
  "v_max3_f32 v134, v134, |v56|, |v57|\n\t" \
  "v_lshlrev_b32 v56, 16, v89\n\t" \
  "v_and_b32 v57, 0xffff0000, v89\n\t" \
  "v_max3_f32 v134, v134, |v56|, |v57|\n\t" \
  "v_lshlrev_b32 v56, 16, v90\n\t" \
  "v_and_b32 v57, 0xffff0000, v90\n\t" \
  "v_max3_f32 v134, v134, |v56|, |v57|\n\t" \
  "v_lshlrev_b32 v56, 16, v91\n\t" \
  "v_and_b32 v57, 0xffff0000, v91\n\t" \
  "v_max3_f32 v134, v134, |v56|, |v57|\n\t" \
  "v_add_u32 v33, 512, v96\n\t" \
  "v_mul_u32_u24 v56, s72, v33\n\t" \
  "v_lshrrev_b32 v56, 22, v56\n\t" \
  "v_mul_u32_u24 v57, 72, v56\n\t" \
  "v_sub_u32 v57, v33, v57\n\t" \
  "v_lshrrev_b32 v58, 1, v57\n\t" \
  "v_lshlrev_b32 v58, 8, v58\n\t" \
  "v_and_b32 v57, 1, v57\n\t" \
  "v_lshl_add_u32 v58, v57, 7, v58\n\t" \
  "v_lshl_add_u32 v58, v56, 4, v58\n\t" \
  "v_add_u32 v58, 139264, v58\n\t" \
  "v_cmp_gt_u32 vcc, 576, v33\n\t" \
  "s_nop 0\n\t" \
  "s_mov_b64 exec, vcc\n\t" \
  "ds_write_b128 v58, v[92:95]\n\t" \
  "s_mov_b64 exec, -1\n\t" \
  "v_lshlrev_b32 v56, 16, v92\n\t" \
  "v_and_b32 v57, 0xffff0000, v92\n\t" \
  "v_max3_f32 v134, v134, |v56|, |v57|\n\t" \
  "v_lshlrev_b32 v56, 16, v93\n\t" \
  "v_and_b32 v57, 0xffff0000, v93\n\t" \
  "v_max3_f32 v134, v134, |v56|, |v57|\n\t" \
  "v_lshlrev_b32 v56, 16, v94\n\t" \
  "v_and_b32 v57, 0xffff0000, v94\n\t" \
  "v_max3_f32 v134, v134, |v56|, |v57|\n\t" \
  "v_lshlrev_b32 v56, 16, v95\n\t" \
  "v_and_b32 v57, 0xffff0000, v95\n\t" \
  "v_max3_f32 v134, v134, |v56|, |v57|\n\t" \
  "s_waitcnt vmcnt(28)\n\t" \
  "s_barrier\n\t" \
  "v_xor_b32 v26, 0, v25\n\t" \
  "v_add_u32 v26, 16384, v26\n\t" \
  "ds_read_b64_tr_b16 a[64:65], v26 offset:0\n\t" \
  "ds_read_b64_tr_b16 a[66:67], v26 offset:512\n\t" \
  "ds_read_b64_tr_b16 a[68:69], v26 offset:8192\n\t" \
  "ds_read_b64_tr_b16 a[70:71], v26 offset:8704\n\t" \
  "v_xor_b32 v26, 32, v25\n\t" \
  "v_add_u32 v26, 16384, v26\n\t" \
  "ds_read_b64_tr_b16 a[72:73], v26 offset:0\n\t" \
  "ds_read_b64_tr_b16 a[74:75], v26 offset:512\n\t" \
  "ds_read_b64_tr_b16 a[76:77], v26 offset:8192\n\t" \
  "ds_read_b64_tr_b16 a[78:79], v26 offset:8704\n\t" \
  "s_waitcnt lgkmcnt(0)\n\t" \
  "s_barrier\n\t" \
  "s_add_u32 s66, s90, 4194304\n\t" \
  "s_addc_u32 s67, s91, 0\n\t" \
  "s_lshl_b32 s55, s70, 12\n\t" \
  "s_add_u32 m0, s55, 16384\n\t" \
  "s_nop 0\n\t" \
  "global_load_lds_dwordx4 v21, s[66:67]\n\t" \
  "s_add_u32 m0, s55, 17408\n\t" \
  "s_nop 0\n\t" \
  "global_load_lds_dwordx4 v22, s[66:67]\n\t" \
  "s_add_u32 m0, s55, 18432\n\t" \
  "s_nop 0\n\t" \
  "global_load_lds_dwordx4 v23, s[66:67]\n\t" \
  "s_add_u32 m0, s55, 19456\n\t" \
  "s_nop 0\n\t" \
  "global_load_lds_dwordx4 v24, s[66:67]\n\t" \
  "s_waitcnt vmcnt(28)\n\t" \
  "s_barrier\n\t" \
  "v_xor_b32 v26, 0, v25\n\t" \
  "v_add_u32 v26, 32768, v26\n\t" \
  "ds_read_b64_tr_b16 a[80:81], v26 offset:0\n\t" \
  "ds_read_b64_tr_b16 a[82:83], v26 offset:512\n\t" \
  "ds_read_b64_tr_b16 a[84:85], v26 offset:8192\n\t" \
  "ds_read_b64_tr_b16 a[86:87], v26 offset:8704\n\t" \
  "v_xor_b32 v26, 32, v25\n\t" \
  "v_add_u32 v26, 32768, v26\n\t" \
  "ds_read_b64_tr_b16 a[88:89], v26 offset:0\n\t" \
  "ds_read_b64_tr_b16 a[90:91], v26 offset:512\n\t" \
  "ds_read_b64_tr_b16 a[92:93], v26 offset:8192\n\t" \
  "ds_read_b64_tr_b16 a[94:95], v26 offset:8704\n\t" \
  "s_waitcnt lgkmcnt(0)\n\t" \
  "s_barrier\n\t" \
  "s_add_u32 s66, s92, 0\n\t" \
  "s_addc_u32 s67, s93, 0\n\t" \
  "s_lshl_b32 s55, s70, 12\n\t" \
  "s_add_u32 m0, s55, 32768\n\t" \
  "s_nop 0\n\t" \
  "global_load_lds_dwordx4 v21, s[66:67]\n\t" \
  "s_add_u32 m0, s55, 33792\n\t" \
  "s_nop 0\n\t" \
  "global_load_lds_dwordx4 v22, s[66:67]\n\t" \
  "s_add_u32 m0, s55, 34816\n\t" \
  "s_nop 0\n\t" \
  "global_load_lds_dwordx4 v23, s[66:67]\n\t" \
  "s_add_u32 m0, s55, 35840\n\t" \
  "s_nop 0\n\t" \
  "global_load_lds_dwordx4 v24, s[66:67]\n\t" \
  "buffer_load_dword v69, off, s[36:39], s61 sc1\n\t" \
  "s_waitcnt vmcnt(28)\n\t" \
  "s_barrier\n\t" \
  "v_xor_b32 v26, 0, v25\n\t" \
  "v_add_u32 v26, 49152, v26\n\t" \
  "ds_read_b64_tr_b16 a[96:97], v26 offset:0\n\t" \
  "ds_read_b64_tr_b16 a[98:99], v26 offset:512\n\t" \
  "ds_read_b64_tr_b16 a[100:101], v26 offset:8192\n\t" \
  "ds_read_b64_tr_b16 a[102:103], v26 offset:8704\n\t" \
  "v_xor_b32 v26, 32, v25\n\t" \
  "v_add_u32 v26, 49152, v26\n\t" \
  "ds_read_b64_tr_b16 a[104:105], v26 offset:0\n\t" \
  "ds_read_b64_tr_b16 a[106:107], v26 offset:512\n\t" \
  "ds_read_b64_tr_b16 a[108:109], v26 offset:8192\n\t" \
  "ds_read_b64_tr_b16 a[110:111], v26 offset:8704\n\t" \
  "s_waitcnt lgkmcnt(0)\n\t" \
  "s_barrier\n\t" \
  "s_add_u32 s66, s92, 524288\n\t" \
  "s_addc_u32 s67, s93, 0\n\t" \
  "s_lshl_b32 s55, s70, 12\n\t" \
  "s_add_u32 m0, s55, 49152\n\t" \
  "s_nop 0\n\t" \
  "global_load_lds_dwordx4 v21, s[66:67]\n\t" \
  "s_add_u32 m0, s55, 50176\n\t" \
  "s_nop 0\n\t" \
  "global_load_lds_dwordx4 v22, s[66:67]\n\t" \
  "s_add_u32 m0, s55, 51200\n\t" \
  "s_nop 0\n\t" \
  "global_load_lds_dwordx4 v23, s[66:67]\n\t" \
  "s_add_u32 m0, s55, 52224\n\t" \
  "s_nop 0\n\t" \
  "global_load_lds_dwordx4 v24, s[66:67]\n\t" \
  "s_waitcnt vmcnt(28)\n\t" \
  "s_barrier\n\t" \
  "v_xor_b32 v26, 0, v25\n\t" \
  "v_add_u32 v26, 65536, v26\n\t" \
  "ds_read_b64_tr_b16 a[112:113], v26 offset:0\n\t" \
  "ds_read_b64_tr_b16 a[114:115], v26 offset:512\n\t" \
  "ds_read_b64_tr_b16 a[116:117], v26 offset:8192\n\t" \
  "ds_read_b64_tr_b16 a[118:119], v26 offset:8704\n\t" \
  "v_xor_b32 v26, 32, v25\n\t" \
  "v_add_u32 v26, 65536, v26\n\t" \
  "ds_read_b64_tr_b16 a[120:121], v26 offset:0\n\t" \
  "ds_read_b64_tr_b16 a[122:123], v26 offset:512\n\t" \
  "ds_read_b64_tr_b16 a[124:125], v26 offset:8192\n\t" \
  "ds_read_b64_tr_b16 a[126:127], v26 offset:8704\n\t" \
  "s_waitcnt lgkmcnt(0)\n\t" \
  "s_barrier\n\t" \
  "s_add_u32 s66, s92, 1048576\n\t" \
  "s_addc_u32 s67, s93, 0\n\t" \
  "s_lshl_b32 s55, s70, 12\n\t" \
  "s_add_u32 m0, s55, 65536\n\t" \
  "s_nop 0\n\t" \
  "global_load_lds_dwordx4 v21, s[66:67]\n\t" \
  "s_add_u32 m0, s55, 66560\n\t" \
  "s_nop 0\n\t" \
  "global_load_lds_dwordx4 v22, s[66:67]\n\t" \
  "s_add_u32 m0, s55, 67584\n\t" \
  "s_nop 0\n\t" \
  "global_load_lds_dwordx4 v23, s[66:67]\n\t" \
  "s_add_u32 m0, s55, 68608\n\t" \
  "s_nop 0\n\t" \
  "global_load_lds_dwordx4 v24, s[66:67]\n\t" \
  "s_waitcnt vmcnt(28)\n\t" \
  "s_barrier\n\t" \
  "v_xor_b32 v26, 0, v25\n\t" \
  "v_add_u32 v26, 81920, v26\n\t" \
  "ds_read_b64_tr_b16 a[128:129], v26 offset:0\n\t" \
  "ds_read_b64_tr_b16 a[130:131], v26 offset:512\n\t" \
  "ds_read_b64_tr_b16 a[132:133], v26 offset:8192\n\t" \
  "ds_read_b64_tr_b16 a[134:135], v26 offset:8704\n\t" \
  "v_xor_b32 v26, 32, v25\n\t" \
  "v_add_u32 v26, 81920, v26\n\t" \
  "ds_read_b64_tr_b16 a[136:137], v26 offset:0\n\t" \
  "ds_read_b64_tr_b16 a[138:139], v26 offset:512\n\t" \
  "ds_read_b64_tr_b16 a[140:141], v26 offset:8192\n\t" \
  "ds_read_b64_tr_b16 a[142:143], v26 offset:8704\n\t" \
  "s_waitcnt lgkmcnt(0)\n\t" \
  "s_barrier\n\t" \
  "s_add_u32 s66, s92, 1572864\n\t" \
  "s_addc_u32 s67, s93, 0\n\t" \
  "s_lshl_b32 s55, s70, 12\n\t" \
  "s_add_u32 m0, s55, 81920\n\t" \
  "s_nop 0\n\t" \
  "global_load_lds_dwordx4 v21, s[66:67]\n\t" \
  "s_add_u32 m0, s55, 82944\n\t" \
  "s_nop 0\n\t" \
  "global_load_lds_dwordx4 v22, s[66:67]\n\t" \
  "s_add_u32 m0, s55, 83968\n\t" \
  "s_nop 0\n\t" \
  "global_load_lds_dwordx4 v23, s[66:67]\n\t" \
  "s_add_u32 m0, s55, 84992\n\t" \
  "s_nop 0\n\t" \
  "global_load_lds_dwordx4 v24, s[66:67]\n\t" \
  "s_waitcnt vmcnt(28)\n\t" \
  "s_barrier\n\t" \
  "v_xor_b32 v26, 0, v25\n\t" \
  "v_add_u32 v26, 98304, v26\n\t" \
  "ds_read_b64_tr_b16 a[144:145], v26 offset:0\n\t" \
  "ds_read_b64_tr_b16 a[146:147], v26 offset:512\n\t" \
  "ds_read_b64_tr_b16 a[148:149], v26 offset:8192\n\t" \
  "ds_read_b64_tr_b16 a[150:151], v26 offset:8704\n\t" \
  "v_xor_b32 v26, 32, v25\n\t" \
  "v_add_u32 v26, 98304, v26\n\t" \
  "ds_read_b64_tr_b16 a[152:153], v26 offset:0\n\t" \
  "ds_read_b64_tr_b16 a[154:155], v26 offset:512\n\t" \
  "ds_read_b64_tr_b16 a[156:157], v26 offset:8192\n\t" \
  "ds_read_b64_tr_b16 a[158:159], v26 offset:8704\n\t" \
  "s_waitcnt lgkmcnt(0)\n\t" \
  "s_barrier\n\t" \
  "s_add_u32 s66, s92, 2097152\n\t" \
  "s_addc_u32 s67, s93, 0\n\t" \
  "s_lshl_b32 s55, s70, 12\n\t" \
  "s_add_u32 m0, s55, 98304\n\t" \
  "s_nop 0\n\t" \
  "global_load_lds_dwordx4 v21, s[66:67]\n\t" \
  "s_add_u32 m0, s55, 99328\n\t" \
  "s_nop 0\n\t" \
  "global_load_lds_dwordx4 v22, s[66:67]\n\t" \
  "s_add_u32 m0, s55, 100352\n\t" \
  "s_nop 0\n\t" \
  "global_load_lds_dwordx4 v23, s[66:67]\n\t" \
  "s_add_u32 m0, s55, 101376\n\t" \
  "s_nop 0\n\t" \
  "global_load_lds_dwordx4 v24, s[66:67]\n\t" \
  "s_waitcnt vmcnt(28)\n\t" \
  "s_barrier\n\t" \
  "v_xor_b32 v26, 0, v25\n\t" \
  "v_add_u32 v26, 114688, v26\n\t" \
  "ds_read_b64_tr_b16 a[160:161], v26 offset:0\n\t" \
  "ds_read_b64_tr_b16 a[162:163], v26 offset:512\n\t" \
  "ds_read_b64_tr_b16 a[164:165], v26 offset:8192\n\t" \
  "ds_read_b64_tr_b16 a[166:167], v26 offset:8704\n\t" \
  "v_xor_b32 v26, 32, v25\n\t" \
  "v_add_u32 v26, 114688, v26\n\t" \
  "ds_read_b64_tr_b16 a[168:169], v26 offset:0\n\t" \
  "ds_read_b64_tr_b16 a[170:171], v26 offset:512\n\t" \
  "ds_read_b64_tr_b16 a[172:173], v26 offset:8192\n\t" \
  "ds_read_b64_tr_b16 a[174:175], v26 offset:8704\n\t" \
  "s_waitcnt lgkmcnt(0)\n\t" \
  "s_barrier\n\t" \
  "s_add_u32 s66, s92, 4194304\n\t" \
  "s_addc_u32 s67, s93, 0\n\t" \
  "s_lshl_b32 s55, s70, 12\n\t" \
  "s_add_u32 m0, s55, 114688\n\t" \
  "s_nop 0\n\t" \
  "global_load_lds_dwordx4 v21, s[66:67]\n\t" \
  "s_add_u32 m0, s55, 115712\n\t" \
  "s_nop 0\n\t" \
  "global_load_lds_dwordx4 v22, s[66:67]\n\t" \
  "s_add_u32 m0, s55, 116736\n\t" \
  "s_nop 0\n\t" \
  "global_load_lds_dwordx4 v23, s[66:67]\n\t" \
  "s_add_u32 m0, s55, 117760\n\t" \
  "s_nop 0\n\t" \
  "global_load_lds_dwordx4 v24, s[66:67]\n\t" \
  "s_waitcnt vmcnt(28)\n\t" \
  "s_barrier\n\t" \
  "v_xor_b32 v26, 0, v25\n\t" \
  "v_add_u32 v26, 0, v26\n\t" \
  "ds_read_b64_tr_b16 a[176:177], v26 offset:0\n\t" \
  "ds_read_b64_tr_b16 a[178:179], v26 offset:512\n\t" \
  "ds_read_b64_tr_b16 a[180:181], v26 offset:8192\n\t" \
  "ds_read_b64_tr_b16 a[182:183], v26 offset:8704\n\t" \
  "v_xor_b32 v26, 32, v25\n\t" \
  "v_add_u32 v26, 0, v26\n\t" \
  "ds_read_b64_tr_b16 a[184:185], v26 offset:0\n\t" \
  "ds_read_b64_tr_b16 a[186:187], v26 offset:512\n\t" \
  "ds_read_b64_tr_b16 a[188:189], v26 offset:8192\n\t" \
  "ds_read_b64_tr_b16 a[190:191], v26 offset:8704\n\t" \
  "s_waitcnt lgkmcnt(0)\n\t" \
  "s_barrier\n\t" \
  "s_add_u32 s66, s88, 0\n\t" \
  "s_addc_u32 s67, s89, 0\n\t" \
  "s_lshl_b32 s55, s70, 12\n\t" \
  "s_add_u32 m0, s55, 0\n\t" \
  "s_nop 0\n\t" \
  "global_load_lds_dwordx4 v21, s[66:67]\n\t" \
  "s_add_u32 m0, s55, 1024\n\t" \
  "s_nop 0\n\t" \
  "global_load_lds_dwordx4 v22, s[66:67]\n\t" \
  "s_add_u32 m0, s55, 2048\n\t" \
  "s_nop 0\n\t" \
  "global_load_lds_dwordx4 v23, s[66:67]\n\t" \
  "s_add_u32 m0, s55, 3072\n\t" \
  "s_nop 0\n\t" \
  "global_load_lds_dwordx4 v24, s[66:67]\n\t" \
  "s_waitcnt vmcnt(28)\n\t" \
  "s_barrier\n\t" \
  "v_xor_b32 v26, 0, v25\n\t" \
  "v_add_u32 v26, 16384, v26\n\t" \
  "ds_read_b64_tr_b16 a[192:193], v26 offset:0\n\t" \
  "ds_read_b64_tr_b16 a[194:195], v26 offset:512\n\t" \
  "ds_read_b64_tr_b16 a[196:197], v26 offset:8192\n\t" \
  "ds_read_b64_tr_b16 a[198:199], v26 offset:8704\n\t" \
  "v_xor_b32 v26, 32, v25\n\t" \
  "v_add_u32 v26, 16384, v26\n\t" \
  "ds_read_b64_tr_b16 a[200:201], v26 offset:0\n\t" \
  "ds_read_b64_tr_b16 a[202:203], v26 offset:512\n\t" \
  "ds_read_b64_tr_b16 a[204:205], v26 offset:8192\n\t" \
  "ds_read_b64_tr_b16 a[206:207], v26 offset:8704\n\t" \
  "s_waitcnt lgkmcnt(0)\n\t" \
  "s_barrier\n\t" \
  "s_add_u32 s66, s88, 524288\n\t" \
  "s_addc_u32 s67, s89, 0\n\t" \
  "s_lshl_b32 s55, s70, 12\n\t" \
  "s_add_u32 m0, s55, 16384\n\t" \
  "s_nop 0\n\t" \
  "global_load_lds_dwordx4 v21, s[66:67]\n\t" \
  "s_add_u32 m0, s55, 17408\n\t" \
  "s_nop 0\n\t" \
  "global_load_lds_dwordx4 v22, s[66:67]\n\t" \
  "s_add_u32 m0, s55, 18432\n\t" \
  "s_nop 0\n\t" \
  "global_load_lds_dwordx4 v23, s[66:67]\n\t" \
  "s_add_u32 m0, s55, 19456\n\t" \
  "s_nop 0\n\t" \
  "global_load_lds_dwordx4 v24, s[66:67]\n\t" \
  "s_waitcnt vmcnt(28)\n\t" \
  "s_barrier\n\t" \
  "v_xor_b32 v26, 0, v25\n\t" \
  "v_add_u32 v26, 32768, v26\n\t" \
  "ds_read_b64_tr_b16 a[208:209], v26 offset:0\n\t" \
  "ds_read_b64_tr_b16 a[210:211], v26 offset:512\n\t" \
  "ds_read_b64_tr_b16 a[212:213], v26 offset:8192\n\t" \
  "ds_read_b64_tr_b16 a[214:215], v26 offset:8704\n\t" \
  "v_xor_b32 v26, 32, v25\n\t" \
  "v_add_u32 v26, 32768, v26\n\t" \
  "ds_read_b64_tr_b16 a[216:217], v26 offset:0\n\t" \
  "ds_read_b64_tr_b16 a[218:219], v26 offset:512\n\t" \
  "ds_read_b64_tr_b16 a[220:221], v26 offset:8192\n\t" \
  "ds_read_b64_tr_b16 a[222:223], v26 offset:8704\n\t" \
  "s_waitcnt lgkmcnt(0)\n\t" \
  "s_barrier\n\t" \
  "s_add_u32 s66, s88, 1048576\n\t" \
  "s_addc_u32 s67, s89, 0\n\t" \
  "s_lshl_b32 s55, s70, 12\n\t" \
  "s_add_u32 m0, s55, 32768\n\t" \
  "s_nop 0\n\t" \
  "global_load_lds_dwordx4 v21, s[66:67]\n\t" \
  "s_add_u32 m0, s55, 33792\n\t" \
  "s_nop 0\n\t" \
  "global_load_lds_dwordx4 v22, s[66:67]\n\t" \
  "s_add_u32 m0, s55, 34816\n\t" \
  "s_nop 0\n\t" \
  "global_load_lds_dwordx4 v23, s[66:67]\n\t" \
  "s_add_u32 m0, s55, 35840\n\t" \
  "s_nop 0\n\t" \
  "global_load_lds_dwordx4 v24, s[66:67]\n\t" \
  "s_mov_b32 s58, 0\n\t" \
  "Lrec_h0_%=:\n\t" \
  "v_cmp_eq_u32 vcc, 0, v69\n\t" \
  "s_nop 0\n\t" \
  "s_cbranch_vccz Lrec_h1_%=\n\t" \
  "s_add_u32 s58, s58, 1\n\t" \
  "s_cmp_gt_u32 s58, 524288\n\t" \
  "s_cbranch_scc1 Lrec_end_%=\n\t" \
  "s_sleep 2\n\t" \
  "buffer_load_dword v69, off, s[36:39], s61 sc1\n\t" \
  "s_waitcnt vmcnt(0)\n\t" \
  "s_branch Lrec_h0_%=\n\t" \
  "Lrec_h1_%=:\n\t" \
  "s_sub_u32 s61, s61, 32768\n\t" \
  "s_mov_b64 exec, s[94:95]\n\t" \
  "buffer_store_dword v69, off, s[36:39], s61 sc1\n\t" \
  "s_mov_b64 exec, -1\n\t" \
  "v_lshlrev_b32 v68, 2, v16\n\t" \
  "s_mov_b32 s55, 61440\n\t" \
  "buffer_load_dword v68, v68, s[36:39], s55 offen sc1\n\t" \
  "s_mov_b32 s57, 0\n\t" \
  "v_mbcnt_lo_u32_b32 v135, -1, 0\n\t" \
  "v_mbcnt_hi_u32_b32 v135, -1, v135\n\t" \
  "v_lshlrev_b32 v135, 2, v135\n\t" \
  "buffer_load_dword v135, v135, s[36:39], s57 offen sc1\n\t" \
  "s_waitcnt vmcnt(28)\n\t" \
  "s_barrier\n\t" \
  "v_xor_b32 v26, 0, v25\n\t" \
  "v_add_u32 v26, 49152, v26\n\t" \
  "ds_read_b64_tr_b16 a[224:225], v26 offset:0\n\t" \
  "ds_read_b64_tr_b16 a[226:227], v26 offset:512\n\t" \
  "ds_read_b64_tr_b16 a[228:229], v26 offset:8192\n\t" \
  "ds_read_b64_tr_b16 a[230:231], v26 offset:8704\n\t" \
  "v_xor_b32 v26, 32, v25\n\t" \
  "v_add_u32 v26, 49152, v26\n\t" \
  "ds_read_b64_tr_b16 a[232:233], v26 offset:0\n\t" \
  "ds_read_b64_tr_b16 a[234:235], v26 offset:512\n\t" \
  "ds_read_b64_tr_b16 a[236:237], v26 offset:8192\n\t" \
  "ds_read_b64_tr_b16 a[238:239], v26 offset:8704\n\t" \
  "s_waitcnt lgkmcnt(0)\n\t" \
  "s_barrier\n\t" \
  "s_add_u32 s66, s90, 0\n\t" \
  "s_addc_u32 s67, s91, 0\n\t" \
  "s_lshl_b32 s55, s70, 12\n\t" \
  "s_add_u32 m0, s55, 49152\n\t" \
  "s_nop 0\n\t" \
  "global_load_lds_dwordx4 v21, s[66:67]\n\t" \
  "s_add_u32 m0, s55, 50176\n\t" \
  "s_nop 0\n\t" \
  "global_load_lds_dwordx4 v22, s[66:67]\n\t" \
  "s_add_u32 m0, s55, 51200\n\t" \
  "s_nop 0\n\t" \
  "global_load_lds_dwordx4 v23, s[66:67]\n\t" \
  "s_add_u32 m0, s55, 52224\n\t" \
  "s_nop 0\n\t" \
  "global_load_lds_dwordx4 v24, s[66:67]\n\t" \
  "s_waitcnt vmcnt(28)\n\t" \
  "s_barrier\n\t" \
  "v_xor_b32 v26, 0, v25\n\t" \
  "v_add_u32 v26, 65536, v26\n\t" \
  "ds_read_b64_tr_b16 a[240:241], v26 offset:0\n\t" \
  "ds_read_b64_tr_b16 a[242:243], v26 offset:512\n\t" \
  "ds_read_b64_tr_b16 a[244:245], v26 offset:8192\n\t" \
  "ds_read_b64_tr_b16 a[246:247], v26 offset:8704\n\t" \
  "v_xor_b32 v26, 32, v25\n\t" \
  "v_add_u32 v26, 65536, v26\n\t" \
  "ds_read_b64_tr_b16 a[248:249], v26 offset:0\n\t" \
  "ds_read_b64_tr_b16 a[250:251], v26 offset:512\n\t" \
  "ds_read_b64_tr_b16 a[252:253], v26 offset:8192\n\t" \
  "ds_read_b64_tr_b16 a[254:255], v26 offset:8704\n\t" \
  "s_waitcnt lgkmcnt(0)\n\t" \
  "s_barrier\n\t" \
  "s_add_u32 s66, s90, 524288\n\t" \
  "s_addc_u32 s67, s91, 0\n\t" \
  "s_lshl_b32 s55, s70, 12\n\t" \
  "s_add_u32 m0, s55, 65536\n\t" \
  "s_nop 0\n\t" \
  "global_load_lds_dwordx4 v21, s[66:67]\n\t" \
  "s_add_u32 m0, s55, 66560\n\t" \
  "s_nop 0\n\t" \
  "global_load_lds_dwordx4 v22, s[66:67]\n\t" \
  "s_add_u32 m0, s55, 67584\n\t" \
  "s_nop 0\n\t" \
  "global_load_lds_dwordx4 v23, s[66:67]\n\t" \
  "s_add_u32 m0, s55, 68608\n\t" \
  "s_nop 0\n\t" \
  "global_load_lds_dwordx4 v24, s[66:67]\n\t" \
  "s_waitcnt vmcnt(28)\n\t" \
  "s_barrier\n\t" \
  "v_xor_b32 v26, 0, v25\n\t" \
  "v_add_u32 v26, 81920, v26\n\t" \
  "ds_read_b64_tr_b16 v[136:137], v26 offset:0\n\t" \
  "ds_read_b64_tr_b16 v[138:139], v26 offset:512\n\t" \
  "ds_read_b64_tr_b16 v[140:141], v26 offset:8192\n\t" \
  "ds_read_b64_tr_b16 v[142:143], v26 offset:8704\n\t" \
  "v_xor_b32 v26, 32, v25\n\t" \
  "v_add_u32 v26, 81920, v26\n\t" \
  "ds_read_b64_tr_b16 v[144:145], v26 offset:0\n\t" \
  "ds_read_b64_tr_b16 v[146:147], v26 offset:512\n\t" \
  "ds_read_b64_tr_b16 v[148:149], v26 offset:8192\n\t" \
  "ds_read_b64_tr_b16 v[150:151], v26 offset:8704\n\t" \
  "s_waitcnt lgkmcnt(0)\n\t" \
  "s_barrier\n\t" \
  "s_add_u32 s66, s92, 3145728\n\t" \
  "s_addc_u32 s67, s93, 0\n\t" \
  "s_lshl_b32 s55, s70, 12\n\t" \
  "s_add_u32 m0, s55, 81920\n\t" \
  "s_nop 0\n\t" \
  "global_load_lds_dwordx4 v21, s[66:67]\n\t" \
  "s_add_u32 m0, s55, 82944\n\t" \
  "s_nop 0\n\t" \
  "global_load_lds_dwordx4 v22, s[66:67]\n\t" \
  "s_add_u32 m0, s55, 83968\n\t" \
  "s_nop 0\n\t" \
  "global_load_lds_dwordx4 v23, s[66:67]\n\t" \
  "s_add_u32 m0, s55, 84992\n\t" \
  "s_nop 0\n\t" \
  "global_load_lds_dwordx4 v24, s[66:67]\n\t" \
  "s_waitcnt vmcnt(28)\n\t" \
  "s_barrier\n\t" \
  "v_xor_b32 v26, 0, v25\n\t" \
  "v_add_u32 v26, 98304, v26\n\t" \
  "ds_read_b64_tr_b16 v[152:153], v26 offset:0\n\t" \
  "ds_read_b64_tr_b16 v[154:155], v26 offset:512\n\t" \
  "ds_read_b64_tr_b16 v[156:157], v26 offset:8192\n\t" \
  "ds_read_b64_tr_b16 v[158:159], v26 offset:8704\n\t" \
  "v_xor_b32 v26, 32, v25\n\t" \
  "v_add_u32 v26, 98304, v26\n\t" \
  "ds_read_b64_tr_b16 v[160:161], v26 offset:0\n\t" \
  "ds_read_b64_tr_b16 v[162:163], v26 offset:512\n\t" \
  "ds_read_b64_tr_b16 v[164:165], v26 offset:8192\n\t" \
  "ds_read_b64_tr_b16 v[166:167], v26 offset:8704\n\t" \
  "s_waitcnt lgkmcnt(0)\n\t" \
  "s_barrier\n\t" \
  "s_add_u32 s66, s92, 3670016\n\t" \
  "s_addc_u32 s67, s93, 0\n\t" \
  "s_lshl_b32 s55, s70, 12\n\t" \
  "s_add_u32 m0, s55, 98304\n\t" \
  "s_nop 0\n\t" \
  "global_load_lds_dwordx4 v21, s[66:67]\n\t" \
  "s_add_u32 m0, s55, 99328\n\t" \
  "s_nop 0\n\t" \
  "global_load_lds_dwordx4 v22, s[66:67]\n\t" \
  "s_add_u32 m0, s55, 100352\n\t" \
  "s_nop 0\n\t" \
  "global_load_lds_dwordx4 v23, s[66:67]\n\t" \
  "s_add_u32 m0, s55, 101376\n\t" \
  "s_nop 0\n\t" \
  "global_load_lds_dwordx4 v24, s[66:67]\n\t" \
  "s_waitcnt vmcnt(28)\n\t" \
  "s_barrier\n\t" \
  "v_xor_b32 v26, 0, v25\n\t" \
  "v_add_u32 v26, 114688, v26\n\t" \
  "ds_read_b64_tr_b16 v[0:1], v26 offset:0\n\t" \
  "ds_read_b64_tr_b16 v[2:3], v26 offset:512\n\t" \
  "ds_read_b64_tr_b16 v[4:5], v26 offset:8192\n\t" \
  "ds_read_b64_tr_b16 v[6:7], v26 offset:8704\n\t" \
  "v_xor_b32 v26, 32, v25\n\t" \
  "v_add_u32 v26, 114688, v26\n\t" \
  "ds_read_b64_tr_b16 v[8:9], v26 offset:0\n\t" \
  "ds_read_b64_tr_b16 v[10:11], v26 offset:512\n\t" \
  "ds_read_b64_tr_b16 v[12:13], v26 offset:8192\n\t" \
  "ds_read_b64_tr_b16 v[14:15], v26 offset:8704\n\t" \
  "s_waitcnt vmcnt(24) lgkmcnt(0)\n\t" \
  "s_barrier\n\t" \
  "v_xor_b32 v26, 0, v25\n\t" \
  "v_add_u32 v26, 0, v26\n\t" \
  "ds_read_b64_tr_b16 v[100:101], v26 offset:0\n\t" \
  "ds_read_b64_tr_b16 v[102:103], v26 offset:512\n\t" \
  "ds_read_b64_tr_b16 v[104:105], v26 offset:8192\n\t" \
  "ds_read_b64_tr_b16 v[106:107], v26 offset:8704\n\t" \
  "v_xor_b32 v26, 32, v25\n\t" \
  "v_add_u32 v26, 0, v26\n\t" \
  "ds_read_b64_tr_b16 v[108:109], v26 offset:0\n\t" \
  "ds_read_b64_tr_b16 v[110:111], v26 offset:512\n\t" \
  "ds_read_b64_tr_b16 v[112:113], v26 offset:8192\n\t" \
  "ds_read_b64_tr_b16 v[114:115], v26 offset:8704\n\t" \
  "ds_write_b128 v123, v[0:3] offset:49152\n\t" \
  "ds_write_b128 v123, v[4:7] offset:50176\n\t" \
  "ds_write_b128 v123, v[8:11] offset:51200\n\t" \
  "ds_write_b128 v123, v[12:15] offset:52224\n\t" \
  "s_waitcnt vmcnt(20) lgkmcnt(0)\n\t" \
  "s_barrier\n\t" \
  "v_xor_b32 v26, 0, v25\n\t" \
  "v_add_u32 v26, 16384, v26\n\t" \
  "ds_read_b64_tr_b16 v[0:1], v26 offset:0\n\t" \
  "ds_read_b64_tr_b16 v[2:3], v26 offset:512\n\t" \
  "ds_read_b64_tr_b16 v[4:5], v26 offset:8192\n\t" \
  "ds_read_b64_tr_b16 v[6:7], v26 offset:8704\n\t" \
  "v_xor_b32 v26, 32, v25\n\t" \
  "v_add_u32 v26, 16384, v26\n\t" \
  "ds_read_b64_tr_b16 v[8:9], v26 offset:0\n\t" \
  "ds_read_b64_tr_b16 v[10:11], v26 offset:512\n\t" \
  "ds_read_b64_tr_b16 v[12:13], v26 offset:8192\n\t" \
  "ds_read_b64_tr_b16 v[14:15], v26 offset:8704\n\t" \
  "ds_write_b128 v122, v[100:103] offset:0\n\t" \
  "ds_write_b128 v122, v[104:107] offset:1024\n\t" \
  "ds_write_b128 v122, v[108:111] offset:2048\n\t" \
  "ds_write_b128 v122, v[112:115] offset:3072\n\t" \
  "s_waitcnt vmcnt(16) lgkmcnt(0)\n\t" \
  "s_barrier\n\t" \
  "v_xor_b32 v26, 0, v25\n\t" \
  "v_add_u32 v26, 32768, v26\n\t" \
  "ds_read_b64_tr_b16 v[100:101], v26 offset:0\n\t" \
  "ds_read_b64_tr_b16 v[102:103], v26 offset:512\n\t" \
  "ds_read_b64_tr_b16 v[104:105], v26 offset:8192\n\t" \
  "ds_read_b64_tr_b16 v[106:107], v26 offset:8704\n\t" \
  "v_xor_b32 v26, 32, v25\n\t" \
  "v_add_u32 v26, 32768, v26\n\t" \
  "ds_read_b64_tr_b16 v[108:109], v26 offset:0\n\t" \
  "ds_read_b64_tr_b16 v[110:111], v26 offset:512\n\t" \
  "ds_read_b64_tr_b16 v[112:113], v26 offset:8192\n\t" \
  "ds_read_b64_tr_b16 v[114:115], v26 offset:8704\n\t" \
  "ds_write_b128 v122, v[0:3] offset:16384\n\t" \
  "ds_write_b128 v122, v[4:7] offset:17408\n\t" \
  "ds_write_b128 v122, v[8:11] offset:18432\n\t" \
  "ds_write_b128 v122, v[12:15] offset:19456\n\t" \
  "v_cmp_eq_u32 vcc, 0, v135\n\t" \
  "s_nop 0\n\t" \
  "s_cmp_eq_u64 vcc, 0\n\t" \
  "s_cselect_b32 s62, 1, 0\n\t" \
  "s_cbranch_scc0 Lrec_r0_%=\n\t" \
  "buffer_load_dwordx4 v[36:39], v125, s[24:27], s46 offen sc1\n\t" \
  "buffer_load_dwordx4 v[40:43], v125, s[24:27], s46 offen offset:1024 sc1\n\t" \
  "buffer_load_dwordx4 v[60:63], v125, s[28:31], s46 offen sc1\n\t" \
  "buffer_load_dwordx4 v[64:67], v125, s[28:31], s46 offen offset:1024 sc1\n\t" \
  "v_mov_b32 v70, v135\n\t" \
  "s_mov_b32 s57, 512\n\t" \
  "v_mbcnt_lo_u32_b32 v135, -1, 0\n\t" \
  "v_mbcnt_hi_u32_b32 v135, -1, v135\n\t" \
  "v_lshlrev_b32 v135, 2, v135\n\t" \
  "buffer_load_dword v135, v135, s[36:39], s57 offen sc1\n\t" \
  "v_lshlrev_b32 v56, 3, v16\n\t" \
  "s_mov_b32 s55, 63488\n\t" \
  "buffer_load_dwordx2 v[56:57], v56, s[36:39], s55 offen sc1\n\t" \
  "Lrec_r0_%=:\n\t" \
  "s_waitcnt vmcnt(12) lgkmcnt(0)\n\t" \
  "s_barrier\n\t" \
  "v_xor_b32 v26, 0, v25\n\t" \
  "v_add_u32 v26, 49152, v26\n\t" \
  "ds_read_b64_tr_b16 v[0:1], v26 offset:0\n\t" \
  "ds_read_b64_tr_b16 v[2:3], v26 offset:512\n\t" \
  "ds_read_b64_tr_b16 v[4:5], v26 offset:8192\n\t" \
  "ds_read_b64_tr_b16 v[6:7], v26 offset:8704\n\t" \
  "v_xor_b32 v26, 32, v25\n\t" \
  "v_add_u32 v26, 49152, v26\n\t" \
  "ds_read_b64_tr_b16 v[8:9], v26 offset:0\n\t" \
  "ds_read_b64_tr_b16 v[10:11], v26 offset:512\n\t" \
  "ds_read_b64_tr_b16 v[12:13], v26 offset:8192\n\t" \
  "ds_read_b64_tr_b16 v[14:15], v26 offset:8704\n\t" \
  "ds_write_b128 v122, v[100:103] offset:32768\n\t" \
  "ds_write_b128 v122, v[104:107] offset:33792\n\t" \
  "ds_write_b128 v122, v[108:111] offset:34816\n\t" \
  "ds_write_b128 v122, v[112:115] offset:35840\n\t" \
  "s_waitcnt vmcnt(8) lgkmcnt(0)\n\t" \
  "s_barrier\n\t" \
  "v_xor_b32 v26, 0, v25\n\t" \
  "v_add_u32 v26, 65536, v26\n\t" \
  "ds_read_b64_tr_b16 v[100:101], v26 offset:0\n\t" \
  "ds_read_b64_tr_b16 v[102:103], v26 offset:512\n\t" \
  "ds_read_b64_tr_b16 v[104:105], v26 offset:8192\n\t" \
  "ds_read_b64_tr_b16 v[106:107], v26 offset:8704\n\t" \
  "v_xor_b32 v26, 32, v25\n\t" \
  "v_add_u32 v26, 65536, v26\n\t" \
  "ds_read_b64_tr_b16 v[108:109], v26 offset:0\n\t" \
  "ds_read_b64_tr_b16 v[110:111], v26 offset:512\n\t" \
  "ds_read_b64_tr_b16 v[112:113], v26 offset:8192\n\t" \
  "ds_read_b64_tr_b16 v[114:115], v26 offset:8704\n\t" \
  "ds_write_b128 v122, v[0:3] offset:49152\n\t" \
  "ds_write_b128 v122, v[4:7] offset:50176\n\t" \
  "ds_write_b128 v122, v[8:11] offset:51200\n\t" \
  "ds_write_b128 v122, v[12:15] offset:52224\n\t" \
  "s_waitcnt vmcnt(4) lgkmcnt(0)\n\t" \
  "s_barrier\n\t" \
  "v_xor_b32 v26, 0, v25\n\t" \
  "v_add_u32 v26, 81920, v26\n\t" \
  "ds_read_b64_tr_b16 v[0:1], v26 offset:0\n\t" \
  "ds_read_b64_tr_b16 v[2:3], v26 offset:512\n\t" \
  "ds_read_b64_tr_b16 v[4:5], v26 offset:8192\n\t" \
  "ds_read_b64_tr_b16 v[6:7], v26 offset:8704\n\t" \
  "v_xor_b32 v26, 32, v25\n\t" \
  "v_add_u32 v26, 81920, v26\n\t" \
  "ds_read_b64_tr_b16 v[8:9], v26 offset:0\n\t" \
  "ds_read_b64_tr_b16 v[10:11], v26 offset:512\n\t" \
  "ds_read_b64_tr_b16 v[12:13], v26 offset:8192\n\t" \
  "ds_read_b64_tr_b16 v[14:15], v26 offset:8704\n\t" \
  "ds_write_b128 v123, v[100:103] offset:0\n\t" \
  "ds_write_b128 v123, v[104:107] offset:1024\n\t" \
  "ds_write_b128 v123, v[108:111] offset:2048\n\t" \
  "ds_write_b128 v123, v[112:115] offset:3072\n\t" \
  "s_waitcnt vmcnt(0) lgkmcnt(0)\n\t" \
  "s_barrier\n\t" \
  "v_xor_b32 v26, 0, v25\n\t" \
  "v_add_u32 v26, 98304, v26\n\t" \
  "ds_read_b64_tr_b16 v[100:101], v26 offset:0\n\t" \
  "ds_read_b64_tr_b16 v[102:103], v26 offset:512\n\t" \
  "ds_read_b64_tr_b16 v[104:105], v26 offset:8192\n\t" \
  "ds_read_b64_tr_b16 v[106:107], v26 offset:8704\n\t" \
  "v_xor_b32 v26, 32, v25\n\t" \
  "v_add_u32 v26, 98304, v26\n\t" \
  "ds_read_b64_tr_b16 v[108:109], v26 offset:0\n\t" \
  "ds_read_b64_tr_b16 v[110:111], v26 offset:512\n\t" \
  "ds_read_b64_tr_b16 v[112:113], v26 offset:8192\n\t" \
  "ds_read_b64_tr_b16 v[114:115], v26 offset:8704\n\t" \
  "ds_write_b128 v123, v[0:3] offset:16384\n\t" \
  "ds_write_b128 v123, v[4:7] offset:17408\n\t" \
  "ds_write_b128 v123, v[8:11] offset:18432\n\t" \
  "ds_write_b128 v123, v[12:15] offset:19456\n\t" \
  "s_waitcnt vmcnt(0) lgkmcnt(0)\n\t" \
  "s_barrier\n\t" \
  "ds_write_b128 v123, v[100:103] offset:32768\n\t" \
  "ds_write_b128 v123, v[104:107] offset:33792\n\t" \
  "ds_write_b128 v123, v[108:111] offset:34816\n\t" \
  "ds_write_b128 v123, v[112:115] offset:35840\n\t" \
  "s_branch Lrec_pb_%=\n\t" \
  "Lrec_ob_%=:\n\t" \
  "s_add_u32 s66, s90, 4194304\n\t" \
  "s_addc_u32 s67, s91, 0\n\t" \
  "s_lshl_b32 s55, s70, 12\n\t" \
  "s_add_u32 m0, s55, 65536\n\t" \
  "s_nop 0\n\t" \
  "global_load_lds_dwordx4 v21, s[66:67]\n\t" \
  "s_add_u32 m0, s55, 66560\n\t" \
  "s_nop 0\n\t" \
  "global_load_lds_dwordx4 v22, s[66:67]\n\t" \
  "s_add_u32 m0, s55, 67584\n\t" \
  "s_nop 0\n\t" \
  "global_load_lds_dwordx4 v23, s[66:67]\n\t" \
  "s_add_u32 m0, s55, 68608\n\t" \
  "s_nop 0\n\t" \
  "global_load_lds_dwordx4 v24, s[66:67]\n\t" \
  "s_add_u32 s66, s92, 0\n\t" \
  "s_addc_u32 s67, s93, 0\n\t" \
  "s_lshl_b32 s55, s70, 12\n\t" \
  "s_add_u32 m0, s55, 49152\n\t" \
  "s_nop 0\n\t" \
  "global_load_lds_dwordx4 v21, s[66:67]\n\t" \
  "s_add_u32 m0, s55, 50176\n\t" \
  "s_nop 0\n\t" \
  "global_load_lds_dwordx4 v22, s[66:67]\n\t" \
  "s_add_u32 m0, s55, 51200\n\t" \
  "s_nop 0\n\t" \
  "global_load_lds_dwordx4 v23, s[66:67]\n\t" \
  "s_add_u32 m0, s55, 52224\n\t" \
  "s_nop 0\n\t" \
  "global_load_lds_dwordx4 v24, s[66:67]\n\t" \
  "s_add_u32 s66, s92, 524288\n\t" \
  "s_addc_u32 s67, s93, 0\n\t" \
  "s_lshl_b32 s55, s70, 12\n\t" \
  "s_add_u32 m0, s55, 32768\n\t" \
  "s_nop 0\n\t" \
  "global_load_lds_dwordx4 v21, s[66:67]\n\t" \
  "s_add_u32 m0, s55, 33792\n\t" \
  "s_nop 0\n\t" \
  "global_load_lds_dwordx4 v22, s[66:67]\n\t" \
  "s_add_u32 m0, s55, 34816\n\t" \
  "s_nop 0\n\t" \
  "global_load_lds_dwordx4 v23, s[66:67]\n\t" \
  "s_add_u32 m0, s55, 35840\n\t" \
  "s_nop 0\n\t" \
  "global_load_lds_dwordx4 v24, s[66:67]\n\t" \
  "s_add_u32 s66, s92, 1048576\n\t" \
  "s_addc_u32 s67, s93, 0\n\t" \
  "s_lshl_b32 s55, s70, 12\n\t" \
  "s_add_u32 m0, s55, 16384\n\t" \
  "s_nop 0\n\t" \
  "global_load_lds_dwordx4 v21, s[66:67]\n\t" \
  "s_add_u32 m0, s55, 17408\n\t" \
  "s_nop 0\n\t" \
  "global_load_lds_dwordx4 v22, s[66:67]\n\t" \
  "s_add_u32 m0, s55, 18432\n\t" \
  "s_nop 0\n\t" \
  "global_load_lds_dwordx4 v23, s[66:67]\n\t" \
  "s_add_u32 m0, s55, 19456\n\t" \
  "s_nop 0\n\t" \
  "global_load_lds_dwordx4 v24, s[66:67]\n\t" \
  "s_add_u32 s66, s92, 1572864\n\t" \
  "s_addc_u32 s67, s93, 0\n\t" \
  "s_lshl_b32 s55, s70, 12\n\t" \
  "s_add_u32 m0, s55, 0\n\t" \
  "s_nop 0\n\t" \
  "global_load_lds_dwordx4 v21, s[66:67]\n\t" \
  "s_add_u32 m0, s55, 1024\n\t" \
  "s_nop 0\n\t" \
  "global_load_lds_dwordx4 v22, s[66:67]\n\t" \
  "s_add_u32 m0, s55, 2048\n\t" \
  "s_nop 0\n\t" \
  "global_load_lds_dwordx4 v23, s[66:67]\n\t" \
  "s_add_u32 m0, s55, 3072\n\t" \
  "s_nop 0\n\t" \
  "global_load_lds_dwordx4 v24, s[66:67]\n\t" \
  "s_waitcnt vmcnt(32)\n\t" \
  "s_barrier\n\t" \
  "v_xor_b32 v26, 0, v25\n\t" \
  "v_add_u32 v26, 98304, v26\n\t" \
  "ds_read_b64_tr_b16 a[144:145], v26 offset:0\n\t" \
  "ds_read_b64_tr_b16 a[146:147], v26 offset:512\n\t" \
  "ds_read_b64_tr_b16 a[148:149], v26 offset:8192\n\t" \
  "ds_read_b64_tr_b16 a[150:151], v26 offset:8704\n\t" \
  "v_xor_b32 v26, 32, v25\n\t" \
  "v_add_u32 v26, 98304, v26\n\t" \
  "ds_read_b64_tr_b16 a[152:153], v26 offset:0\n\t" \
  "ds_read_b64_tr_b16 a[154:155], v26 offset:512\n\t" \
  "ds_read_b64_tr_b16 a[156:157], v26 offset:8192\n\t" \
  "ds_read_b64_tr_b16 a[158:159], v26 offset:8704\n\t" \
  "s_waitcnt lgkmcnt(0)\n\t" \
  "s_barrier\n\t" \
  "s_add_u32 s66, s92, 2097152\n\t" \
  "s_addc_u32 s67, s93, 0\n\t" \
  "s_lshl_b32 s55, s70, 12\n\t" \
  "s_add_u32 m0, s55, 98304\n\t" \
  "s_nop 0\n\t" \
  "global_load_lds_dwordx4 v21, s[66:67]\n\t" \
  "s_add_u32 m0, s55, 99328\n\t" \
  "s_nop 0\n\t" \
  "global_load_lds_dwordx4 v22, s[66:67]\n\t" \
  "s_add_u32 m0, s55, 100352\n\t" \
  "s_nop 0\n\t" \
  "global_load_lds_dwordx4 v23, s[66:67]\n\t" \
  "s_add_u32 m0, s55, 101376\n\t" \
  "s_nop 0\n\t" \
  "global_load_lds_dwordx4 v24, s[66:67]\n\t" \
  "s_waitcnt vmcnt(32)\n\t" \
  "s_barrier\n\t" \
  "v_xor_b32 v26, 0, v25\n\t" \
  "v_add_u32 v26, 114688, v26\n\t" \
  "ds_read_b64_tr_b16 a[160:161], v26 offset:0\n\t" \
  "ds_read_b64_tr_b16 a[162:163], v26 offset:512\n\t" \
  "ds_read_b64_tr_b16 a[164:165], v26 offset:8192\n\t" \
  "ds_read_b64_tr_b16 a[166:167], v26 offset:8704\n\t" \
  "v_xor_b32 v26, 32, v25\n\t" \
  "v_add_u32 v26, 114688, v26\n\t" \
  "ds_read_b64_tr_b16 a[168:169], v26 offset:0\n\t" \
  "ds_read_b64_tr_b16 a[170:171], v26 offset:512\n\t" \
  "ds_read_b64_tr_b16 a[172:173], v26 offset:8192\n\t" \
  "ds_read_b64_tr_b16 a[174:175], v26 offset:8704\n\t" \
  "s_waitcnt lgkmcnt(0)\n\t" \
  "s_barrier\n\t" \
  "s_add_u32 s66, s88, 1572864\n\t" \
  "s_addc_u32 s67, s89, 0\n\t" \
  "s_lshl_b32 s55, s70, 12\n\t" \
  "s_add_u32 m0, s55, 114688\n\t" \
  "s_nop 0\n\t" \
  "global_load_lds_dwordx4 v21, s[66:67]\n\t" \
  "s_add_u32 m0, s55, 115712\n\t" \
  "s_nop 0\n\t" \
  "global_load_lds_dwordx4 v22, s[66:67]\n\t" \
  "s_add_u32 m0, s55, 116736\n\t" \
  "s_nop 0\n\t" \
  "global_load_lds_dwordx4 v23, s[66:67]\n\t" \
  "s_add_u32 m0, s55, 117760\n\t" \
  "s_nop 0\n\t" \
  "global_load_lds_dwordx4 v24, s[66:67]\n\t" \
  "s_waitcnt vmcnt(32)\n\t" \
  "s_barrier\n\t" \
  "v_xor_b32 v26, 0, v25\n\t" \
  "v_add_u32 v26, 81920, v26\n\t" \
  "ds_read_b64_tr_b16 a[176:177], v26 offset:0\n\t" \
  "ds_read_b64_tr_b16 a[178:179], v26 offset:512\n\t" \
  "ds_read_b64_tr_b16 a[180:181], v26 offset:8192\n\t" \
  "ds_read_b64_tr_b16 a[182:183], v26 offset:8704\n\t" \
  "v_xor_b32 v26, 32, v25\n\t" \
  "v_add_u32 v26, 81920, v26\n\t" \
  "ds_read_b64_tr_b16 a[184:185], v26 offset:0\n\t" \
  "ds_read_b64_tr_b16 a[186:187], v26 offset:512\n\t" \
  "ds_read_b64_tr_b16 a[188:189], v26 offset:8192\n\t" \
  "ds_read_b64_tr_b16 a[190:191], v26 offset:8704\n\t" \
  "s_waitcnt lgkmcnt(0)\n\t" \
  "s_barrier\n\t" \
  "s_add_u32 s66, s88, 2097152\n\t" \
  "s_addc_u32 s67, s89, 0\n\t" \
  "s_lshl_b32 s55, s70, 12\n\t" \
  "s_add_u32 m0, s55, 81920\n\t" \
  "s_nop 0\n\t" \
  "global_load_lds_dwordx4 v21, s[66:67]\n\t" \
  "s_add_u32 m0, s55, 82944\n\t" \
  "s_nop 0\n\t" \
  "global_load_lds_dwordx4 v22, s[66:67]\n\t" \
  "s_add_u32 m0, s55, 83968\n\t" \
  "s_nop 0\n\t" \
  "global_load_lds_dwordx4 v23, s[66:67]\n\t" \
  "s_add_u32 m0, s55, 84992\n\t" \
  "s_nop 0\n\t" \
  "global_load_lds_dwordx4 v24, s[66:67]\n\t" \
  "s_waitcnt vmcnt(28)\n\t" \
  "s_barrier\n\t" \
  "v_xor_b32 v26, 0, v25\n\t" \
  "v_add_u32 v26, 65536, v26\n\t" \
  "ds_read_b64_tr_b16 a[192:193], v26 offset:0\n\t" \
  "ds_read_b64_tr_b16 a[194:195], v26 offset:512\n\t" \
  "ds_read_b64_tr_b16 a[196:197], v26 offset:8192\n\t" \
  "ds_read_b64_tr_b16 a[198:199], v26 offset:8704\n\t" \
  "v_xor_b32 v26, 32, v25\n\t" \
  "v_add_u32 v26, 65536, v26\n\t" \
  "ds_read_b64_tr_b16 a[200:201], v26 offset:0\n\t" \
  "ds_read_b64_tr_b16 a[202:203], v26 offset:512\n\t" \
  "ds_read_b64_tr_b16 a[204:205], v26 offset:8192\n\t" \
  "ds_read_b64_tr_b16 a[206:207], v26 offset:8704\n\t" \
  "s_waitcnt lgkmcnt(0)\n\t" \
  "s_barrier\n\t" \
  "s_add_u32 s66, s88, 2621440\n\t" \
  "s_addc_u32 s67, s89, 0\n\t" \
  "s_lshl_b32 s55, s70, 12\n\t" \
  "s_add_u32 m0, s55, 65536\n\t" \
  "s_nop 0\n\t" \
  "global_load_lds_dwordx4 v21, s[66:67]\n\t" \
  "s_add_u32 m0, s55, 66560\n\t" \
  "s_nop 0\n\t" \
  "global_load_lds_dwordx4 v22, s[66:67]\n\t" \
  "s_add_u32 m0, s55, 67584\n\t" \
  "s_nop 0\n\t" \
  "global_load_lds_dwordx4 v23, s[66:67]\n\t" \
  "s_add_u32 m0, s55, 68608\n\t" \
  "s_nop 0\n\t" \
  "global_load_lds_dwordx4 v24, s[66:67]\n\t" \
  "v_xor_b32 v26, 0, v25\n\t" \
  "v_add_u32 v26, 131072, v26\n\t" \
  "ds_read_b64_tr_b16 v[0:1], v26 offset:0\n\t" \
  "ds_read_b64_tr_b16 v[2:3], v26 offset:512\n\t" \
  "ds_read_b64_tr_b16 v[4:5], v26 offset:8192\n\t" \
  "ds_read_b64_tr_b16 v[6:7], v26 offset:8704\n\t" \
  "v_xor_b32 v26, 32, v25\n\t" \
  "v_add_u32 v26, 131072, v26\n\t" \
  "ds_read_b64_tr_b16 v[168:169], v26 offset:0\n\t" \
  "ds_read_b64_tr_b16 v[170:171], v26 offset:512\n\t" \
  "ds_read_b64_tr_b16 v[172:173], v26 offset:8192\n\t" \
  "ds_read_b64_tr_b16 v[174:175], v26 offset:8704\n\t" \
  "s_waitcnt lgkmcnt(0)\n\t" \
  "s_barrier\n\t" \
  "ds_write_b128 v125, v[0:3] offset:0\n\t" \
  "ds_write_b128 v125, v[4:7] offset:1024\n\t" \
  "s_waitcnt lgkmcnt(0)\n\t" \
  "v_mov_b32 v134, 0\n\t" \
  "s_mov_b32 s72, 58255\n\t" \
  "v_add_u32 v33, 0, v96\n\t" \
  "v_mul_u32_u24 v56, s72, v33\n\t" \
  "v_lshrrev_b32 v56, 22, v56\n\t" \
  "v_mul_u32_u24 v57, 72, v56\n\t" \
  "v_sub_u32 v57, v33, v57\n\t" \
  "v_lshrrev_b32 v58, 1, v57\n\t" \
  "v_lshlrev_b32 v58, 8, v58\n\t" \
  "v_and_b32 v57, 1, v57\n\t" \
  "v_lshl_add_u32 v58, v57, 7, v58\n\t" \
  "v_lshl_add_u32 v58, v56, 4, v58\n\t" \
  "v_add_u32 v58, 139264, v58\n\t" \
  "v_cmp_gt_u32 vcc, 576, v33\n\t" \
  "s_nop 0\n\t" \
  "s_mov_b64 exec, vcc\n\t" \
  "ds_write_b128 v58, v[84:87]\n\t" \
  "s_mov_b64 exec, -1\n\t" \
  "v_lshlrev_b32 v56, 16, v84\n\t" \
  "v_and_b32 v57, 0xffff0000, v84\n\t" \
  "v_max3_f32 v134, v134, |v56|, |v57|\n\t" \
  "v_lshlrev_b32 v56, 16, v85\n\t" \
  "v_and_b32 v57, 0xffff0000, v85\n\t" \
  "v_max3_f32 v134, v134, |v56|, |v57|\n\t" \
  "v_lshlrev_b32 v56, 16, v86\n\t" \
  "v_and_b32 v57, 0xffff0000, v86\n\t" \
  "v_max3_f32 v134, v134, |v56|, |v57|\n\t" \
  "v_lshlrev_b32 v56, 16, v87\n\t" \
  "v_and_b32 v57, 0xffff0000, v87\n\t" \
  "v_max3_f32 v134, v134, |v56|, |v57|\n\t" \
  "v_add_u32 v33, 256, v96\n\t" \
  "v_mul_u32_u24 v56, s72, v33\n\t" \
  "v_lshrrev_b32 v56, 22, v56\n\t" \
  "v_mul_u32_u24 v57, 72, v56\n\t" \
  "v_sub_u32 v57, v33, v57\n\t" \
  "v_lshrrev_b32 v58, 1, v57\n\t" \
  "v_lshlrev_b32 v58, 8, v58\n\t" \
  "v_and_b32 v57, 1, v57\n\t" \
  "v_lshl_add_u32 v58, v57, 7, v58\n\t" \
  "v_lshl_add_u32 v58, v56, 4, v58\n\t" \
  "v_add_u32 v58, 139264, v58\n\t" \
  "v_cmp_gt_u32 vcc, 576, v33\n\t" \
  "s_nop 0\n\t" \
  "s_mov_b64 exec, vcc\n\t" \
  "ds_write_b128 v58, v[88:91]\n\t" \
  "s_mov_b64 exec, -1\n\t" \
  "v_lshlrev_b32 v56, 16, v88\n\t" \
  "v_and_b32 v57, 0xffff0000, v88\n\t" \
  "v_max3_f32 v134, v134, |v56|, |v57|\n\t" \
  "v_lshlrev_b32 v56, 16, v89\n\t" \
  "v_and_b32 v57, 0xffff0000, v89\n\t" \
  "v_max3_f32 v134, v134, |v56|, |v57|\n\t" \
  "v_lshlrev_b32 v56, 16, v90\n\t" \
  "v_and_b32 v57, 0xffff0000, v90\n\t" \
  "v_max3_f32 v134, v134, |v56|, |v57|\n\t" \
  "v_lshlrev_b32 v56, 16, v91\n\t" \
  "v_and_b32 v57, 0xffff0000, v91\n\t" \
  "v_max3_f32 v134, v134, |v56|, |v57|\n\t" \
  "v_add_u32 v33, 512, v96\n\t" \
  "v_mul_u32_u24 v56, s72, v33\n\t" \
  "v_lshrrev_b32 v56, 22, v56\n\t" \
  "v_mul_u32_u24 v57, 72, v56\n\t" \
  "v_sub_u32 v57, v33, v57\n\t" \
  "v_lshrrev_b32 v58, 1, v57\n\t" \
  "v_lshlrev_b32 v58, 8, v58\n\t" \
  "v_and_b32 v57, 1, v57\n\t" \
  "v_lshl_add_u32 v58, v57, 7, v58\n\t" \
  "v_lshl_add_u32 v58, v56, 4, v58\n\t" \
  "v_add_u32 v58, 139264, v58\n\t" \
  "v_cmp_gt_u32 vcc, 576, v33\n\t" \
  "s_nop 0\n\t" \
  "s_mov_b64 exec, vcc\n\t" \
  "ds_write_b128 v58, v[92:95]\n\t" \
  "s_mov_b64 exec, -1\n\t" \
  "v_lshlrev_b32 v56, 16, v92\n\t" \
  "v_and_b32 v57, 0xffff0000, v92\n\t" \
  "v_max3_f32 v134, v134, |v56|, |v57|\n\t" \
  "v_lshlrev_b32 v56, 16, v93\n\t" \
  "v_and_b32 v57, 0xffff0000, v93\n\t" \
  "v_max3_f32 v134, v134, |v56|, |v57|\n\t" \
  "v_lshlrev_b32 v56, 16, v94\n\t" \
  "v_and_b32 v57, 0xffff0000, v94\n\t" \
  "v_max3_f32 v134, v134, |v56|, |v57|\n\t" \
  "v_lshlrev_b32 v56, 16, v95\n\t" \
  "v_and_b32 v57, 0xffff0000, v95\n\t" \
  "v_max3_f32 v134, v134, |v56|, |v57|\n\t" \
  "s_waitcnt vmcnt(28)\n\t" \
  "s_barrier\n\t" \
  "v_xor_b32 v26, 0, v25\n\t" \
  "v_add_u32 v26, 49152, v26\n\t" \
  "ds_read_b64_tr_b16 a[208:209], v26 offset:0\n\t" \
  "ds_read_b64_tr_b16 a[210:211], v26 offset:512\n\t" \
  "ds_read_b64_tr_b16 a[212:213], v26 offset:8192\n\t" \
  "ds_read_b64_tr_b16 a[214:215], v26 offset:8704\n\t" \
  "v_xor_b32 v26, 32, v25\n\t" \
  "v_add_u32 v26, 49152, v26\n\t" \
  "ds_read_b64_tr_b16 a[216:217], v26 offset:0\n\t" \
  "ds_read_b64_tr_b16 a[218:219], v26 offset:512\n\t" \
  "ds_read_b64_tr_b16 a[220:221], v26 offset:8192\n\t" \
  "ds_read_b64_tr_b16 a[222:223], v26 offset:8704\n\t" \
  "s_waitcnt lgkmcnt(0)\n\t" \
  "s_barrier\n\t" \
  "s_add_u32 s66, s88, 3145728\n\t" \
  "s_addc_u32 s67, s89, 0\n\t" \
  "s_lshl_b32 s55, s70, 12\n\t" \
  "s_add_u32 m0, s55, 49152\n\t" \
  "s_nop 0\n\t" \
  "global_load_lds_dwordx4 v21, s[66:67]\n\t" \
  "s_add_u32 m0, s55, 50176\n\t" \
  "s_nop 0\n\t" \
  "global_load_lds_dwordx4 v22, s[66:67]\n\t" \
  "s_add_u32 m0, s55, 51200\n\t" \
  "s_nop 0\n\t" \
  "global_load_lds_dwordx4 v23, s[66:67]\n\t" \
  "s_add_u32 m0, s55, 52224\n\t" \
  "s_nop 0\n\t" \
  "global_load_lds_dwordx4 v24, s[66:67]\n\t" \
  "s_waitcnt vmcnt(28)\n\t" \
  "s_barrier\n\t" \
  "v_xor_b32 v26, 0, v25\n\t" \
  "v_add_u32 v26, 32768, v26\n\t" \
  "ds_read_b64_tr_b16 a[224:225], v26 offset:0\n\t" \
  "ds_read_b64_tr_b16 a[226:227], v26 offset:512\n\t" \
  "ds_read_b64_tr_b16 a[228:229], v26 offset:8192\n\t" \
  "ds_read_b64_tr_b16 a[230:231], v26 offset:8704\n\t" \
  "v_xor_b32 v26, 32, v25\n\t" \
  "v_add_u32 v26, 32768, v26\n\t" \
  "ds_read_b64_tr_b16 a[232:233], v26 offset:0\n\t" \
  "ds_read_b64_tr_b16 a[234:235], v26 offset:512\n\t" \
  "ds_read_b64_tr_b16 a[236:237], v26 offset:8192\n\t" \
  "ds_read_b64_tr_b16 a[238:239], v26 offset:8704\n\t" \
  "s_waitcnt lgkmcnt(0)\n\t" \
  "s_barrier\n\t" \
  "s_add_u32 s66, s88, 3670016\n\t" \
  "s_addc_u32 s67, s89, 0\n\t" \
  "s_lshl_b32 s55, s70, 12\n\t" \
  "s_add_u32 m0, s55, 32768\n\t" \
  "s_nop 0\n\t" \
  "global_load_lds_dwordx4 v21, s[66:67]\n\t" \
  "s_add_u32 m0, s55, 33792\n\t" \
  "s_nop 0\n\t" \
  "global_load_lds_dwordx4 v22, s[66:67]\n\t" \
  "s_add_u32 m0, s55, 34816\n\t" \
  "s_nop 0\n\t" \
  "global_load_lds_dwordx4 v23, s[66:67]\n\t" \
  "s_add_u32 m0, s55, 35840\n\t" \
  "s_nop 0\n\t" \
  "global_load_lds_dwordx4 v24, s[66:67]\n\t" \
  "buffer_load_dword v69, off, s[36:39], s61 sc1\n\t" \
  "s_waitcnt vmcnt(28)\n\t" \
  "s_barrier\n\t" \
  "v_xor_b32 v26, 0, v25\n\t" \
  "v_add_u32 v26, 16384, v26\n\t" \
  "ds_read_b64_tr_b16 a[240:241], v26 offset:0\n\t" \
  "ds_read_b64_tr_b16 a[242:243], v26 offset:512\n\t" \
  "ds_read_b64_tr_b16 a[244:245], v26 offset:8192\n\t" \
  "ds_read_b64_tr_b16 a[246:247], v26 offset:8704\n\t" \
  "v_xor_b32 v26, 32, v25\n\t" \
  "v_add_u32 v26, 16384, v26\n\t" \
  "ds_read_b64_tr_b16 a[248:249], v26 offset:0\n\t" \
  "ds_read_b64_tr_b16 a[250:251], v26 offset:512\n\t" \
  "ds_read_b64_tr_b16 a[252:253], v26 offset:8192\n\t" \
  "ds_read_b64_tr_b16 a[254:255], v26 offset:8704\n\t" \
  "s_waitcnt lgkmcnt(0)\n\t" \
  "s_barrier\n\t" \
  "s_add_u32 s66, s88, 4194304\n\t" \
  "s_addc_u32 s67, s89, 0\n\t" \
  "s_lshl_b32 s55, s70, 12\n\t" \
  "s_add_u32 m0, s55, 16384\n\t" \
  "s_nop 0\n\t" \
  "global_load_lds_dwordx4 v21, s[66:67]\n\t" \
  "s_add_u32 m0, s55, 17408\n\t" \
  "s_nop 0\n\t" \
  "global_load_lds_dwordx4 v22, s[66:67]\n\t" \
  "s_add_u32 m0, s55, 18432\n\t" \
  "s_nop 0\n\t" \
  "global_load_lds_dwordx4 v23, s[66:67]\n\t" \
  "s_add_u32 m0, s55, 19456\n\t" \
  "s_nop 0\n\t" \
  "global_load_lds_dwordx4 v24, s[66:67]\n\t" \
  "s_waitcnt vmcnt(28)\n\t" \
  "s_barrier\n\t" \
  "v_xor_b32 v26, 0, v25\n\t" \
  "v_add_u32 v26, 0, v26\n\t" \
  "ds_read_b64_tr_b16 v[136:137], v26 offset:0\n\t" \
  "ds_read_b64_tr_b16 v[138:139], v26 offset:512\n\t" \
  "ds_read_b64_tr_b16 v[140:141], v26 offset:8192\n\t" \
  "ds_read_b64_tr_b16 v[142:143], v26 offset:8704\n\t" \
  "v_xor_b32 v26, 32, v25\n\t" \
  "v_add_u32 v26, 0, v26\n\t" \
  "ds_read_b64_tr_b16 v[144:145], v26 offset:0\n\t" \
  "ds_read_b64_tr_b16 v[146:147], v26 offset:512\n\t" \
  "ds_read_b64_tr_b16 v[148:149], v26 offset:8192\n\t" \
  "ds_read_b64_tr_b16 v[150:151], v26 offset:8704\n\t" \
  "s_waitcnt lgkmcnt(0)\n\t" \
  "s_barrier\n\t" \
  "s_add_u32 s66, s90, 1048576\n\t" \
  "s_addc_u32 s67, s91, 0\n\t" \
  "s_lshl_b32 s55, s70, 12\n\t" \
  "s_add_u32 m0, s55, 0\n\t" \
  "s_nop 0\n\t" \
  "global_load_lds_dwordx4 v21, s[66:67]\n\t" \
  "s_add_u32 m0, s55, 1024\n\t" \
  "s_nop 0\n\t" \
  "global_load_lds_dwordx4 v22, s[66:67]\n\t" \
  "s_add_u32 m0, s55, 2048\n\t" \
  "s_nop 0\n\t" \
  "global_load_lds_dwordx4 v23, s[66:67]\n\t" \
  "s_add_u32 m0, s55, 3072\n\t" \
  "s_nop 0\n\t" \
  "global_load_lds_dwordx4 v24, s[66:67]\n\t" \
  "s_waitcnt vmcnt(28)\n\t" \
  "s_barrier\n\t" \
  "v_xor_b32 v26, 0, v25\n\t" \
  "v_add_u32 v26, 98304, v26\n\t" \
  "ds_read_b64_tr_b16 v[152:153], v26 offset:0\n\t" \
  "ds_read_b64_tr_b16 v[154:155], v26 offset:512\n\t" \
  "ds_read_b64_tr_b16 v[156:157], v26 offset:8192\n\t" \
  "ds_read_b64_tr_b16 v[158:159], v26 offset:8704\n\t" \
  "v_xor_b32 v26, 32, v25\n\t" \
  "v_add_u32 v26, 98304, v26\n\t" \
  "ds_read_b64_tr_b16 v[160:161], v26 offset:0\n\t" \
  "ds_read_b64_tr_b16 v[162:163], v26 offset:512\n\t" \
  "ds_read_b64_tr_b16 v[164:165], v26 offset:8192\n\t" \
  "ds_read_b64_tr_b16 v[166:167], v26 offset:8704\n\t" \
  "s_waitcnt lgkmcnt(0)\n\t" \
  "s_barrier\n\t" \
  "s_add_u32 s66, s90, 1572864\n\t" \
  "s_addc_u32 s67, s91, 0\n\t" \
  "s_lshl_b32 s55, s70, 12\n\t" \
  "s_add_u32 m0, s55, 98304\n\t" \
  "s_nop 0\n\t" \
  "global_load_lds_dwordx4 v21, s[66:67]\n\t" \
  "s_add_u32 m0, s55, 99328\n\t" \
  "s_nop 0\n\t" \
  "global_load_lds_dwordx4 v22, s[66:67]\n\t" \
  "s_add_u32 m0, s55, 100352\n\t" \
  "s_nop 0\n\t" \
  "global_load_lds_dwordx4 v23, s[66:67]\n\t" \
  "s_add_u32 m0, s55, 101376\n\t" \
  "s_nop 0\n\t" \
  "global_load_lds_dwordx4 v24, s[66:67]\n\t" \
  "s_waitcnt vmcnt(28)\n\t" \
  "s_barrier\n\t" \
  "v_xor_b32 v26, 0, v25\n\t" \
  "v_add_u32 v26, 114688, v26\n\t" \
  "ds_read_b64_tr_b16 a[0:1], v26 offset:0\n\t" \
  "ds_read_b64_tr_b16 a[2:3], v26 offset:512\n\t" \
  "ds_read_b64_tr_b16 a[4:5], v26 offset:8192\n\t" \
  "ds_read_b64_tr_b16 a[6:7], v26 offset:8704\n\t" \
  "v_xor_b32 v26, 32, v25\n\t" \
  "v_add_u32 v26, 114688, v26\n\t" \
  "ds_read_b64_tr_b16 a[8:9], v26 offset:0\n\t" \
  "ds_read_b64_tr_b16 a[10:11], v26 offset:512\n\t" \
  "ds_read_b64_tr_b16 a[12:13], v26 offset:8192\n\t" \
  "ds_read_b64_tr_b16 a[14:15], v26 offset:8704\n\t" \
  "s_waitcnt lgkmcnt(0)\n\t" \
  "s_barrier\n\t" \
  "s_add_u32 s66, s90, 2097152\n\t" \
  "s_addc_u32 s67, s91, 0\n\t" \
  "s_lshl_b32 s55, s70, 12\n\t" \
  "s_add_u32 m0, s55, 114688\n\t" \
  "s_nop 0\n\t" \
  "global_load_lds_dwordx4 v21, s[66:67]\n\t" \
  "s_add_u32 m0, s55, 115712\n\t" \
  "s_nop 0\n\t" \
  "global_load_lds_dwordx4 v22, s[66:67]\n\t" \
  "s_add_u32 m0, s55, 116736\n\t" \
  "s_nop 0\n\t" \
  "global_load_lds_dwordx4 v23, s[66:67]\n\t" \
  "s_add_u32 m0, s55, 117760\n\t" \
  "s_nop 0\n\t" \
  "global_load_lds_dwordx4 v24, s[66:67]\n\t" \
  "s_waitcnt vmcnt(28)\n\t" \
  "s_barrier\n\t" \
  "v_xor_b32 v26, 0, v25\n\t" \
  "v_add_u32 v26, 81920, v26\n\t" \
  "ds_read_b64_tr_b16 a[16:17], v26 offset:0\n\t" \
  "ds_read_b64_tr_b16 a[18:19], v26 offset:512\n\t" \
  "ds_read_b64_tr_b16 a[20:21], v26 offset:8192\n\t" \
  "ds_read_b64_tr_b16 a[22:23], v26 offset:8704\n\t" \
  "v_xor_b32 v26, 32, v25\n\t" \
  "v_add_u32 v26, 81920, v26\n\t" \
  "ds_read_b64_tr_b16 a[24:25], v26 offset:0\n\t" \
  "ds_read_b64_tr_b16 a[26:27], v26 offset:512\n\t" \
  "ds_read_b64_tr_b16 a[28:29], v26 offset:8192\n\t" \
  "ds_read_b64_tr_b16 a[30:31], v26 offset:8704\n\t" \
  "s_waitcnt lgkmcnt(0)\n\t" \
  "s_barrier\n\t" \
  "s_add_u32 s66, s92, 3145728\n\t" \
  "s_addc_u32 s67, s93, 0\n\t" \
  "s_lshl_b32 s55, s70, 12\n\t" \
  "s_add_u32 m0, s55, 81920\n\t" \
  "s_nop 0\n\t" \
  "global_load_lds_dwordx4 v21, s[66:67]\n\t" \
  "s_add_u32 m0, s55, 82944\n\t" \
  "s_nop 0\n\t" \
  "global_load_lds_dwordx4 v22, s[66:67]\n\t" \
  "s_add_u32 m0, s55, 83968\n\t" \
  "s_nop 0\n\t" \
  "global_load_lds_dwordx4 v23, s[66:67]\n\t" \
  "s_add_u32 m0, s55, 84992\n\t" \
  "s_nop 0\n\t" \
  "global_load_lds_dwordx4 v24, s[66:67]\n\t" \
  "s_waitcnt vmcnt(28)\n\t" \
  "s_barrier\n\t" \
  "v_xor_b32 v26, 0, v25\n\t" \
  "v_add_u32 v26, 65536, v26\n\t" \
  "ds_read_b64_tr_b16 a[32:33], v26 offset:0\n\t" \
  "ds_read_b64_tr_b16 a[34:35], v26 offset:512\n\t" \
  "ds_read_b64_tr_b16 a[36:37], v26 offset:8192\n\t" \
  "ds_read_b64_tr_b16 a[38:39], v26 offset:8704\n\t" \
  "v_xor_b32 v26, 32, v25\n\t" \
  "v_add_u32 v26, 65536, v26\n\t" \
  "ds_read_b64_tr_b16 a[40:41], v26 offset:0\n\t" \
  "ds_read_b64_tr_b16 a[42:43], v26 offset:512\n\t" \
  "ds_read_b64_tr_b16 a[44:45], v26 offset:8192\n\t" \
  "ds_read_b64_tr_b16 a[46:47], v26 offset:8704\n\t" \
  "s_waitcnt lgkmcnt(0)\n\t" \
  "s_barrier\n\t" \
  "s_add_u32 s66, s90, 524288\n\t" \
  "s_addc_u32 s67, s91, 0\n\t" \
  "s_lshl_b32 s55, s70, 12\n\t" \
  "s_add_u32 m0, s55, 65536\n\t" \
  "s_nop 0\n\t" \
  "global_load_lds_dwordx4 v21, s[66:67]\n\t" \
  "s_add_u32 m0, s55, 66560\n\t" \
  "s_nop 0\n\t" \
  "global_load_lds_dwordx4 v22, s[66:67]\n\t" \
  "s_add_u32 m0, s55, 67584\n\t" \
  "s_nop 0\n\t" \
  "global_load_lds_dwordx4 v23, s[66:67]\n\t" \
  "s_add_u32 m0, s55, 68608\n\t" \
  "s_nop 0\n\t" \
  "global_load_lds_dwordx4 v24, s[66:67]\n\t" \
  "s_waitcnt vmcnt(28)\n\t" \
  "s_barrier\n\t" \
  "v_xor_b32 v26, 0, v25\n\t" \
  "v_add_u32 v26, 49152, v26\n\t" \
  "ds_read_b64_tr_b16 a[48:49], v26 offset:0\n\t" \
  "ds_read_b64_tr_b16 a[50:51], v26 offset:512\n\t" \
  "ds_read_b64_tr_b16 a[52:53], v26 offset:8192\n\t" \
  "ds_read_b64_tr_b16 a[54:55], v26 offset:8704\n\t" \
  "v_xor_b32 v26, 32, v25\n\t" \
  "v_add_u32 v26, 49152, v26\n\t" \
  "ds_read_b64_tr_b16 a[56:57], v26 offset:0\n\t" \
  "ds_read_b64_tr_b16 a[58:59], v26 offset:512\n\t" \
  "ds_read_b64_tr_b16 a[60:61], v26 offset:8192\n\t" \
  "ds_read_b64_tr_b16 a[62:63], v26 offset:8704\n\t" \
  "s_waitcnt lgkmcnt(0)\n\t" \
  "s_barrier\n\t" \
  "s_add_u32 s66, s90, 0\n\t" \
  "s_addc_u32 s67, s91, 0\n\t" \
  "s_lshl_b32 s55, s70, 12\n\t" \
  "s_add_u32 m0, s55, 49152\n\t" \
  "s_nop 0\n\t" \
  "global_load_lds_dwordx4 v21, s[66:67]\n\t" \
  "s_add_u32 m0, s55, 50176\n\t" \
  "s_nop 0\n\t" \
  "global_load_lds_dwordx4 v22, s[66:67]\n\t" \
  "s_add_u32 m0, s55, 51200\n\t" \
  "s_nop 0\n\t" \
  "global_load_lds_dwordx4 v23, s[66:67]\n\t" \
  "s_add_u32 m0, s55, 52224\n\t" \
  "s_nop 0\n\t" \
  "global_load_lds_dwordx4 v24, s[66:67]\n\t" \
  "s_waitcnt vmcnt(28)\n\t" \
  "s_barrier\n\t" \
  "v_xor_b32 v26, 0, v25\n\t" \
  "v_add_u32 v26, 32768, v26\n\t" \
  "ds_read_b64_tr_b16 a[64:65], v26 offset:0\n\t" \
  "ds_read_b64_tr_b16 a[66:67], v26 offset:512\n\t" \
  "ds_read_b64_tr_b16 a[68:69], v26 offset:8192\n\t" \
  "ds_read_b64_tr_b16 a[70:71], v26 offset:8704\n\t" \
  "v_xor_b32 v26, 32, v25\n\t" \
  "v_add_u32 v26, 32768, v26\n\t" \
  "ds_read_b64_tr_b16 a[72:73], v26 offset:0\n\t" \
  "ds_read_b64_tr_b16 a[74:75], v26 offset:512\n\t" \
  "ds_read_b64_tr_b16 a[76:77], v26 offset:8192\n\t" \
  "ds_read_b64_tr_b16 a[78:79], v26 offset:8704\n\t" \
  "s_waitcnt lgkmcnt(0)\n\t" \
  "s_barrier\n\t" \
  "s_add_u32 s66, s88, 1048576\n\t" \
  "s_addc_u32 s67, s89, 0\n\t" \
  "s_lshl_b32 s55, s70, 12\n\t" \
  "s_add_u32 m0, s55, 32768\n\t" \
  "s_nop 0\n\t" \
  "global_load_lds_dwordx4 v21, s[66:67]\n\t" \
  "s_add_u32 m0, s55, 33792\n\t" \
  "s_nop 0\n\t" \
  "global_load_lds_dwordx4 v22, s[66:67]\n\t" \
  "s_add_u32 m0, s55, 34816\n\t" \
  "s_nop 0\n\t" \
  "global_load_lds_dwordx4 v23, s[66:67]\n\t" \
  "s_add_u32 m0, s55, 35840\n\t" \
  "s_nop 0\n\t" \
  "global_load_lds_dwordx4 v24, s[66:67]\n\t" \
  "s_mov_b32 s58, 0\n\t" \
  "Lrec_h0x_%=:\n\t" \
  "v_cmp_eq_u32 vcc, 0, v69\n\t" \
  "s_nop 0\n\t" \
  "s_cbranch_vccz Lrec_h1x_%=\n\t" \
  "s_add_u32 s58, s58, 1\n\t" \
  "s_cmp_gt_u32 s58, 524288\n\t" \
  "s_cbranch_scc1 Lrec_end_%=\n\t" \
  "s_sleep 2\n\t" \
  "buffer_load_dword v69, off, s[36:39], s61 sc1\n\t" \
  "s_waitcnt vmcnt(0)\n\t" \
  "s_branch Lrec_h0x_%=\n\t" \
  "Lrec_h1x_%=:\n\t" \
  "s_sub_u32 s61, s61, 32768\n\t" \
  "s_mov_b64 exec, s[94:95]\n\t" \
  "buffer_store_dword v69, off, s[36:39], s61 sc1\n\t" \
  "s_mov_b64 exec, -1\n\t" \
  "v_lshlrev_b32 v68, 2, v16\n\t" \
  "s_mov_b32 s55, 61440\n\t" \
  "buffer_load_dword v68, v68, s[36:39], s55 offen sc1\n\t" \
  "s_mov_b32 s57, 0\n\t" \
  "v_mbcnt_lo_u32_b32 v135, -1, 0\n\t" \
  "v_mbcnt_hi_u32_b32 v135, -1, v135\n\t" \
  "v_lshlrev_b32 v135, 2, v135\n\t" \
  "buffer_load_dword v135, v135, s[36:39], s57 offen sc1\n\t" \
  "s_waitcnt vmcnt(28)\n\t" \
  "s_barrier\n\t" \
  "v_xor_b32 v26, 0, v25\n\t" \
  "v_add_u32 v26, 16384, v26\n\t" \
  "ds_read_b64_tr_b16 a[80:81], v26 offset:0\n\t" \
  "ds_read_b64_tr_b16 a[82:83], v26 offset:512\n\t" \
  "ds_read_b64_tr_b16 a[84:85], v26 offset:8192\n\t" \
  "ds_read_b64_tr_b16 a[86:87], v26 offset:8704\n\t" \
  "v_xor_b32 v26, 32, v25\n\t" \
  "v_add_u32 v26, 16384, v26\n\t" \
  "ds_read_b64_tr_b16 a[88:89], v26 offset:0\n\t" \
  "ds_read_b64_tr_b16 a[90:91], v26 offset:512\n\t" \
  "ds_read_b64_tr_b16 a[92:93], v26 offset:8192\n\t" \
  "ds_read_b64_tr_b16 a[94:95], v26 offset:8704\n\t" \
  "s_waitcnt lgkmcnt(0)\n\t" \
  "s_barrier\n\t" \
  "s_add_u32 s66, s88, 524288\n\t" \
  "s_addc_u32 s67, s89, 0\n\t" \
  "s_lshl_b32 s55, s70, 12\n\t" \
  "s_add_u32 m0, s55, 16384\n\t" \
  "s_nop 0\n\t" \
  "global_load_lds_dwordx4 v21, s[66:67]\n\t" \
  "s_add_u32 m0, s55, 17408\n\t" \
  "s_nop 0\n\t" \
  "global_load_lds_dwordx4 v22, s[66:67]\n\t" \
  "s_add_u32 m0, s55, 18432\n\t" \
  "s_nop 0\n\t" \
  "global_load_lds_dwordx4 v23, s[66:67]\n\t" \
  "s_add_u32 m0, s55, 19456\n\t" \
  "s_nop 0\n\t" \
  "global_load_lds_dwordx4 v24, s[66:67]\n\t" \
  "s_waitcnt vmcnt(28)\n\t" \
  "s_barrier\n\t" \
  "v_xor_b32 v26, 0, v25\n\t" \
  "v_add_u32 v26, 0, v26\n\t" \
  "ds_read_b64_tr_b16 a[96:97], v26 offset:0\n\t" \
  "ds_read_b64_tr_b16 a[98:99], v26 offset:512\n\t" \
  "ds_read_b64_tr_b16 a[100:101], v26 offset:8192\n\t" \
  "ds_read_b64_tr_b16 a[102:103], v26 offset:8704\n\t" \
  "v_xor_b32 v26, 32, v25\n\t" \
  "v_add_u32 v26, 0, v26\n\t" \
  "ds_read_b64_tr_b16 a[104:105], v26 offset:0\n\t" \
  "ds_read_b64_tr_b16 a[106:107], v26 offset:512\n\t" \
  "ds_read_b64_tr_b16 a[108:109], v26 offset:8192\n\t" \
  "ds_read_b64_tr_b16 a[110:111], v26 offset:8704\n\t" \
  "s_waitcnt lgkmcnt(0)\n\t" \
  "s_barrier\n\t" \
  "s_add_u32 s66, s88, 0\n\t" \
  "s_addc_u32 s67, s89, 0\n\t" \
  "s_lshl_b32 s55, s70, 12\n\t" \
  "s_add_u32 m0, s55, 0\n\t" \
  "s_nop 0\n\t" \
  "global_load_lds_dwordx4 v21, s[66:67]\n\t" \
  "s_add_u32 m0, s55, 1024\n\t" \
  "s_nop 0\n\t" \
  "global_load_lds_dwordx4 v22, s[66:67]\n\t" \
  "s_add_u32 m0, s55, 2048\n\t" \
  "s_nop 0\n\t" \
  "global_load_lds_dwordx4 v23, s[66:67]\n\t" \
  "s_add_u32 m0, s55, 3072\n\t" \
  "s_nop 0\n\t" \
  "global_load_lds_dwordx4 v24, s[66:67]\n\t" \
  "s_waitcnt vmcnt(28)\n\t" \
  "s_barrier\n\t" \
  "v_xor_b32 v26, 0, v25\n\t" \
  "v_add_u32 v26, 98304, v26\n\t" \
  "ds_read_b64_tr_b16 a[112:113], v26 offset:0\n\t" \
  "ds_read_b64_tr_b16 a[114:115], v26 offset:512\n\t" \
  "ds_read_b64_tr_b16 a[116:117], v26 offset:8192\n\t" \
  "ds_read_b64_tr_b16 a[118:119], v26 offset:8704\n\t" \
  "v_xor_b32 v26, 32, v25\n\t" \
  "v_add_u32 v26, 98304, v26\n\t" \
  "ds_read_b64_tr_b16 a[120:121], v26 offset:0\n\t" \
  "ds_read_b64_tr_b16 a[122:123], v26 offset:512\n\t" \
  "ds_read_b64_tr_b16 a[124:125], v26 offset:8192\n\t" \
  "ds_read_b64_tr_b16 a[126:127], v26 offset:8704\n\t" \
  "s_waitcnt lgkmcnt(0)\n\t" \
  "s_barrier\n\t" \
  "s_add_u32 s66, s92, 3670016\n\t" \
  "s_addc_u32 s67, s93, 0\n\t" \
  "s_lshl_b32 s55, s70, 12\n\t" \
  "s_add_u32 m0, s55, 98304\n\t" \
  "s_nop 0\n\t" \
  "global_load_lds_dwordx4 v21, s[66:67]\n\t" \
  "s_add_u32 m0, s55, 99328\n\t" \
  "s_nop 0\n\t" \
  "global_load_lds_dwordx4 v22, s[66:67]\n\t" \
  "s_add_u32 m0, s55, 100352\n\t" \
  "s_nop 0\n\t" \
  "global_load_lds_dwordx4 v23, s[66:67]\n\t" \
  "s_add_u32 m0, s55, 101376\n\t" \
  "s_nop 0\n\t" \
  "global_load_lds_dwordx4 v24, s[66:67]\n\t" \
  "s_waitcnt vmcnt(28)\n\t" \
  "s_barrier\n\t" \
  "v_xor_b32 v26, 0, v25\n\t" \
  "v_add_u32 v26, 114688, v26\n\t" \
  "ds_read_b64_tr_b16 a[128:129], v26 offset:0\n\t" \
  "ds_read_b64_tr_b16 a[130:131], v26 offset:512\n\t" \
  "ds_read_b64_tr_b16 a[132:133], v26 offset:8192\n\t" \
  "ds_read_b64_tr_b16 a[134:135], v26 offset:8704\n\t" \
  "v_xor_b32 v26, 32, v25\n\t" \
  "v_add_u32 v26, 114688, v26\n\t" \
  "ds_read_b64_tr_b16 a[136:137], v26 offset:0\n\t" \
  "ds_read_b64_tr_b16 a[138:139], v26 offset:512\n\t" \
  "ds_read_b64_tr_b16 a[140:141], v26 offset:8192\n\t" \
  "ds_read_b64_tr_b16 a[142:143], v26 offset:8704\n\t" \
  "s_waitcnt lgkmcnt(0)\n\t" \
  "s_barrier\n\t" \
  "s_add_u32 s66, s92, 4194304\n\t" \
  "s_addc_u32 s67, s93, 0\n\t" \
  "s_lshl_b32 s55, s70, 12\n\t" \
  "s_add_u32 m0, s55, 114688\n\t" \
  "s_nop 0\n\t" \
  "global_load_lds_dwordx4 v21, s[66:67]\n\t" \
  "s_add_u32 m0, s55, 115712\n\t" \
  "s_nop 0\n\t" \
  "global_load_lds_dwordx4 v22, s[66:67]\n\t" \
  "s_add_u32 m0, s55, 116736\n\t" \
  "s_nop 0\n\t" \
  "global_load_lds_dwordx4 v23, s[66:67]\n\t" \
  "s_add_u32 m0, s55, 117760\n\t" \
  "s_nop 0\n\t" \
  "global_load_lds_dwordx4 v24, s[66:67]\n\t" \
  "s_waitcnt vmcnt(28)\n\t" \
  "s_barrier\n\t" \
  "v_xor_b32 v26, 0, v25\n\t" \
  "v_add_u32 v26, 81920, v26\n\t" \
  "ds_read_b64_tr_b16 v[0:1], v26 offset:0\n\t" \
  "ds_read_b64_tr_b16 v[2:3], v26 offset:512\n\t" \
  "ds_read_b64_tr_b16 v[4:5], v26 offset:8192\n\t" \
  "ds_read_b64_tr_b16 v[6:7], v26 offset:8704\n\t" \
  "v_xor_b32 v26, 32, v25\n\t" \
  "v_add_u32 v26, 81920, v26\n\t" \
  "ds_read_b64_tr_b16 v[8:9], v26 offset:0\n\t" \
  "ds_read_b64_tr_b16 v[10:11], v26 offset:512\n\t" \
  "ds_read_b64_tr_b16 v[12:13], v26 offset:8192\n\t" \
  "ds_read_b64_tr_b16 v[14:15], v26 offset:8704\n\t" \
  "s_waitcnt vmcnt(24) lgkmcnt(0)\n\t" \
  "s_barrier\n\t" \
  "v_xor_b32 v26, 0, v25\n\t" \
  "v_add_u32 v26, 65536, v26\n\t" \
  "ds_read_b64_tr_b16 v[100:101], v26 offset:0\n\t" \
  "ds_read_b64_tr_b16 v[102:103], v26 offset:512\n\t" \
  "ds_read_b64_tr_b16 v[104:105], v26 offset:8192\n\t" \
  "ds_read_b64_tr_b16 v[106:107], v26 offset:8704\n\t" \
  "v_xor_b32 v26, 32, v25\n\t" \
  "v_add_u32 v26, 65536, v26\n\t" \
  "ds_read_b64_tr_b16 v[108:109], v26 offset:0\n\t" \
  "ds_read_b64_tr_b16 v[110:111], v26 offset:512\n\t" \
  "ds_read_b64_tr_b16 v[112:113], v26 offset:8192\n\t" \
  "ds_read_b64_tr_b16 v[114:115], v26 offset:8704\n\t" \
  "ds_write_b128 v123, v[0:3] offset:16384\n\t" \
  "ds_write_b128 v123, v[4:7] offset:17408\n\t" \
  "ds_write_b128 v123, v[8:11] offset:18432\n\t" \
  "ds_write_b128 v123, v[12:15] offset:19456\n\t" \
  "s_waitcnt vmcnt(20) lgkmcnt(0)\n\t" \
  "s_barrier\n\t" \
  "v_xor_b32 v26, 0, v25\n\t" \
  "v_add_u32 v26, 49152, v26\n\t" \
  "ds_read_b64_tr_b16 v[0:1], v26 offset:0\n\t" \
  "ds_read_b64_tr_b16 v[2:3], v26 offset:512\n\t" \
  "ds_read_b64_tr_b16 v[4:5], v26 offset:8192\n\t" \
  "ds_read_b64_tr_b16 v[6:7], v26 offset:8704\n\t" \
  "v_xor_b32 v26, 32, v25\n\t" \
  "v_add_u32 v26, 49152, v26\n\t" \
  "ds_read_b64_tr_b16 v[8:9], v26 offset:0\n\t" \
  "ds_read_b64_tr_b16 v[10:11], v26 offset:512\n\t" \
  "ds_read_b64_tr_b16 v[12:13], v26 offset:8192\n\t" \
  "ds_read_b64_tr_b16 v[14:15], v26 offset:8704\n\t" \
  "ds_write_b128 v123, v[100:103] offset:0\n\t" \
  "ds_write_b128 v123, v[104:107] offset:1024\n\t" \
  "ds_write_b128 v123, v[108:111] offset:2048\n\t" \
  "ds_write_b128 v123, v[112:115] offset:3072\n\t" \
  "s_waitcnt vmcnt(16) lgkmcnt(0)\n\t" \
  "s_barrier\n\t" \
  "v_xor_b32 v26, 0, v25\n\t" \
  "v_add_u32 v26, 32768, v26\n\t" \
  "ds_read_b64_tr_b16 v[100:101], v26 offset:0\n\t" \
  "ds_read_b64_tr_b16 v[102:103], v26 offset:512\n\t" \
  "ds_read_b64_tr_b16 v[104:105], v26 offset:8192\n\t" \
  "ds_read_b64_tr_b16 v[106:107], v26 offset:8704\n\t" \
  "v_xor_b32 v26, 32, v25\n\t" \
  "v_add_u32 v26, 32768, v26\n\t" \
  "ds_read_b64_tr_b16 v[108:109], v26 offset:0\n\t" \
  "ds_read_b64_tr_b16 v[110:111], v26 offset:512\n\t" \
  "ds_read_b64_tr_b16 v[112:113], v26 offset:8192\n\t" \
  "ds_read_b64_tr_b16 v[114:115], v26 offset:8704\n\t" \
  "ds_write_b128 v122, v[0:3] offset:49152\n\t" \
  "ds_write_b128 v122, v[4:7] offset:50176\n\t" \
  "ds_write_b128 v122, v[8:11] offset:51200\n\t" \
  "ds_write_b128 v122, v[12:15] offset:52224\n\t" \
  "v_cmp_eq_u32 vcc, 0, v135\n\t" \
  "s_nop 0\n\t" \
  "s_cmp_eq_u64 vcc, 0\n\t" \
  "s_cselect_b32 s62, 1, 0\n\t" \
  "s_cbranch_scc0 Lrec_r0x_%=\n\t" \
  "buffer_load_dwordx4 v[36:39], v125, s[24:27], s46 offen sc1\n\t" \
  "buffer_load_dwordx4 v[40:43], v125, s[24:27], s46 offen offset:1024 sc1\n\t" \
  "buffer_load_dwordx4 v[60:63], v125, s[28:31], s46 offen sc1\n\t" \
  "buffer_load_dwordx4 v[64:67], v125, s[28:31], s46 offen offset:1024 sc1\n\t" \
  "v_mov_b32 v70, v135\n\t" \
  "s_mov_b32 s57, 512\n\t" \
  "v_mbcnt_lo_u32_b32 v135, -1, 0\n\t" \
  "v_mbcnt_hi_u32_b32 v135, -1, v135\n\t" \
  "v_lshlrev_b32 v135, 2, v135\n\t" \
  "buffer_load_dword v135, v135, s[36:39], s57 offen sc1\n\t" \
  "v_lshlrev_b32 v56, 3, v16\n\t" \
  "s_mov_b32 s55, 63488\n\t" \
  "buffer_load_dwordx2 v[56:57], v56, s[36:39], s55 offen sc1\n\t" \
  "Lrec_r0x_%=:\n\t" \
  "s_waitcnt vmcnt(12) lgkmcnt(0)\n\t" \
  "s_barrier\n\t" \
  "v_xor_b32 v26, 0, v25\n\t" \
  "v_add_u32 v26, 16384, v26\n\t" \
  "ds_read_b64_tr_b16 v[0:1], v26 offset:0\n\t" \
  "ds_read_b64_tr_b16 v[2:3], v26 offset:512\n\t" \
  "ds_read_b64_tr_b16 v[4:5], v26 offset:8192\n\t" \
  "ds_read_b64_tr_b16 v[6:7], v26 offset:8704\n\t" \
  "v_xor_b32 v26, 32, v25\n\t" \
  "v_add_u32 v26, 16384, v26\n\t" \
  "ds_read_b64_tr_b16 v[8:9], v26 offset:0\n\t" \
  "ds_read_b64_tr_b16 v[10:11], v26 offset:512\n\t" \
  "ds_read_b64_tr_b16 v[12:13], v26 offset:8192\n\t" \
  "ds_read_b64_tr_b16 v[14:15], v26 offset:8704\n\t" \
  "ds_write_b128 v122, v[100:103] offset:32768\n\t" \
  "ds_write_b128 v122, v[104:107] offset:33792\n\t" \
  "ds_write_b128 v122, v[108:111] offset:34816\n\t" \
  "ds_write_b128 v122, v[112:115] offset:35840\n\t" \
  "s_waitcnt vmcnt(8) lgkmcnt(0)\n\t" \
  "s_barrier\n\t" \
  "v_xor_b32 v26, 0, v25\n\t" \
  "v_add_u32 v26, 0, v26\n\t" \
  "ds_read_b64_tr_b16 v[100:101], v26 offset:0\n\t" \
  "ds_read_b64_tr_b16 v[102:103], v26 offset:512\n\t" \
  "ds_read_b64_tr_b16 v[104:105], v26 offset:8192\n\t" \
  "ds_read_b64_tr_b16 v[106:107], v26 offset:8704\n\t" \
  "v_xor_b32 v26, 32, v25\n\t" \
  "v_add_u32 v26, 0, v26\n\t" \
  "ds_read_b64_tr_b16 v[108:109], v26 offset:0\n\t" \
  "ds_read_b64_tr_b16 v[110:111], v26 offset:512\n\t" \
  "ds_read_b64_tr_b16 v[112:113], v26 offset:8192\n\t" \
  "ds_read_b64_tr_b16 v[114:115], v26 offset:8704\n\t" \
  "ds_write_b128 v122, v[0:3] offset:16384\n\t" \
  "ds_write_b128 v122, v[4:7] offset:17408\n\t" \
  "ds_write_b128 v122, v[8:11] offset:18432\n\t" \
  "ds_write_b128 v122, v[12:15] offset:19456\n\t" \
  "s_waitcnt vmcnt(4) lgkmcnt(0)\n\t" \
  "s_barrier\n\t" \
  "v_xor_b32 v26, 0, v25\n\t" \
  "v_add_u32 v26, 98304, v26\n\t" \
  "ds_read_b64_tr_b16 v[0:1], v26 offset:0\n\t" \
  "ds_read_b64_tr_b16 v[2:3], v26 offset:512\n\t" \
  "ds_read_b64_tr_b16 v[4:5], v26 offset:8192\n\t" \
  "ds_read_b64_tr_b16 v[6:7], v26 offset:8704\n\t" \
  "v_xor_b32 v26, 32, v25\n\t" \
  "v_add_u32 v26, 98304, v26\n\t" \
  "ds_read_b64_tr_b16 v[8:9], v26 offset:0\n\t" \
  "ds_read_b64_tr_b16 v[10:11], v26 offset:512\n\t" \
  "ds_read_b64_tr_b16 v[12:13], v26 offset:8192\n\t" \
  "ds_read_b64_tr_b16 v[14:15], v26 offset:8704\n\t" \
  "ds_write_b128 v122, v[100:103] offset:0\n\t" \
  "ds_write_b128 v122, v[104:107] offset:1024\n\t" \
  "ds_write_b128 v122, v[108:111] offset:2048\n\t" \
  "ds_write_b128 v122, v[112:115] offset:3072\n\t" \
  "s_waitcnt vmcnt(0) lgkmcnt(0)\n\t" \
  "s_barrier\n\t" \
  "v_xor_b32 v26, 0, v25\n\t" \
  "v_add_u32 v26, 114688, v26\n\t" \
  "ds_read_b64_tr_b16 v[100:101], v26 offset:0\n\t" \
  "ds_read_b64_tr_b16 v[102:103], v26 offset:512\n\t" \
  "ds_read_b64_tr_b16 v[104:105], v26 offset:8192\n\t" \
  "ds_read_b64_tr_b16 v[106:107], v26 offset:8704\n\t" \
  "v_xor_b32 v26, 32, v25\n\t" \
  "v_add_u32 v26, 114688, v26\n\t" \
  "ds_read_b64_tr_b16 v[108:109], v26 offset:0\n\t" \
  "ds_read_b64_tr_b16 v[110:111], v26 offset:512\n\t" \
  "ds_read_b64_tr_b16 v[112:113], v26 offset:8192\n\t" \
  "ds_read_b64_tr_b16 v[114:115], v26 offset:8704\n\t" \
  "ds_write_b128 v123, v[0:3] offset:32768\n\t" \
  "ds_write_b128 v123, v[4:7] offset:33792\n\t" \
  "ds_write_b128 v123, v[8:11] offset:34816\n\t" \
  "ds_write_b128 v123, v[12:15] offset:35840\n\t" \
  "s_waitcnt vmcnt(0) lgkmcnt(0)\n\t" \
  "s_barrier\n\t" \
  "ds_write_b128 v123, v[100:103] offset:49152\n\t" \
  "ds_write_b128 v123, v[104:107] offset:50176\n\t" \
  "ds_write_b128 v123, v[108:111] offset:51200\n\t" \
  "ds_write_b128 v123, v[112:115] offset:52224\n\t" \
  "Lrec_pb_%=:\n\t" \
  "s_mov_b32 s58, 0\n\t" \
  "Lrec_l0_%=:\n\t" \
  "v_cmp_eq_u32 vcc, 0, v68\n\t" \
  "s_nop 0\n\t" \
  "s_cbranch_vccz Lrec_l1_%=\n\t" \
  "s_add_u32 s58, s58, 1\n\t" \
  "s_cmp_gt_u32 s58, 524288\n\t" \
  "s_cbranch_scc1 Lrec_tmo_%=\n\t" \
  "s_sleep 2\n\t" \
  "v_lshlrev_b32 v68, 2, v16\n\t" \
  "s_mov_b32 s55, 61440\n\t" \
  "buffer_load_dword v68, v68, s[36:39], s55 offen sc1\n\t" \
  "s_waitcnt vmcnt(0)\n\t" \
  "s_branch Lrec_l0_%=\n\t" \
  "Lrec_l1_%=:\n\t" \
  "s_nop 1\n\t" \
  "v_add_f32_dpp v68, v68, v68 row_ror:1 row_mask:0xf bank_mask:0xf\n\t" \
  "s_nop 1\n\t" \
  "v_add_f32_dpp v68, v68, v68 row_ror:2 row_mask:0xf bank_mask:0xf\n\t" \
  "s_nop 1\n\t" \
  "v_add_f32_dpp v68, v68, v68 row_ror:4 row_mask:0xf bank_mask:0xf\n\t" \
  "s_nop 1\n\t" \
  "v_add_f32_dpp v68, v68, v68 row_ror:8 row_mask:0xf bank_mask:0xf\n\t" \
  "s_nop 1\n\t" \
  "v_add_f32_dpp v68, v68, v68 row_bcast:15 row_mask:0xa bank_mask:0xf\n\t" \
  "s_nop 1\n\t" \
  "v_add_f32_dpp v68, v68, v68 row_bcast:31 row_mask:0xc bank_mask:0xf\n\t" \
  "v_mul_f32 v68, 0x3f866666, v68\n\t" \
  "s_nop 0\n\t" \
  "v_readlane_b32 s60, v68, 63\n\t" \
  "s_nop 1\n\t" \
  "s_cmp_eq_u32 s62, 1\n\t" \
  "s_cbranch_scc1 Lrec_r1_%=\n\t" \
  "s_mov_b32 s58, 0\n\t" \
  "Lrec_r2_%=:\n\t" \
  "v_mbcnt_lo_u32_b32 v135, -1, 0\n\t" \
  "v_mbcnt_hi_u32_b32 v135, -1, v135\n\t" \
  "v_lshlrev_b32 v135, 2, v135\n\t" \
  "buffer_load_dword v135, v135, s[36:39], s57 offen sc1\n\t" \
  "s_waitcnt vmcnt(0)\n\t" \
  "v_cmp_eq_u32 vcc, 0, v135\n\t" \
  "s_nop 0\n\t" \
  "s_cmp_eq_u64 vcc, 0\n\t" \
  "s_cselect_b32 s62, 1, 0\n\t" \
  "s_cbranch_scc1 Lrec_r3_%=\n\t" \
  "s_add_u32 s58, s58, 1\n\t" \
  "s_cmp_gt_u32 s58, 524288\n\t" \
  "s_cbranch_scc1 Lrec_tmo_%=\n\t" \
  "s_sleep 2\n\t" \
  "s_branch Lrec_r2_%=\n\t" \
  "Lrec_r3_%=:\n\t" \
  "buffer_load_dwordx4 v[36:39], v125, s[24:27], s46 offen sc1\n\t" \
  "buffer_load_dwordx4 v[40:43], v125, s[24:27], s46 offen offset:1024 sc1\n\t" \
  "buffer_load_dwordx4 v[60:63], v125, s[28:31], s46 offen sc1\n\t" \
  "buffer_load_dwordx4 v[64:67], v125, s[28:31], s46 offen offset:1024 sc1\n\t" \
  "v_mov_b32 v70, v135\n\t" \
  "s_mov_b32 s57, 512\n\t" \
  "v_mbcnt_lo_u32_b32 v135, -1, 0\n\t" \
  "v_mbcnt_hi_u32_b32 v135, -1, v135\n\t" \
  "v_lshlrev_b32 v135, 2, v135\n\t" \
  "buffer_load_dword v135, v135, s[36:39], s57 offen sc1\n\t" \
  "v_lshlrev_b32 v56, 3, v16\n\t" \
  "s_mov_b32 s55, 63488\n\t" \
  "buffer_load_dwordx2 v[56:57], v56, s[36:39], s55 offen sc1\n\t" \
  "Lrec_r1_%=:\n\t" \
  "s_waitcnt vmcnt(0) lgkmcnt(0)\n\t" \
  "s_barrier\n\t" \
  "v_mov_b32 v71, v135\n\t" \
  "v_mov_b32 v135, v70\n\t" \
  "v_cmp_eq_u32 vcc, 0, v135\n\t" \
  "s_nop 0\n\t" \
  "v_max_f32_dpp v135, v135, v135 row_ror:1 row_mask:0xf bank_mask:0xf\n\t" \
  "s_cmp_eq_u64 vcc, 0\n\t" \
  "s_cselect_b32 s78, 1, 0\n\t" \
  "v_max_f32_dpp v135, v135, v135 row_ror:2 row_mask:0xf bank_mask:0xf\n\t" \
  "s_nop 1\n\t" \
  "v_max_f32_dpp v135, v135, v135 row_ror:4 row_mask:0xf bank_mask:0xf\n\t" \
  "s_nop 1\n\t" \
  "v_max_f32_dpp v135, v135, v135 row_ror:8 row_mask:0xf bank_mask:0xf\n\t" \
  "s_nop 1\n\t" \
  "v_max_f32_dpp v135, v135, v135 row_bcast:15 row_mask:0xa bank_mask:0xf\n\t" \
  "v_mul_f32 v135, s60, v135\n\t" \
  "s_nop 0\n\t" \
  "v_readlane_b32 s71, v135, 31\n\t" \
  "s_nop 1\n\t" \
  "s_lshr_b32 s71, s71, 23\n\t" \
  "s_mov_b32 s52, s71\n\t" \
  "v_mov_b32 v135, v71\n\t" \
  "s_mov_b32 s58, 0\n\t" \
  "Lrec_c0_%=:\n\t" \
  "v_min_u32 v58, v56, v57\n\t" \
  "v_cmp_eq_u32 vcc, 0, v58\n\t" \
  "s_nop 0\n\t" \
  "s_cbranch_vccz Lrec_c1_%=\n\t" \
  "s_add_u32 s58, s58, 1\n\t" \
  "s_cmp_gt_u32 s58, 524288\n\t" \
  "s_cbranch_scc1 Lrec_tmo_%=\n\t" \
  "s_sleep 2\n\t" \
  "v_lshlrev_b32 v56, 3, v16\n\t" \
  "s_mov_b32 s55, 63488\n\t" \
  "buffer_load_dwordx2 v[56:57], v56, s[36:39], s55 offen sc1\n\t" \
  "s_waitcnt vmcnt(0)\n\t" \
  "s_branch Lrec_c0_%=\n\t" \
  "Lrec_c1_%=:\n\t" \
  "v_max_f32 v56, v56, v57\n\t" \
  "s_nop 1\n\t" \
  "v_max_f32_dpp v56, v56, v56 row_ror:1 row_mask:0xf bank_mask:0xf\n\t" \
  "s_nop 1\n\t" \
  "v_max_f32_dpp v56, v56, v56 row_ror:2 row_mask:0xf bank_mask:0xf\n\t" \
  "s_nop 1\n\t" \
  "v_max_f32_dpp v56, v56, v56 row_ror:4 row_mask:0xf bank_mask:0xf\n\t" \
  "s_nop 1\n\t" \
  "v_max_f32_dpp v56, v56, v56 row_ror:8 row_mask:0xf bank_mask:0xf\n\t" \
  "s_nop 1\n\t" \
  "v_max_f32_dpp v56, v56, v56 row_bcast:15 row_mask:0xa bank_mask:0xf\n\t" \
  "s_nop 1\n\t" \
  "v_max_f32_dpp v56, v56, v56 row_bcast:31 row_mask:0xc bank_mask:0xf\n\t" \
  "v_sqrt_f32 v56, v56\n\t" \
  "s_nop 0\n\t" \
  "v_mul_f32 v56, 0x41c3d70a, v56\n\t" \
  "v_mul_f32 v56, s60, v56\n\t" \
  "s_nop 0\n\t" \
  "v_readlane_b32 s53, v56, 63\n\t" \
  "s_nop 1\n\t" \
  "s_lshr_b32 s53, s53, 23\n\t" \
  "s_sub_i32 s53, s53, 126\n\t" \
  "s_lshl_b32 s55, s48, 23\n\t" \
  "s_sub_i32 s49, 1040356334, s55\n\t" \
  "s_sub_i32 s50, s49, s55\n\t" \
  "s_add_i32 s55, s48, 126\n\t" \
  "s_mul_i32 s55, s55, 8388736\n\t" \
  "v_mov_b32 v120, s55\n\t" \
  "v_mov_b32 v121, s55\n\t" \
  "s_sub_i32 s54, s53, s48\n\t" \
  "v_cvt_f32_i32 v133, s48\n\t" \
  "ds_write_b128 v125, v[36:39] offset:17408\n\t" \
  "ds_write_b128 v125, v[40:43] offset:18432\n\t" \
  "v_cmp_eq_u32 vcc, 0, v135\n\t" \
  "v_max_f32_dpp v135, v135, v135 row_ror:1 row_mask:0xf bank_mask:0xf\n\t" \
  "s_cmp_eq_u64 vcc, 0\n\t" \
  "s_cselect_b32 s78, 1, 0\n\t" \
  "v_max_f32_dpp v135, v135, v135 row_ror:2 row_mask:0xf bank_mask:0xf\n\t" \
  "s_nop 1\n\t" \
  "v_max_f32_dpp v135, v135, v135 row_ror:4 row_mask:0xf bank_mask:0xf\n\t" \
  "s_nop 1\n\t" \
  "v_max_f32_dpp v135, v135, v135 row_ror:8 row_mask:0xf bank_mask:0xf\n\t" \
  "s_nop 1\n\t" \
  "v_max_f32_dpp v135, v135, v135 row_bcast:15 row_mask:0xa bank_mask:0xf\n\t" \
  "v_mul_f32 v135, s60, v135\n\t" \
  "s_nop 0\n\t" \
  "v_readlane_b32 s71, v135, 31\n\t" \
  "s_nop 1\n\t" \
  "s_lshr_b32 s71, s71, 23\n\t" \
  "s_waitcnt lgkmcnt(0)\n\t" \
  "v_mov_b32 v36, 0\n\t" \
  "v_mov_b32 v37, 0\n\t" \
  "v_mov_b32 v38, 0\n\t" \
  "v_mov_b32 v39, 0\n\t" \
  "v_mov_b32 v40, 0\n\t" \
  "v_mov_b32 v41, 0\n\t" \
  "v_mov_b32 v42, 0\n\t" \
  "v_mov_b32 v43, 0\n\t" \
  "ds_read_b128 v[68:71], v122 offset:0\n\t" \
  "ds_read_b128 v[72:75], v122 offset:1024\n\t" \
  "ds_read_b128 v[76:79], v122 offset:2048\n\t" \
  "ds_read_b128 v[80:83], v122 offset:3072\n\t" \
  "ds_read_b128 v[84:87], v122 offset:16384\n\t" \
  "ds_read_b128 v[88:91], v122 offset:17408\n\t" \
  "ds_read_b128 v[92:95], v122 offset:18432\n\t" \
  "ds_read_b128 v[96:99], v122 offset:19456\n\t" \
  "ds_read_b128 v[100:103], v122 offset:32768\n\t" \
  "ds_read_b128 v[104:107], v122 offset:33792\n\t" \
  "ds_read_b128 v[108:111], v122 offset:34816\n\t" \
  "ds_read_b128 v[112:115], v122 offset:35840\n\t" \
  "v_mov_b32 v44, 0\n\t" \
  "v_mov_b32 v45, 0\n\t" \
  "v_mov_b32 v46, 0\n\t" \
  "v_mov_b32 v47, 0\n\t" \
  "v_mov_b32 v48, 0\n\t" \
  "v_mov_b32 v49, 0\n\t" \
  "v_mov_b32 v50, 0\n\t" \
  "v_mov_b32 v51, 0\n\t" \
  "v_mov_b32 v52, 0\n\t" \
  "v_mov_b32 v53, 0\n\t" \
  "v_mov_b32 v54, 0\n\t" \
  "v_mov_b32 v55, 0\n\t" \
  "v_mov_b32 v56, 0\n\t" \
  "v_mov_b32 v57, 0\n\t" \
  "v_mov_b32 v58, 0\n\t" \
  "v_mov_b32 v59, 0\n\t" \
  "v_mbcnt_lo_u32_b32 v32, -1, 0\n\t" \
  "v_mbcnt_hi_u32_b32 v32, -1, v32\n\t" \
  "v_and_b32 v33, 7, v32\n\t" \
  "v_lshrrev_b32 v35, 3, v32\n\t" \
  "v_and_b32 v34, 3, v35\n\t" \
  "s_lshl_b32 s55, s70, 11\n\t" \
  "v_lshlrev_b32 v35, 8, v35\n\t" \
  "v_lshl_add_u32 v35, v33, 4, v35\n\t" \
  "v_add_u32 v24, s55, v35\n\t" \
  "s_lshl_b32 s55, s70, 13\n\t" \
  "s_add_u32 s55, s55, 128\n\t" \
  "v_lshrrev_b32 v35, 2, v33\n\t" \
  "v_lshl_add_u32 v35, v34, 1, v35\n\t" \
  "v_lshlrev_b32 v35, 10, v35\n\t" \
  "v_and_b32 v25, 3, v33\n\t" \
  "v_lshl_add_u32 v35, v25, 4, v35\n\t" \
  "v_add_u32 v25, s55, v35\n\t" \
  "v_lshrrev_b32 v35, 5, v32\n\t" \
  "v_lshl_add_u32 v25, v35, 9, v25\n\t" \
  "v_lshrrev_b32 v35, 2, v33\n\t" \
  "v_lshlrev_b32 v35, 7, v35\n\t" \
  "v_lshl_add_u32 v35, v34, 5, v35\n\t" \
  "v_and_b32 v33, 3, v33\n\t" \
  "v_lshl_add_u32 v35, v33, 2, v35\n\t" \
  "s_lshl_b32 s55, s70, 8\n\t" \
  "s_lshr_b32 s56, s70, 1\n\t" \
  "s_mul_i32 s56, s56, 4096\n\t" \
  "s_add_u32 s56, s56, s55\n\t" \
  "s_add_u32 s56, s56, 143360\n\t" \
  "v_add_u32 v30, s56, v35\n\t" \
  "s_add_u32 s56, s55, 162816\n\t" \
  "v_lshlrev_b32 v31, 2, v32\n\t" \
  "v_add_u32 v31, s56, v31\n\t" \
  "s_lshl_b32 s55, s70, 9\n\t" \
  "s_add_u32 s55, s55, 139264\n\t" \
  "v_lshrrev_b32 v32, 5, v32\n\t" \
  "v_lshl_add_u32 v35, v32, 8, v35\n\t" \
  "v_add_u32 v26, s55, v35\n\t" \
  "v_lshl_add_u32 v30, v32, 4, v30\n\t" \
  "v_add_u32 v27, 2048, v26\n\t" \
  "v_add_u32 v28, 4608, v26\n\t" \
  "v_add_u32 v29, 6656, v26\n\t" \
  "v_mov_b32 v35, 0\n\t" \
  "v_mov_b32 v135, v31\n\t" \
  "s_mov_b32 s86, 0\n\t" \
  "s_mov_b32 s87, 0\n\t" \
  "s_branch Lrec_top_%=\n\t" \
  "Lrec_poll_%=:\n\t" \
  "buffer_load_dwordx4 v[0:3], v24, s[20:23], s45 offen sc1\n\t" \
  "buffer_load_dwordx4 v[4:7], v24, s[20:23], s61 offen sc1\n\t" \
  "buffer_load_dwordx4 v[8:11], v24, s[20:23], s62 offen sc1\n\t" \
  "buffer_load_dwordx4 v[12:15], v24, s[20:23], s63 offen sc1\n\t" \
  "buffer_load_dwordx4 v[16:19], v25, s[20:23], s45 offen sc1\n\t" \
  "s_waitcnt vmcnt(5)\n\t" \
  "ds_write_b128 v125, v[36:39] offset:17408\n\t" \
  "ds_write_b128 v125, v[40:43] offset:18432\n\t" \
  "v_cmp_eq_u32 vcc, 0, v135\n\t" \
  "v_max_f32_dpp v135, v135, v135 row_ror:1 row_mask:0xf bank_mask:0xf\n\t" \
  "s_cmp_eq_u64 vcc, 0\n\t" \
  "s_cselect_b32 s78, 1, 0\n\t" \
  "v_max_f32_dpp v135, v135, v135 row_ror:2 row_mask:0xf bank_mask:0xf\n\t" \
  "s_nop 1\n\t" \
  "v_max_f32_dpp v135, v135, v135 row_ror:4 row_mask:0xf bank_mask:0xf\n\t" \
  "s_nop 1\n\t" \
  "v_max_f32_dpp v135, v135, v135 row_ror:8 row_mask:0xf bank_mask:0xf\n\t" \
  "s_nop 1\n\t" \
  "v_max_f32_dpp v135, v135, v135 row_bcast:15 row_mask:0xa bank_mask:0xf\n\t" \
  "v_mul_f32 v135, s60, v135\n\t" \
  "s_nop 0\n\t" \
  "v_readlane_b32 s71, v135, 31\n\t" \
  "s_nop 1\n\t" \
  "s_lshr_b32 s71, s71, 23\n\t" \
  "s_waitcnt lgkmcnt(0)\n\t" \
  "v_mov_b32 v36, 0\n\t" \
  "v_mov_b32 v37, 0\n\t" \
  "v_mov_b32 v38, 0\n\t" \
  "v_mov_b32 v39, 0\n\t" \
  "v_mov_b32 v40, 0\n\t" \
  "v_mov_b32 v41, 0\n\t" \
  "v_mov_b32 v42, 0\n\t" \
  "v_mov_b32 v43, 0\n\t" \
  "Lrec_dec_%=:\n\t" \
  "v_mov_b32 v20, 0\n\t" \
  "v_mov_b32 v135, v31\n\t" \
  "s_cmp_lt_u32 s44, 63\n\t" \
  "s_waitcnt vmcnt(4)\n\t" \
  "v_or3_b32 v20, v20, v0, v2\n\t" \
  "v_cvt_f32_i32 v0, v0\n\t" \
  "v_cvt_f32_i32 v2, v2\n\t" \
  "v_cvt_f32_i32 v1, v1\n\t" \
  "v_cvt_f32_i32 v3, v3\n\t" \
  "v_cvt_pk_bf16_f32 v22, v0, v2\n\t" \
  "v_cvt_pk_bf16_f32 v23, v1, v3\n\t" \
  "ds_write2_b32 v26, v22, v23 offset1:4\n\t" \
  "s_waitcnt vmcnt(3)\n\t" \
  "v_or3_b32 v20, v20, v4, v6\n\t" \
  "v_cvt_f32_i32 v4, v4\n\t" \
  "v_cvt_f32_i32 v6, v6\n\t" \
  "v_cvt_f32_i32 v5, v5\n\t" \
  "v_cvt_f32_i32 v7, v7\n\t" \
  "v_cvt_pk_bf16_f32 v22, v4, v6\n\t" \
  "v_cvt_pk_bf16_f32 v23, v5, v7\n\t" \
  "ds_write2_b32 v27, v22, v23 offset1:4\n\t" \
  "s_waitcnt vmcnt(2)\n\t" \
  "v_or3_b32 v20, v20, v8, v10\n\t" \
  "v_cvt_f32_i32 v8, v8\n\t" \
  "v_cvt_f32_i32 v10, v10\n\t" \
  "v_cvt_f32_i32 v9, v9\n\t" \
  "v_cvt_f32_i32 v11, v11\n\t" \
  "v_cvt_pk_bf16_f32 v22, v8, v10\n\t" \
  "v_cvt_pk_bf16_f32 v23, v9, v11\n\t" \
  "ds_write2_b32 v28, v22, v23 offset1:4\n\t" \
  "s_waitcnt vmcnt(1)\n\t" \
  "v_or3_b32 v20, v20, v12, v14\n\t" \
  "v_cvt_f32_i32 v12, v12\n\t" \
  "v_cvt_f32_i32 v14, v14\n\t" \
  "v_cvt_f32_i32 v13, v13\n\t" \
  "v_cvt_f32_i32 v15, v15\n\t" \
  "v_cvt_pk_bf16_f32 v22, v12, v14\n\t" \
  "v_cvt_pk_bf16_f32 v23, v13, v15\n\t" \
  "ds_write2_b32 v29, v22, v23 offset1:4\n\t" \
  "s_waitcnt vmcnt(0)\n\t" \
  "v_or3_b32 v20, v20, v16, v18\n\t" \
  "v_permlane32_swap_b32 v16, v17\n\t" \
  "v_permlane32_swap_b32 v18, v19\n\t" \
  "v_add_u32 v16, v16, v17\n\t" \
  "v_add_u32 v18, v18, v19\n\t" \
  "v_cvt_f32_i32 v16, v16\n\t" \
  "v_cvt_f32_i32 v18, v18\n\t" \
  "v_cvt_pk_bf16_f32 v22, v16, v18\n\t" \
  "ds_write_b32 v30, v22\n\t" \
  "v_and_b32 v20, 63, v20\n\t" \
  "v_cmp_ne_u32 vcc, 0, v20\n\t" \
  "v_max3_f32 v134, |v0|, |v1|, |v2|\n\t" \
  "v_max3_f32 v134, v134, |v3|, |v4|\n\t" \
  "v_max3_f32 v134, v134, |v5|, |v6|\n\t" \
  "v_max3_f32 v134, v134, |v7|, |v8|\n\t" \
  "v_max3_f32 v134, v134, |v9|, |v10|\n\t" \
  "v_max3_f32 v134, v134, |v11|, |v12|\n\t" \
  "v_max3_f32 v134, v134, |v13|, |v14|\n\t" \
  "v_max3_f32 v134, v134, |v15|, |v16|\n\t" \
  "v_max_f32 v134, v134, |v18|\n\t" \
  "s_cbranch_vccnz Lrec_retry_%=\n\t" \
  "s_cbranch_scc0 Lrec_fin_%=\n\t" \
  "Lrec_top_%=:\n\t" \
  "s_waitcnt lgkmcnt(0)\n\t" \
  "s_barrier\n\t" \
  "ds_read_b128 v[0:3], v124 offset:0\n\t" \
  "ds_read_b128 v[4:7], v124 offset:1024\n\t" \
  "ds_read_b128 v[20:23], v124 offset:5120\n\t" \
  "s_mov_b64 exec, s[94:95]\n\t" \
  "buffer_store_dword v35, off, s[36:39], s96 sc1\n\t" \
  "s_mov_b64 exec, -1\n\t" \
  "ds_read_b128 v[12:15], v124 offset:3072\n\t" \
  "s_waitcnt lgkmcnt(3)\n\t" \
  "v_smfmac_f32_16x16x64_bf16 v[36:39], v[0:3], v[68:75], v132\n\t" \
  "ds_read_b128 v[8:11], v124 offset:2048\n\t" \
  "v_smfmac_f32_16x16x64_bf16 v[40:43], v[0:3], v[76:83], v132\n\t" \
  "ds_read_b128 v[16:19], v124 offset:4096\n\t" \
  "s_waitcnt lgkmcnt(4)\n\t" \
  "v_smfmac_f32_16x16x64_bf16 v[40:43], v[4:7], v[92:99], v132\n\t" \
  "ds_read_b128 v[72:75], v122 offset:50176\n\t" \
  "v_smfmac_f32_16x16x64_bf16 v[36:39], v[4:7], v[84:91], v132\n\t" \
  "ds_read_b128 v[24:27], v124 offset:6144\n\t" \
  "s_waitcnt lgkmcnt(3)\n\t" \
  "v_smfmac_f32_16x16x64_bf16 v[40:43], v[8:11], v[108:115], v132\n\t" \
  "v_smfmac_f32_16x16x64_bf16 v[36:39], v[8:11], v[100:107], v132\n\t" \
  "v_smfmac_f32_16x16x64_bf16 v[36:39], v[12:15], a[0:7], v132\n\t" \
  "ds_read_b128 v[88:91], v123 offset:1024\n\t" \
  "v_smfmac_f32_16x16x64_bf16 v[40:43], v[12:15], a[8:15], v132\n\t" \
  "ds_read_b128 v[68:71], v122 offset:49152\n\t" \
  "s_waitcnt lgkmcnt(4)\n\t" \
  "v_smfmac_f32_16x16x64_bf16 v[40:43], v[16:19], a[24:31], v132\n\t" \
  "ds_read_b128 v[28:31], v124 offset:7168\n\t" \
  "v_smfmac_f32_16x16x64_bf16 v[40:43], v[20:23], a[40:47], v132\n\t" \
  "ds_read_b128 v[96:99], v123 offset:3072\n\t" \
  "s_waitcnt lgkmcnt(4)\n\t" \
  "v_smfmac_f32_16x16x64_bf16 v[40:43], v[24:27], a[56:63], v132\n\t" \
  "ds_read_b128 v[80:83], v122 offset:52224\n\t" \
  "v_smfmac_f32_16x16x64_bf16 v[36:39], v[16:19], a[16:23], v132\n\t" \
  "ds_read_b128 v[32:35], v124 offset:8192\n\t" \
  "v_smfmac_f32_16x16x64_bf16 v[36:39], v[20:23], a[32:39], v132\n\t" \
  "ds_read_b128 v[92:95], v123 offset:2048\n\t" \
  "s_waitcnt lgkmcnt(4)\n\t" \
  "v_smfmac_f32_16x16x64_bf16 v[40:43], v[28:31], a[72:79], v132\n\t" \
  "ds_read_b128 v[76:79], v122 offset:51200\n\t" \
  "v_smfmac_f32_16x16x64_bf16 v[36:39], v[24:27], a[48:55], v132\n\t" \
  "v_smfmac_f32_16x16x64_bf16 v[36:39], v[28:31], a[64:71], v132\n\t" \
  "ds_read_b128 v[84:87], v123 offset:0\n\t" \
  "s_waitcnt lgkmcnt(3)\n\t" \
  "v_smfmac_f32_16x16x64_bf16 v[40:43], v[32:35], a[88:95], v132\n\t" \
  "v_smfmac_f32_16x16x64_bf16 v[36:39], v[32:35], a[80:87], v132\n\t" \
  "ds_read_b128 v[112:115], v124 offset:16384\n\t" \
  "v_smfmac_f32_16x16x64_bf16 v[44:47], v[0:3], v[68:75], v132\n\t" \
  "ds_read_b128 v[100:103], v124 offset:13312\n\t" \
  "s_waitcnt lgkmcnt(2)\n\t" \
  "v_smfmac_f32_16x16x64_bf16 v[44:47], v[4:7], v[84:91], v132\n\t" \
  "ds_read_b128 v[88:91], v124 offset:10240\n\t" \
  "v_permlane32_swap_b32 v39, v43\n\t" \
  "v_permlane32_swap_b32 v36, v40\n\t" \
  "v_add_f32 v39, v39, v43\n\t" \
  "v_permlane32_swap_b32 v37, v41\n\t" \
  "v_add_f32 v37, v37, v41\n\t" \
  "v_smfmac_f32_16x16x64_bf16 v[48:51], v[0:3], v[76:83], v132\n\t" \
  "v_permlane32_swap_b32 v38, v42\n\t" \
  "v_smfmac_f32_16x16x64_bf16 v[48:51], v[4:7], v[92:99], v132\n\t" \
  "ds_read_b128 v[104:107], v124 offset:14336\n\t" \
  "v_add_f32 v36, v36, v40\n\t" \
  "v_smfmac_f32_16x16x64_bf16 v[44:47], v[8:11], a[96:103], v132\n\t" \
  "ds_write_b32 v135, v134\n\t" \
  "v_add_f32 v38, v38, v42\n\t" \
  "v_smfmac_f32_16x16x64_bf16 v[48:51], v[8:11], a[104:111], v132\n\t" \
  "ds_read_b128 v[96:99], v124 offset:12288\n\t" \
  "v_cvt_pk_bf16_f32 v36, v36, v37\n\t" \
  "v_smfmac_f32_16x16x64_bf16 v[44:47], v[12:15], a[112:119], v132\n\t" \
  "v_cvt_pk_bf16_f32 v37, v38, v39\n\t" \
  "v_smfmac_f32_16x16x64_bf16 v[44:47], v[16:19], a[128:135], v132\n\t" \
  "ds_write_b64 v126, v[36:37]\n\t" \
  "v_smfmac_f32_16x16x64_bf16 v[48:51], v[12:15], a[120:127], v132\n\t" \
  "ds_read_b128 v[92:95], v124 offset:11264\n\t" \
  "v_smfmac_f32_16x16x64_bf16 v[44:47], v[20:23], a[144:151], v132\n\t" \
  "ds_read_b128 v[84:87], v124 offset:9216\n\t" \
  "v_smfmac_f32_16x16x64_bf16 v[44:47], v[24:27], a[160:167], v132\n\t" \
  "ds_read_b128 v[108:111], v124 offset:15360\n\t" \
  "v_smfmac_f32_16x16x64_bf16 v[44:47], v[28:31], a[176:183], v132\n\t" \
  "s_waitcnt lgkmcnt(3)\n\t" \
  "s_barrier\n\t" \
  "v_smfmac_f32_16x16x64_bf16 v[48:51], v[16:19], a[136:143], v132\n\t" \
  "ds_read_b128 v[40:43], v124 offset:23552\n\t" \
  "v_smfmac_f32_16x16x64_bf16 v[48:51], v[20:23], a[152:159], v132\n\t" \
  "ds_read_b64_tr_b16 v[74:75], v127 offset:576\n\t" \
  "v_smfmac_f32_16x16x64_bf16 v[48:51], v[24:27], a[168:175], v132\n\t" \
  "ds_read_b64_tr_b16 v[82:83], v127 offset:1600\n\t" \
  "ds_read_b64_tr_b16 v[68:69], v127 offset:0\n\t" \
  "v_smfmac_f32_16x16x64_bf16 v[48:51], v[28:31], a[184:191], v132\n\t" \
  "ds_read_b64_tr_b16 v[72:73], v127 offset:512\n\t" \
  "s_waitcnt lgkmcnt(4)\n\t" \
  "v_max3_f32 v134, v40, v41, v42\n\t" \
  "v_smfmac_f32_16x16x64_bf16 v[44:47], v[32:35], a[192:199], v132\n\t" \
  "ds_read_b64_tr_b16 v[70:71], v127 offset:64\n\t" \
  "v_max_f32 v134, v134, v43\n\t" \
  "v_smfmac_f32_16x16x64_bf16 v[56:59], v[0:3], a[216:223], v132\n\t" \
  "ds_read_b64_tr_b16 v[80:81], v127 offset:1536\n\t" \
  "v_max_f32_dpp v134, v134, v134 row_ror:1 row_mask:0xf bank_mask:0xf\n\t" \
  "v_smfmac_f32_16x16x64_bf16 v[48:51], v[32:35], a[200:207], v132\n\t" \
  "ds_read_b64_tr_b16 v[78:79], v127 offset:1088\n\t" \
  "v_max_f32_dpp v134, v134, v134 row_ror:2 row_mask:0xf bank_mask:0xf\n\t" \
  "v_smfmac_f32_16x16x64_bf16 v[52:55], v[0:3], a[208:215], v132\n\t" \
  "ds_read_b64_tr_b16 v[76:77], v127 offset:1024\n\t" \
  "s_waitcnt lgkmcnt(3)\n\t" \
  "v_mfma_f32_16x16x32_bf16 v[40:43], v[100:103], v[68:71], 0\n\t" \
  "s_nop 0\n\t" \
  "v_permlane32_swap_b32 v44, v48\n\t" \
  "v_permlane32_swap_b32 v46, v50\n\t" \
  "v_mfma_f32_16x16x32_bf16 v[36:39], v[84:87], v[68:71], 0\n\t" \
  "v_add_f32 v44, v44, v48\n\t" \
  "v_permlane32_swap_b32 v45, v49\n\t" \
  "v_mfma_f32_16x16x32_bf16 v[40:43], v[104:107], v[72:75], v[40:43]\n\t" \
  "v_permlane32_swap_b32 v47, v51\n\t" \
  "v_add_f32 v45, v45, v49\n\t" \
  "v_add_f32 v46, v46, v50\n\t" \
  "v_smfmac_f32_16x16x64_bf16 v[52:55], v[4:7], a[224:231], v132\n\t" \
  "ds_read_b128 v[100:103], v125 offset:0\n\t" \
  "v_cvt_pk_bf16_f32 v44, v44, v45\n\t" \
  "v_add_f32 v47, v47, v51\n\t" \
  "v_smfmac_f32_16x16x64_bf16 v[56:59], v[4:7], a[232:239], v132\n\t" \
  "ds_read_b128 v[104:107], v125 offset:1024\n\t" \
  "v_cvt_pk_bf16_f32 v45, v46, v47\n\t" \
  "v_mfma_f32_16x16x32_bf16 v[36:39], v[88:91], v[72:75], v[36:39]\n\t" \
  "ds_write_b64 v126, v[44:45] offset:2048\n\t" \
  "s_waitcnt lgkmcnt(3)\n\t" \
  "v_mfma_f32_16x16x32_bf16 v[40:43], v[108:111], v[76:79], v[40:43]\n\t" \
  "ds_read_b128 v[108:111], v123 offset:16384\n\t" \
  "v_max_f32_dpp v134, v134, v134 row_ror:4 row_mask:0xf bank_mask:0xf\n\t" \
  "v_mfma_f32_16x16x32_bf16 v[36:39], v[92:95], v[76:79], v[36:39]\n\t" \
  "ds_read_b128 v[0:3], v123 offset:18432\n\t" \
  "v_max_f32_dpp v134, v134, v134 row_ror:8 row_mask:0xf bank_mask:0xf\n\t" \
  "v_smfmac_f32_16x16x64_bf16 v[56:59], v[8:11], a[248:255], v132\n\t" \
  "ds_read_b128 v[4:7], v123 offset:19456\n\t" \
  "v_max_f32_dpp v134, v134, v134 row_bcast:15 row_mask:0xa bank_mask:0xf\n\t" \
  "v_mfma_f32_16x16x32_bf16 v[40:43], v[112:115], v[80:83], v[40:43]\n\t" \
  "s_waitcnt lgkmcnt(3)\n\t" \
  "s_barrier\n\t" \
  "v_mfma_f32_16x16x32_bf16 v[36:39], v[96:99], v[80:83], v[36:39]\n\t" \
  "ds_read_b64_tr_b16 v[96:97], v127 offset:3584\n\t" \
  "ds_read_b64_tr_b16 v[98:99], v127 offset:3648\n\t" \
  "v_smfmac_f32_16x16x64_bf16 v[56:59], v[12:15], v[144:151], v132\n\t" \
  "ds_read_b64_tr_b16 v[92:93], v127 offset:3072\n\t" \
  "v_max_f32_dpp v134, v134, v134 row_bcast:31 row_mask:0xc bank_mask:0xf\n\t" \
  "v_smfmac_f32_16x16x64_bf16 v[52:55], v[8:11], a[240:247], v132\n\t" \
  "ds_read_b64_tr_b16 v[84:85], v127 offset:2048\n\t" \
  "v_readlane_b32 s55, v134, 63\n\t" \
  "v_smfmac_f32_16x16x64_bf16 v[52:55], v[12:15], v[136:143], v132\n\t" \
  "ds_read_b128 v[112:115], v123 offset:17408\n\t" \
  "v_cndmask_b32 v49, v41, v37, s[64:65]\n\t" \
  "v_smfmac_f32_16x16x64_bf16 v[56:59], v[16:19], v[160:167], v132\n\t" \
  "ds_read_b64_tr_b16 v[90:91], v127 offset:2624\n\t" \
  "v_cndmask_b32 v50, v42, v38, s[64:65]\n\t" \
  "v_smfmac_f32_16x16x64_bf16 v[52:55], v[16:19], v[152:159], v132\n\t" \
  "ds_read_b64_tr_b16 v[94:95], v127 offset:3136\n\t" \
  "ds_read_b64_tr_b16 v[88:89], v127 offset:2560\n\t" \
  "v_smfmac_f32_16x16x64_bf16 v[52:55], v[20:23], v[100:107], v132\n\t" \
  "ds_read_b64_tr_b16 v[86:87], v127 offset:2112\n\t" \
  "v_cndmask_b32 v48, v40, v36, s[64:65]\n\t" \
  "v_max3_f32 v134, v48, v49, v50\n\t" \
  "v_smfmac_f32_16x16x64_bf16 v[56:59], v[20:23], v[168:175], v132\n\t" \
  "ds_read_b128 v[100:103], v123 offset:32768\n\t" \
  "s_waitcnt lgkmcnt(10)\n\t" \
  "v_smfmac_f32_16x16x64_bf16 v[56:59], v[24:27], v[0:7], v132\n\t" \
  "ds_read_b128 v[0:3], v123 offset:49152\n\t" \
  "s_waitcnt lgkmcnt(2)\n\t" \
  "v_mfma_f32_16x16x32_bf16 v[44:47], v[84:87], v[68:71], 0\n\t" \
  "ds_read_b128 v[4:7], v123 offset:50176\n\t" \
  "v_cndmask_b32 v51, v43, v39, s[64:65]\n\t" \
  "v_mfma_f32_16x16x32_bf16 v[44:47], v[88:91], v[72:75], v[44:47]\n\t" \
  "ds_read_b128 v[104:107], v123 offset:33792\n\t" \
  "v_max_f32 v134, v134, v51\n\t" \
  "v_mfma_f32_16x16x32_bf16 v[44:47], v[92:95], v[76:79], v[44:47]\n\t" \
  "s_lshr_b32 s55, s55, 23\n\t" \
  "v_mul_f32 v134, s49, v134\n\t" \
  "v_mov_b32 v38, v134\n\t" \
  "v_mfma_f32_16x16x32_bf16 v[44:47], v[96:99], v[80:83], v[44:47]\n\t" \
  "s_add_i32 s55, s55, s54\n\t" \
  "v_permlane32_swap_b32 v134, v38\n\t" \
  "v_smfmac_f32_16x16x64_bf16 v[52:55], v[24:27], v[108:115], v132\n\t" \
  "ds_read_b128 v[108:111], v123 offset:34816\n\t" \
  "s_waitcnt lgkmcnt(1)\n\t" \
  "v_smfmac_f32_16x16x64_bf16 v[52:55], v[28:31], v[100:107], v132\n\t" \
  "ds_read_b128 v[112:115], v123 offset:35840\n\t" \
  "v_cndmask_b32 v36, v46, v44, s[64:65]\n\t" \
  "v_cndmask_b32 v37, v47, v45, s[64:65]\n\t" \
  "v_max_f32 v135, v36, v37\n\t" \
  "v_max_f32 v134, v134, v38\n\t" \
  "v_mul_f32 v135, s50, v135\n\t" \
  "v_max_f32 v134, v134, v135\n\t" \
  "v_smfmac_f32_16x16x64_bf16 v[52:55], v[32:35], v[0:7], v132\n\t" \
  "ds_read_b128 v[104:107], v123 offset:52224\n\t" \
  "v_max_f32_dpp v134, v134, v134 row_ror:8 row_mask:0xf bank_mask:0xf\n\t" \
  "v_mov_b32 v39, v134\n\t" \
  "s_waitcnt lgkmcnt(1)\n\t" \
  "v_smfmac_f32_16x16x64_bf16 v[56:59], v[28:31], v[108:115], v132\n\t" \
  "ds_read_b128 v[100:103], v123 offset:51200\n\t" \
  "v_permlane16_swap_b32 v134, v39\n\t" \
  "v_max_f32 v134, v134, v39\n\t" \
  "v_fma_f32 v48, v48, s49, -v134\n\t" \
  "v_fma_f32 v49, v49, s49, -v134\n\t" \
  "v_exp_f32 v48, v48\n\t" \
  "v_fma_f32 v50, v50, s49, -v134\n\t" \
  "v_exp_f32 v49, v49\n\t" \
  "v_fma_f32 v51, v51, s49, -v134\n\t" \
  "v_exp_f32 v51, v51\n\t" \
  "v_exp_f32 v50, v50\n\t" \
  "v_cvt_pk_bf16_f32 v40, v48, v49\n\t" \
  "v_cvt_pk_bf16_f32 v41, v50, v51\n\t" \
  "v_add_f32 v38, v134, v133\n\t" \
  "v_fma_f32 v36, v36, s50, -v38\n\t" \
  "v_mov_b32_dpp v43, v41 row_ror:8 row_mask:0xf bank_mask:0xf\n\t" \
  "v_mov_b32_dpp v42, v40 row_ror:8 row_mask:0xf bank_mask:0xf\n\t" \
  "v_exp_f32 v36, v36\n\t" \
  "v_fma_f32 v37, v37, s50, -v38\n\t" \
  "v_mfma_f32_16x16x32_bf16 v[84:87], v[60:63], v[40:43], 0\n\t" \
  "v_exp_f32 v37, v37\n\t" \
  "v_mfma_f32_16x16x32_bf16 v[92:95], v[116:119], v[40:43], 0\n\t" \
  "v_cvt_pk_bf16_f32 v46, v36, v37\n\t" \
  "s_waitcnt lgkmcnt(0)\n\t" \
  "v_smfmac_f32_16x16x64_bf16 v[56:59], v[32:35], v[100:107], v132\n\t" \
  "s_max_i32 s55, s55, s52\n\t" \
  "v_mov_b32_dpp v47, v46 row_ror:8 row_mask:0xf bank_mask:0xf\n\t" \
  "v_mfma_f32_16x16x32_bf16 v[88:91], v[64:67], v[40:43], 0\n\t" \
  "s_sub_i32 s55, 157, s55\n\t" \
  "v_cvt_pk_bf16_f32 v9, v54, v55\n\t" \
  "v_cvt_pk_bf16_f32 v8, v52, v53\n\t" \
  "v_mfma_f32_16x16x16_bf16 v[92:95], v[120:121], v[46:47], v[92:95]\n\t" \
  "s_nop 0\n\t" \
  "v_cvt_pk_bf16_f32 v10, v56, v57\n\t" \
  "v_cvt_pk_bf16_f32 v11, v58, v59\n\t" \
  "v_mfma_f32_16x16x16_bf16 v[84:87], v[8:9], v[46:47], v[84:87]\n\t" \
  "v_mfma_f32_16x16x16_bf16 v[88:91], v[10:11], v[46:47], v[88:91]\n\t" \
  "s_min_i32 s55, s55, 50\n\t" \
  "s_max_i32 s59, s55, -40\n\t" \
  "s_add_i32 s55, s59, 127\n\t" \
  "v_mov_b32 v23, 0\n\t" \
  "v_rcp_f32 v92, v92\n\t" \
  "s_lshl_b32 s51, s55, 23\n\t" \
  "v_mul_f32 v92, s51, v92\n\t" \
  "v_pk_mul_f32 v[84:85], v[84:85], v[92:93] op_sel_hi:[1,0]\n\t" \
  "v_pk_mul_f32 v[88:89], v[88:89], v[92:93] op_sel_hi:[1,0]\n\t" \
  "v_pk_mul_f32 v[86:87], v[86:87], v[92:93] op_sel_hi:[1,0]\n\t" \
  "v_cvt_pk_bf16_f32 v84, v84, v85\n\t" \
  "v_cvt_pk_bf16_f32 v85, v86, v87\n\t" \
  "v_pk_mul_f32 v[90:91], v[90:91], v[92:93] op_sel_hi:[1,0]\n\t" \
  "v_cvt_pk_bf16_f32 v87, v90, v91\n\t" \
  "v_cvt_pk_bf16_f32 v86, v88, v89\n\t" \
  "ds_write_b128 v128, v[84:87]\n\t" \
  "v_mov_b32 v15, 0\n\t" \
  "v_mov_b32 v13, 0\n\t" \
  "v_mov_b32 v14, 0\n\t" \
  "v_mov_b32 v12, 0\n\t" \
  "v_mov_b32 v22, 0\n\t" \
  "v_mov_b32 v20, 0\n\t" \
  "v_mov_b32 v19, 0\n\t" \
  "v_mov_b32 v21, 0\n\t" \
  "v_mov_b32 v18, 0\n\t" \
  "s_waitcnt lgkmcnt(0)\n\t" \
  "s_barrier\n\t" \
  "ds_read_b128 v[68:71], v129 offset:0\n\t" \
  "ds_read_b128 v[72:75], v129 offset:512\n\t" \
  "ds_read_b128 v[76:79], v129 offset:1024\n\t" \
  "v_mov_b32 v17, 0\n\t" \
  "v_mov_b32 v16, 0\n\t" \
  "ds_read_b128 v[80:83], v129 offset:1536\n\t" \
  "ds_read_b128 v[104:107], v124 offset:22528\n\t" \
  "s_waitcnt lgkmcnt(4)\n\t" \
  "v_smfmac_f32_16x16x64_bf16 v[12:15], v[68:71], v[176:183], v132\n\t" \
  "s_waitcnt lgkmcnt(3)\n\t" \
  "v_smfmac_f32_16x16x64_bf16 v[12:15], v[72:75], v[184:191], v132\n\t" \
  "s_waitcnt lgkmcnt(2)\n\t" \
  "v_smfmac_f32_16x16x64_bf16 v[12:15], v[76:79], v[192:199], v132\n\t" \
  "s_waitcnt lgkmcnt(1)\n\t" \
  "v_smfmac_f32_16x16x64_bf16 v[12:15], v[80:83], v[200:207], v132\n\t" \
  "v_smfmac_f32_16x16x64_bf16 v[16:19], v[68:71], v[208:215], v132\n\t" \
  "v_smfmac_f32_16x16x64_bf16 v[16:19], v[72:75], v[216:223], v132\n\t" \
  "ds_read_b128 v[100:103], v124 offset:21504\n\t" \
  "v_cvt_i32_f32 v13, v13\n\t" \
  "v_cvt_i32_f32 v12, v12\n\t" \
  "v_and_or_b32 v12, v12, s73, 1\n\t" \
  "v_smfmac_f32_16x16x64_bf16 v[16:19], v[76:79], v[224:231], v132\n\t" \
  "global_atomic_add_x2 v131, v[12:13], s[34:35] offset:0\n\t" \
  "v_cvt_i32_f32 v14, v14\n\t" \
  "v_and_or_b32 v14, v14, s73, 1\n\t" \
  "v_smfmac_f32_16x16x64_bf16 v[16:19], v[80:83], v[232:239], v132\n\t" \
  "v_cvt_i32_f32 v15, v15\n\t" \
  "s_waitcnt lgkmcnt(0)\n\t" \
  "v_smfmac_f32_16x16x64_bf16 v[20:23], v[100:103], v[240:247], v132\n\t" \
  "global_atomic_add_x2 v131, v[14:15], s[34:35] offset:256\n\t" \
  "v_smfmac_f32_16x16x64_bf16 v[20:23], v[104:107], v[248:255], v132\n\t" \
  "s_nop 0\n\t" \
  "v_cvt_i32_f32 v19, v19\n\t" \
  "v_cvt_i32_f32 v18, v18\n\t" \
  "v_and_or_b32 v18, v18, s73, 1\n\t" \
  "global_atomic_add_x2 v131, v[18:19], s[34:35] offset:2304\n\t" \
  "v_cvt_i32_f32 v16, v16\n\t" \
  "v_cvt_i32_f32 v17, v17\n\t" \
  "v_and_or_b32 v16, v16, s73, 1\n\t" \
  "global_atomic_add_x2 v131, v[16:17], s[34:35] offset:2048\n\t" \
  "v_permlane32_swap_b32 v21, v23\n\t" \
  "v_permlane32_swap_b32 v20, v22\n\t" \
  "v_add_f32 v20, v20, v22\n\t" \
  "v_cvt_i32_f32 v20, v20\n\t" \
  "v_and_or_b32 v20, v20, s73, 1\n\t" \
  "v_add_f32 v21, v21, v23\n\t" \
  "v_cvt_i32_f32 v21, v21\n\t" \
  "s_mov_b64 exec, s[98:99]\n\t" \
  "global_atomic_add_x2 v130, v[20:21], s[34:35]\n\t" \
  "s_mov_b64 exec, -1\n\t" \
  "s_cmp_eq_u32 s44, 0\n\t" \
  "s_cbranch_scc0 Lrec_nh_%=\n\t" \
  "s_lshl_b32 s55, s68, 1\n\t" \
  "s_add_u32 s55, s55, s69\n\t" \
  "s_lshl_b32 s55, s55, 9\n\t" \
  "s_add_u32 s55, s55, 33152\n\t" \
  "v_mov_b32 v34, 0\n\t" \
  "s_mov_b64 exec, s[94:95]\n\t" \
  "buffer_store_dword v34, off, s[20:23], s55 sc1\n\t" \
  "s_mov_b64 exec, -1\n\t" \
  "Lrec_nh_%=:\n\t" \
  "s_cmp_eq_u32 s78, 1\n\t" \
  "s_cbranch_scc1 Lrec_rd_%=\n\t" \
  "s_mov_b32 s58, 0\n\t" \
  "Lrec_spb_%=:\n\t" \
  "v_mbcnt_lo_u32_b32 v135, -1, 0\n\t" \
  "v_mbcnt_hi_u32_b32 v135, -1, v135\n\t" \
  "v_lshlrev_b32 v135, 2, v135\n\t" \
  "buffer_load_dword v135, v135, s[36:39], s57 offen sc1\n\t" \
  "s_waitcnt vmcnt(0)\n\t" \
  "v_cmp_eq_u32 vcc, 0, v135\n\t" \
  "v_max_f32_dpp v135, v135, v135 row_ror:1 row_mask:0xf bank_mask:0xf\n\t" \
  "s_cmp_eq_u64 vcc, 0\n\t" \
  "s_cselect_b32 s78, 1, 0\n\t" \
  "v_max_f32_dpp v135, v135, v135 row_ror:2 row_mask:0xf bank_mask:0xf\n\t" \
  "s_nop 1\n\t" \
  "v_max_f32_dpp v135, v135, v135 row_ror:4 row_mask:0xf bank_mask:0xf\n\t" \
  "s_nop 1\n\t" \
  "v_max_f32_dpp v135, v135, v135 row_ror:8 row_mask:0xf bank_mask:0xf\n\t" \
  "s_nop 1\n\t" \
  "v_max_f32_dpp v135, v135, v135 row_bcast:15 row_mask:0xa bank_mask:0xf\n\t" \
  "v_mul_f32 v135, s60, v135\n\t" \
  "s_nop 0\n\t" \
  "v_readlane_b32 s71, v135, 31\n\t" \
  "s_nop 1\n\t" \
  "s_lshr_b32 s71, s71, 23\n\t" \
  "s_cmp_eq_u32 s78, 1\n\t" \
  "s_cbranch_scc1 Lrec_sqb_%=\n\t" \
  "s_add_u32 s58, s58, 1\n\t" \
  "s_cmp_gt_u32 s58, 524288\n\t" \
  "s_cbranch_scc1 Lrec_tmo_%=\n\t" \
  "s_sleep 2\n\t" \
  "s_branch Lrec_spb_%=\n\t" \
  "Lrec_sqb_%=:\n\t" \
  "Lrec_rd_%=:\n\t" \
  "s_mov_b32 s52, s71\n\t" \
  "s_add_u32 s57, s57, 512\n\t" \
  "s_add_u32 s46, s46, 262144\n\t" \
  "buffer_load_dwordx4 v[36:39], v125, s[24:27], s46 offen sc1\n\t" \
  "buffer_load_dwordx4 v[40:43], v125, s[24:27], s46 offen offset:1024 sc1\n\t" \
  "buffer_load_dwordx4 v[60:63], v125, s[28:31], s46 offen sc1\n\t" \
  "buffer_load_dwordx4 v[64:67], v125, s[28:31], s46 offen offset:1024 sc1\n\t" \
  "v_mbcnt_lo_u32_b32 v135, -1, 0\n\t" \
  "v_mbcnt_hi_u32_b32 v135, -1, v135\n\t" \
  "v_lshlrev_b32 v135, 2, v135\n\t" \
  "buffer_load_dword v135, v135, s[36:39], s57 offen sc1\n\t" \
  "s_add_u32 s34, s34, 32768\n\t" \
  "s_addc_u32 s35, s35, 0\n\t" \
  "s_cmp_eq_u32 s69, 0\n\t" \
  "s_cbranch_scc0 Lrec_pva_%=\n\t" \
  "s_lshl_b32 s55, s70, 9\n\t" \
  "v_add_u32 v134, s55, v127\n\t" \
  "ds_read_b64_tr_b16 v[8:9], v134 offset:2048\n\t" \
  "ds_read_b64_tr_b16 v[10:11], v134 offset:2112\n\t" \
  "s_waitcnt lgkmcnt(0)\n\t" \
  "Lrec_pva_%=:\n\t" \
  "s_lshl_b32 s55, s70, 10\n\t" \
  "s_add_u32 s55, s55, 131072\n\t" \
  "v_subrev_u32 v134, s55, v125\n\t" \
  "buffer_store_dwordx4 v[8:11], v134, s[40:43], s47 offen sc1\n\t" \
  "s_sub_i32 s55, 127, s48\n\t" \
  "s_lshl_b32 s55, s55, 23\n\t" \
  "v_mov_b32 v32, s55\n\t" \
  "s_cmp_eq_u32 s69, 0\n\t" \
  "s_cselect_b64 exec, s[94:95], 0\n\t" \
  "buffer_store_dword v32, off, s[36:39], s97 sc1\n\t" \
  "s_mov_b64 exec, -1\n\t" \
  "s_add_u32 s97, s97, 512\n\t" \
  "s_cmp_ge_u32 s44, 62\n\t" \
  "s_cbranch_scc0 Lrec_na_%=\n\t" \
  "s_add_u32 s55, s44, 1\n\t" \
  "v_mov_b32 v35, s55\n\t" \
  "s_mov_b64 exec, s[94:95]\n\t" \
  "buffer_store_dword v35, off, s[36:39], s96 sc1\n\t" \
  "s_mov_b64 exec, -1\n\t" \
  "Lrec_na_%=:\n\t" \
  "ds_read_b128 v[68:71], v122 offset:0\n\t" \
  "ds_read_b128 v[72:75], v122 offset:1024\n\t" \
  "ds_read_b128 v[76:79], v122 offset:2048\n\t" \
  "ds_read_b128 v[80:83], v122 offset:3072\n\t" \
  "ds_read_b128 v[84:87], v122 offset:16384\n\t" \
  "ds_read_b128 v[88:91], v122 offset:17408\n\t" \
  "ds_read_b128 v[92:95], v122 offset:18432\n\t" \
  "ds_read_b128 v[96:99], v122 offset:19456\n\t" \
  "ds_read_b128 v[100:103], v122 offset:32768\n\t" \
  "ds_read_b128 v[104:107], v122 offset:33792\n\t" \
  "ds_read_b128 v[108:111], v122 offset:34816\n\t" \
  "ds_read_b128 v[112:115], v122 offset:35840\n\t" \
  "v_mov_b32 v44, 0\n\t" \
  "v_mov_b32 v45, 0\n\t" \
  "v_mov_b32 v46, 0\n\t" \
  "v_mov_b32 v47, 0\n\t" \
  "v_mov_b32 v48, 0\n\t" \
  "v_mov_b32 v49, 0\n\t" \
  "v_mov_b32 v50, 0\n\t" \
  "v_mov_b32 v51, 0\n\t" \
  "v_mov_b32 v52, 0\n\t" \
  "v_mov_b32 v53, 0\n\t" \
  "v_mov_b32 v54, 0\n\t" \
  "v_mov_b32 v55, 0\n\t" \
  "v_mov_b32 v56, 0\n\t" \
  "v_mov_b32 v57, 0\n\t" \
  "v_mov_b32 v58, 0\n\t" \
  "v_mov_b32 v59, 0\n\t" \
  "s_mov_b32 s48, s59\n\t" \
  "s_lshl_b32 s55, s48, 23\n\t" \
  "s_sub_i32 s49, 1040356334, s55\n\t" \
  "s_sub_i32 s50, s49, s55\n\t" \
  "s_add_i32 s55, s48, 126\n\t" \
  "s_mul_i32 s55, s55, 8388736\n\t" \
  "v_mov_b32 v120, s55\n\t" \
  "v_mov_b32 v121, s55\n\t" \
  "s_sub_i32 s54, s53, s48\n\t" \
  "v_cvt_f32_i32 v133, s48\n\t" \
  "s_add_u32 s44, s44, 1\n\t" \
  "s_add_u32 s47, s47, 131072\n\t" \
  "s_add_u32 s45, s45, 32768\n\t" \
  "s_add_u32 s61, s45, 8192\n\t" \
  "s_add_u32 s62, s45, 16384\n\t" \
  "s_add_u32 s63, s45, 24576\n\t" \
  "s_mov_b32 s58, 0\n\t" \
  "v_mbcnt_lo_u32_b32 v32, -1, 0\n\t" \
  "v_mbcnt_hi_u32_b32 v32, -1, v32\n\t" \
  "v_and_b32 v33, 7, v32\n\t" \
  "v_lshrrev_b32 v35, 3, v32\n\t" \
  "v_and_b32 v34, 3, v35\n\t" \
  "s_lshl_b32 s55, s70, 11\n\t" \
  "v_lshlrev_b32 v35, 8, v35\n\t" \
  "v_lshl_add_u32 v35, v33, 4, v35\n\t" \
  "v_add_u32 v24, s55, v35\n\t" \
  "s_lshl_b32 s55, s70, 13\n\t" \
  "s_add_u32 s55, s55, 128\n\t" \
  "v_lshrrev_b32 v35, 2, v33\n\t" \
  "v_lshl_add_u32 v35, v34, 1, v35\n\t" \
  "v_lshlrev_b32 v35, 10, v35\n\t" \
  "v_and_b32 v25, 3, v33\n\t" \
  "v_lshl_add_u32 v35, v25, 4, v35\n\t" \
  "v_add_u32 v25, s55, v35\n\t" \
  "v_lshrrev_b32 v35, 5, v32\n\t" \
  "v_lshl_add_u32 v25, v35, 9, v25\n\t" \
  "v_lshrrev_b32 v35, 2, v33\n\t" \
  "v_lshlrev_b32 v35, 7, v35\n\t" \
  "v_lshl_add_u32 v35, v34, 5, v35\n\t" \
  "v_and_b32 v33, 3, v33\n\t" \
  "v_lshl_add_u32 v35, v33, 2, v35\n\t" \
  "s_lshl_b32 s55, s70, 8\n\t" \
  "s_lshr_b32 s56, s70, 1\n\t" \
  "s_mul_i32 s56, s56, 4096\n\t" \
  "s_add_u32 s56, s56, s55\n\t" \
  "s_add_u32 s56, s56, 143360\n\t" \
  "v_add_u32 v30, s56, v35\n\t" \
  "s_add_u32 s56, s55, 162816\n\t" \
  "v_lshlrev_b32 v31, 2, v32\n\t" \
  "v_add_u32 v31, s56, v31\n\t" \
  "s_lshl_b32 s55, s70, 9\n\t" \
  "s_add_u32 s55, s55, 139264\n\t" \
  "v_lshrrev_b32 v32, 5, v32\n\t" \
  "v_lshl_add_u32 v35, v32, 8, v35\n\t" \
  "v_add_u32 v26, s55, v35\n\t" \
  "v_lshl_add_u32 v30, v32, 4, v30\n\t" \
  "v_add_u32 v27, 2048, v26\n\t" \
  "v_add_u32 v28, 4608, v26\n\t" \
  "v_add_u32 v29, 6656, v26\n\t" \
  "v_mov_b32 v35, s44\n\t" \
  "s_cmp_eq_u32 s44, 56\n\t" \
  "s_cbranch_scc1 Lrec_fl_%=\n\t" \
  "Lrec_fm_%=:\n\t" \
  "s_cmp_eq_u32 s44, 1\n\t" \
  "s_cbranch_scc1 Lrec_al_%=\n\t" \
  "s_sleep 5\n\t" \
  "s_branch Lrec_poll_%=\n\t" \
  "Lrec_retry_%=:\n\t" \
  "s_add_u32 s58, s58, 1\n\t" \
  "s_cmp_gt_u32 s58, 524288\n\t" \
  "s_cbranch_scc1 Lrec_tmo_%=\n\t" \
  "buffer_load_dwordx4 v[0:3], v24, s[20:23], s45 offen sc1\n\t" \
  "buffer_load_dwordx4 v[4:7], v24, s[20:23], s61 offen sc1\n\t" \
  "buffer_load_dwordx4 v[8:11], v24, s[20:23], s62 offen sc1\n\t" \
  "buffer_load_dwordx4 v[12:15], v24, s[20:23], s63 offen sc1\n\t" \
  "buffer_load_dwordx4 v[16:19], v25, s[20:23], s45 offen sc1\n\t" \
  "s_branch Lrec_dec_%=\n\t" \
  "Lrec_al_%=:\n\t" \
  "v_mbcnt_lo_u32_b32 v23, -1, 0\n\t" \
  "v_mbcnt_hi_u32_b32 v23, -1, v23\n\t" \
  "v_lshlrev_b32 v23, 9, v23\n\t" \
  "s_mov_b32 s55, 32768\n\t" \
  "s_mov_b32 s56, 0\n\t" \
  "s_mov_b32 s72, 0\n\t" \
  "Lrec_an_%=:\n\t" \
  "buffer_load_dword v22, v23, s[20:23], s55 offen offset:384 sc1\n\t" \
  "s_add_u32 s56, s56, 1\n\t" \
  "s_cmp_gt_u32 s56, 2000\n\t" \
  "s_cbranch_scc1 Lrec_poll_%=\n\t" \
  "s_waitcnt vmcnt(0)\n\t" \
  "v_cmp_ne_u32 vcc, 0, v22\n\t" \
  "s_nop 0\n\t" \
  "s_cbranch_vccz Lrec_am_%=\n\t" \
  "s_mov_b32 s72, 1\n\t" \
  "s_sleep 1\n\t" \
  "s_branch Lrec_an_%=\n\t" \
  "Lrec_am_%=:\n\t" \
  "s_cmp_eq_u32 s72, 0\n\t" \
  "s_cbranch_scc1 Lrec_poll_%=\n\t" \
  "s_sleep 4\n\t" \
  "s_branch Lrec_poll_%=\n\t" \
  "Lrec_fl_%=:\n\t" \
  "v_mbcnt_lo_u32_b32 v23, -1, 0\n\t" \
  "v_mbcnt_hi_u32_b32 v23, -1, v23\n\t" \
  "v_min_u32 v23, 23, v23\n\t" \
  "v_lshlrev_b32 v23, 2, v23\n\t" \
  "s_mov_b32 s55, 60672\n\t" \
  "s_mov_b32 s56, 0\n\t" \
  "Lrec_fn_%=:\n\t" \
  "buffer_load_dword v22, v23, s[36:39], s55 offen sc1\n\t" \
  "s_add_u32 s56, s56, 1\n\t" \
  "s_cmp_gt_u32 s56, 524288\n\t" \
  "s_cbranch_scc1 Lrec_tmo_%=\n\t" \
  "s_waitcnt vmcnt(0)\n\t" \
  "v_cmp_eq_u32 vcc, 0, v22\n\t" \
  "s_nop 0\n\t" \
  "s_cbranch_vccz Lrec_poll_%=\n\t" \
  "s_sleep 2\n\t" \
  "s_branch Lrec_fn_%=\n\t" \
  "Lrec_fin_%=:\n\t" \
  "s_waitcnt lgkmcnt(0)\n\t" \
  "s_barrier\n\t" \
  "s_mov_b64 exec, s[94:95]\n\t" \
  "buffer_store_dword v35, off, s[36:39], s96 sc1\n\t" \
  "s_mov_b64 exec, -1\n\t" \
  "ds_read_b128 v[0:3], v124 offset:0\n\t" \
  "ds_read_b128 v[4:7], v124 offset:1024\n\t" \
  "ds_read_b128 v[8:11], v124 offset:2048\n\t" \
  "ds_read_b128 v[12:15], v124 offset:3072\n\t" \
  "ds_read_b128 v[16:19], v124 offset:4096\n\t" \
  "ds_read_b128 v[20:23], v124 offset:5120\n\t" \
  "ds_read_b128 v[24:27], v124 offset:6144\n\t" \
  "ds_read_b128 v[28:31], v124 offset:7168\n\t" \
  "ds_read_b128 v[32:35], v124 offset:8192\n\t" \
  "s_cmp_eq_u32 s69, 0\n\t" \
  "s_cbranch_scc0 Lrec_fv_%=\n\t" \
  "ds_read_b128 v[68:71], v122 offset:49152\n\t" \
  "ds_read_b128 v[72:75], v122 offset:50176\n\t" \
  "ds_read_b128 v[76:79], v122 offset:51200\n\t" \
  "ds_read_b128 v[80:83], v122 offset:52224\n\t" \
  "s_waitcnt lgkmcnt(0)\n\t" \
  "ds_read_b128 v[84:87], v123 offset:0\n\t" \
  "ds_read_b128 v[88:91], v123 offset:1024\n\t" \
  "ds_read_b128 v[92:95], v123 offset:2048\n\t" \
  "ds_read_b128 v[96:99], v123 offset:3072\n\t" \
  "s_waitcnt lgkmcnt(0)\n\t" \
  "v_smfmac_f32_16x16x64_bf16 v[44:47], v[0:3], v[68:75], v132\n\t" \
  "v_smfmac_f32_16x16x64_bf16 v[48:51], v[0:3], v[76:83], v132\n\t" \
  "v_smfmac_f32_16x16x64_bf16 v[44:47], v[4:7], v[84:91], v132\n\t" \
  "v_smfmac_f32_16x16x64_bf16 v[48:51], v[4:7], v[92:99], v132\n\t" \
  "v_smfmac_f32_16x16x64_bf16 v[44:47], v[8:11], a[96:103], v132\n\t" \
  "v_smfmac_f32_16x16x64_bf16 v[48:51], v[8:11], a[104:111], v132\n\t" \
  "v_smfmac_f32_16x16x64_bf16 v[44:47], v[12:15], a[112:119], v132\n\t" \
  "v_smfmac_f32_16x16x64_bf16 v[48:51], v[12:15], a[120:127], v132\n\t" \
  "v_smfmac_f32_16x16x64_bf16 v[44:47], v[16:19], a[128:135], v132\n\t" \
  "v_smfmac_f32_16x16x64_bf16 v[48:51], v[16:19], a[136:143], v132\n\t" \
  "v_smfmac_f32_16x16x64_bf16 v[44:47], v[20:23], a[144:151], v132\n\t" \
  "v_smfmac_f32_16x16x64_bf16 v[48:51], v[20:23], a[152:159], v132\n\t" \
  "v_smfmac_f32_16x16x64_bf16 v[44:47], v[24:27], a[160:167], v132\n\t" \
  "v_smfmac_f32_16x16x64_bf16 v[48:51], v[24:27], a[168:175], v132\n\t" \
  "v_smfmac_f32_16x16x64_bf16 v[44:47], v[28:31], a[176:183], v132\n\t" \
  "v_smfmac_f32_16x16x64_bf16 v[48:51], v[28:31], a[184:191], v132\n\t" \
  "v_smfmac_f32_16x16x64_bf16 v[44:47], v[32:35], a[192:199], v132\n\t" \
  "v_smfmac_f32_16x16x64_bf16 v[48:51], v[32:35], a[200:207], v132\n\t" \
  "s_nop 7\n\t" \
  "v_permlane32_swap_b32 v44, v48\n\t" \
  "v_permlane32_swap_b32 v45, v49\n\t" \
  "v_permlane32_swap_b32 v46, v50\n\t" \
  "v_permlane32_swap_b32 v47, v51\n\t" \
  "v_add_f32 v44, v44, v48\n\t" \
  "v_add_f32 v45, v45, v49\n\t" \
  "v_add_f32 v46, v46, v50\n\t" \
  "v_add_f32 v47, v47, v51\n\t" \
  "v_cvt_pk_bf16_f32 v44, v44, v45\n\t" \
  "v_cvt_pk_bf16_f32 v45, v46, v47\n\t" \
  "ds_write_b64 v126, v[44:45] offset:2048\n\t" \
  "s_waitcnt lgkmcnt(0)\n\t" \
  "s_barrier\n\t" \
  "s_branch Lrec_fp_%=\n\t" \
  "Lrec_fv_%=:\n\t" \
  "s_waitcnt lgkmcnt(0)\n\t" \
  "ds_read_b128 v[68:71], v125 offset:0\n\t" \
  "ds_read_b128 v[72:75], v125 offset:1024\n\t" \
  "ds_read_b128 v[76:79], v123 offset:16384\n\t" \
  "ds_read_b128 v[80:83], v123 offset:17408\n\t" \
  "ds_read_b128 v[84:87], v123 offset:18432\n\t" \
  "ds_read_b128 v[88:91], v123 offset:19456\n\t" \
  "ds_read_b128 v[92:95], v123 offset:32768\n\t" \
  "ds_read_b128 v[96:99], v123 offset:33792\n\t" \
  "ds_read_b128 v[100:103], v123 offset:34816\n\t" \
  "ds_read_b128 v[104:107], v123 offset:35840\n\t" \
  "ds_read_b128 v[108:111], v123 offset:49152\n\t" \
  "ds_read_b128 v[112:115], v123 offset:50176\n\t" \
  "v_smfmac_f32_16x16x64_bf16 v[52:55], v[0:3], a[208:215], v132\n\t" \
  "v_smfmac_f32_16x16x64_bf16 v[56:59], v[0:3], a[216:223], v132\n\t" \
  "v_smfmac_f32_16x16x64_bf16 v[52:55], v[4:7], a[224:231], v132\n\t" \
  "v_smfmac_f32_16x16x64_bf16 v[56:59], v[4:7], a[232:239], v132\n\t" \
  "v_smfmac_f32_16x16x64_bf16 v[52:55], v[8:11], a[240:247], v132\n\t" \
  "v_smfmac_f32_16x16x64_bf16 v[56:59], v[8:11], a[248:255], v132\n\t" \
  "v_smfmac_f32_16x16x64_bf16 v[52:55], v[12:15], v[136:143], v132\n\t" \
  "v_smfmac_f32_16x16x64_bf16 v[56:59], v[12:15], v[144:151], v132\n\t" \
  "v_smfmac_f32_16x16x64_bf16 v[52:55], v[16:19], v[152:159], v132\n\t" \
  "v_smfmac_f32_16x16x64_bf16 v[56:59], v[16:19], v[160:167], v132\n\t" \
  "v_smfmac_f32_16x16x64_bf16 v[56:59], v[20:23], v[168:175], v132\n\t" \
  "s_waitcnt lgkmcnt(0)\n\t" \
  "v_smfmac_f32_16x16x64_bf16 v[52:55], v[20:23], v[68:75], v132\n\t" \
  "ds_read_b128 v[68:71], v123 offset:51200\n\t" \
  "ds_read_b128 v[72:75], v123 offset:52224\n\t" \
  "v_smfmac_f32_16x16x64_bf16 v[52:55], v[24:27], v[76:83], v132\n\t" \
  "v_smfmac_f32_16x16x64_bf16 v[56:59], v[24:27], v[84:91], v132\n\t" \
  "v_smfmac_f32_16x16x64_bf16 v[52:55], v[28:31], v[92:99], v132\n\t" \
  "v_smfmac_f32_16x16x64_bf16 v[56:59], v[28:31], v[100:107], v132\n\t" \
  "v_smfmac_f32_16x16x64_bf16 v[52:55], v[32:35], v[108:115], v132\n\t" \
  "s_waitcnt lgkmcnt(0)\n\t" \
  "v_smfmac_f32_16x16x64_bf16 v[56:59], v[32:35], v[68:75], v132\n\t" \
  "s_nop 7\n\t" \
  "v_cvt_pk_bf16_f32 v8, v52, v53\n\t" \
  "v_cvt_pk_bf16_f32 v9, v54, v55\n\t" \
  "v_cvt_pk_bf16_f32 v10, v56, v57\n\t" \
  "v_cvt_pk_bf16_f32 v11, v58, v59\n\t" \
  "Lrec_fp_%=:\n\t" \
  "s_cmp_eq_u32 s69, 0\n\t" \
  "s_cbranch_scc0 Lrec_pvb_%=\n\t" \
  "s_lshl_b32 s55, s70, 9\n\t" \
  "v_add_u32 v134, s55, v127\n\t" \
  "ds_read_b64_tr_b16 v[8:9], v134 offset:2048\n\t" \
  "ds_read_b64_tr_b16 v[10:11], v134 offset:2112\n\t" \
  "s_waitcnt lgkmcnt(0)\n\t" \
  "Lrec_pvb_%=:\n\t" \
  "s_lshl_b32 s55, s70, 10\n\t" \
  "s_add_u32 s55, s55, 131072\n\t" \
  "v_subrev_u32 v134, s55, v125\n\t" \
  "buffer_store_dwordx4 v[8:11], v134, s[40:43], s47 offen sc1\n\t" \
  "s_sub_i32 s55, 127, s48\n\t" \
  "s_lshl_b32 s55, s55, 23\n\t" \
  "v_mov_b32 v32, s55\n\t" \
  "s_cmp_eq_u32 s69, 0\n\t" \
  "s_cselect_b64 exec, s[94:95], 0\n\t" \
  "buffer_store_dword v32, off, s[36:39], s97 sc1\n\t" \
  "s_mov_b64 exec, -1\n\t" \
  "s_add_u32 s97, s97, 512\n\t" \
  "v_mov_b32 v35, 64\n\t" \
  "s_mov_b64 exec, s[94:95]\n\t" \
  "buffer_store_dword v35, off, s[36:39], s96 sc1\n\t" \
  "s_mov_b64 exec, -1\n\t" \
  "s_branch Lrec_end_%=\n\t" \
  "Lrec_tmo_%=:\n\t" \
  "v_mov_b32 v134, 1\n\t" \
  "v_mov_b32 v135, 60416\n\t" \
  "buffer_store_dword v134, v135, s[36:39], 0 offen sc1\n\t" \
  "Lrec_end_%=:\n\t" \
  "s_waitcnt vmcnt(0)\n\t"
#define REC_CLOB "v0","v1","v2","v3","v4","v5","v6","v7","v8","v9","v10","v11","v12","v13","v14","v15","v16","v17","v18","v19","v20","v21","v22","v23","v24","v25","v26","v27","v28","v29","v30","v31","v32","v33","v34","v35","v36","v37","v38","v39","v40","v41","v42","v43","v44","v45","v46","v47","v48","v49","v50","v51","v52","v53","v54","v55","v56","v57","v58","v59","v60","v61","v62","v63","v64","v65","v66","v67","v68","v69","v70","v71","v72","v73","v74","v75","v76","v77","v78","v79","v80","v81","v82","v83","v84","v85","v86","v87","v88","v89","v90","v91","v92","v93","v94","v95","v96","v97","v98","v99","v100","v101","v102","v103","v104","v105","v106","v107","v108","v109","v110","v111","v112","v113","v114","v115","v116","v117","v118","v119","v120","v121","v122","v123","v124","v125","v126","v127","v128","v129","v130","v131","v132","v133","v134","v135","v136","v137","v138","v139","v140","v141","v142","v143","v144","v145","v146","v147","v148","v149","v150","v151","v152","v153","v154","v155","v156","v157","v158","v159","v160","v161","v162","v163","v164","v165","v166","v167","v168","v169","v170","v171","v172","v173","v174","v175","v176","v177","v178","v179","v180","v181","v182","v183","v184","v185","v186","v187","v188","v189","v190","v191","v192","v193","v194","v195","v196","v197","v198","v199","v200","v201","v202","v203","v204","v205","v206","v207","v208","v209","v210","v211","v212","v213","v214","v215","v216","v217","v218","v219","v220","v221","v222","v223","v224","v225","v226","v227","v228","v229","v230","v231","v232","v233","v234","v235","v236","v237","v238","v239","v240","v241","v242","v243","v244","v245","v246","v247","v248","v249","v250","v251","v252","v253","v254","v255","a0","a1","a2","a3","a4","a5","a6","a7","a8","a9","a10","a11","a12","a13","a14","a15","a16","a17","a18","a19","a20","a21","a22","a23","a24","a25","a26","a27","a28","a29","a30","a31","a32","a33","a34","a35","a36","a37","a38","a39","a40","a41","a42","a43","a44","a45","a46","a47","a48","a49","a50","a51","a52","a53","a54","a55","a56","a57","a58","a59","a60","a61","a62","a63","a64","a65","a66","a67","a68","a69","a70","a71","a72","a73","a74","a75","a76","a77","a78","a79","a80","a81","a82","a83","a84","a85","a86","a87","a88","a89","a90","a91","a92","a93","a94","a95","a96","a97","a98","a99","a100","a101","a102","a103","a104","a105","a106","a107","a108","a109","a110","a111","a112","a113","a114","a115","a116","a117","a118","a119","a120","a121","a122","a123","a124","a125","a126","a127","a128","a129","a130","a131","a132","a133","a134","a135","a136","a137","a138","a139","a140","a141","a142","a143","a144","a145","a146","a147","a148","a149","a150","a151","a152","a153","a154","a155","a156","a157","a158","a159","a160","a161","a162","a163","a164","a165","a166","a167","a168","a169","a170","a171","a172","a173","a174","a175","a176","a177","a178","a179","a180","a181","a182","a183","a184","a185","a186","a187","a188","a189","a190","a191","a192","a193","a194","a195","a196","a197","a198","a199","a200","a201","a202","a203","a204","a205","a206","a207","a208","a209","a210","a211","a212","a213","a214","a215","a216","a217","a218","a219","a220","a221","a222","a223","a224","a225","a226","a227","a228","a229","a230","a231","a232","a233","a234","a235","a236","a237","a238","a239","a240","a241","a242","a243","a244","a245","a246","a247","a248","a249","a250","a251","a252","a253","a254","a255","s20","s21","s22","s23","s24","s25","s26","s27","s28","s29","s30","s31","s34","s35","s36","s37","s38","s39","s40","s41","s42","s43","s44","s45","s46","s47","s48","s49","s50","s51","s52","s53","s54","s55","s56","s57","s58","s59","s60","s61","s62","s63","s64","s65","s66","s67","s68","s69","s70","s71","s72","s73","s74","s75","s76","s77","s78","s79","s80","s81","s82","s83","s84","s85","s86","s87","s88","s89","s90","s91","s92","s93","s94","s95","s96","s97","s98","s99","vcc","scc","memory"
#define QKV_CLOB "v0","v1","v2","v3","v4","v5","v6","v7","v8","v9","v10","v11","v12","v13","v14","v15","v16","v17","v18","v19","v20","v21","v22","v23","v24","v25","v26","v27","v28","v29","v30","v31","v32","v33","v34","v35","v36","v37","v38","v39","v40","v41","v42","v43","v44","v45","v46","v47","v48","v49","v50","v51","v52","v53","v54","v55","v56","v57","v58","v59","v60","v61","v62","v63","v64","v65","v66","v67","v68","v69","v70","v71","v72","v73","v74","v75","v76","v77","v78","v79","v80","v81","v82","v83","v84","v85","v86","v87","a0","a1","a2","a3","a4","a5","a6","a7","a8","a9","a10","a11","a12","a13","a14","a15","a16","a17","a18","a19","a20","a21","a22","a23","a24","a25","a26","a27","a28","a29","a30","a31","a32","a33","a34","a35","a36","a37","a38","a39","a40","a41","a42","a43","a44","a45","a46","a47","a48","a49","a50","a51","a52","a53","a54","a55","a56","a57","a58","a59","a60","a61","a62","a63","a64","a65","a66","a67","a68","a69","a70","a71","a72","a73","a74","a75","a76","a77","a78","a79","a80","a81","a82","a83","a84","a85","a86","a87","a88","a89","a90","a91","a92","a93","a94","a95","a96","a97","a98","a99","a100","a101","a102","a103","a104","a105","a106","a107","a108","a109","a110","a111","a112","a113","a114","a115","a116","a117","a118","a119","a120","a121","a122","a123","a124","a125","a126","a127","a128","a129","a130","a131","a132","a133","a134","a135","a136","a137","a138","a139","a140","a141","a142","a143","s20","s21","s22","s23","s24","s25","s26","s27","s28","s29","s40","s41","s42","s43","s44","s45","s46","s47","s48","s49","s50","s51","s52","s53","s54","s55","s56","s57","s58","s59","vcc","scc","memory"
#define OPJ_ASM \
  "s_load_dwordx2 s[24:25], %0, 40\n\t" \
  "s_load_dwordx2 s[26:27], %0, 0\n\t" \
  "s_load_dwordx2 s[28:29], %0, 80\n\t" \
  "s_and_b32 s30, %1, 255\n\t" \
  "s_bfe_u32 s55, %1, 0x80008\n\t" \
  "s_add_u32 s49, s55, 112\n\t" \
  "s_cmp_eq_u32 s55, 3\n\t" \
  "s_cselect_b32 s31, s49, s55\n\t" \
  "s_lshr_b32 s45, %1, 16\n\t" \
  "s_mov_b64 exec, -1\n\t" \
  "s_waitcnt lgkmcnt(0)\n\t" \
  "s_mov_b32 s20, s28\n\t" \
  "s_and_b32 s21, s29, 0xffff\n\t" \
  "s_mov_b32 s22, 113246208\n\t" \
  "s_mov_b32 s23, 0x20000\n\t" \
  "s_mov_b32 s36, s26\n\t" \
  "s_and_b32 s37, s27, 0xffff\n\t" \
  "s_mov_b32 s38, 2359296\n\t" \
  "s_mov_b32 s39, 0x20000\n\t" \
  "v_mbcnt_lo_u32_b32 v198, -1, 0\n\t" \
  "v_mbcnt_hi_u32_b32 v198, -1, v198\n\t" \
  "s_mul_i32 s40, s45, 1179648\n\t" \
  "s_mul_i32 s41, s30, 96\n\t" \
  "s_add_u32 s40, s40, s41\n\t" \
  "s_add_u32 s46, s24, s40\n\t" \
  "s_addc_u32 s47, s25, 0\n\t" \
  "s_mul_i32 s54, s45, 40960\n\t" \
  "s_movk_i32 s40, 10923\n\t" \
  "v_add_u32 v162, 0, v198\n\t" \
  "v_mul_u32_u24 v199, s40, v162\n\t" \
  "v_lshrrev_b32 v199, 16, v199\n\t" \
  "v_mul_u32_u24 v199, 1056, v199\n\t" \
  "v_lshl_add_u32 v200, v162, 4, v199\n\t" \
  "v_add_u32 v162, 64, v198\n\t" \
  "v_mul_u32_u24 v199, s40, v162\n\t" \
  "v_lshrrev_b32 v199, 16, v199\n\t" \
  "v_mul_u32_u24 v199, 1056, v199\n\t" \
  "v_lshl_add_u32 v201, v162, 4, v199\n\t" \
  "v_add_u32 v162, 128, v198\n\t" \
  "v_mul_u32_u24 v199, s40, v162\n\t" \
  "v_lshrrev_b32 v199, 16, v199\n\t" \
  "v_mul_u32_u24 v199, 1056, v199\n\t" \
  "v_lshl_add_u32 v202, v162, 4, v199\n\t" \
  "v_lshrrev_b32 v162, 4, v198\n\t" \
  "v_lshlrev_b32 v162, 2, v162\n\t" \
  "v_bfe_u32 v199, v198, 2, 2\n\t" \
  "v_add_u32 v162, v162, v199\n\t" \
  "v_mul_u32_u24 v162, 96, v162\n\t" \
  "v_add_u32 v162, s54, v162\n\t" \
  "v_and_b32 v199, 3, v198\n\t" \
  "v_lshl_add_u32 v203, v199, 4, v162\n\t" \
  "v_lshl_add_u32 v204, v199, 3, v162\n\t" \
  "s_add_u32 m0, s54, 0\n\t" \
  "s_nop 0\n\t" \
  "global_load_lds_dwordx4 v200, s[46:47]\n\t" \
  "s_add_u32 m0, s54, 1024\n\t" \
  "s_nop 0\n\t" \
  "global_load_lds_dwordx4 v201, s[46:47]\n\t" \
  "s_add_u32 m0, s54, 2048\n\t" \
  "s_nop 0\n\t" \
  "global_load_lds_dwordx4 v202, s[46:47]\n\t" \
  "s_add_u32 s46, s46, 36864\n\t" \
  "s_addc_u32 s47, s47, 0\n\t" \
  "s_add_u32 m0, s54, 3072\n\t" \
  "s_nop 0\n\t" \
  "global_load_lds_dwordx4 v200, s[46:47]\n\t" \
  "s_add_u32 m0, s54, 4096\n\t" \
  "s_nop 0\n\t" \
  "global_load_lds_dwordx4 v201, s[46:47]\n\t" \
  "s_add_u32 m0, s54, 5120\n\t" \
  "s_nop 0\n\t" \
  "global_load_lds_dwordx4 v202, s[46:47]\n\t" \
  "s_add_u32 s46, s46, 36864\n\t" \
  "s_addc_u32 s47, s47, 0\n\t" \
  "s_add_u32 m0, s54, 6144\n\t" \
  "s_nop 0\n\t" \
  "global_load_lds_dwordx4 v200, s[46:47]\n\t" \
  "s_add_u32 m0, s54, 7168\n\t" \
  "s_nop 0\n\t" \
  "global_load_lds_dwordx4 v201, s[46:47]\n\t" \
  "s_add_u32 m0, s54, 8192\n\t" \
  "s_nop 0\n\t" \
  "global_load_lds_dwordx4 v202, s[46:47]\n\t" \
  "s_add_u32 s46, s46, 36864\n\t" \
  "s_addc_u32 s47, s47, 0\n\t" \
  "s_add_u32 m0, s54, 9216\n\t" \
  "s_nop 0\n\t" \
  "global_load_lds_dwordx4 v200, s[46:47]\n\t" \
  "s_add_u32 m0, s54, 10240\n\t" \
  "s_nop 0\n\t" \
  "global_load_lds_dwordx4 v201, s[46:47]\n\t" \
  "s_add_u32 m0, s54, 11264\n\t" \
  "s_nop 0\n\t" \
  "global_load_lds_dwordx4 v202, s[46:47]\n\t" \
  "s_add_u32 s46, s46, 36864\n\t" \
  "s_addc_u32 s47, s47, 0\n\t" \
  "s_add_u32 m0, s54, 12288\n\t" \
  "s_nop 0\n\t" \
  "global_load_lds_dwordx4 v200, s[46:47]\n\t" \
  "s_add_u32 m0, s54, 13312\n\t" \
  "s_nop 0\n\t" \
  "global_load_lds_dwordx4 v201, s[46:47]\n\t" \
  "s_add_u32 m0, s54, 14336\n\t" \
  "s_nop 0\n\t" \
  "global_load_lds_dwordx4 v202, s[46:47]\n\t" \
  "s_add_u32 s46, s46, 36864\n\t" \
  "s_addc_u32 s47, s47, 0\n\t" \
  "s_add_u32 m0, s54, 15360\n\t" \
  "s_nop 0\n\t" \
  "global_load_lds_dwordx4 v200, s[46:47]\n\t" \
  "s_add_u32 m0, s54, 16384\n\t" \
  "s_nop 0\n\t" \
  "global_load_lds_dwordx4 v201, s[46:47]\n\t" \
  "s_add_u32 m0, s54, 17408\n\t" \
  "s_nop 0\n\t" \
  "global_load_lds_dwordx4 v202, s[46:47]\n\t" \
  "s_add_u32 s46, s46, 36864\n\t" \
  "s_addc_u32 s47, s47, 0\n\t" \
  "s_sleep 32\n\t" \
  "s_add_u32 m0, s54, 18432\n\t" \
  "s_nop 0\n\t" \
  "global_load_lds_dwordx4 v200, s[46:47]\n\t" \
  "s_add_u32 m0, s54, 19456\n\t" \
  "s_nop 0\n\t" \
  "global_load_lds_dwordx4 v201, s[46:47]\n\t" \
  "s_add_u32 m0, s54, 20480\n\t" \
  "s_nop 0\n\t" \
  "global_load_lds_dwordx4 v202, s[46:47]\n\t" \
  "s_add_u32 s46, s46, 36864\n\t" \
  "s_addc_u32 s47, s47, 0\n\t" \
  "s_add_u32 m0, s54, 21504\n\t" \
  "s_nop 0\n\t" \
  "global_load_lds_dwordx4 v200, s[46:47]\n\t" \
  "s_add_u32 m0, s54, 22528\n\t" \
  "s_nop 0\n\t" \
  "global_load_lds_dwordx4 v201, s[46:47]\n\t" \
  "s_add_u32 m0, s54, 23552\n\t" \
  "s_nop 0\n\t" \
  "global_load_lds_dwordx4 v202, s[46:47]\n\t" \
  "s_add_u32 s46, s46, 36864\n\t" \
  "s_addc_u32 s47, s47, 0\n\t" \
  "s_add_u32 m0, s54, 24576\n\t" \
  "s_nop 0\n\t" \
  "global_load_lds_dwordx4 v200, s[46:47]\n\t" \
  "s_add_u32 m0, s54, 25600\n\t" \
  "s_nop 0\n\t" \
  "global_load_lds_dwordx4 v201, s[46:47]\n\t" \
  "s_add_u32 m0, s54, 26624\n\t" \
  "s_nop 0\n\t" \
  "global_load_lds_dwordx4 v202, s[46:47]\n\t" \
  "s_add_u32 s46, s46, 36864\n\t" \
  "s_addc_u32 s47, s47, 0\n\t" \
  "s_add_u32 m0, s54, 27648\n\t" \
  "s_nop 0\n\t" \
  "global_load_lds_dwordx4 v200, s[46:47]\n\t" \
  "s_add_u32 m0, s54, 28672\n\t" \
  "s_nop 0\n\t" \
  "global_load_lds_dwordx4 v201, s[46:47]\n\t" \
  "s_add_u32 m0, s54, 29696\n\t" \
  "s_nop 0\n\t" \
  "global_load_lds_dwordx4 v202, s[46:47]\n\t" \
  "s_add_u32 s46, s46, 36864\n\t" \
  "s_addc_u32 s47, s47, 0\n\t" \
  "s_add_u32 m0, s54, 30720\n\t" \
  "s_nop 0\n\t" \
  "global_load_lds_dwordx4 v200, s[46:47]\n\t" \
  "s_add_u32 m0, s54, 31744\n\t" \
  "s_nop 0\n\t" \
  "global_load_lds_dwordx4 v201, s[46:47]\n\t" \
  "s_add_u32 m0, s54, 32768\n\t" \
  "s_nop 0\n\t" \
  "global_load_lds_dwordx4 v202, s[46:47]\n\t" \
  "s_add_u32 s46, s46, 36864\n\t" \
  "s_addc_u32 s47, s47, 0\n\t" \
  "s_add_u32 m0, s54, 33792\n\t" \
  "s_nop 0\n\t" \
  "global_load_lds_dwordx4 v200, s[46:47]\n\t" \
  "s_add_u32 m0, s54, 34816\n\t" \
  "s_nop 0\n\t" \
  "global_load_lds_dwordx4 v201, s[46:47]\n\t" \
  "s_add_u32 m0, s54, 35840\n\t" \
  "s_nop 0\n\t" \
  "global_load_lds_dwordx4 v202, s[46:47]\n\t" \
  "s_add_u32 s46, s46, 36864\n\t" \
  "s_addc_u32 s47, s47, 0\n\t" \
  "s_waitcnt vmcnt(18)\n\t" \
  "ds_read_b64_tr_b16 a[0:1], v203 offset:0\n\t" \
  "ds_read_b64_tr_b16 a[2:3], v203 offset:1536\n\t" \
  "ds_read_b64_tr_b16 a[4:5], v203 offset:8\n\t" \
  "ds_read_b64_tr_b16 a[6:7], v203 offset:1544\n\t" \
  "ds_read_b64_tr_b16 v[0:1], v204 offset:64\n\t" \
  "ds_read_b64_tr_b16 v[2:3], v204 offset:1600\n\t" \
  "ds_read_b64_tr_b16 a[8:9], v203 offset:3072\n\t" \
  "ds_read_b64_tr_b16 a[10:11], v203 offset:4608\n\t" \
  "ds_read_b64_tr_b16 a[12:13], v203 offset:3080\n\t" \
  "ds_read_b64_tr_b16 a[14:15], v203 offset:4616\n\t" \
  "ds_read_b64_tr_b16 v[4:5], v204 offset:3136\n\t" \
  "ds_read_b64_tr_b16 v[6:7], v204 offset:4672\n\t" \
  "ds_read_b64_tr_b16 a[16:17], v203 offset:6144\n\t" \
  "ds_read_b64_tr_b16 a[18:19], v203 offset:7680\n\t" \
  "ds_read_b64_tr_b16 a[20:21], v203 offset:6152\n\t" \
  "ds_read_b64_tr_b16 a[22:23], v203 offset:7688\n\t" \
  "ds_read_b64_tr_b16 v[8:9], v204 offset:6208\n\t" \
  "ds_read_b64_tr_b16 v[10:11], v204 offset:7744\n\t" \
  "ds_read_b64_tr_b16 a[24:25], v203 offset:9216\n\t" \
  "ds_read_b64_tr_b16 a[26:27], v203 offset:10752\n\t" \
  "ds_read_b64_tr_b16 a[28:29], v203 offset:9224\n\t" \
  "ds_read_b64_tr_b16 a[30:31], v203 offset:10760\n\t" \
  "ds_read_b64_tr_b16 v[12:13], v204 offset:9280\n\t" \
  "ds_read_b64_tr_b16 v[14:15], v204 offset:10816\n\t" \
  "ds_read_b64_tr_b16 a[32:33], v203 offset:12288\n\t" \
  "ds_read_b64_tr_b16 a[34:35], v203 offset:13824\n\t" \
  "ds_read_b64_tr_b16 a[36:37], v203 offset:12296\n\t" \
  "ds_read_b64_tr_b16 a[38:39], v203 offset:13832\n\t" \
  "ds_read_b64_tr_b16 v[16:17], v204 offset:12352\n\t" \
  "ds_read_b64_tr_b16 v[18:19], v204 offset:13888\n\t" \
  "ds_read_b64_tr_b16 a[40:41], v203 offset:15360\n\t" \
  "ds_read_b64_tr_b16 a[42:43], v203 offset:16896\n\t" \
  "ds_read_b64_tr_b16 a[44:45], v203 offset:15368\n\t" \
  "ds_read_b64_tr_b16 a[46:47], v203 offset:16904\n\t" \
  "ds_read_b64_tr_b16 v[20:21], v204 offset:15424\n\t" \
  "ds_read_b64_tr_b16 v[22:23], v204 offset:16960\n\t" \
  "s_waitcnt lgkmcnt(0)\n\t" \
  "s_sleep 32\n\t" \
  "s_add_u32 m0, s54, 0\n\t" \
  "s_nop 0\n\t" \
  "global_load_lds_dwordx4 v200, s[46:47]\n\t" \
  "s_add_u32 m0, s54, 1024\n\t" \
  "s_nop 0\n\t" \
  "global_load_lds_dwordx4 v201, s[46:47]\n\t" \
  "s_add_u32 m0, s54, 2048\n\t" \
  "s_nop 0\n\t" \
  "global_load_lds_dwordx4 v202, s[46:47]\n\t" \
  "s_add_u32 s46, s46, 36864\n\t" \
  "s_addc_u32 s47, s47, 0\n\t" \
  "s_add_u32 m0, s54, 3072\n\t" \
  "s_nop 0\n\t" \
  "global_load_lds_dwordx4 v200, s[46:47]\n\t" \
  "s_add_u32 m0, s54, 4096\n\t" \
  "s_nop 0\n\t" \
  "global_load_lds_dwordx4 v201, s[46:47]\n\t" \
  "s_add_u32 m0, s54, 5120\n\t" \
  "s_nop 0\n\t" \
  "global_load_lds_dwordx4 v202, s[46:47]\n\t" \
  "s_add_u32 s46, s46, 36864\n\t" \
  "s_addc_u32 s47, s47, 0\n\t" \
  "s_add_u32 m0, s54, 6144\n\t" \
  "s_nop 0\n\t" \
  "global_load_lds_dwordx4 v200, s[46:47]\n\t" \
  "s_add_u32 m0, s54, 7168\n\t" \
  "s_nop 0\n\t" \
  "global_load_lds_dwordx4 v201, s[46:47]\n\t" \
  "s_add_u32 m0, s54, 8192\n\t" \
  "s_nop 0\n\t" \
  "global_load_lds_dwordx4 v202, s[46:47]\n\t" \
  "s_add_u32 s46, s46, 36864\n\t" \
  "s_addc_u32 s47, s47, 0\n\t" \
  "s_add_u32 m0, s54, 9216\n\t" \
  "s_nop 0\n\t" \
  "global_load_lds_dwordx4 v200, s[46:47]\n\t" \
  "s_add_u32 m0, s54, 10240\n\t" \
  "s_nop 0\n\t" \
  "global_load_lds_dwordx4 v201, s[46:47]\n\t" \
  "s_add_u32 m0, s54, 11264\n\t" \
  "s_nop 0\n\t" \
  "global_load_lds_dwordx4 v202, s[46:47]\n\t" \
  "s_add_u32 s46, s46, 36864\n\t" \
  "s_addc_u32 s47, s47, 0\n\t" \
  "s_add_u32 m0, s54, 12288\n\t" \
  "s_nop 0\n\t" \
  "global_load_lds_dwordx4 v200, s[46:47]\n\t" \
  "s_add_u32 m0, s54, 13312\n\t" \
  "s_nop 0\n\t" \
  "global_load_lds_dwordx4 v201, s[46:47]\n\t" \
  "s_add_u32 m0, s54, 14336\n\t" \
  "s_nop 0\n\t" \
  "global_load_lds_dwordx4 v202, s[46:47]\n\t" \
  "s_add_u32 s46, s46, 36864\n\t" \
  "s_addc_u32 s47, s47, 0\n\t" \
  "s_add_u32 m0, s54, 15360\n\t" \
  "s_nop 0\n\t" \
  "global_load_lds_dwordx4 v200, s[46:47]\n\t" \
  "s_add_u32 m0, s54, 16384\n\t" \
  "s_nop 0\n\t" \
  "global_load_lds_dwordx4 v201, s[46:47]\n\t" \
  "s_add_u32 m0, s54, 17408\n\t" \
  "s_nop 0\n\t" \
  "global_load_lds_dwordx4 v202, s[46:47]\n\t" \
  "s_add_u32 s46, s46, 36864\n\t" \
  "s_addc_u32 s47, s47, 0\n\t" \
  "s_waitcnt vmcnt(18)\n\t" \
  "ds_read_b64_tr_b16 a[48:49], v203 offset:18432\n\t" \
  "ds_read_b64_tr_b16 a[50:51], v203 offset:19968\n\t" \
  "ds_read_b64_tr_b16 a[52:53], v203 offset:18440\n\t" \
  "ds_read_b64_tr_b16 a[54:55], v203 offset:19976\n\t" \
  "ds_read_b64_tr_b16 v[24:25], v204 offset:18496\n\t" \
  "ds_read_b64_tr_b16 v[26:27], v204 offset:20032\n\t" \
  "ds_read_b64_tr_b16 a[56:57], v203 offset:21504\n\t" \
  "ds_read_b64_tr_b16 a[58:59], v203 offset:23040\n\t" \
  "ds_read_b64_tr_b16 a[60:61], v203 offset:21512\n\t" \
  "ds_read_b64_tr_b16 a[62:63], v203 offset:23048\n\t" \
  "ds_read_b64_tr_b16 v[28:29], v204 offset:21568\n\t" \
  "ds_read_b64_tr_b16 v[30:31], v204 offset:23104\n\t" \
  "ds_read_b64_tr_b16 a[64:65], v203 offset:24576\n\t" \
  "ds_read_b64_tr_b16 a[66:67], v203 offset:26112\n\t" \
  "ds_read_b64_tr_b16 a[68:69], v203 offset:24584\n\t" \
  "ds_read_b64_tr_b16 a[70:71], v203 offset:26120\n\t" \
  "ds_read_b64_tr_b16 v[32:33], v204 offset:24640\n\t" \
  "ds_read_b64_tr_b16 v[34:35], v204 offset:26176\n\t" \
  "ds_read_b64_tr_b16 a[72:73], v203 offset:27648\n\t" \
  "ds_read_b64_tr_b16 a[74:75], v203 offset:29184\n\t" \
  "ds_read_b64_tr_b16 a[76:77], v203 offset:27656\n\t" \
  "ds_read_b64_tr_b16 a[78:79], v203 offset:29192\n\t" \
  "ds_read_b64_tr_b16 v[36:37], v204 offset:27712\n\t" \
  "ds_read_b64_tr_b16 v[38:39], v204 offset:29248\n\t" \
  "ds_read_b64_tr_b16 a[80:81], v203 offset:30720\n\t" \
  "ds_read_b64_tr_b16 a[82:83], v203 offset:32256\n\t" \
  "ds_read_b64_tr_b16 a[84:85], v203 offset:30728\n\t" \
  "ds_read_b64_tr_b16 a[86:87], v203 offset:32264\n\t" \
  "ds_read_b64_tr_b16 v[40:41], v204 offset:30784\n\t" \
  "ds_read_b64_tr_b16 v[42:43], v204 offset:32320\n\t" \
  "ds_read_b64_tr_b16 a[88:89], v203 offset:33792\n\t" \
  "ds_read_b64_tr_b16 a[90:91], v203 offset:35328\n\t" \
  "ds_read_b64_tr_b16 a[92:93], v203 offset:33800\n\t" \
  "ds_read_b64_tr_b16 a[94:95], v203 offset:35336\n\t" \
  "ds_read_b64_tr_b16 v[44:45], v204 offset:33856\n\t" \
  "ds_read_b64_tr_b16 v[46:47], v204 offset:35392\n\t" \
  "s_waitcnt lgkmcnt(0)\n\t" \
  "s_sleep 32\n\t" \
  "s_add_u32 m0, s54, 18432\n\t" \
  "s_nop 0\n\t" \
  "global_load_lds_dwordx4 v200, s[46:47]\n\t" \
  "s_add_u32 m0, s54, 19456\n\t" \
  "s_nop 0\n\t" \
  "global_load_lds_dwordx4 v201, s[46:47]\n\t" \
  "s_add_u32 m0, s54, 20480\n\t" \
  "s_nop 0\n\t" \
  "global_load_lds_dwordx4 v202, s[46:47]\n\t" \
  "s_add_u32 s46, s46, 36864\n\t" \
  "s_addc_u32 s47, s47, 0\n\t" \
  "s_add_u32 m0, s54, 21504\n\t" \
  "s_nop 0\n\t" \
  "global_load_lds_dwordx4 v200, s[46:47]\n\t" \
  "s_add_u32 m0, s54, 22528\n\t" \
  "s_nop 0\n\t" \
  "global_load_lds_dwordx4 v201, s[46:47]\n\t" \
  "s_add_u32 m0, s54, 23552\n\t" \
  "s_nop 0\n\t" \
  "global_load_lds_dwordx4 v202, s[46:47]\n\t" \
  "s_add_u32 s46, s46, 36864\n\t" \
  "s_addc_u32 s47, s47, 0\n\t" \
  "s_add_u32 m0, s54, 24576\n\t" \
  "s_nop 0\n\t" \
  "global_load_lds_dwordx4 v200, s[46:47]\n\t" \
  "s_add_u32 m0, s54, 25600\n\t" \
  "s_nop 0\n\t" \
  "global_load_lds_dwordx4 v201, s[46:47]\n\t" \
  "s_add_u32 m0, s54, 26624\n\t" \
  "s_nop 0\n\t" \
  "global_load_lds_dwordx4 v202, s[46:47]\n\t" \
  "s_add_u32 s46, s46, 36864\n\t" \
  "s_addc_u32 s47, s47, 0\n\t" \
  "s_add_u32 m0, s54, 27648\n\t" \
  "s_nop 0\n\t" \
  "global_load_lds_dwordx4 v200, s[46:47]\n\t" \
  "s_add_u32 m0, s54, 28672\n\t" \
  "s_nop 0\n\t" \
  "global_load_lds_dwordx4 v201, s[46:47]\n\t" \
  "s_add_u32 m0, s54, 29696\n\t" \
  "s_nop 0\n\t" \
  "global_load_lds_dwordx4 v202, s[46:47]\n\t" \
  "s_add_u32 s46, s46, 36864\n\t" \
  "s_addc_u32 s47, s47, 0\n\t" \
  "s_add_u32 m0, s54, 30720\n\t" \
  "s_nop 0\n\t" \
  "global_load_lds_dwordx4 v200, s[46:47]\n\t" \
  "s_add_u32 m0, s54, 31744\n\t" \
  "s_nop 0\n\t" \
  "global_load_lds_dwordx4 v201, s[46:47]\n\t" \
  "s_add_u32 m0, s54, 32768\n\t" \
  "s_nop 0\n\t" \
  "global_load_lds_dwordx4 v202, s[46:47]\n\t" \
  "s_add_u32 s46, s46, 36864\n\t" \
  "s_addc_u32 s47, s47, 0\n\t" \
  "s_add_u32 m0, s54, 33792\n\t" \
  "s_nop 0\n\t" \
  "global_load_lds_dwordx4 v200, s[46:47]\n\t" \
  "s_add_u32 m0, s54, 34816\n\t" \
  "s_nop 0\n\t" \
  "global_load_lds_dwordx4 v201, s[46:47]\n\t" \
  "s_add_u32 m0, s54, 35840\n\t" \
  "s_nop 0\n\t" \
  "global_load_lds_dwordx4 v202, s[46:47]\n\t" \
  "s_add_u32 s46, s46, 36864\n\t" \
  "s_addc_u32 s47, s47, 0\n\t" \
  "s_waitcnt vmcnt(18)\n\t" \
  "ds_read_b64_tr_b16 a[96:97], v203 offset:0\n\t" \
  "ds_read_b64_tr_b16 a[98:99], v203 offset:1536\n\t" \
  "ds_read_b64_tr_b16 a[100:101], v203 offset:8\n\t" \
  "ds_read_b64_tr_b16 a[102:103], v203 offset:1544\n\t" \
  "ds_read_b64_tr_b16 v[48:49], v204 offset:64\n\t" \
  "ds_read_b64_tr_b16 v[50:51], v204 offset:1600\n\t" \
  "ds_read_b64_tr_b16 a[104:105], v203 offset:3072\n\t" \
  "ds_read_b64_tr_b16 a[106:107], v203 offset:4608\n\t" \
  "ds_read_b64_tr_b16 a[108:109], v203 offset:3080\n\t" \
  "ds_read_b64_tr_b16 a[110:111], v203 offset:4616\n\t" \
  "ds_read_b64_tr_b16 v[52:53], v204 offset:3136\n\t" \
  "ds_read_b64_tr_b16 v[54:55], v204 offset:4672\n\t" \
  "ds_read_b64_tr_b16 a[112:113], v203 offset:6144\n\t" \
  "ds_read_b64_tr_b16 a[114:115], v203 offset:7680\n\t" \
  "ds_read_b64_tr_b16 a[116:117], v203 offset:6152\n\t" \
  "ds_read_b64_tr_b16 a[118:119], v203 offset:7688\n\t" \
  "ds_read_b64_tr_b16 v[56:57], v204 offset:6208\n\t" \
  "ds_read_b64_tr_b16 v[58:59], v204 offset:7744\n\t" \
  "ds_read_b64_tr_b16 a[120:121], v203 offset:9216\n\t" \
  "ds_read_b64_tr_b16 a[122:123], v203 offset:10752\n\t" \
  "ds_read_b64_tr_b16 a[124:125], v203 offset:9224\n\t" \
  "ds_read_b64_tr_b16 a[126:127], v203 offset:10760\n\t" \
  "ds_read_b64_tr_b16 v[60:61], v204 offset:9280\n\t" \
  "ds_read_b64_tr_b16 v[62:63], v204 offset:10816\n\t" \
  "ds_read_b64_tr_b16 a[128:129], v203 offset:12288\n\t" \
  "ds_read_b64_tr_b16 a[130:131], v203 offset:13824\n\t" \
  "ds_read_b64_tr_b16 a[132:133], v203 offset:12296\n\t" \
  "ds_read_b64_tr_b16 a[134:135], v203 offset:13832\n\t" \
  "ds_read_b64_tr_b16 v[64:65], v204 offset:12352\n\t" \
  "ds_read_b64_tr_b16 v[66:67], v204 offset:13888\n\t" \
  "ds_read_b64_tr_b16 a[136:137], v203 offset:15360\n\t" \
  "ds_read_b64_tr_b16 a[138:139], v203 offset:16896\n\t" \
  "ds_read_b64_tr_b16 a[140:141], v203 offset:15368\n\t" \
  "ds_read_b64_tr_b16 a[142:143], v203 offset:16904\n\t" \
  "ds_read_b64_tr_b16 v[68:69], v204 offset:15424\n\t" \
  "ds_read_b64_tr_b16 v[70:71], v204 offset:16960\n\t" \
  "s_waitcnt lgkmcnt(0)\n\t" \
  "s_sleep 32\n\t" \
  "s_add_u32 m0, s54, 0\n\t" \
  "s_nop 0\n\t" \
  "global_load_lds_dwordx4 v200, s[46:47]\n\t" \
  "s_add_u32 m0, s54, 1024\n\t" \
  "s_nop 0\n\t" \
  "global_load_lds_dwordx4 v201, s[46:47]\n\t" \
  "s_add_u32 m0, s54, 2048\n\t" \
  "s_nop 0\n\t" \
  "global_load_lds_dwordx4 v202, s[46:47]\n\t" \
  "s_add_u32 s46, s46, 36864\n\t" \
  "s_addc_u32 s47, s47, 0\n\t" \
  "s_add_u32 m0, s54, 3072\n\t" \
  "s_nop 0\n\t" \
  "global_load_lds_dwordx4 v200, s[46:47]\n\t" \
  "s_add_u32 m0, s54, 4096\n\t" \
  "s_nop 0\n\t" \
  "global_load_lds_dwordx4 v201, s[46:47]\n\t" \
  "s_add_u32 m0, s54, 5120\n\t" \
  "s_nop 0\n\t" \
  "global_load_lds_dwordx4 v202, s[46:47]\n\t" \
  "s_add_u32 s46, s46, 36864\n\t" \
  "s_addc_u32 s47, s47, 0\n\t" \
  "s_add_u32 m0, s54, 6144\n\t" \
  "s_nop 0\n\t" \
  "global_load_lds_dwordx4 v200, s[46:47]\n\t" \
  "s_add_u32 m0, s54, 7168\n\t" \
  "s_nop 0\n\t" \
  "global_load_lds_dwordx4 v201, s[46:47]\n\t" \
  "s_add_u32 m0, s54, 8192\n\t" \
  "s_nop 0\n\t" \
  "global_load_lds_dwordx4 v202, s[46:47]\n\t" \
  "s_add_u32 s46, s46, 36864\n\t" \
  "s_addc_u32 s47, s47, 0\n\t" \
  "s_add_u32 m0, s54, 9216\n\t" \
  "s_nop 0\n\t" \
  "global_load_lds_dwordx4 v200, s[46:47]\n\t" \
  "s_add_u32 m0, s54, 10240\n\t" \
  "s_nop 0\n\t" \
  "global_load_lds_dwordx4 v201, s[46:47]\n\t" \
  "s_add_u32 m0, s54, 11264\n\t" \
  "s_nop 0\n\t" \
  "global_load_lds_dwordx4 v202, s[46:47]\n\t" \
  "s_add_u32 s46, s46, 36864\n\t" \
  "s_addc_u32 s47, s47, 0\n\t" \
  "s_add_u32 m0, s54, 12288\n\t" \
  "s_nop 0\n\t" \
  "global_load_lds_dwordx4 v200, s[46:47]\n\t" \
  "s_add_u32 m0, s54, 13312\n\t" \
  "s_nop 0\n\t" \
  "global_load_lds_dwordx4 v201, s[46:47]\n\t" \
  "s_add_u32 m0, s54, 14336\n\t" \
  "s_nop 0\n\t" \
  "global_load_lds_dwordx4 v202, s[46:47]\n\t" \
  "s_add_u32 s46, s46, 36864\n\t" \
  "s_addc_u32 s47, s47, 0\n\t" \
  "s_add_u32 m0, s54, 15360\n\t" \
  "s_nop 0\n\t" \
  "global_load_lds_dwordx4 v200, s[46:47]\n\t" \
  "s_add_u32 m0, s54, 16384\n\t" \
  "s_nop 0\n\t" \
  "global_load_lds_dwordx4 v201, s[46:47]\n\t" \
  "s_add_u32 m0, s54, 17408\n\t" \
  "s_nop 0\n\t" \
  "global_load_lds_dwordx4 v202, s[46:47]\n\t" \
  "s_add_u32 s46, s46, 36864\n\t" \
  "s_addc_u32 s47, s47, 0\n\t" \
  "s_waitcnt vmcnt(18)\n\t" \
  "ds_read_b64_tr_b16 a[144:145], v203 offset:18432\n\t" \
  "ds_read_b64_tr_b16 a[146:147], v203 offset:19968\n\t" \
  "ds_read_b64_tr_b16 a[148:149], v203 offset:18440\n\t" \
  "ds_read_b64_tr_b16 a[150:151], v203 offset:19976\n\t" \
  "ds_read_b64_tr_b16 v[72:73], v204 offset:18496\n\t" \
  "ds_read_b64_tr_b16 v[74:75], v204 offset:20032\n\t" \
  "ds_read_b64_tr_b16 a[152:153], v203 offset:21504\n\t" \
  "ds_read_b64_tr_b16 a[154:155], v203 offset:23040\n\t" \
  "ds_read_b64_tr_b16 a[156:157], v203 offset:21512\n\t" \
  "ds_read_b64_tr_b16 a[158:159], v203 offset:23048\n\t" \
  "ds_read_b64_tr_b16 v[76:77], v204 offset:21568\n\t" \
  "ds_read_b64_tr_b16 v[78:79], v204 offset:23104\n\t" \
  "ds_read_b64_tr_b16 a[160:161], v203 offset:24576\n\t" \
  "ds_read_b64_tr_b16 a[162:163], v203 offset:26112\n\t" \
  "ds_read_b64_tr_b16 a[164:165], v203 offset:24584\n\t" \
  "ds_read_b64_tr_b16 a[166:167], v203 offset:26120\n\t" \
  "ds_read_b64_tr_b16 v[80:81], v204 offset:24640\n\t" \
  "ds_read_b64_tr_b16 v[82:83], v204 offset:26176\n\t" \
  "ds_read_b64_tr_b16 a[168:169], v203 offset:27648\n\t" \
  "ds_read_b64_tr_b16 a[170:171], v203 offset:29184\n\t" \
  "ds_read_b64_tr_b16 a[172:173], v203 offset:27656\n\t" \
  "ds_read_b64_tr_b16 a[174:175], v203 offset:29192\n\t" \
  "ds_read_b64_tr_b16 v[84:85], v204 offset:27712\n\t" \
  "ds_read_b64_tr_b16 v[86:87], v204 offset:29248\n\t" \
  "ds_read_b64_tr_b16 a[176:177], v203 offset:30720\n\t" \
  "ds_read_b64_tr_b16 a[178:179], v203 offset:32256\n\t" \
  "ds_read_b64_tr_b16 a[180:181], v203 offset:30728\n\t" \
  "ds_read_b64_tr_b16 a[182:183], v203 offset:32264\n\t" \
  "ds_read_b64_tr_b16 v[88:89], v204 offset:30784\n\t" \
  "ds_read_b64_tr_b16 v[90:91], v204 offset:32320\n\t" \
  "ds_read_b64_tr_b16 a[184:185], v203 offset:33792\n\t" \
  "ds_read_b64_tr_b16 a[186:187], v203 offset:35328\n\t" \
  "ds_read_b64_tr_b16 a[188:189], v203 offset:33800\n\t" \
  "ds_read_b64_tr_b16 a[190:191], v203 offset:35336\n\t" \
  "ds_read_b64_tr_b16 v[92:93], v204 offset:33856\n\t" \
  "ds_read_b64_tr_b16 v[94:95], v204 offset:35392\n\t" \
  "s_waitcnt lgkmcnt(0)\n\t" \
  "s_sleep 32\n\t" \
  "s_add_u32 m0, s54, 18432\n\t" \
  "s_nop 0\n\t" \
  "global_load_lds_dwordx4 v200, s[46:47]\n\t" \
  "s_add_u32 m0, s54, 19456\n\t" \
  "s_nop 0\n\t" \
  "global_load_lds_dwordx4 v201, s[46:47]\n\t" \
  "s_add_u32 m0, s54, 20480\n\t" \
  "s_nop 0\n\t" \
  "global_load_lds_dwordx4 v202, s[46:47]\n\t" \
  "s_add_u32 s46, s46, 36864\n\t" \
  "s_addc_u32 s47, s47, 0\n\t" \
  "s_add_u32 m0, s54, 21504\n\t" \
  "s_nop 0\n\t" \
  "global_load_lds_dwordx4 v200, s[46:47]\n\t" \
  "s_add_u32 m0, s54, 22528\n\t" \
  "s_nop 0\n\t" \
  "global_load_lds_dwordx4 v201, s[46:47]\n\t" \
  "s_add_u32 m0, s54, 23552\n\t" \
  "s_nop 0\n\t" \
  "global_load_lds_dwordx4 v202, s[46:47]\n\t" \
  "s_add_u32 s46, s46, 36864\n\t" \
  "s_addc_u32 s47, s47, 0\n\t" \
  "s_waitcnt vmcnt(6)\n\t" \
  "ds_read_b64_tr_b16 a[192:193], v203 offset:0\n\t" \
  "ds_read_b64_tr_b16 a[194:195], v203 offset:1536\n\t" \
  "ds_read_b64_tr_b16 a[196:197], v203 offset:8\n\t" \
  "ds_read_b64_tr_b16 a[198:199], v203 offset:1544\n\t" \
  "ds_read_b64_tr_b16 v[96:97], v204 offset:64\n\t" \
  "ds_read_b64_tr_b16 v[98:99], v204 offset:1600\n\t" \
  "ds_read_b64_tr_b16 a[200:201], v203 offset:3072\n\t" \
  "ds_read_b64_tr_b16 a[202:203], v203 offset:4608\n\t" \
  "ds_read_b64_tr_b16 a[204:205], v203 offset:3080\n\t" \
  "ds_read_b64_tr_b16 a[206:207], v203 offset:4616\n\t" \
  "ds_read_b64_tr_b16 v[100:101], v204 offset:3136\n\t" \
  "ds_read_b64_tr_b16 v[102:103], v204 offset:4672\n\t" \
  "ds_read_b64_tr_b16 a[208:209], v203 offset:6144\n\t" \
  "ds_read_b64_tr_b16 a[210:211], v203 offset:7680\n\t" \
  "ds_read_b64_tr_b16 a[212:213], v203 offset:6152\n\t" \
  "ds_read_b64_tr_b16 a[214:215], v203 offset:7688\n\t" \
  "ds_read_b64_tr_b16 v[104:105], v204 offset:6208\n\t" \
  "ds_read_b64_tr_b16 v[106:107], v204 offset:7744\n\t" \
  "ds_read_b64_tr_b16 a[216:217], v203 offset:9216\n\t" \
  "ds_read_b64_tr_b16 a[218:219], v203 offset:10752\n\t" \
  "ds_read_b64_tr_b16 a[220:221], v203 offset:9224\n\t" \
  "ds_read_b64_tr_b16 a[222:223], v203 offset:10760\n\t" \
  "ds_read_b64_tr_b16 v[108:109], v204 offset:9280\n\t" \
  "ds_read_b64_tr_b16 v[110:111], v204 offset:10816\n\t" \
  "ds_read_b64_tr_b16 a[224:225], v203 offset:12288\n\t" \
  "ds_read_b64_tr_b16 a[226:227], v203 offset:13824\n\t" \
  "ds_read_b64_tr_b16 a[228:229], v203 offset:12296\n\t" \
  "ds_read_b64_tr_b16 a[230:231], v203 offset:13832\n\t" \
  "ds_read_b64_tr_b16 v[112:113], v204 offset:12352\n\t" \
  "ds_read_b64_tr_b16 v[114:115], v204 offset:13888\n\t" \
  "ds_read_b64_tr_b16 a[232:233], v203 offset:15360\n\t" \
  "ds_read_b64_tr_b16 a[234:235], v203 offset:16896\n\t" \
  "ds_read_b64_tr_b16 a[236:237], v203 offset:15368\n\t" \
  "ds_read_b64_tr_b16 a[238:239], v203 offset:16904\n\t" \
  "ds_read_b64_tr_b16 v[116:117], v204 offset:15424\n\t" \
  "ds_read_b64_tr_b16 v[118:119], v204 offset:16960\n\t" \
  "s_waitcnt lgkmcnt(0)\n\t" \
  "s_waitcnt vmcnt(0)\n\t" \
  "ds_read_b64_tr_b16 a[240:241], v203 offset:18432\n\t" \
  "ds_read_b64_tr_b16 a[242:243], v203 offset:19968\n\t" \
  "ds_read_b64_tr_b16 a[244:245], v203 offset:18440\n\t" \
  "ds_read_b64_tr_b16 a[246:247], v203 offset:19976\n\t" \
  "ds_read_b64_tr_b16 v[120:121], v204 offset:18496\n\t" \
  "ds_read_b64_tr_b16 v[122:123], v204 offset:20032\n\t" \
  "ds_read_b64_tr_b16 a[248:249], v203 offset:21504\n\t" \
  "ds_read_b64_tr_b16 a[250:251], v203 offset:23040\n\t" \
  "ds_read_b64_tr_b16 a[252:253], v203 offset:21512\n\t" \
  "ds_read_b64_tr_b16 a[254:255], v203 offset:23048\n\t" \
  "ds_read_b64_tr_b16 v[124:125], v204 offset:21568\n\t" \
  "ds_read_b64_tr_b16 v[126:127], v204 offset:23104\n\t" \
  "s_waitcnt lgkmcnt(0)\n\t" \
  "v_lshlrev_b32 v188, 4, v198\n\t" \
  "s_lshl_b32 s40, s45, 15\n\t" \
  "v_add_u32 v189, s40, v188\n\t" \
  "s_barrier\n\t" \
  "ds_write_b128 v189, v[0:3] offset:0\n\t" \
  "ds_write_b128 v189, v[4:7] offset:1024\n\t" \
  "ds_write_b128 v189, v[8:11] offset:2048\n\t" \
  "ds_write_b128 v189, v[12:15] offset:3072\n\t" \
  "ds_write_b128 v189, v[16:19] offset:4096\n\t" \
  "ds_write_b128 v189, v[20:23] offset:5120\n\t" \
  "ds_write_b128 v189, v[24:27] offset:6144\n\t" \
  "ds_write_b128 v189, v[28:31] offset:7168\n\t" \
  "ds_write_b128 v189, v[32:35] offset:8192\n\t" \
  "ds_write_b128 v189, v[36:39] offset:9216\n\t" \
  "ds_write_b128 v189, v[40:43] offset:10240\n\t" \
  "ds_write_b128 v189, v[44:47] offset:11264\n\t" \
  "ds_write_b128 v189, v[48:51] offset:12288\n\t" \
  "ds_write_b128 v189, v[52:55] offset:13312\n\t" \
  "ds_write_b128 v189, v[56:59] offset:14336\n\t" \
  "ds_write_b128 v189, v[60:63] offset:15360\n\t" \
  "ds_write_b128 v189, v[64:67] offset:16384\n\t" \
  "ds_write_b128 v189, v[68:71] offset:17408\n\t" \
  "ds_write_b128 v189, v[72:75] offset:18432\n\t" \
  "ds_write_b128 v189, v[76:79] offset:19456\n\t" \
  "ds_write_b128 v189, v[80:83] offset:20480\n\t" \
  "ds_write_b128 v189, v[84:87] offset:21504\n\t" \
  "ds_write_b128 v189, v[88:91] offset:22528\n\t" \
  "ds_write_b128 v189, v[92:95] offset:23552\n\t" \
  "ds_write_b128 v189, v[96:99] offset:24576\n\t" \
  "ds_write_b128 v189, v[100:103] offset:25600\n\t" \
  "ds_write_b128 v189, v[104:107] offset:26624\n\t" \
  "ds_write_b128 v189, v[108:111] offset:27648\n\t" \
  "ds_write_b128 v189, v[112:115] offset:28672\n\t" \
  "ds_write_b128 v189, v[116:119] offset:29696\n\t" \
  "ds_write_b128 v189, v[120:123] offset:30720\n\t" \
  "ds_write_b128 v189, v[124:127] offset:31744\n\t" \
  "v_and_b32 v162, 15, v198\n\t" \
  "v_mul_u32_u24 v194, 1152, v162\n\t" \
  "s_mul_i32 s41, s30, 96\n\t" \
  "v_add_u32 v194, s41, v194\n\t" \
  "v_and_b32 v199, 48, v198\n\t" \
  "v_add_u32 v193, v194, v199\n\t" \
  "v_lshrrev_b32 v199, 1, v199\n\t" \
  "v_add_u32 v194, v194, v199\n\t" \
  "v_lshrrev_b32 v199, 5, v198\n\t" \
  "v_lshlrev_b32 v199, 8, v199\n\t" \
  "v_lshl_add_u32 v195, v162, 4, v199\n\t" \
  "v_bfe_u32 v199, v198, 4, 1\n\t" \
  "v_lshl_add_u32 v195, v199, 3, v195\n\t" \
  "v_lshlrev_b32 v197, 3, v198\n\t" \
  "v_mov_b32 v163, 0\n\t" \
  "s_mul_i32 s40, s45, 3072\n\t" \
  "s_add_u32 s40, s40, 131072\n\t" \
  "v_add_u32 v196, s40, v188\n\t" \
  "v_min_u32 v162, 5, v198\n\t" \
  "v_subrev_u32 v192, 3, v162\n\t" \
  "v_min_u32 v192, v192, v162\n\t" \
  "s_mul_i32 s40, s45, 6\n\t" \
  "v_add_u32 v162, s40, v162\n\t" \
  "v_lshlrev_b32 v162, 7, v162\n\t" \
  "v_add_u32 v190, 2138112, v162\n\t" \
  "s_lshl_b32 s40, s30, 2\n\t" \
  "s_add_u32 s40, s40, s55\n\t" \
  "s_lshl_b32 s40, s40, 7\n\t" \
  "s_add_u32 s40, s40, 2142208\n\t" \
  "s_add_u32 s56, s28, s40\n\t" \
  "s_addc_u32 s57, s29, 0\n\t" \
  "s_mul_i32 s54, s45, 32768\n\t" \
  "s_add_u32 s54, s54, 77594624\n\t" \
  "s_mov_b32 s51, 0\n\t" \
  "s_mov_b32 s52, -1\n\t" \
  "s_mov_b32 s44, 0\n\t" \
  "s_mov_b32 s48, s31\n\t" \
  "s_waitcnt lgkmcnt(0)\n\t" \
  "s_branch Lblk_%=\n\t" \
  "Ltop_%=:\n\t" \
  "ds_read_b128 v[164:167], v189 offset:0\n\t" \
  "ds_read_b128 v[168:171], v189 offset:1024\n\t" \
  "s_add_u32 s48, s31, 3\n\t" \
  "s_cmp_lt_u32 s48, 112\n\t" \
  "s_cselect_b32 s48, s48, s49\n\t" \
  "s_add_u32 s41, s31, 4\n\t" \
  "s_cmp_lt_u32 s31, 112\n\t" \
  "s_cselect_b32 s48, s48, s41\n\t" \
  "s_lshr_b32 s43, s48, 1\n\t" \
  "s_cmp_gt_u32 s44, s43\n\t" \
  "s_cselect_b32 s50, 1, 0\n\t" \
  "s_cmp_lt_u32 s48, 112\n\t" \
  "s_cselect_b32 s50, s50, 0\n\t" \
  "s_add_u32 s41, s48, 3\n\t" \
  "s_lshr_b32 s41, s41, 1\n\t" \
  "s_min_u32 s41, s41, 63\n\t" \
  "s_cmp_le_u32 s44, s41\n\t" \
  "s_cselect_b32 s59, 1, 0\n\t" \
  "s_cbranch_scc0 Lnn_%=\n\t" \
  "buffer_load_dword v191, v190, s[20:23], 0 offen sc1\n\t" \
  "Lnn_%=:\n\t" \
  "s_mul_i32 s58, s31, 18432\n\t" \
  "s_mul_i32 s40, s30, 1536\n\t" \
  "s_add_u32 s58, s58, s40\n\t" \
  "s_add_u32 s58, s58, 6291456\n\t" \
  "s_mul_i32 s53, s31, 18432\n\t" \
  "s_and_b32 s40, s51, 3\n\t" \
  "s_cmp_eq_u32 s40, s45\n\t" \
  "s_cselect_b32 s61, 1, 0\n\t" \
  "s_cbranch_scc0 Lnq_%=\n\t" \
  "buffer_load_dwordx4 v[156:159], v193, s[36:39], s53 offen\n\t" \
  "buffer_load_dwordx2 v[160:161], v194, s[36:39], s53 offen offset:64\n\t" \
  "Lnq_%=:\n\t" \
  "s_cmp_ge_u32 s31, 112\n\t" \
  "s_cbranch_scc1 Ltl_%=\n\t" \
  "s_mul_i32 s62, s48, 131072\n\t" \
  "s_add_u32 s62, s62, s54\n\t" \
  "s_add_u32 s63, s62, 4096\n\t" \
  "s_add_u32 s64, s62, 8192\n\t" \
  "s_add_u32 s65, s62, 12288\n\t" \
  "s_add_u32 s66, s62, 16384\n\t" \
  "s_add_u32 s67, s62, 20480\n\t" \
  "s_add_u32 s68, s62, 24576\n\t" \
  "s_add_u32 s69, s62, 28672\n\t" \
  "s_cmp_eq_u32 s50, 1\n\t" \
  "s_cbranch_scc0 Lnp_%=\n\t" \
  "s_waitcnt vmcnt(31)\n\t" \
  "v_mfma_f32_16x16x32_bf16 v[144:147], a[0:3], v[0:3], 0\n\t" \
  "ds_read_b128 v[172:175], v189 offset:2048\n\t" \
  "v_mfma_f32_16x16x32_bf16 v[148:151], a[4:7], v[0:3], 0\n\t" \
  "s_waitcnt lgkmcnt(2)\n\t" \
  "v_mfma_f32_16x16x32_bf16 v[152:155], v[164:167], v[0:3], 0\n\t" \
  "buffer_load_dwordx4 v[0:3], v188, s[20:23], s62 offen offset:0 sc1\n\t" \
  "s_waitcnt vmcnt(31)\n\t" \
  "v_mfma_f32_16x16x32_bf16 v[144:147], a[8:11], v[4:7], v[144:147]\n\t" \
  "ds_read_b128 v[164:167], v189 offset:3072\n\t" \
  "v_mfma_f32_16x16x32_bf16 v[148:151], a[12:15], v[4:7], v[148:151]\n\t" \
  "s_waitcnt lgkmcnt(2)\n\t" \
  "v_mfma_f32_16x16x32_bf16 v[152:155], v[168:171], v[4:7], v[152:155]\n\t" \
  "buffer_load_dwordx4 v[4:7], v188, s[20:23], s62 offen offset:1024 sc1\n\t" \
  "s_waitcnt vmcnt(31)\n\t" \
  "v_mfma_f32_16x16x32_bf16 v[144:147], a[16:19], v[8:11], v[144:147]\n\t" \
  "ds_read_b128 v[168:171], v189 offset:4096\n\t" \
  "v_mfma_f32_16x16x32_bf16 v[148:151], a[20:23], v[8:11], v[148:151]\n\t" \
  "s_waitcnt lgkmcnt(2)\n\t" \
  "v_mfma_f32_16x16x32_bf16 v[152:155], v[172:175], v[8:11], v[152:155]\n\t" \
  "buffer_load_dwordx4 v[8:11], v188, s[20:23], s62 offen offset:2048 sc1\n\t" \
  "s_waitcnt vmcnt(31)\n\t" \
  "v_mfma_f32_16x16x32_bf16 v[144:147], a[24:27], v[12:15], v[144:147]\n\t" \
  "ds_read_b128 v[172:175], v189 offset:5120\n\t" \
  "v_mfma_f32_16x16x32_bf16 v[148:151], a[28:31], v[12:15], v[148:151]\n\t" \
  "s_waitcnt lgkmcnt(2)\n\t" \
  "v_mfma_f32_16x16x32_bf16 v[152:155], v[164:167], v[12:15], v[152:155]\n\t" \
  "buffer_load_dwordx4 v[12:15], v188, s[20:23], s62 offen offset:3072 sc1\n\t" \
  "s_waitcnt vmcnt(31)\n\t" \
  "v_mfma_f32_16x16x32_bf16 v[144:147], a[32:35], v[16:19], v[144:147]\n\t" \
  "ds_read_b128 v[164:167], v189 offset:6144\n\t" \
  "v_mfma_f32_16x16x32_bf16 v[148:151], a[36:39], v[16:19], v[148:151]\n\t" \
  "s_waitcnt lgkmcnt(2)\n\t" \
  "v_mfma_f32_16x16x32_bf16 v[152:155], v[168:171], v[16:19], v[152:155]\n\t" \
  "buffer_load_dwordx4 v[16:19], v188, s[20:23], s63 offen offset:0 sc1\n\t" \
  "s_waitcnt vmcnt(31)\n\t" \
  "v_mfma_f32_16x16x32_bf16 v[144:147], a[40:43], v[20:23], v[144:147]\n\t" \
  "ds_read_b128 v[168:171], v189 offset:7168\n\t" \
  "v_mfma_f32_16x16x32_bf16 v[148:151], a[44:47], v[20:23], v[148:151]\n\t" \
  "s_waitcnt lgkmcnt(2)\n\t" \
  "v_mfma_f32_16x16x32_bf16 v[152:155], v[172:175], v[20:23], v[152:155]\n\t" \
  "buffer_load_dwordx4 v[20:23], v188, s[20:23], s63 offen offset:1024 sc1\n\t" \
  "s_waitcnt vmcnt(31)\n\t" \
  "v_mfma_f32_16x16x32_bf16 v[144:147], a[48:51], v[24:27], v[144:147]\n\t" \
  "ds_read_b128 v[172:175], v189 offset:8192\n\t" \
  "v_mfma_f32_16x16x32_bf16 v[148:151], a[52:55], v[24:27], v[148:151]\n\t" \
  "s_waitcnt lgkmcnt(2)\n\t" \
  "v_mfma_f32_16x16x32_bf16 v[152:155], v[164:167], v[24:27], v[152:155]\n\t" \
  "buffer_load_dwordx4 v[24:27], v188, s[20:23], s63 offen offset:2048 sc1\n\t" \
  "s_waitcnt vmcnt(31)\n\t" \
  "v_mfma_f32_16x16x32_bf16 v[144:147], a[56:59], v[28:31], v[144:147]\n\t" \
  "ds_read_b128 v[164:167], v189 offset:9216\n\t" \
  "v_mfma_f32_16x16x32_bf16 v[148:151], a[60:63], v[28:31], v[148:151]\n\t" \
  "s_waitcnt lgkmcnt(2)\n\t" \
  "v_mfma_f32_16x16x32_bf16 v[152:155], v[168:171], v[28:31], v[152:155]\n\t" \
  "buffer_load_dwordx4 v[28:31], v188, s[20:23], s63 offen offset:3072 sc1\n\t" \
  "s_waitcnt vmcnt(31)\n\t" \
  "v_mfma_f32_16x16x32_bf16 v[144:147], a[64:67], v[32:35], v[144:147]\n\t" \
  "ds_read_b128 v[168:171], v189 offset:10240\n\t" \
  "v_mfma_f32_16x16x32_bf16 v[148:151], a[68:71], v[32:35], v[148:151]\n\t" \
  "s_waitcnt lgkmcnt(2)\n\t" \
  "v_mfma_f32_16x16x32_bf16 v[152:155], v[172:175], v[32:35], v[152:155]\n\t" \
  "buffer_load_dwordx4 v[32:35], v188, s[20:23], s64 offen offset:0 sc1\n\t" \
  "s_waitcnt vmcnt(31)\n\t" \
  "v_mfma_f32_16x16x32_bf16 v[144:147], a[72:75], v[36:39], v[144:147]\n\t" \
  "ds_read_b128 v[172:175], v189 offset:11264\n\t" \
  "v_mfma_f32_16x16x32_bf16 v[148:151], a[76:79], v[36:39], v[148:151]\n\t" \
  "s_waitcnt lgkmcnt(2)\n\t" \
  "v_mfma_f32_16x16x32_bf16 v[152:155], v[164:167], v[36:39], v[152:155]\n\t" \
  "buffer_load_dwordx4 v[36:39], v188, s[20:23], s64 offen offset:1024 sc1\n\t" \
  "s_waitcnt vmcnt(31)\n\t" \
  "v_mfma_f32_16x16x32_bf16 v[144:147], a[80:83], v[40:43], v[144:147]\n\t" \
  "ds_read_b128 v[164:167], v189 offset:12288\n\t" \
  "v_mfma_f32_16x16x32_bf16 v[148:151], a[84:87], v[40:43], v[148:151]\n\t" \
  "s_waitcnt lgkmcnt(2)\n\t" \
  "v_mfma_f32_16x16x32_bf16 v[152:155], v[168:171], v[40:43], v[152:155]\n\t" \
  "buffer_load_dwordx4 v[40:43], v188, s[20:23], s64 offen offset:2048 sc1\n\t" \
  "s_waitcnt vmcnt(31)\n\t" \
  "v_mfma_f32_16x16x32_bf16 v[144:147], a[88:91], v[44:47], v[144:147]\n\t" \
  "ds_read_b128 v[168:171], v189 offset:13312\n\t" \
  "v_mfma_f32_16x16x32_bf16 v[148:151], a[92:95], v[44:47], v[148:151]\n\t" \
  "s_waitcnt lgkmcnt(2)\n\t" \
  "v_mfma_f32_16x16x32_bf16 v[152:155], v[172:175], v[44:47], v[152:155]\n\t" \
  "buffer_load_dwordx4 v[44:47], v188, s[20:23], s64 offen offset:3072 sc1\n\t" \
  "s_waitcnt vmcnt(31)\n\t" \
  "v_mfma_f32_16x16x32_bf16 v[144:147], a[96:99], v[48:51], v[144:147]\n\t" \
  "ds_read_b128 v[172:175], v189 offset:14336\n\t" \
  "v_mfma_f32_16x16x32_bf16 v[148:151], a[100:103], v[48:51], v[148:151]\n\t" \
  "s_waitcnt lgkmcnt(2)\n\t" \
  "v_mfma_f32_16x16x32_bf16 v[152:155], v[164:167], v[48:51], v[152:155]\n\t" \
  "buffer_load_dwordx4 v[48:51], v188, s[20:23], s65 offen offset:0 sc1\n\t" \
  "s_waitcnt vmcnt(31)\n\t" \
  "v_mfma_f32_16x16x32_bf16 v[144:147], a[104:107], v[52:55], v[144:147]\n\t" \
  "ds_read_b128 v[164:167], v189 offset:15360\n\t" \
  "v_mfma_f32_16x16x32_bf16 v[148:151], a[108:111], v[52:55], v[148:151]\n\t" \
  "s_waitcnt lgkmcnt(2)\n\t" \
  "v_mfma_f32_16x16x32_bf16 v[152:155], v[168:171], v[52:55], v[152:155]\n\t" \
  "buffer_load_dwordx4 v[52:55], v188, s[20:23], s65 offen offset:1024 sc1\n\t" \
  "s_waitcnt vmcnt(31)\n\t" \
  "v_mfma_f32_16x16x32_bf16 v[144:147], a[112:115], v[56:59], v[144:147]\n\t" \
  "ds_read_b128 v[168:171], v189 offset:16384\n\t" \
  "v_mfma_f32_16x16x32_bf16 v[148:151], a[116:119], v[56:59], v[148:151]\n\t" \
  "s_waitcnt lgkmcnt(2)\n\t" \
  "v_mfma_f32_16x16x32_bf16 v[152:155], v[172:175], v[56:59], v[152:155]\n\t" \
  "buffer_load_dwordx4 v[56:59], v188, s[20:23], s65 offen offset:2048 sc1\n\t" \
  "s_waitcnt vmcnt(31)\n\t" \
  "v_mfma_f32_16x16x32_bf16 v[144:147], a[120:123], v[60:63], v[144:147]\n\t" \
  "ds_read_b128 v[172:175], v189 offset:17408\n\t" \
  "v_mfma_f32_16x16x32_bf16 v[148:151], a[124:127], v[60:63], v[148:151]\n\t" \
  "s_waitcnt lgkmcnt(2)\n\t" \
  "v_mfma_f32_16x16x32_bf16 v[152:155], v[164:167], v[60:63], v[152:155]\n\t" \
  "buffer_load_dwordx4 v[60:63], v188, s[20:23], s65 offen offset:3072 sc1\n\t" \
  "s_waitcnt vmcnt(31)\n\t" \
  "v_mfma_f32_16x16x32_bf16 v[144:147], a[128:131], v[64:67], v[144:147]\n\t" \
  "ds_read_b128 v[164:167], v189 offset:18432\n\t" \
  "v_mfma_f32_16x16x32_bf16 v[148:151], a[132:135], v[64:67], v[148:151]\n\t" \
  "s_waitcnt lgkmcnt(2)\n\t" \
  "v_mfma_f32_16x16x32_bf16 v[152:155], v[168:171], v[64:67], v[152:155]\n\t" \
  "buffer_load_dwordx4 v[64:67], v188, s[20:23], s66 offen offset:0 sc1\n\t" \
  "s_waitcnt vmcnt(31)\n\t" \
  "v_mfma_f32_16x16x32_bf16 v[144:147], a[136:139], v[68:71], v[144:147]\n\t" \
  "ds_read_b128 v[168:171], v189 offset:19456\n\t" \
  "v_mfma_f32_16x16x32_bf16 v[148:151], a[140:143], v[68:71], v[148:151]\n\t" \
  "s_waitcnt lgkmcnt(2)\n\t" \
  "v_mfma_f32_16x16x32_bf16 v[152:155], v[172:175], v[68:71], v[152:155]\n\t" \
  "buffer_load_dwordx4 v[68:71], v188, s[20:23], s66 offen offset:1024 sc1\n\t" \
  "s_waitcnt vmcnt(31)\n\t" \
  "v_mfma_f32_16x16x32_bf16 v[144:147], a[144:147], v[72:75], v[144:147]\n\t" \
  "ds_read_b128 v[172:175], v189 offset:20480\n\t" \
  "v_mfma_f32_16x16x32_bf16 v[148:151], a[148:151], v[72:75], v[148:151]\n\t" \
  "s_waitcnt lgkmcnt(2)\n\t" \
  "v_mfma_f32_16x16x32_bf16 v[152:155], v[164:167], v[72:75], v[152:155]\n\t" \
  "buffer_load_dwordx4 v[72:75], v188, s[20:23], s66 offen offset:2048 sc1\n\t" \
  "s_waitcnt vmcnt(31)\n\t" \
  "v_mfma_f32_16x16x32_bf16 v[144:147], a[152:155], v[76:79], v[144:147]\n\t" \
  "ds_read_b128 v[164:167], v189 offset:21504\n\t" \
  "v_mfma_f32_16x16x32_bf16 v[148:151], a[156:159], v[76:79], v[148:151]\n\t" \
  "s_waitcnt lgkmcnt(2)\n\t" \
  "v_mfma_f32_16x16x32_bf16 v[152:155], v[168:171], v[76:79], v[152:155]\n\t" \
  "buffer_load_dwordx4 v[76:79], v188, s[20:23], s66 offen offset:3072 sc1\n\t" \
  "s_waitcnt vmcnt(31)\n\t" \
  "v_mfma_f32_16x16x32_bf16 v[144:147], a[160:163], v[80:83], v[144:147]\n\t" \
  "ds_read_b128 v[168:171], v189 offset:22528\n\t" \
  "v_mfma_f32_16x16x32_bf16 v[148:151], a[164:167], v[80:83], v[148:151]\n\t" \
  "s_waitcnt lgkmcnt(2)\n\t" \
  "v_mfma_f32_16x16x32_bf16 v[152:155], v[172:175], v[80:83], v[152:155]\n\t" \
  "buffer_load_dwordx4 v[80:83], v188, s[20:23], s67 offen offset:0 sc1\n\t" \
  "s_waitcnt vmcnt(31)\n\t" \
  "v_mfma_f32_16x16x32_bf16 v[144:147], a[168:171], v[84:87], v[144:147]\n\t" \
  "ds_read_b128 v[172:175], v189 offset:23552\n\t" \
  "v_mfma_f32_16x16x32_bf16 v[148:151], a[172:175], v[84:87], v[148:151]\n\t" \
  "s_waitcnt lgkmcnt(2)\n\t" \
  "v_mfma_f32_16x16x32_bf16 v[152:155], v[164:167], v[84:87], v[152:155]\n\t" \
  "buffer_load_dwordx4 v[84:87], v188, s[20:23], s67 offen offset:1024 sc1\n\t" \
  "s_waitcnt vmcnt(31)\n\t" \
  "v_mfma_f32_16x16x32_bf16 v[144:147], a[176:179], v[88:91], v[144:147]\n\t" \
  "ds_read_b128 v[164:167], v189 offset:24576\n\t" \
  "v_mfma_f32_16x16x32_bf16 v[148:151], a[180:183], v[88:91], v[148:151]\n\t" \
  "s_waitcnt lgkmcnt(2)\n\t" \
  "v_mfma_f32_16x16x32_bf16 v[152:155], v[168:171], v[88:91], v[152:155]\n\t" \
  "buffer_load_dwordx4 v[88:91], v188, s[20:23], s67 offen offset:2048 sc1\n\t" \
  "s_waitcnt vmcnt(31)\n\t" \
  "v_mfma_f32_16x16x32_bf16 v[144:147], a[184:187], v[92:95], v[144:147]\n\t" \
  "ds_read_b128 v[168:171], v189 offset:25600\n\t" \
  "v_mfma_f32_16x16x32_bf16 v[148:151], a[188:191], v[92:95], v[148:151]\n\t" \
  "s_waitcnt lgkmcnt(2)\n\t" \
  "v_mfma_f32_16x16x32_bf16 v[152:155], v[172:175], v[92:95], v[152:155]\n\t" \
  "buffer_load_dwordx4 v[92:95], v188, s[20:23], s67 offen offset:3072 sc1\n\t" \
  "s_waitcnt vmcnt(31)\n\t" \
  "v_mfma_f32_16x16x32_bf16 v[144:147], a[192:195], v[96:99], v[144:147]\n\t" \
  "ds_read_b128 v[172:175], v189 offset:26624\n\t" \
  "v_mfma_f32_16x16x32_bf16 v[148:151], a[196:199], v[96:99], v[148:151]\n\t" \
  "s_waitcnt lgkmcnt(2)\n\t" \
  "v_mfma_f32_16x16x32_bf16 v[152:155], v[164:167], v[96:99], v[152:155]\n\t" \
  "buffer_load_dwordx4 v[96:99], v188, s[20:23], s68 offen offset:0 sc1\n\t" \
  "s_waitcnt vmcnt(31)\n\t" \
  "v_mfma_f32_16x16x32_bf16 v[144:147], a[200:203], v[100:103], v[144:147]\n\t" \
  "ds_read_b128 v[164:167], v189 offset:27648\n\t" \
  "v_mfma_f32_16x16x32_bf16 v[148:151], a[204:207], v[100:103], v[148:151]\n\t" \
  "s_waitcnt lgkmcnt(2)\n\t" \
  "v_mfma_f32_16x16x32_bf16 v[152:155], v[168:171], v[100:103], v[152:155]\n\t" \
  "buffer_load_dwordx4 v[100:103], v188, s[20:23], s68 offen offset:1024 sc1\n\t" \
  "s_waitcnt vmcnt(31)\n\t" \
  "v_mfma_f32_16x16x32_bf16 v[144:147], a[208:211], v[104:107], v[144:147]\n\t" \
  "ds_read_b128 v[168:171], v189 offset:28672\n\t" \
  "v_mfma_f32_16x16x32_bf16 v[148:151], a[212:215], v[104:107], v[148:151]\n\t" \
  "s_waitcnt lgkmcnt(2)\n\t" \
  "v_mfma_f32_16x16x32_bf16 v[152:155], v[172:175], v[104:107], v[152:155]\n\t" \
  "buffer_load_dwordx4 v[104:107], v188, s[20:23], s68 offen offset:2048 sc1\n\t" \
  "s_waitcnt vmcnt(31)\n\t" \
  "v_mfma_f32_16x16x32_bf16 v[144:147], a[216:219], v[108:111], v[144:147]\n\t" \
  "ds_read_b128 v[172:175], v189 offset:29696\n\t" \
  "v_mfma_f32_16x16x32_bf16 v[148:151], a[220:223], v[108:111], v[148:151]\n\t" \
  "s_waitcnt lgkmcnt(2)\n\t" \
  "v_mfma_f32_16x16x32_bf16 v[152:155], v[164:167], v[108:111], v[152:155]\n\t" \
  "buffer_load_dwordx4 v[108:111], v188, s[20:23], s68 offen offset:3072 sc1\n\t" \
  "s_waitcnt vmcnt(31)\n\t" \
  "v_mfma_f32_16x16x32_bf16 v[144:147], a[224:227], v[112:115], v[144:147]\n\t" \
  "ds_read_b128 v[164:167], v189 offset:30720\n\t" \
  "v_mfma_f32_16x16x32_bf16 v[148:151], a[228:231], v[112:115], v[148:151]\n\t" \
  "s_waitcnt lgkmcnt(2)\n\t" \
  "v_mfma_f32_16x16x32_bf16 v[152:155], v[168:171], v[112:115], v[152:155]\n\t" \
  "buffer_load_dwordx4 v[112:115], v188, s[20:23], s69 offen offset:0 sc1\n\t" \
  "s_waitcnt vmcnt(31)\n\t" \
  "v_mfma_f32_16x16x32_bf16 v[144:147], a[232:235], v[116:119], v[144:147]\n\t" \
  "ds_read_b128 v[168:171], v189 offset:31744\n\t" \
  "v_mfma_f32_16x16x32_bf16 v[148:151], a[236:239], v[116:119], v[148:151]\n\t" \
  "s_waitcnt lgkmcnt(2)\n\t" \
  "v_mfma_f32_16x16x32_bf16 v[152:155], v[172:175], v[116:119], v[152:155]\n\t" \
  "buffer_load_dwordx4 v[116:119], v188, s[20:23], s69 offen offset:1024 sc1\n\t" \
  "s_waitcnt vmcnt(31)\n\t" \
  "v_mfma_f32_16x16x32_bf16 v[144:147], a[240:243], v[120:123], v[144:147]\n\t" \
  "v_mfma_f32_16x16x32_bf16 v[148:151], a[244:247], v[120:123], v[148:151]\n\t" \
  "s_waitcnt lgkmcnt(1)\n\t" \
  "v_mfma_f32_16x16x32_bf16 v[152:155], v[164:167], v[120:123], v[152:155]\n\t" \
  "buffer_load_dwordx4 v[120:123], v188, s[20:23], s69 offen offset:2048 sc1\n\t" \
  "s_waitcnt vmcnt(31)\n\t" \
  "v_mfma_f32_16x16x32_bf16 v[144:147], a[248:251], v[124:127], v[144:147]\n\t" \
  "v_mfma_f32_16x16x32_bf16 v[148:151], a[252:255], v[124:127], v[148:151]\n\t" \
  "s_waitcnt lgkmcnt(0)\n\t" \
  "v_mfma_f32_16x16x32_bf16 v[152:155], v[168:171], v[124:127], v[152:155]\n\t" \
  "buffer_load_dwordx4 v[124:127], v188, s[20:23], s69 offen offset:3072 sc1\n\t" \
  "s_waitcnt vmcnt(32)\n\t" \
  "s_branch Lkd_%=\n\t" \
  "Ltl_%=:\n\t" \
  "s_mov_b32 s71, 0\n\t" \
  "Ltm_%=:\n\t" \
  "v_mov_b32 v200, 0\n\t" \
  "s_waitcnt vmcnt(31)\n\t" \
  "v_mfma_f32_16x16x32_bf16 v[144:147], a[0:3], v[0:3], 0\n\t" \
  "ds_read_b128 v[172:175], v189 offset:2048\n\t" \
  "v_mfma_f32_16x16x32_bf16 v[148:151], a[4:7], v[0:3], 0\n\t" \
  "s_waitcnt lgkmcnt(2)\n\t" \
  "v_mfma_f32_16x16x32_bf16 v[152:155], v[164:167], v[0:3], 0\n\t" \
  "v_max3_u32 v200, v200, v0, v1\n\t" \
  "v_max3_u32 v200, v200, v2, v3\n\t" \
  "s_waitcnt vmcnt(30)\n\t" \
  "v_mfma_f32_16x16x32_bf16 v[144:147], a[8:11], v[4:7], v[144:147]\n\t" \
  "ds_read_b128 v[164:167], v189 offset:3072\n\t" \
  "v_mfma_f32_16x16x32_bf16 v[148:151], a[12:15], v[4:7], v[148:151]\n\t" \
  "s_waitcnt lgkmcnt(2)\n\t" \
  "v_mfma_f32_16x16x32_bf16 v[152:155], v[168:171], v[4:7], v[152:155]\n\t" \
  "v_max3_u32 v200, v200, v4, v5\n\t" \
  "v_max3_u32 v200, v200, v6, v7\n\t" \
  "s_waitcnt vmcnt(29)\n\t" \
  "v_mfma_f32_16x16x32_bf16 v[144:147], a[16:19], v[8:11], v[144:147]\n\t" \
  "ds_read_b128 v[168:171], v189 offset:4096\n\t" \
  "v_mfma_f32_16x16x32_bf16 v[148:151], a[20:23], v[8:11], v[148:151]\n\t" \
  "s_waitcnt lgkmcnt(2)\n\t" \
  "v_mfma_f32_16x16x32_bf16 v[152:155], v[172:175], v[8:11], v[152:155]\n\t" \
  "v_max3_u32 v200, v200, v8, v9\n\t" \
  "v_max3_u32 v200, v200, v10, v11\n\t" \
  "s_waitcnt vmcnt(28)\n\t" \
  "v_mfma_f32_16x16x32_bf16 v[144:147], a[24:27], v[12:15], v[144:147]\n\t" \
  "ds_read_b128 v[172:175], v189 offset:5120\n\t" \
  "v_mfma_f32_16x16x32_bf16 v[148:151], a[28:31], v[12:15], v[148:151]\n\t" \
  "s_waitcnt lgkmcnt(2)\n\t" \
  "v_mfma_f32_16x16x32_bf16 v[152:155], v[164:167], v[12:15], v[152:155]\n\t" \
  "v_max3_u32 v200, v200, v12, v13\n\t" \
  "v_max3_u32 v200, v200, v14, v15\n\t" \
  "s_waitcnt vmcnt(27)\n\t" \
  "v_mfma_f32_16x16x32_bf16 v[144:147], a[32:35], v[16:19], v[144:147]\n\t" \
  "ds_read_b128 v[164:167], v189 offset:6144\n\t" \
  "v_mfma_f32_16x16x32_bf16 v[148:151], a[36:39], v[16:19], v[148:151]\n\t" \
  "s_waitcnt lgkmcnt(2)\n\t" \
  "v_mfma_f32_16x16x32_bf16 v[152:155], v[168:171], v[16:19], v[152:155]\n\t" \
  "v_max3_u32 v200, v200, v16, v17\n\t" \
  "v_max3_u32 v200, v200, v18, v19\n\t" \
  "s_waitcnt vmcnt(26)\n\t" \
  "v_mfma_f32_16x16x32_bf16 v[144:147], a[40:43], v[20:23], v[144:147]\n\t" \
  "ds_read_b128 v[168:171], v189 offset:7168\n\t" \
  "v_mfma_f32_16x16x32_bf16 v[148:151], a[44:47], v[20:23], v[148:151]\n\t" \
  "s_waitcnt lgkmcnt(2)\n\t" \
  "v_mfma_f32_16x16x32_bf16 v[152:155], v[172:175], v[20:23], v[152:155]\n\t" \
  "v_max3_u32 v200, v200, v20, v21\n\t" \
  "v_max3_u32 v200, v200, v22, v23\n\t" \
  "s_waitcnt vmcnt(25)\n\t" \
  "v_mfma_f32_16x16x32_bf16 v[144:147], a[48:51], v[24:27], v[144:147]\n\t" \
  "ds_read_b128 v[172:175], v189 offset:8192\n\t" \
  "v_mfma_f32_16x16x32_bf16 v[148:151], a[52:55], v[24:27], v[148:151]\n\t" \
  "s_waitcnt lgkmcnt(2)\n\t" \
  "v_mfma_f32_16x16x32_bf16 v[152:155], v[164:167], v[24:27], v[152:155]\n\t" \
  "v_max3_u32 v200, v200, v24, v25\n\t" \
  "v_max3_u32 v200, v200, v26, v27\n\t" \
  "s_waitcnt vmcnt(24)\n\t" \
  "v_mfma_f32_16x16x32_bf16 v[144:147], a[56:59], v[28:31], v[144:147]\n\t" \
  "ds_read_b128 v[164:167], v189 offset:9216\n\t" \
  "v_mfma_f32_16x16x32_bf16 v[148:151], a[60:63], v[28:31], v[148:151]\n\t" \
  "s_waitcnt lgkmcnt(2)\n\t" \
  "v_mfma_f32_16x16x32_bf16 v[152:155], v[168:171], v[28:31], v[152:155]\n\t" \
  "v_max3_u32 v200, v200, v28, v29\n\t" \
  "v_max3_u32 v200, v200, v30, v31\n\t" \
  "s_waitcnt vmcnt(23)\n\t" \
  "v_mfma_f32_16x16x32_bf16 v[144:147], a[64:67], v[32:35], v[144:147]\n\t" \
  "ds_read_b128 v[168:171], v189 offset:10240\n\t" \
  "v_mfma_f32_16x16x32_bf16 v[148:151], a[68:71], v[32:35], v[148:151]\n\t" \
  "s_waitcnt lgkmcnt(2)\n\t" \
  "v_mfma_f32_16x16x32_bf16 v[152:155], v[172:175], v[32:35], v[152:155]\n\t" \
  "v_max3_u32 v200, v200, v32, v33\n\t" \
  "v_max3_u32 v200, v200, v34, v35\n\t" \
  "s_waitcnt vmcnt(22)\n\t" \
  "v_mfma_f32_16x16x32_bf16 v[144:147], a[72:75], v[36:39], v[144:147]\n\t" \
  "ds_read_b128 v[172:175], v189 offset:11264\n\t" \
  "v_mfma_f32_16x16x32_bf16 v[148:151], a[76:79], v[36:39], v[148:151]\n\t" \
  "s_waitcnt lgkmcnt(2)\n\t" \
  "v_mfma_f32_16x16x32_bf16 v[152:155], v[164:167], v[36:39], v[152:155]\n\t" \
  "v_max3_u32 v200, v200, v36, v37\n\t" \
  "v_max3_u32 v200, v200, v38, v39\n\t" \
  "s_waitcnt vmcnt(21)\n\t" \
  "v_mfma_f32_16x16x32_bf16 v[144:147], a[80:83], v[40:43], v[144:147]\n\t" \
  "ds_read_b128 v[164:167], v189 offset:12288\n\t" \
  "v_mfma_f32_16x16x32_bf16 v[148:151], a[84:87], v[40:43], v[148:151]\n\t" \
  "s_waitcnt lgkmcnt(2)\n\t" \
  "v_mfma_f32_16x16x32_bf16 v[152:155], v[168:171], v[40:43], v[152:155]\n\t" \
  "v_max3_u32 v200, v200, v40, v41\n\t" \
  "v_max3_u32 v200, v200, v42, v43\n\t" \
  "s_waitcnt vmcnt(20)\n\t" \
  "v_mfma_f32_16x16x32_bf16 v[144:147], a[88:91], v[44:47], v[144:147]\n\t" \
  "ds_read_b128 v[168:171], v189 offset:13312\n\t" \
  "v_mfma_f32_16x16x32_bf16 v[148:151], a[92:95], v[44:47], v[148:151]\n\t" \
  "s_waitcnt lgkmcnt(2)\n\t" \
  "v_mfma_f32_16x16x32_bf16 v[152:155], v[172:175], v[44:47], v[152:155]\n\t" \
  "v_max3_u32 v200, v200, v44, v45\n\t" \
  "v_max3_u32 v200, v200, v46, v47\n\t" \
  "s_waitcnt vmcnt(19)\n\t" \
  "v_mfma_f32_16x16x32_bf16 v[144:147], a[96:99], v[48:51], v[144:147]\n\t" \
  "ds_read_b128 v[172:175], v189 offset:14336\n\t" \
  "v_mfma_f32_16x16x32_bf16 v[148:151], a[100:103], v[48:51], v[148:151]\n\t" \
  "s_waitcnt lgkmcnt(2)\n\t" \
  "v_mfma_f32_16x16x32_bf16 v[152:155], v[164:167], v[48:51], v[152:155]\n\t" \
  "v_max3_u32 v200, v200, v48, v49\n\t" \
  "v_max3_u32 v200, v200, v50, v51\n\t" \
  "s_waitcnt vmcnt(18)\n\t" \
  "v_mfma_f32_16x16x32_bf16 v[144:147], a[104:107], v[52:55], v[144:147]\n\t" \
  "ds_read_b128 v[164:167], v189 offset:15360\n\t" \
  "v_mfma_f32_16x16x32_bf16 v[148:151], a[108:111], v[52:55], v[148:151]\n\t" \
  "s_waitcnt lgkmcnt(2)\n\t" \
  "v_mfma_f32_16x16x32_bf16 v[152:155], v[168:171], v[52:55], v[152:155]\n\t" \
  "v_max3_u32 v200, v200, v52, v53\n\t" \
  "v_max3_u32 v200, v200, v54, v55\n\t" \
  "s_waitcnt vmcnt(17)\n\t" \
  "v_mfma_f32_16x16x32_bf16 v[144:147], a[112:115], v[56:59], v[144:147]\n\t" \
  "ds_read_b128 v[168:171], v189 offset:16384\n\t" \
  "v_mfma_f32_16x16x32_bf16 v[148:151], a[116:119], v[56:59], v[148:151]\n\t" \
  "s_waitcnt lgkmcnt(2)\n\t" \
  "v_mfma_f32_16x16x32_bf16 v[152:155], v[172:175], v[56:59], v[152:155]\n\t" \
  "v_max3_u32 v200, v200, v56, v57\n\t" \
  "v_max3_u32 v200, v200, v58, v59\n\t" \
  "s_waitcnt vmcnt(16)\n\t" \
  "v_mfma_f32_16x16x32_bf16 v[144:147], a[120:123], v[60:63], v[144:147]\n\t" \
  "ds_read_b128 v[172:175], v189 offset:17408\n\t" \
  "v_mfma_f32_16x16x32_bf16 v[148:151], a[124:127], v[60:63], v[148:151]\n\t" \
  "s_waitcnt lgkmcnt(2)\n\t" \
  "v_mfma_f32_16x16x32_bf16 v[152:155], v[164:167], v[60:63], v[152:155]\n\t" \
  "v_max3_u32 v200, v200, v60, v61\n\t" \
  "v_max3_u32 v200, v200, v62, v63\n\t" \
  "s_waitcnt vmcnt(15)\n\t" \
  "v_mfma_f32_16x16x32_bf16 v[144:147], a[128:131], v[64:67], v[144:147]\n\t" \
  "ds_read_b128 v[164:167], v189 offset:18432\n\t" \
  "v_mfma_f32_16x16x32_bf16 v[148:151], a[132:135], v[64:67], v[148:151]\n\t" \
  "s_waitcnt lgkmcnt(2)\n\t" \
  "v_mfma_f32_16x16x32_bf16 v[152:155], v[168:171], v[64:67], v[152:155]\n\t" \
  "v_max3_u32 v200, v200, v64, v65\n\t" \
  "v_max3_u32 v200, v200, v66, v67\n\t" \
  "s_waitcnt vmcnt(14)\n\t" \
  "v_mfma_f32_16x16x32_bf16 v[144:147], a[136:139], v[68:71], v[144:147]\n\t" \
  "ds_read_b128 v[168:171], v189 offset:19456\n\t" \
  "v_mfma_f32_16x16x32_bf16 v[148:151], a[140:143], v[68:71], v[148:151]\n\t" \
  "s_waitcnt lgkmcnt(2)\n\t" \
  "v_mfma_f32_16x16x32_bf16 v[152:155], v[172:175], v[68:71], v[152:155]\n\t" \
  "v_max3_u32 v200, v200, v68, v69\n\t" \
  "v_max3_u32 v200, v200, v70, v71\n\t" \
  "s_waitcnt vmcnt(13)\n\t" \
  "v_mfma_f32_16x16x32_bf16 v[144:147], a[144:147], v[72:75], v[144:147]\n\t" \
  "ds_read_b128 v[172:175], v189 offset:20480\n\t" \
  "v_mfma_f32_16x16x32_bf16 v[148:151], a[148:151], v[72:75], v[148:151]\n\t" \
  "s_waitcnt lgkmcnt(2)\n\t" \
  "v_mfma_f32_16x16x32_bf16 v[152:155], v[164:167], v[72:75], v[152:155]\n\t" \
  "v_max3_u32 v200, v200, v72, v73\n\t" \
  "v_max3_u32 v200, v200, v74, v75\n\t" \
  "s_waitcnt vmcnt(12)\n\t" \
  "v_mfma_f32_16x16x32_bf16 v[144:147], a[152:155], v[76:79], v[144:147]\n\t" \
  "ds_read_b128 v[164:167], v189 offset:21504\n\t" \
  "v_mfma_f32_16x16x32_bf16 v[148:151], a[156:159], v[76:79], v[148:151]\n\t" \
  "s_waitcnt lgkmcnt(2)\n\t" \
  "v_mfma_f32_16x16x32_bf16 v[152:155], v[168:171], v[76:79], v[152:155]\n\t" \
  "v_max3_u32 v200, v200, v76, v77\n\t" \
  "v_max3_u32 v200, v200, v78, v79\n\t" \
  "s_waitcnt vmcnt(11)\n\t" \
  "v_mfma_f32_16x16x32_bf16 v[144:147], a[160:163], v[80:83], v[144:147]\n\t" \
  "ds_read_b128 v[168:171], v189 offset:22528\n\t" \
  "v_mfma_f32_16x16x32_bf16 v[148:151], a[164:167], v[80:83], v[148:151]\n\t" \
  "s_waitcnt lgkmcnt(2)\n\t" \
  "v_mfma_f32_16x16x32_bf16 v[152:155], v[172:175], v[80:83], v[152:155]\n\t" \
  "v_max3_u32 v200, v200, v80, v81\n\t" \
  "v_max3_u32 v200, v200, v82, v83\n\t" \
  "s_waitcnt vmcnt(10)\n\t" \
  "v_mfma_f32_16x16x32_bf16 v[144:147], a[168:171], v[84:87], v[144:147]\n\t" \
  "ds_read_b128 v[172:175], v189 offset:23552\n\t" \
  "v_mfma_f32_16x16x32_bf16 v[148:151], a[172:175], v[84:87], v[148:151]\n\t" \
  "s_waitcnt lgkmcnt(2)\n\t" \
  "v_mfma_f32_16x16x32_bf16 v[152:155], v[164:167], v[84:87], v[152:155]\n\t" \
  "v_max3_u32 v200, v200, v84, v85\n\t" \
  "v_max3_u32 v200, v200, v86, v87\n\t" \
  "s_waitcnt vmcnt(9)\n\t" \
  "v_mfma_f32_16x16x32_bf16 v[144:147], a[176:179], v[88:91], v[144:147]\n\t" \
  "ds_read_b128 v[164:167], v189 offset:24576\n\t" \
  "v_mfma_f32_16x16x32_bf16 v[148:151], a[180:183], v[88:91], v[148:151]\n\t" \
  "s_waitcnt lgkmcnt(2)\n\t" \
  "v_mfma_f32_16x16x32_bf16 v[152:155], v[168:171], v[88:91], v[152:155]\n\t" \
  "v_max3_u32 v200, v200, v88, v89\n\t" \
  "v_max3_u32 v200, v200, v90, v91\n\t" \
  "s_waitcnt vmcnt(8)\n\t" \
  "v_mfma_f32_16x16x32_bf16 v[144:147], a[184:187], v[92:95], v[144:147]\n\t" \
  "ds_read_b128 v[168:171], v189 offset:25600\n\t" \
  "v_mfma_f32_16x16x32_bf16 v[148:151], a[188:191], v[92:95], v[148:151]\n\t" \
  "s_waitcnt lgkmcnt(2)\n\t" \
  "v_mfma_f32_16x16x32_bf16 v[152:155], v[172:175], v[92:95], v[152:155]\n\t" \
  "v_max3_u32 v200, v200, v92, v93\n\t" \
  "v_max3_u32 v200, v200, v94, v95\n\t" \
  "s_waitcnt vmcnt(7)\n\t" \
  "v_mfma_f32_16x16x32_bf16 v[144:147], a[192:195], v[96:99], v[144:147]\n\t" \
  "ds_read_b128 v[172:175], v189 offset:26624\n\t" \
  "v_mfma_f32_16x16x32_bf16 v[148:151], a[196:199], v[96:99], v[148:151]\n\t" \
  "s_waitcnt lgkmcnt(2)\n\t" \
  "v_mfma_f32_16x16x32_bf16 v[152:155], v[164:167], v[96:99], v[152:155]\n\t" \
  "v_max3_u32 v200, v200, v96, v97\n\t" \
  "v_max3_u32 v200, v200, v98, v99\n\t" \
  "s_waitcnt vmcnt(6)\n\t" \
  "v_mfma_f32_16x16x32_bf16 v[144:147], a[200:203], v[100:103], v[144:147]\n\t" \
  "ds_read_b128 v[164:167], v189 offset:27648\n\t" \
  "v_mfma_f32_16x16x32_bf16 v[148:151], a[204:207], v[100:103], v[148:151]\n\t" \
  "s_waitcnt lgkmcnt(2)\n\t" \
  "v_mfma_f32_16x16x32_bf16 v[152:155], v[168:171], v[100:103], v[152:155]\n\t" \
  "v_max3_u32 v200, v200, v100, v101\n\t" \
  "v_max3_u32 v200, v200, v102, v103\n\t" \
  "s_waitcnt vmcnt(5)\n\t" \
  "v_mfma_f32_16x16x32_bf16 v[144:147], a[208:211], v[104:107], v[144:147]\n\t" \
  "ds_read_b128 v[168:171], v189 offset:28672\n\t" \
  "v_mfma_f32_16x16x32_bf16 v[148:151], a[212:215], v[104:107], v[148:151]\n\t" \
  "s_waitcnt lgkmcnt(2)\n\t" \
  "v_mfma_f32_16x16x32_bf16 v[152:155], v[172:175], v[104:107], v[152:155]\n\t" \
  "v_max3_u32 v200, v200, v104, v105\n\t" \
  "v_max3_u32 v200, v200, v106, v107\n\t" \
  "s_waitcnt vmcnt(4)\n\t" \
  "v_mfma_f32_16x16x32_bf16 v[144:147], a[216:219], v[108:111], v[144:147]\n\t" \
  "ds_read_b128 v[172:175], v189 offset:29696\n\t" \
  "v_mfma_f32_16x16x32_bf16 v[148:151], a[220:223], v[108:111], v[148:151]\n\t" \
  "s_waitcnt lgkmcnt(2)\n\t" \
  "v_mfma_f32_16x16x32_bf16 v[152:155], v[164:167], v[108:111], v[152:155]\n\t" \
  "v_max3_u32 v200, v200, v108, v109\n\t" \
  "v_max3_u32 v200, v200, v110, v111\n\t" \
  "s_waitcnt vmcnt(3)\n\t" \
  "v_mfma_f32_16x16x32_bf16 v[144:147], a[224:227], v[112:115], v[144:147]\n\t" \
  "ds_read_b128 v[164:167], v189 offset:30720\n\t" \
  "v_mfma_f32_16x16x32_bf16 v[148:151], a[228:231], v[112:115], v[148:151]\n\t" \
  "s_waitcnt lgkmcnt(2)\n\t" \
  "v_mfma_f32_16x16x32_bf16 v[152:155], v[168:171], v[112:115], v[152:155]\n\t" \
  "v_max3_u32 v200, v200, v112, v113\n\t" \
  "v_max3_u32 v200, v200, v114, v115\n\t" \
  "s_waitcnt vmcnt(2)\n\t" \
  "v_mfma_f32_16x16x32_bf16 v[144:147], a[232:235], v[116:119], v[144:147]\n\t" \
  "ds_read_b128 v[168:171], v189 offset:31744\n\t" \
  "v_mfma_f32_16x16x32_bf16 v[148:151], a[236:239], v[116:119], v[148:151]\n\t" \
  "s_waitcnt lgkmcnt(2)\n\t" \
  "v_mfma_f32_16x16x32_bf16 v[152:155], v[172:175], v[116:119], v[152:155]\n\t" \
  "v_max3_u32 v200, v200, v116, v117\n\t" \
  "v_max3_u32 v200, v200, v118, v119\n\t" \
  "s_waitcnt vmcnt(1)\n\t" \
  "v_mfma_f32_16x16x32_bf16 v[144:147], a[240:243], v[120:123], v[144:147]\n\t" \
  "v_mfma_f32_16x16x32_bf16 v[148:151], a[244:247], v[120:123], v[148:151]\n\t" \
  "s_waitcnt lgkmcnt(1)\n\t" \
  "v_mfma_f32_16x16x32_bf16 v[152:155], v[164:167], v[120:123], v[152:155]\n\t" \
  "v_max3_u32 v200, v200, v120, v121\n\t" \
  "v_max3_u32 v200, v200, v122, v123\n\t" \
  "s_waitcnt vmcnt(0)\n\t" \
  "v_mfma_f32_16x16x32_bf16 v[144:147], a[248:251], v[124:127], v[144:147]\n\t" \
  "v_mfma_f32_16x16x32_bf16 v[148:151], a[252:255], v[124:127], v[148:151]\n\t" \
  "s_waitcnt lgkmcnt(0)\n\t" \
  "v_mfma_f32_16x16x32_bf16 v[152:155], v[168:171], v[124:127], v[152:155]\n\t" \
  "v_max3_u32 v200, v200, v124, v125\n\t" \
  "v_max3_u32 v200, v200, v126, v127\n\t" \
  "s_waitcnt vmcnt(0)\n\t" \
  "v_cmp_eq_u32 vcc, -1, v200\n\t" \
  "s_nop 0\n\t" \
  "s_cbranch_vccz Ltn_%=\n\t" \
  "s_add_u32 s71, s71, 1\n\t" \
  "s_cmp_gt_u32 s71, 4096\n\t" \
  "s_cbranch_scc1 Ltn_%=\n\t" \
  "buffer_load_dwordx4 v[0:3], v188, s[20:23], s62 offen offset:0 sc1\n\t" \
  "buffer_load_dwordx4 v[4:7], v188, s[20:23], s62 offen offset:1024 sc1\n\t" \
  "buffer_load_dwordx4 v[8:11], v188, s[20:23], s62 offen offset:2048 sc1\n\t" \
  "buffer_load_dwordx4 v[12:15], v188, s[20:23], s62 offen offset:3072 sc1\n\t" \
  "buffer_load_dwordx4 v[16:19], v188, s[20:23], s63 offen offset:0 sc1\n\t" \
  "buffer_load_dwordx4 v[20:23], v188, s[20:23], s63 offen offset:1024 sc1\n\t" \
  "buffer_load_dwordx4 v[24:27], v188, s[20:23], s63 offen offset:2048 sc1\n\t" \
  "buffer_load_dwordx4 v[28:31], v188, s[20:23], s63 offen offset:3072 sc1\n\t" \
  "buffer_load_dwordx4 v[32:35], v188, s[20:23], s64 offen offset:0 sc1\n\t" \
  "buffer_load_dwordx4 v[36:39], v188, s[20:23], s64 offen offset:1024 sc1\n\t" \
  "buffer_load_dwordx4 v[40:43], v188, s[20:23], s64 offen offset:2048 sc1\n\t" \
  "buffer_load_dwordx4 v[44:47], v188, s[20:23], s64 offen offset:3072 sc1\n\t" \
  "buffer_load_dwordx4 v[48:51], v188, s[20:23], s65 offen offset:0 sc1\n\t" \
  "buffer_load_dwordx4 v[52:55], v188, s[20:23], s65 offen offset:1024 sc1\n\t" \
  "buffer_load_dwordx4 v[56:59], v188, s[20:23], s65 offen offset:2048 sc1\n\t" \
  "buffer_load_dwordx4 v[60:63], v188, s[20:23], s65 offen offset:3072 sc1\n\t" \
  "buffer_load_dwordx4 v[64:67], v188, s[20:23], s66 offen offset:0 sc1\n\t" \
  "buffer_load_dwordx4 v[68:71], v188, s[20:23], s66 offen offset:1024 sc1\n\t" \
  "buffer_load_dwordx4 v[72:75], v188, s[20:23], s66 offen offset:2048 sc1\n\t" \
  "buffer_load_dwordx4 v[76:79], v188, s[20:23], s66 offen offset:3072 sc1\n\t" \
  "buffer_load_dwordx4 v[80:83], v188, s[20:23], s67 offen offset:0 sc1\n\t" \
  "buffer_load_dwordx4 v[84:87], v188, s[20:23], s67 offen offset:1024 sc1\n\t" \
  "buffer_load_dwordx4 v[88:91], v188, s[20:23], s67 offen offset:2048 sc1\n\t" \
  "buffer_load_dwordx4 v[92:95], v188, s[20:23], s67 offen offset:3072 sc1\n\t" \
  "buffer_load_dwordx4 v[96:99], v188, s[20:23], s68 offen offset:0 sc1\n\t" \
  "buffer_load_dwordx4 v[100:103], v188, s[20:23], s68 offen offset:1024 sc1\n\t" \
  "buffer_load_dwordx4 v[104:107], v188, s[20:23], s68 offen offset:2048 sc1\n\t" \
  "buffer_load_dwordx4 v[108:111], v188, s[20:23], s68 offen offset:3072 sc1\n\t" \
  "buffer_load_dwordx4 v[112:115], v188, s[20:23], s69 offen offset:0 sc1\n\t" \
  "buffer_load_dwordx4 v[116:119], v188, s[20:23], s69 offen offset:1024 sc1\n\t" \
  "buffer_load_dwordx4 v[120:123], v188, s[20:23], s69 offen offset:2048 sc1\n\t" \
  "buffer_load_dwordx4 v[124:127], v188, s[20:23], s69 offen offset:3072 sc1\n\t" \
  "ds_read_b128 v[164:167], v189 offset:0\n\t" \
  "ds_read_b128 v[168:171], v189 offset:1024\n\t" \
  "s_branch Ltm_%=\n\t" \
  "Ltn_%=:\n\t" \
  "s_mul_i32 s62, s48, 131072\n\t" \
  "s_add_u32 s62, s62, s54\n\t" \
  "s_add_u32 s63, s62, 4096\n\t" \
  "s_add_u32 s64, s62, 8192\n\t" \
  "s_add_u32 s65, s62, 12288\n\t" \
  "s_add_u32 s66, s62, 16384\n\t" \
  "s_add_u32 s67, s62, 20480\n\t" \
  "s_add_u32 s68, s62, 24576\n\t" \
  "s_add_u32 s69, s62, 28672\n\t" \
  "s_branch Lkd_%=\n\t" \
  "Lnp_%=:\n\t" \
  "s_waitcnt vmcnt(31)\n\t" \
  "v_mfma_f32_16x16x32_bf16 v[144:147], a[0:3], v[0:3], 0\n\t" \
  "ds_read_b128 v[172:175], v189 offset:2048\n\t" \
  "v_mfma_f32_16x16x32_bf16 v[148:151], a[4:7], v[0:3], 0\n\t" \
  "s_waitcnt lgkmcnt(2)\n\t" \
  "v_mfma_f32_16x16x32_bf16 v[152:155], v[164:167], v[0:3], 0\n\t" \
  "s_waitcnt vmcnt(30)\n\t" \
  "v_mfma_f32_16x16x32_bf16 v[144:147], a[8:11], v[4:7], v[144:147]\n\t" \
  "ds_read_b128 v[164:167], v189 offset:3072\n\t" \
  "v_mfma_f32_16x16x32_bf16 v[148:151], a[12:15], v[4:7], v[148:151]\n\t" \
  "s_waitcnt lgkmcnt(2)\n\t" \
  "v_mfma_f32_16x16x32_bf16 v[152:155], v[168:171], v[4:7], v[152:155]\n\t" \
  "s_waitcnt vmcnt(29)\n\t" \
  "v_mfma_f32_16x16x32_bf16 v[144:147], a[16:19], v[8:11], v[144:147]\n\t" \
  "ds_read_b128 v[168:171], v189 offset:4096\n\t" \
  "v_mfma_f32_16x16x32_bf16 v[148:151], a[20:23], v[8:11], v[148:151]\n\t" \
  "s_waitcnt lgkmcnt(2)\n\t" \
  "v_mfma_f32_16x16x32_bf16 v[152:155], v[172:175], v[8:11], v[152:155]\n\t" \
  "s_waitcnt vmcnt(28)\n\t" \
  "v_mfma_f32_16x16x32_bf16 v[144:147], a[24:27], v[12:15], v[144:147]\n\t" \
  "ds_read_b128 v[172:175], v189 offset:5120\n\t" \
  "v_mfma_f32_16x16x32_bf16 v[148:151], a[28:31], v[12:15], v[148:151]\n\t" \
  "s_waitcnt lgkmcnt(2)\n\t" \
  "v_mfma_f32_16x16x32_bf16 v[152:155], v[164:167], v[12:15], v[152:155]\n\t" \
  "s_waitcnt vmcnt(27)\n\t" \
  "v_mfma_f32_16x16x32_bf16 v[144:147], a[32:35], v[16:19], v[144:147]\n\t" \
  "ds_read_b128 v[164:167], v189 offset:6144\n\t" \
  "v_mfma_f32_16x16x32_bf16 v[148:151], a[36:39], v[16:19], v[148:151]\n\t" \
  "s_waitcnt lgkmcnt(2)\n\t" \
  "v_mfma_f32_16x16x32_bf16 v[152:155], v[168:171], v[16:19], v[152:155]\n\t" \
  "s_waitcnt vmcnt(26)\n\t" \
  "v_mfma_f32_16x16x32_bf16 v[144:147], a[40:43], v[20:23], v[144:147]\n\t" \
  "ds_read_b128 v[168:171], v189 offset:7168\n\t" \
  "v_mfma_f32_16x16x32_bf16 v[148:151], a[44:47], v[20:23], v[148:151]\n\t" \
  "s_waitcnt lgkmcnt(2)\n\t" \
  "v_mfma_f32_16x16x32_bf16 v[152:155], v[172:175], v[20:23], v[152:155]\n\t" \
  "s_waitcnt vmcnt(25)\n\t" \
  "v_mfma_f32_16x16x32_bf16 v[144:147], a[48:51], v[24:27], v[144:147]\n\t" \
  "ds_read_b128 v[172:175], v189 offset:8192\n\t" \
  "v_mfma_f32_16x16x32_bf16 v[148:151], a[52:55], v[24:27], v[148:151]\n\t" \
  "s_waitcnt lgkmcnt(2)\n\t" \
  "v_mfma_f32_16x16x32_bf16 v[152:155], v[164:167], v[24:27], v[152:155]\n\t" \
  "s_waitcnt vmcnt(24)\n\t" \
  "v_mfma_f32_16x16x32_bf16 v[144:147], a[56:59], v[28:31], v[144:147]\n\t" \
  "ds_read_b128 v[164:167], v189 offset:9216\n\t" \
  "v_mfma_f32_16x16x32_bf16 v[148:151], a[60:63], v[28:31], v[148:151]\n\t" \
  "s_waitcnt lgkmcnt(2)\n\t" \
  "v_mfma_f32_16x16x32_bf16 v[152:155], v[168:171], v[28:31], v[152:155]\n\t" \
  "s_waitcnt vmcnt(23)\n\t" \
  "v_mfma_f32_16x16x32_bf16 v[144:147], a[64:67], v[32:35], v[144:147]\n\t" \
  "ds_read_b128 v[168:171], v189 offset:10240\n\t" \
  "v_mfma_f32_16x16x32_bf16 v[148:151], a[68:71], v[32:35], v[148:151]\n\t" \
  "s_waitcnt lgkmcnt(2)\n\t" \
  "v_mfma_f32_16x16x32_bf16 v[152:155], v[172:175], v[32:35], v[152:155]\n\t" \
  "s_waitcnt vmcnt(22)\n\t" \
  "v_mfma_f32_16x16x32_bf16 v[144:147], a[72:75], v[36:39], v[144:147]\n\t" \
  "ds_read_b128 v[172:175], v189 offset:11264\n\t" \
  "v_mfma_f32_16x16x32_bf16 v[148:151], a[76:79], v[36:39], v[148:151]\n\t" \
  "s_waitcnt lgkmcnt(2)\n\t" \
  "v_mfma_f32_16x16x32_bf16 v[152:155], v[164:167], v[36:39], v[152:155]\n\t" \
  "s_waitcnt vmcnt(21)\n\t" \
  "v_mfma_f32_16x16x32_bf16 v[144:147], a[80:83], v[40:43], v[144:147]\n\t" \
  "ds_read_b128 v[164:167], v189 offset:12288\n\t" \
  "v_mfma_f32_16x16x32_bf16 v[148:151], a[84:87], v[40:43], v[148:151]\n\t" \
  "s_waitcnt lgkmcnt(2)\n\t" \
  "v_mfma_f32_16x16x32_bf16 v[152:155], v[168:171], v[40:43], v[152:155]\n\t" \
  "s_waitcnt vmcnt(20)\n\t" \
  "v_mfma_f32_16x16x32_bf16 v[144:147], a[88:91], v[44:47], v[144:147]\n\t" \
  "ds_read_b128 v[168:171], v189 offset:13312\n\t" \
  "v_mfma_f32_16x16x32_bf16 v[148:151], a[92:95], v[44:47], v[148:151]\n\t" \
  "s_waitcnt lgkmcnt(2)\n\t" \
  "v_mfma_f32_16x16x32_bf16 v[152:155], v[172:175], v[44:47], v[152:155]\n\t" \
  "s_waitcnt vmcnt(19)\n\t" \
  "v_mfma_f32_16x16x32_bf16 v[144:147], a[96:99], v[48:51], v[144:147]\n\t" \
  "ds_read_b128 v[172:175], v189 offset:14336\n\t" \
  "v_mfma_f32_16x16x32_bf16 v[148:151], a[100:103], v[48:51], v[148:151]\n\t" \
  "s_waitcnt lgkmcnt(2)\n\t" \
  "v_mfma_f32_16x16x32_bf16 v[152:155], v[164:167], v[48:51], v[152:155]\n\t" \
  "s_waitcnt vmcnt(18)\n\t" \
  "v_mfma_f32_16x16x32_bf16 v[144:147], a[104:107], v[52:55], v[144:147]\n\t" \
  "ds_read_b128 v[164:167], v189 offset:15360\n\t" \
  "v_mfma_f32_16x16x32_bf16 v[148:151], a[108:111], v[52:55], v[148:151]\n\t" \
  "s_waitcnt lgkmcnt(2)\n\t" \
  "v_mfma_f32_16x16x32_bf16 v[152:155], v[168:171], v[52:55], v[152:155]\n\t" \
  "s_waitcnt vmcnt(17)\n\t" \
  "v_mfma_f32_16x16x32_bf16 v[144:147], a[112:115], v[56:59], v[144:147]\n\t" \
  "ds_read_b128 v[168:171], v189 offset:16384\n\t" \
  "v_mfma_f32_16x16x32_bf16 v[148:151], a[116:119], v[56:59], v[148:151]\n\t" \
  "s_waitcnt lgkmcnt(2)\n\t" \
  "v_mfma_f32_16x16x32_bf16 v[152:155], v[172:175], v[56:59], v[152:155]\n\t" \
  "s_waitcnt vmcnt(16)\n\t" \
  "v_mfma_f32_16x16x32_bf16 v[144:147], a[120:123], v[60:63], v[144:147]\n\t" \
  "ds_read_b128 v[172:175], v189 offset:17408\n\t" \
  "v_mfma_f32_16x16x32_bf16 v[148:151], a[124:127], v[60:63], v[148:151]\n\t" \
  "s_waitcnt lgkmcnt(2)\n\t" \
  "v_mfma_f32_16x16x32_bf16 v[152:155], v[164:167], v[60:63], v[152:155]\n\t" \
  "s_waitcnt vmcnt(15)\n\t" \
  "v_mfma_f32_16x16x32_bf16 v[144:147], a[128:131], v[64:67], v[144:147]\n\t" \
  "ds_read_b128 v[164:167], v189 offset:18432\n\t" \
  "v_mfma_f32_16x16x32_bf16 v[148:151], a[132:135], v[64:67], v[148:151]\n\t" \
  "s_waitcnt lgkmcnt(2)\n\t" \
  "v_mfma_f32_16x16x32_bf16 v[152:155], v[168:171], v[64:67], v[152:155]\n\t" \
  "s_waitcnt vmcnt(14)\n\t" \
  "v_mfma_f32_16x16x32_bf16 v[144:147], a[136:139], v[68:71], v[144:147]\n\t" \
  "ds_read_b128 v[168:171], v189 offset:19456\n\t" \
  "v_mfma_f32_16x16x32_bf16 v[148:151], a[140:143], v[68:71], v[148:151]\n\t" \
  "s_waitcnt lgkmcnt(2)\n\t" \
  "v_mfma_f32_16x16x32_bf16 v[152:155], v[172:175], v[68:71], v[152:155]\n\t" \
  "s_waitcnt vmcnt(13)\n\t" \
  "v_mfma_f32_16x16x32_bf16 v[144:147], a[144:147], v[72:75], v[144:147]\n\t" \
  "ds_read_b128 v[172:175], v189 offset:20480\n\t" \
  "v_mfma_f32_16x16x32_bf16 v[148:151], a[148:151], v[72:75], v[148:151]\n\t" \
  "s_waitcnt lgkmcnt(2)\n\t" \
  "v_mfma_f32_16x16x32_bf16 v[152:155], v[164:167], v[72:75], v[152:155]\n\t" \
  "s_waitcnt vmcnt(12)\n\t" \
  "v_mfma_f32_16x16x32_bf16 v[144:147], a[152:155], v[76:79], v[144:147]\n\t" \
  "ds_read_b128 v[164:167], v189 offset:21504\n\t" \
  "v_mfma_f32_16x16x32_bf16 v[148:151], a[156:159], v[76:79], v[148:151]\n\t" \
  "s_waitcnt lgkmcnt(2)\n\t" \
  "v_mfma_f32_16x16x32_bf16 v[152:155], v[168:171], v[76:79], v[152:155]\n\t" \
  "s_waitcnt vmcnt(11)\n\t" \
  "v_mfma_f32_16x16x32_bf16 v[144:147], a[160:163], v[80:83], v[144:147]\n\t" \
  "ds_read_b128 v[168:171], v189 offset:22528\n\t" \
  "v_mfma_f32_16x16x32_bf16 v[148:151], a[164:167], v[80:83], v[148:151]\n\t" \
  "s_waitcnt lgkmcnt(2)\n\t" \
  "v_mfma_f32_16x16x32_bf16 v[152:155], v[172:175], v[80:83], v[152:155]\n\t" \
  "s_waitcnt vmcnt(10)\n\t" \
  "v_mfma_f32_16x16x32_bf16 v[144:147], a[168:171], v[84:87], v[144:147]\n\t" \
  "ds_read_b128 v[172:175], v189 offset:23552\n\t" \
  "v_mfma_f32_16x16x32_bf16 v[148:151], a[172:175], v[84:87], v[148:151]\n\t" \
  "s_waitcnt lgkmcnt(2)\n\t" \
  "v_mfma_f32_16x16x32_bf16 v[152:155], v[164:167], v[84:87], v[152:155]\n\t" \
  "s_waitcnt vmcnt(9)\n\t" \
  "v_mfma_f32_16x16x32_bf16 v[144:147], a[176:179], v[88:91], v[144:147]\n\t" \
  "ds_read_b128 v[164:167], v189 offset:24576\n\t" \
  "v_mfma_f32_16x16x32_bf16 v[148:151], a[180:183], v[88:91], v[148:151]\n\t" \
  "s_waitcnt lgkmcnt(2)\n\t" \
  "v_mfma_f32_16x16x32_bf16 v[152:155], v[168:171], v[88:91], v[152:155]\n\t" \
  "s_waitcnt vmcnt(8)\n\t" \
  "v_mfma_f32_16x16x32_bf16 v[144:147], a[184:187], v[92:95], v[144:147]\n\t" \
  "ds_read_b128 v[168:171], v189 offset:25600\n\t" \
  "v_mfma_f32_16x16x32_bf16 v[148:151], a[188:191], v[92:95], v[148:151]\n\t" \
  "s_waitcnt lgkmcnt(2)\n\t" \
  "v_mfma_f32_16x16x32_bf16 v[152:155], v[172:175], v[92:95], v[152:155]\n\t" \
  "s_waitcnt vmcnt(7)\n\t" \
  "v_mfma_f32_16x16x32_bf16 v[144:147], a[192:195], v[96:99], v[144:147]\n\t" \
  "ds_read_b128 v[172:175], v189 offset:26624\n\t" \
  "v_mfma_f32_16x16x32_bf16 v[148:151], a[196:199], v[96:99], v[148:151]\n\t" \
  "s_waitcnt lgkmcnt(2)\n\t" \
  "v_mfma_f32_16x16x32_bf16 v[152:155], v[164:167], v[96:99], v[152:155]\n\t" \
  "s_waitcnt vmcnt(6)\n\t" \
  "v_mfma_f32_16x16x32_bf16 v[144:147], a[200:203], v[100:103], v[144:147]\n\t" \
  "ds_read_b128 v[164:167], v189 offset:27648\n\t" \
  "v_mfma_f32_16x16x32_bf16 v[148:151], a[204:207], v[100:103], v[148:151]\n\t" \
  "s_waitcnt lgkmcnt(2)\n\t" \
  "v_mfma_f32_16x16x32_bf16 v[152:155], v[168:171], v[100:103], v[152:155]\n\t" \
  "s_waitcnt vmcnt(5)\n\t" \
  "v_mfma_f32_16x16x32_bf16 v[144:147], a[208:211], v[104:107], v[144:147]\n\t" \
  "ds_read_b128 v[168:171], v189 offset:28672\n\t" \
  "v_mfma_f32_16x16x32_bf16 v[148:151], a[212:215], v[104:107], v[148:151]\n\t" \
  "s_waitcnt lgkmcnt(2)\n\t" \
  "v_mfma_f32_16x16x32_bf16 v[152:155], v[172:175], v[104:107], v[152:155]\n\t" \
  "s_waitcnt vmcnt(4)\n\t" \
  "v_mfma_f32_16x16x32_bf16 v[144:147], a[216:219], v[108:111], v[144:147]\n\t" \
  "ds_read_b128 v[172:175], v189 offset:29696\n\t" \
  "v_mfma_f32_16x16x32_bf16 v[148:151], a[220:223], v[108:111], v[148:151]\n\t" \
  "s_waitcnt lgkmcnt(2)\n\t" \
  "v_mfma_f32_16x16x32_bf16 v[152:155], v[164:167], v[108:111], v[152:155]\n\t" \
  "s_waitcnt vmcnt(3)\n\t" \
  "v_mfma_f32_16x16x32_bf16 v[144:147], a[224:227], v[112:115], v[144:147]\n\t" \
  "ds_read_b128 v[164:167], v189 offset:30720\n\t" \
  "v_mfma_f32_16x16x32_bf16 v[148:151], a[228:231], v[112:115], v[148:151]\n\t" \
  "s_waitcnt lgkmcnt(2)\n\t" \
  "v_mfma_f32_16x16x32_bf16 v[152:155], v[168:171], v[112:115], v[152:155]\n\t" \
  "s_waitcnt vmcnt(2)\n\t" \
  "v_mfma_f32_16x16x32_bf16 v[144:147], a[232:235], v[116:119], v[144:147]\n\t" \
  "ds_read_b128 v[168:171], v189 offset:31744\n\t" \
  "v_mfma_f32_16x16x32_bf16 v[148:151], a[236:239], v[116:119], v[148:151]\n\t" \
  "s_waitcnt lgkmcnt(2)\n\t" \
  "v_mfma_f32_16x16x32_bf16 v[152:155], v[172:175], v[116:119], v[152:155]\n\t" \
  "s_waitcnt vmcnt(1)\n\t" \
  "v_mfma_f32_16x16x32_bf16 v[144:147], a[240:243], v[120:123], v[144:147]\n\t" \
  "v_mfma_f32_16x16x32_bf16 v[148:151], a[244:247], v[120:123], v[148:151]\n\t" \
  "s_waitcnt lgkmcnt(1)\n\t" \
  "v_mfma_f32_16x16x32_bf16 v[152:155], v[164:167], v[120:123], v[152:155]\n\t" \
  "s_waitcnt vmcnt(0)\n\t" \
  "v_mfma_f32_16x16x32_bf16 v[144:147], a[248:251], v[124:127], v[144:147]\n\t" \
  "v_mfma_f32_16x16x32_bf16 v[148:151], a[252:255], v[124:127], v[148:151]\n\t" \
  "s_waitcnt lgkmcnt(0)\n\t" \
  "v_mfma_f32_16x16x32_bf16 v[152:155], v[168:171], v[124:127], v[152:155]\n\t" \
  "s_waitcnt vmcnt(0)\n\t" \
  "Lkd_%=:\n\t" \
  "s_and_b32 s41, s51, 1\n\t" \
  "s_mul_i32 s41, s41, 12288\n\t" \
  "v_add_u32 v162, s41, v196\n\t" \
  "s_mov_b32 s60, s50\n\t" \
  "s_nop 3\n\t" \
  "ds_write_b128 v162, v[144:147] offset:0\n\t" \
  "ds_write_b128 v162, v[148:151] offset:1024\n\t" \
  "ds_write_b128 v162, v[152:155] offset:2048\n\t" \
  "s_cmp_eq_u32 s59, 1\n\t" \
  "s_cbranch_scc0 Lne_%=\n\t" \
  "v_max_u32 v191, v191, v192\n\t" \
  "s_nop 1\n\t" \
  "v_min_u32_dpp v191, v191, v191 row_ror:1 row_mask:0xf bank_mask:0xf\n\t" \
  "s_nop 1\n\t" \
  "v_min_u32_dpp v191, v191, v191 row_ror:2 row_mask:0xf bank_mask:0xf\n\t" \
  "s_nop 1\n\t" \
  "v_min_u32_dpp v191, v191, v191 row_ror:4 row_mask:0xf bank_mask:0xf\n\t" \
  "s_nop 1\n\t" \
  "v_min_u32_dpp v191, v191, v191 row_ror:8 row_mask:0xf bank_mask:0xf\n\t" \
  "s_nop 1\n\t" \
  "v_min_u32_dpp v191, v191, v191 row_bcast:15 row_mask:0xa bank_mask:0xf\n\t" \
  "s_nop 1\n\t" \
  "v_min_u32_dpp v191, v191, v191 row_bcast:31 row_mask:0xc bank_mask:0xf\n\t" \
  "s_nop 0\n\t" \
  "v_readlane_b32 s44, v191, 63\n\t" \
  "s_nop 1\n\t" \
  "Lne_%=:\n\t" \
  "s_cmp_lt_i32 s52, 0\n\t" \
  "s_cbranch_scc1 Lnf_%=\n\t" \
  "s_and_b32 s40, s51, s50\n\t" \
  "s_cbranch_scc1 Lng_%=\n\t" \
  "v_mov_b32 v162, s52\n\t" \
  "s_mov_b64 exec, 1\n\t" \
  "global_atomic_umax v163, v162, s[56:57]\n\t" \
  "s_mov_b64 exec, -1\n\t" \
  "Lng_%=:\n\t" \
  "s_mov_b32 s52, -1\n\t" \
  "Lnf_%=:\n\t" \
  "s_waitcnt lgkmcnt(0)\n\t" \
  "s_barrier\n\t" \
  "s_cmp_eq_u32 s61, 1\n\t" \
  "s_cbranch_scc0 Lnr_%=\n\t" \
  "s_add_u32 s41, s41, 131072\n\t" \
  "s_add_u32 s40, s45, 1\n\t" \
  "s_and_b32 s40, s40, 3\n\t" \
  "s_mul_i32 s40, s40, 3072\n\t" \
  "s_add_u32 s40, s40, s41\n\t" \
  "v_add_u32 v162, s40, v188\n\t" \
  "ds_read_b128 v[164:167], v162 offset:0\n\t" \
  "ds_read_b128 v[168:171], v162 offset:1024\n\t" \
  "ds_read_b128 v[172:175], v162 offset:2048\n\t" \
  "s_add_u32 s40, s45, 2\n\t" \
  "s_and_b32 s40, s40, 3\n\t" \
  "s_mul_i32 s40, s40, 3072\n\t" \
  "s_add_u32 s40, s40, s41\n\t" \
  "v_add_u32 v162, s40, v188\n\t" \
  "ds_read_b128 v[176:179], v162 offset:0\n\t" \
  "ds_read_b128 v[180:183], v162 offset:1024\n\t" \
  "ds_read_b128 v[184:187], v162 offset:2048\n\t" \
  "s_waitcnt lgkmcnt(3)\n\t" \
  "v_pk_add_f32 v[144:145], v[144:145], v[164:165]\n\t" \
  "v_pk_add_f32 v[146:147], v[146:147], v[166:167]\n\t" \
  "v_pk_add_f32 v[148:149], v[148:149], v[168:169]\n\t" \
  "v_pk_add_f32 v[150:151], v[150:151], v[170:171]\n\t" \
  "v_pk_add_f32 v[152:153], v[152:153], v[172:173]\n\t" \
  "v_pk_add_f32 v[154:155], v[154:155], v[174:175]\n\t" \
  "s_add_u32 s40, s45, 3\n\t" \
  "s_and_b32 s40, s40, 3\n\t" \
  "s_mul_i32 s40, s40, 3072\n\t" \
  "s_add_u32 s40, s40, s41\n\t" \
  "v_add_u32 v162, s40, v188\n\t" \
  "ds_read_b128 v[164:167], v162 offset:0\n\t" \
  "ds_read_b128 v[168:171], v162 offset:1024\n\t" \
  "ds_read_b128 v[172:175], v162 offset:2048\n\t" \
  "s_waitcnt lgkmcnt(3)\n\t" \
  "v_pk_add_f32 v[144:145], v[144:145], v[176:177]\n\t" \
  "v_pk_add_f32 v[146:147], v[146:147], v[178:179]\n\t" \
  "v_pk_add_f32 v[148:149], v[148:149], v[180:181]\n\t" \
  "v_pk_add_f32 v[150:151], v[150:151], v[182:183]\n\t" \
  "v_pk_add_f32 v[152:153], v[152:153], v[184:185]\n\t" \
  "v_pk_add_f32 v[154:155], v[154:155], v[186:187]\n\t" \
  "v_lshlrev_b32 v176, 16, v156\n\t" \
  "v_and_b32 v177, 0xffff0000, v156\n\t" \
  "v_lshlrev_b32 v178, 16, v157\n\t" \
  "v_and_b32 v179, 0xffff0000, v157\n\t" \
  "v_lshlrev_b32 v180, 16, v158\n\t" \
  "v_and_b32 v181, 0xffff0000, v158\n\t" \
  "v_lshlrev_b32 v182, 16, v159\n\t" \
  "v_and_b32 v183, 0xffff0000, v159\n\t" \
  "v_lshlrev_b32 v184, 16, v160\n\t" \
  "v_and_b32 v185, 0xffff0000, v160\n\t" \
  "v_lshlrev_b32 v186, 16, v161\n\t" \
  "v_and_b32 v187, 0xffff0000, v161\n\t" \
  "v_pk_add_f32 v[144:145], v[144:145], v[176:177]\n\t" \
  "v_pk_add_f32 v[146:147], v[146:147], v[178:179]\n\t" \
  "v_pk_add_f32 v[148:149], v[148:149], v[180:181]\n\t" \
  "v_pk_add_f32 v[150:151], v[150:151], v[182:183]\n\t" \
  "v_pk_add_f32 v[152:153], v[152:153], v[184:185]\n\t" \
  "v_pk_add_f32 v[154:155], v[154:155], v[186:187]\n\t" \
  "s_waitcnt lgkmcnt(0)\n\t" \
  "v_pk_add_f32 v[144:145], v[144:145], v[164:165]\n\t" \
  "v_pk_add_f32 v[146:147], v[146:147], v[166:167]\n\t" \
  "v_pk_add_f32 v[148:149], v[148:149], v[168:169]\n\t" \
  "v_pk_add_f32 v[150:151], v[150:151], v[170:171]\n\t" \
  "v_pk_add_f32 v[152:153], v[152:153], v[172:173]\n\t" \
  "v_pk_add_f32 v[154:155], v[154:155], v[174:175]\n\t" \
  "v_cvt_pk_bf16_f32 v156, v144, v145\n\t" \
  "v_cvt_pk_bf16_f32 v157, v146, v147\n\t" \
  "v_cvt_pk_bf16_f32 v158, v148, v149\n\t" \
  "v_cvt_pk_bf16_f32 v159, v150, v151\n\t" \
  "v_cvt_pk_bf16_f32 v160, v152, v153\n\t" \
  "v_cvt_pk_bf16_f32 v161, v154, v155\n\t" \
  "buffer_store_dwordx4 v[156:159], v188, s[20:23], s58 offen sc1\n\t" \
  "buffer_store_dwordx2 v[160:161], v195, s[20:23], s58 offen offset:1024 sc1\n\t" \
  "v_pk_mul_f32 v[178:179], v[144:145], v[144:145]\n\t" \
  "v_pk_add_f32 v[176:177], v[144:145], v[146:147]\n\t" \
  "v_pk_fma_f32 v[178:179], v[146:147], v[146:147], v[178:179]\n\t" \
  "v_pk_fma_f32 v[178:179], v[148:149], v[148:149], v[178:179]\n\t" \
  "v_pk_fma_f32 v[178:179], v[150:151], v[150:151], v[178:179]\n\t" \
  "v_pk_fma_f32 v[178:179], v[152:153], v[152:153], v[178:179]\n\t" \
  "v_pk_fma_f32 v[178:179], v[154:155], v[154:155], v[178:179]\n\t" \
  "v_pk_add_f32 v[176:177], v[176:177], v[148:149]\n\t" \
  "v_pk_add_f32 v[176:177], v[176:177], v[150:151]\n\t" \
  "v_pk_add_f32 v[176:177], v[176:177], v[152:153]\n\t" \
  "v_pk_add_f32 v[176:177], v[176:177], v[154:155]\n\t" \
  "v_add_f32 v176, v176, v177\n\t" \
  "v_add_f32 v177, v178, v179\n\t" \
  "v_mov_b32 v178, v176\n\t" \
  "v_mov_b32 v179, v177\n\t" \
  "s_nop 0\n\t" \
  "v_permlane16_swap_b32 v176, v178\n\t" \
  "v_permlane16_swap_b32 v177, v179\n\t" \
  "v_add_f32 v176, v176, v178\n\t" \
  "v_add_f32 v177, v177, v179\n\t" \
  "v_mov_b32 v178, v176\n\t" \
  "v_mov_b32 v179, v177\n\t" \
  "s_nop 0\n\t" \
  "v_permlane32_swap_b32 v176, v178\n\t" \
  "v_permlane32_swap_b32 v177, v179\n\t" \
  "v_add_f32 v176, v176, v178\n\t" \
  "v_add_f32 v177, v177, v179\n\t" \
  "s_mul_i32 s40, s31, 12\n\t" \
  "s_add_u32 s40, s40, s30\n\t" \
  "s_lshl_b32 s40, s40, 7\n\t" \
  "s_add_u32 s40, s40, 9437184\n\t" \
  "s_mov_b64 exec, 0xffff\n\t" \
  "buffer_store_dwordx2 v[176:177], v197, s[20:23], s40 offen sc1\n\t" \
  "s_mov_b64 exec, -1\n\t" \
  "s_mov_b32 s52, s48\n\t" \
  "s_cmp_ge_u32 s31, 112\n\t" \
  "s_cselect_b32 s52, -1, s52\n\t" \
  "Lnr_%=:\n\t" \
  "s_add_u32 s51, s51, 1\n\t" \
  "s_mov_b32 s31, s48\n\t" \
  "s_cmp_eq_u32 s48, s49\n\t" \
  "s_cbranch_scc0 Lnd_%=\n\t" \
  "s_waitcnt vmcnt(0)\n\t" \
  "Lnd_%=:\n\t" \
  "s_barrier\n\t" \
  "s_cmp_eq_u32 s60, 1\n\t" \
  "s_cbranch_scc1 Ltop_%=\n\t" \
  "s_cmp_gt_u32 s44, s43\n\t" \
  "s_cbranch_scc0 Lnb_%=\n\t" \
  "buffer_load_dwordx4 v[0:3], v188, s[20:23], s62 offen offset:0 sc1\n\t" \
  "buffer_load_dwordx4 v[4:7], v188, s[20:23], s62 offen offset:1024 sc1\n\t" \
  "buffer_load_dwordx4 v[8:11], v188, s[20:23], s62 offen offset:2048 sc1\n\t" \
  "buffer_load_dwordx4 v[12:15], v188, s[20:23], s62 offen offset:3072 sc1\n\t" \
  "buffer_load_dwordx4 v[16:19], v188, s[20:23], s63 offen offset:0 sc1\n\t" \
  "buffer_load_dwordx4 v[20:23], v188, s[20:23], s63 offen offset:1024 sc1\n\t" \
  "buffer_load_dwordx4 v[24:27], v188, s[20:23], s63 offen offset:2048 sc1\n\t" \
  "buffer_load_dwordx4 v[28:31], v188, s[20:23], s63 offen offset:3072 sc1\n\t" \
  "buffer_load_dwordx4 v[32:35], v188, s[20:23], s64 offen offset:0 sc1\n\t" \
  "buffer_load_dwordx4 v[36:39], v188, s[20:23], s64 offen offset:1024 sc1\n\t" \
  "buffer_load_dwordx4 v[40:43], v188, s[20:23], s64 offen offset:2048 sc1\n\t" \
  "buffer_load_dwordx4 v[44:47], v188, s[20:23], s64 offen offset:3072 sc1\n\t" \
  "buffer_load_dwordx4 v[48:51], v188, s[20:23], s65 offen offset:0 sc1\n\t" \
  "buffer_load_dwordx4 v[52:55], v188, s[20:23], s65 offen offset:1024 sc1\n\t" \
  "buffer_load_dwordx4 v[56:59], v188, s[20:23], s65 offen offset:2048 sc1\n\t" \
  "buffer_load_dwordx4 v[60:63], v188, s[20:23], s65 offen offset:3072 sc1\n\t" \
  "buffer_load_dwordx4 v[64:67], v188, s[20:23], s66 offen offset:0 sc1\n\t" \
  "buffer_load_dwordx4 v[68:71], v188, s[20:23], s66 offen offset:1024 sc1\n\t" \
  "buffer_load_dwordx4 v[72:75], v188, s[20:23], s66 offen offset:2048 sc1\n\t" \
  "buffer_load_dwordx4 v[76:79], v188, s[20:23], s66 offen offset:3072 sc1\n\t" \
  "buffer_load_dwordx4 v[80:83], v188, s[20:23], s67 offen offset:0 sc1\n\t" \
  "buffer_load_dwordx4 v[84:87], v188, s[20:23], s67 offen offset:1024 sc1\n\t" \
  "buffer_load_dwordx4 v[88:91], v188, s[20:23], s67 offen offset:2048 sc1\n\t" \
  "buffer_load_dwordx4 v[92:95], v188, s[20:23], s67 offen offset:3072 sc1\n\t" \
  "buffer_load_dwordx4 v[96:99], v188, s[20:23], s68 offen offset:0 sc1\n\t" \
  "buffer_load_dwordx4 v[100:103], v188, s[20:23], s68 offen offset:1024 sc1\n\t" \
  "buffer_load_dwordx4 v[104:107], v188, s[20:23], s68 offen offset:2048 sc1\n\t" \
  "buffer_load_dwordx4 v[108:111], v188, s[20:23], s68 offen offset:3072 sc1\n\t" \
  "buffer_load_dwordx4 v[112:115], v188, s[20:23], s69 offen offset:0 sc1\n\t" \
  "buffer_load_dwordx4 v[116:119], v188, s[20:23], s69 offen offset:1024 sc1\n\t" \
  "buffer_load_dwordx4 v[120:123], v188, s[20:23], s69 offen offset:2048 sc1\n\t" \
  "buffer_load_dwordx4 v[124:127], v188, s[20:23], s69 offen offset:3072 sc1\n\t" \
  "s_cmp_ge_u32 s48, 112\n\t" \
  "s_cselect_b32 s40, s45, 1\n\t" \
  "s_cmp_eq_u32 s40, 0\n\t" \
  "s_cbranch_scc0 Lbh1_%=\n\t" \
  "s_add_u32 s40, s48, 4\n\t" \
  "v_mov_b32 v162, s40\n\t" \
  "s_mov_b64 exec, 1\n\t" \
  "global_atomic_umax v163, v162, s[56:57]\n\t" \
  "s_mov_b64 exec, -1\n\t" \
  "Lbh1_%=:\n\t" \
  "s_cmp_lt_i32 s52, 0\n\t" \
  "s_cbranch_scc1 Ltop_%=\n\t" \
  "s_waitcnt vmcnt(32)\n\t" \
  "v_mov_b32 v162, s52\n\t" \
  "s_mov_b64 exec, 1\n\t" \
  "global_atomic_umax v163, v162, s[56:57]\n\t" \
  "s_mov_b64 exec, -1\n\t" \
  "s_mov_b32 s52, -1\n\t" \
  "s_branch Ltop_%=\n\t" \
  "Lnb_%=:\n\t" \
  "s_cmp_lt_i32 s52, 0\n\t" \
  "s_cbranch_scc1 Lnc_%=\n\t" \
  "s_waitcnt vmcnt(0)\n\t" \
  "v_mov_b32 v162, s52\n\t" \
  "s_mov_b64 exec, 1\n\t" \
  "global_atomic_umax v163, v162, s[56:57]\n\t" \
  "s_mov_b64 exec, -1\n\t" \
  "s_mov_b32 s52, -1\n\t" \
  "Lnc_%=:\n\t" \
  "s_cmp_lt_u32 s48, 128\n\t" \
  "s_cbranch_scc0 Lend_%=\n\t" \
  "Lblk_%=:\n\t" \
  "s_lshr_b32 s43, s48, 1\n\t" \
  "s_mul_i32 s62, s48, 131072\n\t" \
  "s_add_u32 s62, s62, s54\n\t" \
  "s_add_u32 s63, s62, 4096\n\t" \
  "s_add_u32 s64, s62, 8192\n\t" \
  "s_add_u32 s65, s62, 12288\n\t" \
  "s_add_u32 s66, s62, 16384\n\t" \
  "s_add_u32 s67, s62, 20480\n\t" \
  "s_add_u32 s68, s62, 24576\n\t" \
  "s_add_u32 s69, s62, 28672\n\t" \
  "s_mov_b32 s42, 0\n\t" \
  "La_sp_%=:\n\t" \
  "buffer_load_dword v191, v190, s[20:23], 0 offen sc1\n\t" \
  "s_waitcnt vmcnt(0)\n\t" \
  "v_max_u32 v191, v191, v192\n\t" \
  "s_nop 1\n\t" \
  "v_min_u32_dpp v191, v191, v191 row_ror:1 row_mask:0xf bank_mask:0xf\n\t" \
  "s_nop 1\n\t" \
  "v_min_u32_dpp v191, v191, v191 row_ror:2 row_mask:0xf bank_mask:0xf\n\t" \
  "s_nop 1\n\t" \
  "v_min_u32_dpp v191, v191, v191 row_ror:4 row_mask:0xf bank_mask:0xf\n\t" \
  "s_nop 1\n\t" \
  "v_min_u32_dpp v191, v191, v191 row_ror:8 row_mask:0xf bank_mask:0xf\n\t" \
  "s_nop 1\n\t" \
  "v_min_u32_dpp v191, v191, v191 row_bcast:15 row_mask:0xa bank_mask:0xf\n\t" \
  "s_nop 1\n\t" \
  "v_min_u32_dpp v191, v191, v191 row_bcast:31 row_mask:0xc bank_mask:0xf\n\t" \
  "s_nop 0\n\t" \
  "v_readlane_b32 s44, v191, 63\n\t" \
  "s_nop 1\n\t" \
  "s_cmp_gt_u32 s44, s43\n\t" \
  "s_cbranch_scc1 La_ok_%=\n\t" \
  "s_add_u32 s42, s42, 1\n\t" \
  "s_cmp_gt_u32 s42, 1048576\n\t" \
  "s_cbranch_scc1 Ltmo_%=\n\t" \
  "s_and_b32 s40, s42, 255\n\t" \
  "s_cbranch_scc1 La_sl_%=\n\t" \
  "s_mov_b32 s40, 2157568\n\t" \
  "buffer_load_dword v191, off, s[20:23], s40 sc1\n\t" \
  "s_waitcnt vmcnt(0)\n\t" \
  "v_cmp_ne_u32 vcc, 0, v191\n\t" \
  "s_nop 0\n\t" \
  "s_cbranch_vccnz Lend_%=\n\t" \
  "La_sl_%=:\n\t" \
  "s_sleep 1\n\t" \
  "s_sub_u32 s40, s43, s44\n\t" \
  "s_cmp_lt_u32 s40, 4\n\t" \
  "s_cbranch_scc1 La_sp_%=\n\t" \
  "s_sleep 100\n\t" \
  "s_branch La_sp_%=\n\t" \
  "La_ok_%=:\n\t" \
  "buffer_load_dwordx4 v[0:3], v188, s[20:23], s62 offen offset:0 sc1\n\t" \
  "buffer_load_dwordx4 v[4:7], v188, s[20:23], s62 offen offset:1024 sc1\n\t" \
  "buffer_load_dwordx4 v[8:11], v188, s[20:23], s62 offen offset:2048 sc1\n\t" \
  "buffer_load_dwordx4 v[12:15], v188, s[20:23], s62 offen offset:3072 sc1\n\t" \
  "buffer_load_dwordx4 v[16:19], v188, s[20:23], s63 offen offset:0 sc1\n\t" \
  "buffer_load_dwordx4 v[20:23], v188, s[20:23], s63 offen offset:1024 sc1\n\t" \
  "buffer_load_dwordx4 v[24:27], v188, s[20:23], s63 offen offset:2048 sc1\n\t" \
  "buffer_load_dwordx4 v[28:31], v188, s[20:23], s63 offen offset:3072 sc1\n\t" \
  "buffer_load_dwordx4 v[32:35], v188, s[20:23], s64 offen offset:0 sc1\n\t" \
  "buffer_load_dwordx4 v[36:39], v188, s[20:23], s64 offen offset:1024 sc1\n\t" \
  "buffer_load_dwordx4 v[40:43], v188, s[20:23], s64 offen offset:2048 sc1\n\t" \
  "buffer_load_dwordx4 v[44:47], v188, s[20:23], s64 offen offset:3072 sc1\n\t" \
  "buffer_load_dwordx4 v[48:51], v188, s[20:23], s65 offen offset:0 sc1\n\t" \
  "buffer_load_dwordx4 v[52:55], v188, s[20:23], s65 offen offset:1024 sc1\n\t" \
  "buffer_load_dwordx4 v[56:59], v188, s[20:23], s65 offen offset:2048 sc1\n\t" \
  "buffer_load_dwordx4 v[60:63], v188, s[20:23], s65 offen offset:3072 sc1\n\t" \
  "buffer_load_dwordx4 v[64:67], v188, s[20:23], s66 offen offset:0 sc1\n\t" \
  "buffer_load_dwordx4 v[68:71], v188, s[20:23], s66 offen offset:1024 sc1\n\t" \
  "buffer_load_dwordx4 v[72:75], v188, s[20:23], s66 offen offset:2048 sc1\n\t" \
  "buffer_load_dwordx4 v[76:79], v188, s[20:23], s66 offen offset:3072 sc1\n\t" \
  "buffer_load_dwordx4 v[80:83], v188, s[20:23], s67 offen offset:0 sc1\n\t" \
  "buffer_load_dwordx4 v[84:87], v188, s[20:23], s67 offen offset:1024 sc1\n\t" \
  "buffer_load_dwordx4 v[88:91], v188, s[20:23], s67 offen offset:2048 sc1\n\t" \
  "buffer_load_dwordx4 v[92:95], v188, s[20:23], s67 offen offset:3072 sc1\n\t" \
  "buffer_load_dwordx4 v[96:99], v188, s[20:23], s68 offen offset:0 sc1\n\t" \
  "buffer_load_dwordx4 v[100:103], v188, s[20:23], s68 offen offset:1024 sc1\n\t" \
  "buffer_load_dwordx4 v[104:107], v188, s[20:23], s68 offen offset:2048 sc1\n\t" \
  "buffer_load_dwordx4 v[108:111], v188, s[20:23], s68 offen offset:3072 sc1\n\t" \
  "buffer_load_dwordx4 v[112:115], v188, s[20:23], s69 offen offset:0 sc1\n\t" \
  "buffer_load_dwordx4 v[116:119], v188, s[20:23], s69 offen offset:1024 sc1\n\t" \
  "buffer_load_dwordx4 v[120:123], v188, s[20:23], s69 offen offset:2048 sc1\n\t" \
  "buffer_load_dwordx4 v[124:127], v188, s[20:23], s69 offen offset:3072 sc1\n\t" \
  "s_cmp_ge_u32 s48, 112\n\t" \
  "s_cselect_b32 s40, s45, 1\n\t" \
  "s_cmp_eq_u32 s40, 0\n\t" \
  "s_cbranch_scc0 Lbh2_%=\n\t" \
  "s_add_u32 s40, s48, 4\n\t" \
  "v_mov_b32 v162, s40\n\t" \
  "s_mov_b64 exec, 1\n\t" \
  "global_atomic_umax v163, v162, s[56:57]\n\t" \
  "s_mov_b64 exec, -1\n\t" \
  "Lbh2_%=:\n\t" \
  "s_branch Ltop_%=\n\t" \
  "s_branch Lend_%=\n\t" \
  "Ltmo_%=:\n\t" \
  "v_mov_b32 v162, 1\n\t" \
  "s_mov_b32 s40, 2157568\n\t" \
  "buffer_store_dword v162, off, s[20:23], s40 sc1\n\t" \
  "Lend_%=:\n\t" \
  "s_waitcnt vmcnt(0) lgkmcnt(0)\n\t"
#define DOWN_ASM \
  "s_load_dwordx2 s[24:25], %0, 64\n\t" \
  "s_load_dwordx2 s[26:27], %0, 72\n\t" \
  "s_load_dwordx2 s[28:29], %0, 80\n\t" \
  "s_and_b32 s30, %1, 255\n\t" \
  "s_bfe_u32 s55, %1, 0x80008\n\t" \
  "s_add_u32 s49, s55, 112\n\t" \
  "s_cmp_eq_u32 s55, 3\n\t" \
  "s_cselect_b32 s31, s49, s55\n\t" \
  "s_lshr_b32 s45, %1, 16\n\t" \
  "s_mov_b64 exec, -1\n\t" \
  "s_waitcnt lgkmcnt(0)\n\t" \
  "s_mov_b32 s20, s28\n\t" \
  "s_and_b32 s21, s29, 0xffff\n\t" \
  "s_mov_b32 s22, 113246208\n\t" \
  "s_mov_b32 s23, 0x20000\n\t" \
  "s_mov_b32 s36, s26\n\t" \
  "s_and_b32 s37, s27, 0xffff\n\t" \
  "s_mov_b32 s38, 2359296\n\t" \
  "s_mov_b32 s39, 0x20000\n\t" \
  "v_mbcnt_lo_u32_b32 v198, -1, 0\n\t" \
  "v_mbcnt_hi_u32_b32 v198, -1, v198\n\t" \
  "s_mul_i32 s40, s45, 1327104\n\t" \
  "s_mul_i32 s41, s30, 96\n\t" \
  "s_add_u32 s40, s40, s41\n\t" \
  "s_add_u32 s46, s24, s40\n\t" \
  "s_addc_u32 s47, s25, 0\n\t" \
  "s_mul_i32 s54, s45, 40960\n\t" \
  "s_movk_i32 s40, 10923\n\t" \
  "v_add_u32 v162, 0, v198\n\t" \
  "v_mul_u32_u24 v199, s40, v162\n\t" \
  "v_lshrrev_b32 v199, 16, v199\n\t" \
  "v_mul_u32_u24 v199, 1056, v199\n\t" \
  "v_lshl_add_u32 v200, v162, 4, v199\n\t" \
  "v_add_u32 v162, 64, v198\n\t" \
  "v_mul_u32_u24 v199, s40, v162\n\t" \
  "v_lshrrev_b32 v199, 16, v199\n\t" \
  "v_mul_u32_u24 v199, 1056, v199\n\t" \
  "v_lshl_add_u32 v201, v162, 4, v199\n\t" \
  "v_add_u32 v162, 128, v198\n\t" \
  "v_mul_u32_u24 v199, s40, v162\n\t" \
  "v_lshrrev_b32 v199, 16, v199\n\t" \
  "v_mul_u32_u24 v199, 1056, v199\n\t" \
  "v_lshl_add_u32 v202, v162, 4, v199\n\t" \
  "v_lshrrev_b32 v162, 4, v198\n\t" \
  "v_lshlrev_b32 v162, 3, v162\n\t" \
  "v_bfe_u32 v199, v198, 2, 2\n\t" \
  "v_add_u32 v162, v162, v199\n\t" \
  "v_mul_u32_u24 v162, 96, v162\n\t" \
  "v_add_u32 v162, s54, v162\n\t" \
  "v_and_b32 v199, 3, v198\n\t" \
  "v_lshl_add_u32 v203, v199, 4, v162\n\t" \
  "v_lshl_add_u32 v204, v199, 3, v162\n\t" \
  "s_add_u32 m0, s54, 0\n\t" \
  "s_nop 0\n\t" \
  "global_load_lds_dwordx4 v200, s[46:47]\n\t" \
  "s_add_u32 m0, s54, 1024\n\t" \
  "s_nop 0\n\t" \
  "global_load_lds_dwordx4 v201, s[46:47]\n\t" \
  "s_add_u32 m0, s54, 2048\n\t" \
  "s_nop 0\n\t" \
  "global_load_lds_dwordx4 v202, s[46:47]\n\t" \
  "s_add_u32 s46, s46, 36864\n\t" \
  "s_addc_u32 s47, s47, 0\n\t" \
  "s_add_u32 m0, s54, 3072\n\t" \
  "s_nop 0\n\t" \
  "global_load_lds_dwordx4 v200, s[46:47]\n\t" \
  "s_add_u32 m0, s54, 4096\n\t" \
  "s_nop 0\n\t" \
  "global_load_lds_dwordx4 v201, s[46:47]\n\t" \
  "s_add_u32 m0, s54, 5120\n\t" \
  "s_nop 0\n\t" \
  "global_load_lds_dwordx4 v202, s[46:47]\n\t" \
  "s_add_u32 s46, s46, 36864\n\t" \
  "s_addc_u32 s47, s47, 0\n\t" \
  "s_add_u32 m0, s54, 6144\n\t" \
  "s_nop 0\n\t" \
  "global_load_lds_dwordx4 v200, s[46:47]\n\t" \
  "s_add_u32 m0, s54, 7168\n\t" \
  "s_nop 0\n\t" \
  "global_load_lds_dwordx4 v201, s[46:47]\n\t" \
  "s_add_u32 m0, s54, 8192\n\t" \
  "s_nop 0\n\t" \
  "global_load_lds_dwordx4 v202, s[46:47]\n\t" \
  "s_add_u32 s46, s46, 36864\n\t" \
  "s_addc_u32 s47, s47, 0\n\t" \
  "s_add_u32 m0, s54, 9216\n\t" \
  "s_nop 0\n\t" \
  "global_load_lds_dwordx4 v200, s[46:47]\n\t" \
  "s_add_u32 m0, s54, 10240\n\t" \
  "s_nop 0\n\t" \
  "global_load_lds_dwordx4 v201, s[46:47]\n\t" \
  "s_add_u32 m0, s54, 11264\n\t" \
  "s_nop 0\n\t" \
  "global_load_lds_dwordx4 v202, s[46:47]\n\t" \
  "s_add_u32 s46, s46, 36864\n\t" \
  "s_addc_u32 s47, s47, 0\n\t" \
  "s_add_u32 m0, s54, 12288\n\t" \
  "s_nop 0\n\t" \
  "global_load_lds_dwordx4 v200, s[46:47]\n\t" \
  "s_add_u32 m0, s54, 13312\n\t" \
  "s_nop 0\n\t" \
  "global_load_lds_dwordx4 v201, s[46:47]\n\t" \
  "s_add_u32 m0, s54, 14336\n\t" \
  "s_nop 0\n\t" \
  "global_load_lds_dwordx4 v202, s[46:47]\n\t" \
  "s_add_u32 s46, s46, 36864\n\t" \
  "s_addc_u32 s47, s47, 0\n\t" \
  "s_add_u32 m0, s54, 15360\n\t" \
  "s_nop 0\n\t" \
  "global_load_lds_dwordx4 v200, s[46:47]\n\t" \
  "s_add_u32 m0, s54, 16384\n\t" \
  "s_nop 0\n\t" \
  "global_load_lds_dwordx4 v201, s[46:47]\n\t" \
  "s_add_u32 m0, s54, 17408\n\t" \
  "s_nop 0\n\t" \
  "global_load_lds_dwordx4 v202, s[46:47]\n\t" \
  "s_add_u32 s46, s46, 36864\n\t" \
  "s_addc_u32 s47, s47, 0\n\t" \
  "s_sleep 64\n\t" \
  "s_add_u32 m0, s54, 18432\n\t" \
  "s_nop 0\n\t" \
  "global_load_lds_dwordx4 v200, s[46:47]\n\t" \
  "s_add_u32 m0, s54, 19456\n\t" \
  "s_nop 0\n\t" \
  "global_load_lds_dwordx4 v201, s[46:47]\n\t" \
  "s_add_u32 m0, s54, 20480\n\t" \
  "s_nop 0\n\t" \
  "global_load_lds_dwordx4 v202, s[46:47]\n\t" \
  "s_add_u32 s46, s46, 36864\n\t" \
  "s_addc_u32 s47, s47, 0\n\t" \
  "s_add_u32 m0, s54, 21504\n\t" \
  "s_nop 0\n\t" \
  "global_load_lds_dwordx4 v200, s[46:47]\n\t" \
  "s_add_u32 m0, s54, 22528\n\t" \
  "s_nop 0\n\t" \
  "global_load_lds_dwordx4 v201, s[46:47]\n\t" \
  "s_add_u32 m0, s54, 23552\n\t" \
  "s_nop 0\n\t" \
  "global_load_lds_dwordx4 v202, s[46:47]\n\t" \
  "s_add_u32 s46, s46, 36864\n\t" \
  "s_addc_u32 s47, s47, 0\n\t" \
  "s_add_u32 m0, s54, 24576\n\t" \
  "s_nop 0\n\t" \
  "global_load_lds_dwordx4 v200, s[46:47]\n\t" \
  "s_add_u32 m0, s54, 25600\n\t" \
  "s_nop 0\n\t" \
  "global_load_lds_dwordx4 v201, s[46:47]\n\t" \
  "s_add_u32 m0, s54, 26624\n\t" \
  "s_nop 0\n\t" \
  "global_load_lds_dwordx4 v202, s[46:47]\n\t" \
  "s_add_u32 s46, s46, 36864\n\t" \
  "s_addc_u32 s47, s47, 0\n\t" \
  "s_add_u32 m0, s54, 27648\n\t" \
  "s_nop 0\n\t" \
  "global_load_lds_dwordx4 v200, s[46:47]\n\t" \
  "s_add_u32 m0, s54, 28672\n\t" \
  "s_nop 0\n\t" \
  "global_load_lds_dwordx4 v201, s[46:47]\n\t" \
  "s_add_u32 m0, s54, 29696\n\t" \
  "s_nop 0\n\t" \
  "global_load_lds_dwordx4 v202, s[46:47]\n\t" \
  "s_add_u32 s46, s46, 36864\n\t" \
  "s_addc_u32 s47, s47, 0\n\t" \
  "s_add_u32 m0, s54, 30720\n\t" \
  "s_nop 0\n\t" \
  "global_load_lds_dwordx4 v200, s[46:47]\n\t" \
  "s_add_u32 m0, s54, 31744\n\t" \
  "s_nop 0\n\t" \
  "global_load_lds_dwordx4 v201, s[46:47]\n\t" \
  "s_add_u32 m0, s54, 32768\n\t" \
  "s_nop 0\n\t" \
  "global_load_lds_dwordx4 v202, s[46:47]\n\t" \
  "s_add_u32 s46, s46, 36864\n\t" \
  "s_addc_u32 s47, s47, 0\n\t" \
  "s_add_u32 m0, s54, 33792\n\t" \
  "s_nop 0\n\t" \
  "global_load_lds_dwordx4 v200, s[46:47]\n\t" \
  "s_add_u32 m0, s54, 34816\n\t" \
  "s_nop 0\n\t" \
  "global_load_lds_dwordx4 v201, s[46:47]\n\t" \
  "s_add_u32 m0, s54, 35840\n\t" \
  "s_nop 0\n\t" \
  "global_load_lds_dwordx4 v202, s[46:47]\n\t" \
  "s_add_u32 s46, s46, 36864\n\t" \
  "s_addc_u32 s47, s47, 0\n\t" \
  "s_waitcnt vmcnt(18)\n\t" \
  "ds_read_b64_tr_b16 a[0:1], v203 offset:0\n\t" \
  "ds_read_b64_tr_b16 a[2:3], v203 offset:384\n\t" \
  "ds_read_b64_tr_b16 a[4:5], v203 offset:8\n\t" \
  "ds_read_b64_tr_b16 a[6:7], v203 offset:392\n\t" \
  "ds_read_b64_tr_b16 v[0:1], v204 offset:64\n\t" \
  "ds_read_b64_tr_b16 v[2:3], v204 offset:448\n\t" \
  "ds_read_b64_tr_b16 a[8:9], v203 offset:3072\n\t" \
  "ds_read_b64_tr_b16 a[10:11], v203 offset:3456\n\t" \
  "ds_read_b64_tr_b16 a[12:13], v203 offset:3080\n\t" \
  "ds_read_b64_tr_b16 a[14:15], v203 offset:3464\n\t" \
  "ds_read_b64_tr_b16 v[4:5], v204 offset:3136\n\t" \
  "ds_read_b64_tr_b16 v[6:7], v204 offset:3520\n\t" \
  "ds_read_b64_tr_b16 a[16:17], v203 offset:6144\n\t" \
  "ds_read_b64_tr_b16 a[18:19], v203 offset:6528\n\t" \
  "ds_read_b64_tr_b16 a[20:21], v203 offset:6152\n\t" \
  "ds_read_b64_tr_b16 a[22:23], v203 offset:6536\n\t" \
  "ds_read_b64_tr_b16 v[8:9], v204 offset:6208\n\t" \
  "ds_read_b64_tr_b16 v[10:11], v204 offset:6592\n\t" \
  "ds_read_b64_tr_b16 a[24:25], v203 offset:9216\n\t" \
  "ds_read_b64_tr_b16 a[26:27], v203 offset:9600\n\t" \
  "ds_read_b64_tr_b16 a[28:29], v203 offset:9224\n\t" \
  "ds_read_b64_tr_b16 a[30:31], v203 offset:9608\n\t" \
  "ds_read_b64_tr_b16 v[12:13], v204 offset:9280\n\t" \
  "ds_read_b64_tr_b16 v[14:15], v204 offset:9664\n\t" \
  "ds_read_b64_tr_b16 a[32:33], v203 offset:12288\n\t" \
  "ds_read_b64_tr_b16 a[34:35], v203 offset:12672\n\t" \
  "ds_read_b64_tr_b16 a[36:37], v203 offset:12296\n\t" \
  "ds_read_b64_tr_b16 a[38:39], v203 offset:12680\n\t" \
  "ds_read_b64_tr_b16 v[16:17], v204 offset:12352\n\t" \
  "ds_read_b64_tr_b16 v[18:19], v204 offset:12736\n\t" \
  "ds_read_b64_tr_b16 a[40:41], v203 offset:15360\n\t" \
  "ds_read_b64_tr_b16 a[42:43], v203 offset:15744\n\t" \
  "ds_read_b64_tr_b16 a[44:45], v203 offset:15368\n\t" \
  "ds_read_b64_tr_b16 a[46:47], v203 offset:15752\n\t" \
  "ds_read_b64_tr_b16 v[20:21], v204 offset:15424\n\t" \
  "ds_read_b64_tr_b16 v[22:23], v204 offset:15808\n\t" \
  "s_waitcnt lgkmcnt(0)\n\t" \
  "s_sleep 64\n\t" \
  "s_add_u32 m0, s54, 0\n\t" \
  "s_nop 0\n\t" \
  "global_load_lds_dwordx4 v200, s[46:47]\n\t" \
  "s_add_u32 m0, s54, 1024\n\t" \
  "s_nop 0\n\t" \
  "global_load_lds_dwordx4 v201, s[46:47]\n\t" \
  "s_add_u32 m0, s54, 2048\n\t" \
  "s_nop 0\n\t" \
  "global_load_lds_dwordx4 v202, s[46:47]\n\t" \
  "s_add_u32 s46, s46, 36864\n\t" \
  "s_addc_u32 s47, s47, 0\n\t" \
  "s_add_u32 m0, s54, 3072\n\t" \
  "s_nop 0\n\t" \
  "global_load_lds_dwordx4 v200, s[46:47]\n\t" \
  "s_add_u32 m0, s54, 4096\n\t" \
  "s_nop 0\n\t" \
  "global_load_lds_dwordx4 v201, s[46:47]\n\t" \
  "s_add_u32 m0, s54, 5120\n\t" \
  "s_nop 0\n\t" \
  "global_load_lds_dwordx4 v202, s[46:47]\n\t" \
  "s_add_u32 s46, s46, 36864\n\t" \
  "s_addc_u32 s47, s47, 0\n\t" \
  "s_add_u32 m0, s54, 6144\n\t" \
  "s_nop 0\n\t" \
  "global_load_lds_dwordx4 v200, s[46:47]\n\t" \
  "s_add_u32 m0, s54, 7168\n\t" \
  "s_nop 0\n\t" \
  "global_load_lds_dwordx4 v201, s[46:47]\n\t" \
  "s_add_u32 m0, s54, 8192\n\t" \
  "s_nop 0\n\t" \
  "global_load_lds_dwordx4 v202, s[46:47]\n\t" \
  "s_add_u32 s46, s46, 36864\n\t" \
  "s_addc_u32 s47, s47, 0\n\t" \
  "s_add_u32 m0, s54, 9216\n\t" \
  "s_nop 0\n\t" \
  "global_load_lds_dwordx4 v200, s[46:47]\n\t" \
  "s_add_u32 m0, s54, 10240\n\t" \
  "s_nop 0\n\t" \
  "global_load_lds_dwordx4 v201, s[46:47]\n\t" \
  "s_add_u32 m0, s54, 11264\n\t" \
  "s_nop 0\n\t" \
  "global_load_lds_dwordx4 v202, s[46:47]\n\t" \
  "s_add_u32 s46, s46, 36864\n\t" \
  "s_addc_u32 s47, s47, 0\n\t" \
  "s_add_u32 m0, s54, 12288\n\t" \
  "s_nop 0\n\t" \
  "global_load_lds_dwordx4 v200, s[46:47]\n\t" \
  "s_add_u32 m0, s54, 13312\n\t" \
  "s_nop 0\n\t" \
  "global_load_lds_dwordx4 v201, s[46:47]\n\t" \
  "s_add_u32 m0, s54, 14336\n\t" \
  "s_nop 0\n\t" \
  "global_load_lds_dwordx4 v202, s[46:47]\n\t" \
  "s_add_u32 s46, s46, 36864\n\t" \
  "s_addc_u32 s47, s47, 0\n\t" \
  "s_add_u32 m0, s54, 15360\n\t" \
  "s_nop 0\n\t" \
  "global_load_lds_dwordx4 v200, s[46:47]\n\t" \
  "s_add_u32 m0, s54, 16384\n\t" \
  "s_nop 0\n\t" \
  "global_load_lds_dwordx4 v201, s[46:47]\n\t" \
  "s_add_u32 m0, s54, 17408\n\t" \
  "s_nop 0\n\t" \
  "global_load_lds_dwordx4 v202, s[46:47]\n\t" \
  "s_add_u32 s46, s46, 36864\n\t" \
  "s_addc_u32 s47, s47, 0\n\t" \
  "s_waitcnt vmcnt(18)\n\t" \
  "ds_read_b64_tr_b16 a[48:49], v203 offset:18432\n\t" \
  "ds_read_b64_tr_b16 a[50:51], v203 offset:18816\n\t" \
  "ds_read_b64_tr_b16 a[52:53], v203 offset:18440\n\t" \
  "ds_read_b64_tr_b16 a[54:55], v203 offset:18824\n\t" \
  "ds_read_b64_tr_b16 v[24:25], v204 offset:18496\n\t" \
  "ds_read_b64_tr_b16 v[26:27], v204 offset:18880\n\t" \
  "ds_read_b64_tr_b16 a[56:57], v203 offset:21504\n\t" \
  "ds_read_b64_tr_b16 a[58:59], v203 offset:21888\n\t" \
  "ds_read_b64_tr_b16 a[60:61], v203 offset:21512\n\t" \
  "ds_read_b64_tr_b16 a[62:63], v203 offset:21896\n\t" \
  "ds_read_b64_tr_b16 v[28:29], v204 offset:21568\n\t" \
  "ds_read_b64_tr_b16 v[30:31], v204 offset:21952\n\t" \
  "ds_read_b64_tr_b16 a[64:65], v203 offset:24576\n\t" \
  "ds_read_b64_tr_b16 a[66:67], v203 offset:24960\n\t" \
  "ds_read_b64_tr_b16 a[68:69], v203 offset:24584\n\t" \
  "ds_read_b64_tr_b16 a[70:71], v203 offset:24968\n\t" \
  "ds_read_b64_tr_b16 v[32:33], v204 offset:24640\n\t" \
  "ds_read_b64_tr_b16 v[34:35], v204 offset:25024\n\t" \
  "ds_read_b64_tr_b16 a[72:73], v203 offset:27648\n\t" \
  "ds_read_b64_tr_b16 a[74:75], v203 offset:28032\n\t" \
  "ds_read_b64_tr_b16 a[76:77], v203 offset:27656\n\t" \
  "ds_read_b64_tr_b16 a[78:79], v203 offset:28040\n\t" \
  "ds_read_b64_tr_b16 v[36:37], v204 offset:27712\n\t" \
  "ds_read_b64_tr_b16 v[38:39], v204 offset:28096\n\t" \
  "ds_read_b64_tr_b16 a[80:81], v203 offset:30720\n\t" \
  "ds_read_b64_tr_b16 a[82:83], v203 offset:31104\n\t" \
  "ds_read_b64_tr_b16 a[84:85], v203 offset:30728\n\t" \
  "ds_read_b64_tr_b16 a[86:87], v203 offset:31112\n\t" \
  "ds_read_b64_tr_b16 v[40:41], v204 offset:30784\n\t" \
  "ds_read_b64_tr_b16 v[42:43], v204 offset:31168\n\t" \
  "ds_read_b64_tr_b16 a[88:89], v203 offset:33792\n\t" \
  "ds_read_b64_tr_b16 a[90:91], v203 offset:34176\n\t" \
  "ds_read_b64_tr_b16 a[92:93], v203 offset:33800\n\t" \
  "ds_read_b64_tr_b16 a[94:95], v203 offset:34184\n\t" \
  "ds_read_b64_tr_b16 v[44:45], v204 offset:33856\n\t" \
  "ds_read_b64_tr_b16 v[46:47], v204 offset:34240\n\t" \
  "s_waitcnt lgkmcnt(0)\n\t" \
  "s_sleep 64\n\t" \
  "s_add_u32 m0, s54, 18432\n\t" \
  "s_nop 0\n\t" \
  "global_load_lds_dwordx4 v200, s[46:47]\n\t" \
  "s_add_u32 m0, s54, 19456\n\t" \
  "s_nop 0\n\t" \
  "global_load_lds_dwordx4 v201, s[46:47]\n\t" \
  "s_add_u32 m0, s54, 20480\n\t" \
  "s_nop 0\n\t" \
  "global_load_lds_dwordx4 v202, s[46:47]\n\t" \
  "s_add_u32 s46, s46, 36864\n\t" \
  "s_addc_u32 s47, s47, 0\n\t" \
  "s_add_u32 m0, s54, 21504\n\t" \
  "s_nop 0\n\t" \
  "global_load_lds_dwordx4 v200, s[46:47]\n\t" \
  "s_add_u32 m0, s54, 22528\n\t" \
  "s_nop 0\n\t" \
  "global_load_lds_dwordx4 v201, s[46:47]\n\t" \
  "s_add_u32 m0, s54, 23552\n\t" \
  "s_nop 0\n\t" \
  "global_load_lds_dwordx4 v202, s[46:47]\n\t" \
  "s_add_u32 s46, s46, 36864\n\t" \
  "s_addc_u32 s47, s47, 0\n\t" \
  "s_add_u32 m0, s54, 24576\n\t" \
  "s_nop 0\n\t" \
  "global_load_lds_dwordx4 v200, s[46:47]\n\t" \
  "s_add_u32 m0, s54, 25600\n\t" \
  "s_nop 0\n\t" \
  "global_load_lds_dwordx4 v201, s[46:47]\n\t" \
  "s_add_u32 m0, s54, 26624\n\t" \
  "s_nop 0\n\t" \
  "global_load_lds_dwordx4 v202, s[46:47]\n\t" \
  "s_add_u32 s46, s46, 36864\n\t" \
  "s_addc_u32 s47, s47, 0\n\t" \
  "s_add_u32 m0, s54, 27648\n\t" \
  "s_nop 0\n\t" \
  "global_load_lds_dwordx4 v200, s[46:47]\n\t" \
  "s_add_u32 m0, s54, 28672\n\t" \
  "s_nop 0\n\t" \
  "global_load_lds_dwordx4 v201, s[46:47]\n\t" \
  "s_add_u32 m0, s54, 29696\n\t" \
  "s_nop 0\n\t" \
  "global_load_lds_dwordx4 v202, s[46:47]\n\t" \
  "s_add_u32 s46, s46, 36864\n\t" \
  "s_addc_u32 s47, s47, 0\n\t" \
  "s_add_u32 m0, s54, 30720\n\t" \
  "s_nop 0\n\t" \
  "global_load_lds_dwordx4 v200, s[46:47]\n\t" \
  "s_add_u32 m0, s54, 31744\n\t" \
  "s_nop 0\n\t" \
  "global_load_lds_dwordx4 v201, s[46:47]\n\t" \
  "s_add_u32 m0, s54, 32768\n\t" \
  "s_nop 0\n\t" \
  "global_load_lds_dwordx4 v202, s[46:47]\n\t" \
  "s_add_u32 s46, s46, 36864\n\t" \
  "s_addc_u32 s47, s47, 0\n\t" \
  "s_add_u32 m0, s54, 33792\n\t" \
  "s_nop 0\n\t" \
  "global_load_lds_dwordx4 v200, s[46:47]\n\t" \
  "s_add_u32 m0, s54, 34816\n\t" \
  "s_nop 0\n\t" \
  "global_load_lds_dwordx4 v201, s[46:47]\n\t" \
  "s_add_u32 m0, s54, 35840\n\t" \
  "s_nop 0\n\t" \
  "global_load_lds_dwordx4 v202, s[46:47]\n\t" \
  "s_add_u32 s46, s46, 36864\n\t" \
  "s_addc_u32 s47, s47, 0\n\t" \
  "s_waitcnt vmcnt(18)\n\t" \
  "ds_read_b64_tr_b16 a[96:97], v203 offset:0\n\t" \
  "ds_read_b64_tr_b16 a[98:99], v203 offset:384\n\t" \
  "ds_read_b64_tr_b16 a[100:101], v203 offset:8\n\t" \
  "ds_read_b64_tr_b16 a[102:103], v203 offset:392\n\t" \
  "ds_read_b64_tr_b16 v[48:49], v204 offset:64\n\t" \
  "ds_read_b64_tr_b16 v[50:51], v204 offset:448\n\t" \
  "ds_read_b64_tr_b16 a[104:105], v203 offset:3072\n\t" \
  "ds_read_b64_tr_b16 a[106:107], v203 offset:3456\n\t" \
  "ds_read_b64_tr_b16 a[108:109], v203 offset:3080\n\t" \
  "ds_read_b64_tr_b16 a[110:111], v203 offset:3464\n\t" \
  "ds_read_b64_tr_b16 v[52:53], v204 offset:3136\n\t" \
  "ds_read_b64_tr_b16 v[54:55], v204 offset:3520\n\t" \
  "ds_read_b64_tr_b16 a[112:113], v203 offset:6144\n\t" \
  "ds_read_b64_tr_b16 a[114:115], v203 offset:6528\n\t" \
  "ds_read_b64_tr_b16 a[116:117], v203 offset:6152\n\t" \
  "ds_read_b64_tr_b16 a[118:119], v203 offset:6536\n\t" \
  "ds_read_b64_tr_b16 v[56:57], v204 offset:6208\n\t" \
  "ds_read_b64_tr_b16 v[58:59], v204 offset:6592\n\t" \
  "ds_read_b64_tr_b16 a[120:121], v203 offset:9216\n\t" \
  "ds_read_b64_tr_b16 a[122:123], v203 offset:9600\n\t" \
  "ds_read_b64_tr_b16 a[124:125], v203 offset:9224\n\t" \
  "ds_read_b64_tr_b16 a[126:127], v203 offset:9608\n\t" \
  "ds_read_b64_tr_b16 v[60:61], v204 offset:9280\n\t" \
  "ds_read_b64_tr_b16 v[62:63], v204 offset:9664\n\t" \
  "ds_read_b64_tr_b16 a[128:129], v203 offset:12288\n\t" \
  "ds_read_b64_tr_b16 a[130:131], v203 offset:12672\n\t" \
  "ds_read_b64_tr_b16 a[132:133], v203 offset:12296\n\t" \
  "ds_read_b64_tr_b16 a[134:135], v203 offset:12680\n\t" \
  "ds_read_b64_tr_b16 v[64:65], v204 offset:12352\n\t" \
  "ds_read_b64_tr_b16 v[66:67], v204 offset:12736\n\t" \
  "ds_read_b64_tr_b16 a[136:137], v203 offset:15360\n\t" \
  "ds_read_b64_tr_b16 a[138:139], v203 offset:15744\n\t" \
  "ds_read_b64_tr_b16 a[140:141], v203 offset:15368\n\t" \
  "ds_read_b64_tr_b16 a[142:143], v203 offset:15752\n\t" \
  "ds_read_b64_tr_b16 v[68:69], v204 offset:15424\n\t" \
  "ds_read_b64_tr_b16 v[70:71], v204 offset:15808\n\t" \
  "s_waitcnt lgkmcnt(0)\n\t" \
  "s_sleep 64\n\t" \
  "s_add_u32 m0, s54, 0\n\t" \
  "s_nop 0\n\t" \
  "global_load_lds_dwordx4 v200, s[46:47]\n\t" \
  "s_add_u32 m0, s54, 1024\n\t" \
  "s_nop 0\n\t" \
  "global_load_lds_dwordx4 v201, s[46:47]\n\t" \
  "s_add_u32 m0, s54, 2048\n\t" \
  "s_nop 0\n\t" \
  "global_load_lds_dwordx4 v202, s[46:47]\n\t" \
  "s_add_u32 s46, s46, 36864\n\t" \
  "s_addc_u32 s47, s47, 0\n\t" \
  "s_add_u32 m0, s54, 3072\n\t" \
  "s_nop 0\n\t" \
  "global_load_lds_dwordx4 v200, s[46:47]\n\t" \
  "s_add_u32 m0, s54, 4096\n\t" \
  "s_nop 0\n\t" \
  "global_load_lds_dwordx4 v201, s[46:47]\n\t" \
  "s_add_u32 m0, s54, 5120\n\t" \
  "s_nop 0\n\t" \
  "global_load_lds_dwordx4 v202, s[46:47]\n\t" \
  "s_add_u32 s46, s46, 36864\n\t" \
  "s_addc_u32 s47, s47, 0\n\t" \
  "s_add_u32 m0, s54, 6144\n\t" \
  "s_nop 0\n\t" \
  "global_load_lds_dwordx4 v200, s[46:47]\n\t" \
  "s_add_u32 m0, s54, 7168\n\t" \
  "s_nop 0\n\t" \
  "global_load_lds_dwordx4 v201, s[46:47]\n\t" \
  "s_add_u32 m0, s54, 8192\n\t" \
  "s_nop 0\n\t" \
  "global_load_lds_dwordx4 v202, s[46:47]\n\t" \
  "s_add_u32 s46, s46, 36864\n\t" \
  "s_addc_u32 s47, s47, 0\n\t" \
  "s_add_u32 m0, s54, 9216\n\t" \
  "s_nop 0\n\t" \
  "global_load_lds_dwordx4 v200, s[46:47]\n\t" \
  "s_add_u32 m0, s54, 10240\n\t" \
  "s_nop 0\n\t" \
  "global_load_lds_dwordx4 v201, s[46:47]\n\t" \
  "s_add_u32 m0, s54, 11264\n\t" \
  "s_nop 0\n\t" \
  "global_load_lds_dwordx4 v202, s[46:47]\n\t" \
  "s_add_u32 s46, s46, 36864\n\t" \
  "s_addc_u32 s47, s47, 0\n\t" \
  "s_add_u32 m0, s54, 12288\n\t" \
  "s_nop 0\n\t" \
  "global_load_lds_dwordx4 v200, s[46:47]\n\t" \
  "s_add_u32 m0, s54, 13312\n\t" \
  "s_nop 0\n\t" \
  "global_load_lds_dwordx4 v201, s[46:47]\n\t" \
  "s_add_u32 m0, s54, 14336\n\t" \
  "s_nop 0\n\t" \
  "global_load_lds_dwordx4 v202, s[46:47]\n\t" \
  "s_add_u32 s46, s46, 36864\n\t" \
  "s_addc_u32 s47, s47, 0\n\t" \
  "s_add_u32 m0, s54, 15360\n\t" \
  "s_nop 0\n\t" \
  "global_load_lds_dwordx4 v200, s[46:47]\n\t" \
  "s_add_u32 m0, s54, 16384\n\t" \
  "s_nop 0\n\t" \
  "global_load_lds_dwordx4 v201, s[46:47]\n\t" \
  "s_add_u32 m0, s54, 17408\n\t" \
  "s_nop 0\n\t" \
  "global_load_lds_dwordx4 v202, s[46:47]\n\t" \
  "s_add_u32 s46, s46, 36864\n\t" \
  "s_addc_u32 s47, s47, 0\n\t" \
  "s_waitcnt vmcnt(18)\n\t" \
  "ds_read_b64_tr_b16 a[144:145], v203 offset:18432\n\t" \
  "ds_read_b64_tr_b16 a[146:147], v203 offset:18816\n\t" \
  "ds_read_b64_tr_b16 a[148:149], v203 offset:18440\n\t" \
  "ds_read_b64_tr_b16 a[150:151], v203 offset:18824\n\t" \
  "ds_read_b64_tr_b16 v[72:73], v204 offset:18496\n\t" \
  "ds_read_b64_tr_b16 v[74:75], v204 offset:18880\n\t" \
  "ds_read_b64_tr_b16 a[152:153], v203 offset:21504\n\t" \
  "ds_read_b64_tr_b16 a[154:155], v203 offset:21888\n\t" \
  "ds_read_b64_tr_b16 a[156:157], v203 offset:21512\n\t" \
  "ds_read_b64_tr_b16 a[158:159], v203 offset:21896\n\t" \
  "ds_read_b64_tr_b16 v[76:77], v204 offset:21568\n\t" \
  "ds_read_b64_tr_b16 v[78:79], v204 offset:21952\n\t" \
  "ds_read_b64_tr_b16 a[160:161], v203 offset:24576\n\t" \
  "ds_read_b64_tr_b16 a[162:163], v203 offset:24960\n\t" \
  "ds_read_b64_tr_b16 a[164:165], v203 offset:24584\n\t" \
  "ds_read_b64_tr_b16 a[166:167], v203 offset:24968\n\t" \
  "ds_read_b64_tr_b16 v[80:81], v204 offset:24640\n\t" \
  "ds_read_b64_tr_b16 v[82:83], v204 offset:25024\n\t" \
  "ds_read_b64_tr_b16 a[168:169], v203 offset:27648\n\t" \
  "ds_read_b64_tr_b16 a[170:171], v203 offset:28032\n\t" \
  "ds_read_b64_tr_b16 a[172:173], v203 offset:27656\n\t" \
  "ds_read_b64_tr_b16 a[174:175], v203 offset:28040\n\t" \
  "ds_read_b64_tr_b16 v[84:85], v204 offset:27712\n\t" \
  "ds_read_b64_tr_b16 v[86:87], v204 offset:28096\n\t" \
  "ds_read_b64_tr_b16 a[176:177], v203 offset:30720\n\t" \
  "ds_read_b64_tr_b16 a[178:179], v203 offset:31104\n\t" \
  "ds_read_b64_tr_b16 a[180:181], v203 offset:30728\n\t" \
  "ds_read_b64_tr_b16 a[182:183], v203 offset:31112\n\t" \
  "ds_read_b64_tr_b16 v[88:89], v204 offset:30784\n\t" \
  "ds_read_b64_tr_b16 v[90:91], v204 offset:31168\n\t" \
  "ds_read_b64_tr_b16 a[184:185], v203 offset:33792\n\t" \
  "ds_read_b64_tr_b16 a[186:187], v203 offset:34176\n\t" \
  "ds_read_b64_tr_b16 a[188:189], v203 offset:33800\n\t" \
  "ds_read_b64_tr_b16 a[190:191], v203 offset:34184\n\t" \
  "ds_read_b64_tr_b16 v[92:93], v204 offset:33856\n\t" \
  "ds_read_b64_tr_b16 v[94:95], v204 offset:34240\n\t" \
  "s_waitcnt lgkmcnt(0)\n\t" \
  "s_sleep 64\n\t" \
  "s_add_u32 m0, s54, 18432\n\t" \
  "s_nop 0\n\t" \
  "global_load_lds_dwordx4 v200, s[46:47]\n\t" \
  "s_add_u32 m0, s54, 19456\n\t" \
  "s_nop 0\n\t" \
  "global_load_lds_dwordx4 v201, s[46:47]\n\t" \
  "s_add_u32 m0, s54, 20480\n\t" \
  "s_nop 0\n\t" \
  "global_load_lds_dwordx4 v202, s[46:47]\n\t" \
  "s_add_u32 s46, s46, 36864\n\t" \
  "s_addc_u32 s47, s47, 0\n\t" \
  "s_add_u32 m0, s54, 21504\n\t" \
  "s_nop 0\n\t" \
  "global_load_lds_dwordx4 v200, s[46:47]\n\t" \
  "s_add_u32 m0, s54, 22528\n\t" \
  "s_nop 0\n\t" \
  "global_load_lds_dwordx4 v201, s[46:47]\n\t" \
  "s_add_u32 m0, s54, 23552\n\t" \
  "s_nop 0\n\t" \
  "global_load_lds_dwordx4 v202, s[46:47]\n\t" \
  "s_add_u32 s46, s46, 36864\n\t" \
  "s_addc_u32 s47, s47, 0\n\t" \
  "s_add_u32 m0, s54, 24576\n\t" \
  "s_nop 0\n\t" \
  "global_load_lds_dwordx4 v200, s[46:47]\n\t" \
  "s_add_u32 m0, s54, 25600\n\t" \
  "s_nop 0\n\t" \
  "global_load_lds_dwordx4 v201, s[46:47]\n\t" \
  "s_add_u32 m0, s54, 26624\n\t" \
  "s_nop 0\n\t" \
  "global_load_lds_dwordx4 v202, s[46:47]\n\t" \
  "s_add_u32 s46, s46, 36864\n\t" \
  "s_addc_u32 s47, s47, 0\n\t" \
  "s_add_u32 m0, s54, 27648\n\t" \
  "s_nop 0\n\t" \
  "global_load_lds_dwordx4 v200, s[46:47]\n\t" \
  "s_add_u32 m0, s54, 28672\n\t" \
  "s_nop 0\n\t" \
  "global_load_lds_dwordx4 v201, s[46:47]\n\t" \
  "s_add_u32 m0, s54, 29696\n\t" \
  "s_nop 0\n\t" \
  "global_load_lds_dwordx4 v202, s[46:47]\n\t" \
  "s_add_u32 s46, s46, 36864\n\t" \
  "s_addc_u32 s47, s47, 0\n\t" \
  "s_add_u32 m0, s54, 30720\n\t" \
  "s_nop 0\n\t" \
  "global_load_lds_dwordx4 v200, s[46:47]\n\t" \
  "s_add_u32 m0, s54, 31744\n\t" \
  "s_nop 0\n\t" \
  "global_load_lds_dwordx4 v201, s[46:47]\n\t" \
  "s_add_u32 m0, s54, 32768\n\t" \
  "s_nop 0\n\t" \
  "global_load_lds_dwordx4 v202, s[46:47]\n\t" \
  "s_add_u32 s46, s46, 36864\n\t" \
  "s_addc_u32 s47, s47, 0\n\t" \
  "s_add_u32 m0, s54, 33792\n\t" \
  "s_nop 0\n\t" \
  "global_load_lds_dwordx4 v200, s[46:47]\n\t" \
  "s_add_u32 m0, s54, 34816\n\t" \
  "s_nop 0\n\t" \
  "global_load_lds_dwordx4 v201, s[46:47]\n\t" \
  "s_add_u32 m0, s54, 35840\n\t" \
  "s_nop 0\n\t" \
  "global_load_lds_dwordx4 v202, s[46:47]\n\t" \
  "s_add_u32 s46, s46, 36864\n\t" \
  "s_addc_u32 s47, s47, 0\n\t" \
  "s_waitcnt vmcnt(18)\n\t" \
  "ds_read_b64_tr_b16 a[192:193], v203 offset:0\n\t" \
  "ds_read_b64_tr_b16 a[194:195], v203 offset:384\n\t" \
  "ds_read_b64_tr_b16 a[196:197], v203 offset:8\n\t" \
  "ds_read_b64_tr_b16 a[198:199], v203 offset:392\n\t" \
  "ds_read_b64_tr_b16 v[96:97], v204 offset:64\n\t" \
  "ds_read_b64_tr_b16 v[98:99], v204 offset:448\n\t" \
  "ds_read_b64_tr_b16 a[200:201], v203 offset:3072\n\t" \
  "ds_read_b64_tr_b16 a[202:203], v203 offset:3456\n\t" \
  "ds_read_b64_tr_b16 a[204:205], v203 offset:3080\n\t" \
  "ds_read_b64_tr_b16 a[206:207], v203 offset:3464\n\t" \
  "ds_read_b64_tr_b16 v[100:101], v204 offset:3136\n\t" \
  "ds_read_b64_tr_b16 v[102:103], v204 offset:3520\n\t" \
  "ds_read_b64_tr_b16 a[208:209], v203 offset:6144\n\t" \
  "ds_read_b64_tr_b16 a[210:211], v203 offset:6528\n\t" \
  "ds_read_b64_tr_b16 a[212:213], v203 offset:6152\n\t" \
  "ds_read_b64_tr_b16 a[214:215], v203 offset:6536\n\t" \
  "ds_read_b64_tr_b16 v[104:105], v204 offset:6208\n\t" \
  "ds_read_b64_tr_b16 v[106:107], v204 offset:6592\n\t" \
  "ds_read_b64_tr_b16 a[216:217], v203 offset:9216\n\t" \
  "ds_read_b64_tr_b16 a[218:219], v203 offset:9600\n\t" \
  "ds_read_b64_tr_b16 a[220:221], v203 offset:9224\n\t" \
  "ds_read_b64_tr_b16 a[222:223], v203 offset:9608\n\t" \
  "ds_read_b64_tr_b16 v[108:109], v204 offset:9280\n\t" \
  "ds_read_b64_tr_b16 v[110:111], v204 offset:9664\n\t" \
  "ds_read_b64_tr_b16 a[224:225], v203 offset:12288\n\t" \
  "ds_read_b64_tr_b16 a[226:227], v203 offset:12672\n\t" \
  "ds_read_b64_tr_b16 a[228:229], v203 offset:12296\n\t" \
  "ds_read_b64_tr_b16 a[230:231], v203 offset:12680\n\t" \
  "ds_read_b64_tr_b16 v[112:113], v204 offset:12352\n\t" \
  "ds_read_b64_tr_b16 v[114:115], v204 offset:12736\n\t" \
  "ds_read_b64_tr_b16 a[232:233], v203 offset:15360\n\t" \
  "ds_read_b64_tr_b16 a[234:235], v203 offset:15744\n\t" \
  "ds_read_b64_tr_b16 a[236:237], v203 offset:15368\n\t" \
  "ds_read_b64_tr_b16 a[238:239], v203 offset:15752\n\t" \
  "ds_read_b64_tr_b16 v[116:117], v204 offset:15424\n\t" \
  "ds_read_b64_tr_b16 v[118:119], v204 offset:15808\n\t" \
  "s_waitcnt lgkmcnt(0)\n\t" \
  "s_waitcnt vmcnt(0)\n\t" \
  "ds_read_b64_tr_b16 a[240:241], v203 offset:18432\n\t" \
  "ds_read_b64_tr_b16 a[242:243], v203 offset:18816\n\t" \
  "ds_read_b64_tr_b16 a[244:245], v203 offset:18440\n\t" \
  "ds_read_b64_tr_b16 a[246:247], v203 offset:18824\n\t" \
  "ds_read_b64_tr_b16 v[120:121], v204 offset:18496\n\t" \
  "ds_read_b64_tr_b16 v[122:123], v204 offset:18880\n\t" \
  "ds_read_b64_tr_b16 a[248:249], v203 offset:21504\n\t" \
  "ds_read_b64_tr_b16 a[250:251], v203 offset:21888\n\t" \
  "ds_read_b64_tr_b16 a[252:253], v203 offset:21512\n\t" \
  "ds_read_b64_tr_b16 a[254:255], v203 offset:21896\n\t" \
  "ds_read_b64_tr_b16 v[124:125], v204 offset:21568\n\t" \
  "ds_read_b64_tr_b16 v[126:127], v204 offset:21952\n\t" \
  "ds_read_b64_tr_b16 v[224:225], v203 offset:24576\n\t" \
  "ds_read_b64_tr_b16 v[226:227], v203 offset:24960\n\t" \
  "ds_read_b64_tr_b16 v[228:229], v203 offset:24584\n\t" \
  "ds_read_b64_tr_b16 v[230:231], v203 offset:24968\n\t" \
  "ds_read_b64_tr_b16 v[208:209], v204 offset:24640\n\t" \
  "ds_read_b64_tr_b16 v[210:211], v204 offset:25024\n\t" \
  "ds_read_b64_tr_b16 v[232:233], v203 offset:27648\n\t" \
  "ds_read_b64_tr_b16 v[234:235], v203 offset:28032\n\t" \
  "ds_read_b64_tr_b16 v[236:237], v203 offset:27656\n\t" \
  "ds_read_b64_tr_b16 v[238:239], v203 offset:28040\n\t" \
  "ds_read_b64_tr_b16 v[212:213], v204 offset:27712\n\t" \
  "ds_read_b64_tr_b16 v[214:215], v204 offset:28096\n\t" \
  "ds_read_b64_tr_b16 v[240:241], v203 offset:30720\n\t" \
  "ds_read_b64_tr_b16 v[242:243], v203 offset:31104\n\t" \
  "ds_read_b64_tr_b16 v[244:245], v203 offset:30728\n\t" \
  "ds_read_b64_tr_b16 v[246:247], v203 offset:31112\n\t" \
  "ds_read_b64_tr_b16 v[216:217], v204 offset:30784\n\t" \
  "ds_read_b64_tr_b16 v[218:219], v204 offset:31168\n\t" \
  "ds_read_b64_tr_b16 v[248:249], v203 offset:33792\n\t" \
  "ds_read_b64_tr_b16 v[250:251], v203 offset:34176\n\t" \
  "ds_read_b64_tr_b16 v[252:253], v203 offset:33800\n\t" \
  "ds_read_b64_tr_b16 v[254:255], v203 offset:34184\n\t" \
  "ds_read_b64_tr_b16 v[220:221], v204 offset:33856\n\t" \
  "ds_read_b64_tr_b16 v[222:223], v204 offset:34240\n\t" \
  "s_waitcnt lgkmcnt(0)\n\t" \
  "v_lshlrev_b32 v188, 4, v198\n\t" \
  "s_lshl_b32 s40, s45, 15\n\t" \
  "v_add_u32 v189, s40, v188\n\t" \
  "s_barrier\n\t" \
  "ds_write_b128 v189, v[0:3] offset:0\n\t" \
  "ds_write_b128 v189, v[4:7] offset:1024\n\t" \
  "ds_write_b128 v189, v[8:11] offset:2048\n\t" \
  "ds_write_b128 v189, v[12:15] offset:3072\n\t" \
  "ds_write_b128 v189, v[16:19] offset:4096\n\t" \
  "ds_write_b128 v189, v[20:23] offset:5120\n\t" \
  "ds_write_b128 v189, v[24:27] offset:6144\n\t" \
  "ds_write_b128 v189, v[28:31] offset:7168\n\t" \
  "ds_write_b128 v189, v[32:35] offset:8192\n\t" \
  "ds_write_b128 v189, v[36:39] offset:9216\n\t" \
  "ds_write_b128 v189, v[40:43] offset:10240\n\t" \
  "ds_write_b128 v189, v[44:47] offset:11264\n\t" \
  "ds_write_b128 v189, v[48:51] offset:12288\n\t" \
  "ds_write_b128 v189, v[52:55] offset:13312\n\t" \
  "ds_write_b128 v189, v[56:59] offset:14336\n\t" \
  "ds_write_b128 v189, v[60:63] offset:15360\n\t" \
  "ds_write_b128 v189, v[64:67] offset:16384\n\t" \
  "ds_write_b128 v189, v[68:71] offset:17408\n\t" \
  "ds_write_b128 v189, v[72:75] offset:18432\n\t" \
  "ds_write_b128 v189, v[76:79] offset:19456\n\t" \
  "ds_write_b128 v189, v[80:83] offset:20480\n\t" \
  "ds_write_b128 v189, v[84:87] offset:21504\n\t" \
  "ds_write_b128 v189, v[88:91] offset:22528\n\t" \
  "ds_write_b128 v189, v[92:95] offset:23552\n\t" \
  "ds_write_b128 v189, v[96:99] offset:24576\n\t" \
  "ds_write_b128 v189, v[100:103] offset:25600\n\t" \
  "ds_write_b128 v189, v[104:107] offset:26624\n\t" \
  "ds_write_b128 v189, v[108:111] offset:27648\n\t" \
  "ds_write_b128 v189, v[112:115] offset:28672\n\t" \
  "ds_write_b128 v189, v[116:119] offset:29696\n\t" \
  "ds_write_b128 v189, v[120:123] offset:30720\n\t" \
  "ds_write_b128 v189, v[124:127] offset:31744\n\t" \
  "v_and_b32 v162, 15, v198\n\t" \
  "v_mul_u32_u24 v194, 1152, v162\n\t" \
  "s_mul_i32 s41, s30, 96\n\t" \
  "v_add_u32 v194, s41, v194\n\t" \
  "v_and_b32 v199, 48, v198\n\t" \
  "v_add_u32 v193, v194, v199\n\t" \
  "v_lshrrev_b32 v199, 1, v199\n\t" \
  "v_add_u32 v194, v194, v199\n\t" \
  "v_lshrrev_b32 v199, 5, v198\n\t" \
  "v_lshlrev_b32 v199, 8, v199\n\t" \
  "v_lshl_add_u32 v195, v162, 4, v199\n\t" \
  "v_bfe_u32 v199, v198, 4, 1\n\t" \
  "v_lshl_add_u32 v195, v199, 3, v195\n\t" \
  "v_lshlrev_b32 v197, 3, v198\n\t" \
  "v_mov_b32 v163, 0\n\t" \
  "s_mul_i32 s40, s45, 3072\n\t" \
  "s_add_u32 s40, s40, 131072\n\t" \
  "v_add_u32 v196, s40, v188\n\t" \
  "v_min_u32 v162, 17, v198\n\t" \
  "s_mul_i32 s40, s45, 18\n\t" \
  "v_add_u32 v162, s40, v162\n\t" \
  "v_lshlrev_b32 v162, 7, v162\n\t" \
  "v_add_u32 v190, 2148352, v162\n\t" \
  "s_mul_i32 s54, s45, 36864\n\t" \
  "s_add_u32 s54, s54, 94371840\n\t" \
  "s_mov_b32 s51, 0\n\t" \
  "s_mov_b32 s52, -1\n\t" \
  "s_mov_b32 s44, 0\n\t" \
  "s_mov_b32 s48, s31\n\t" \
  "s_waitcnt lgkmcnt(0)\n\t" \
  "s_branch Lblk_%=\n\t" \
  "Ltop_%=:\n\t" \
  "ds_read_b128 v[164:167], v189 offset:0\n\t" \
  "ds_read_b128 v[168:171], v189 offset:1024\n\t" \
  "s_add_u32 s48, s31, 3\n\t" \
  "s_cmp_lt_u32 s48, 112\n\t" \
  "s_cselect_b32 s48, s48, s49\n\t" \
  "s_add_u32 s41, s31, 4\n\t" \
  "s_cmp_lt_u32 s31, 112\n\t" \
  "s_cselect_b32 s48, s48, s41\n\t" \
  "s_lshr_b32 s43, s48, 1\n\t" \
  "s_cmp_gt_u32 s44, s43\n\t" \
  "s_cselect_b32 s50, 1, 0\n\t" \
  "s_cmp_lt_u32 s48, 112\n\t" \
  "s_cselect_b32 s50, s50, 0\n\t" \
  "s_add_u32 s41, s48, 3\n\t" \
  "s_lshr_b32 s41, s41, 1\n\t" \
  "s_min_u32 s41, s41, 63\n\t" \
  "s_cmp_le_u32 s44, s41\n\t" \
  "s_cselect_b32 s59, 1, 0\n\t" \
  "s_cbranch_scc0 Lnn_%=\n\t" \
  "buffer_load_dword v191, v190, s[20:23], 0 offen sc1\n\t" \
  "Lnn_%=:\n\t" \
  "s_mul_i32 s58, s31, 18432\n\t" \
  "s_mul_i32 s40, s30, 1536\n\t" \
  "s_add_u32 s58, s58, s40\n\t" \
  "s_add_u32 s58, s58, 6291456\n\t" \
  "s_mul_i32 s53, s31, 18432\n\t" \
  "s_and_b32 s40, s51, 3\n\t" \
  "s_cmp_eq_u32 s40, s45\n\t" \
  "s_cselect_b32 s61, 1, 0\n\t" \
  "s_cbranch_scc0 Lnq_%=\n\t" \
  "buffer_load_dwordx4 v[156:159], v188, s[20:23], s58 offen sc1\n\t" \
  "buffer_load_dwordx2 v[160:161], v195, s[20:23], s58 offen offset:1024 sc1\n\t" \
  "Lnq_%=:\n\t" \
  "s_cmp_ge_u32 s31, 112\n\t" \
  "s_cbranch_scc1 Ltl_%=\n\t" \
  "s_mul_i32 s62, s48, 147456\n\t" \
  "s_add_u32 s62, s62, s54\n\t" \
  "s_add_u32 s63, s62, 4096\n\t" \
  "s_add_u32 s64, s62, 8192\n\t" \
  "s_add_u32 s65, s62, 12288\n\t" \
  "s_add_u32 s66, s62, 16384\n\t" \
  "s_add_u32 s67, s62, 20480\n\t" \
  "s_add_u32 s68, s62, 24576\n\t" \
  "s_add_u32 s69, s62, 28672\n\t" \
  "s_add_u32 s70, s62, 32768\n\t" \
  "s_cmp_eq_u32 s50, 1\n\t" \
  "s_cbranch_scc0 Lnp_%=\n\t" \
  "s_waitcnt vmcnt(35)\n\t" \
  "v_mfma_f32_16x16x32_bf16 v[144:147], a[0:3], v[0:3], 0\n\t" \
  "ds_read_b128 v[172:175], v189 offset:2048\n\t" \
  "v_mfma_f32_16x16x32_bf16 v[148:151], a[4:7], v[0:3], 0\n\t" \
  "s_waitcnt lgkmcnt(2)\n\t" \
  "v_mfma_f32_16x16x32_bf16 v[152:155], v[164:167], v[0:3], 0\n\t" \
  "buffer_load_dwordx4 v[0:3], v188, s[20:23], s62 offen offset:0 sc1\n\t" \
  "s_waitcnt vmcnt(35)\n\t" \
  "v_mfma_f32_16x16x32_bf16 v[144:147], a[8:11], v[4:7], v[144:147]\n\t" \
  "ds_read_b128 v[164:167], v189 offset:3072\n\t" \
  "v_mfma_f32_16x16x32_bf16 v[148:151], a[12:15], v[4:7], v[148:151]\n\t" \
  "s_waitcnt lgkmcnt(2)\n\t" \
  "v_mfma_f32_16x16x32_bf16 v[152:155], v[168:171], v[4:7], v[152:155]\n\t" \
  "buffer_load_dwordx4 v[4:7], v188, s[20:23], s62 offen offset:1024 sc1\n\t" \
  "s_waitcnt vmcnt(35)\n\t" \
  "v_mfma_f32_16x16x32_bf16 v[144:147], a[16:19], v[8:11], v[144:147]\n\t" \
  "ds_read_b128 v[168:171], v189 offset:4096\n\t" \
  "v_mfma_f32_16x16x32_bf16 v[148:151], a[20:23], v[8:11], v[148:151]\n\t" \
  "s_waitcnt lgkmcnt(2)\n\t" \
  "v_mfma_f32_16x16x32_bf16 v[152:155], v[172:175], v[8:11], v[152:155]\n\t" \
  "buffer_load_dwordx4 v[8:11], v188, s[20:23], s62 offen offset:2048 sc1\n\t" \
  "s_waitcnt vmcnt(35)\n\t" \
  "v_mfma_f32_16x16x32_bf16 v[144:147], a[24:27], v[12:15], v[144:147]\n\t" \
  "ds_read_b128 v[172:175], v189 offset:5120\n\t" \
  "v_mfma_f32_16x16x32_bf16 v[148:151], a[28:31], v[12:15], v[148:151]\n\t" \
  "s_waitcnt lgkmcnt(2)\n\t" \
  "v_mfma_f32_16x16x32_bf16 v[152:155], v[164:167], v[12:15], v[152:155]\n\t" \
  "buffer_load_dwordx4 v[12:15], v188, s[20:23], s62 offen offset:3072 sc1\n\t" \
  "s_waitcnt vmcnt(35)\n\t" \
  "v_mfma_f32_16x16x32_bf16 v[144:147], a[32:35], v[16:19], v[144:147]\n\t" \
  "ds_read_b128 v[164:167], v189 offset:6144\n\t" \
  "v_mfma_f32_16x16x32_bf16 v[148:151], a[36:39], v[16:19], v[148:151]\n\t" \
  "s_waitcnt lgkmcnt(2)\n\t" \
  "v_mfma_f32_16x16x32_bf16 v[152:155], v[168:171], v[16:19], v[152:155]\n\t" \
  "buffer_load_dwordx4 v[16:19], v188, s[20:23], s63 offen offset:0 sc1\n\t" \
  "s_waitcnt vmcnt(35)\n\t" \
  "v_mfma_f32_16x16x32_bf16 v[144:147], a[40:43], v[20:23], v[144:147]\n\t" \
  "ds_read_b128 v[168:171], v189 offset:7168\n\t" \
  "v_mfma_f32_16x16x32_bf16 v[148:151], a[44:47], v[20:23], v[148:151]\n\t" \
  "s_waitcnt lgkmcnt(2)\n\t" \
  "v_mfma_f32_16x16x32_bf16 v[152:155], v[172:175], v[20:23], v[152:155]\n\t" \
  "buffer_load_dwordx4 v[20:23], v188, s[20:23], s63 offen offset:1024 sc1\n\t" \
  "s_waitcnt vmcnt(35)\n\t" \
  "v_mfma_f32_16x16x32_bf16 v[144:147], a[48:51], v[24:27], v[144:147]\n\t" \
  "ds_read_b128 v[172:175], v189 offset:8192\n\t" \
  "v_mfma_f32_16x16x32_bf16 v[148:151], a[52:55], v[24:27], v[148:151]\n\t" \
  "s_waitcnt lgkmcnt(2)\n\t" \
  "v_mfma_f32_16x16x32_bf16 v[152:155], v[164:167], v[24:27], v[152:155]\n\t" \
  "buffer_load_dwordx4 v[24:27], v188, s[20:23], s63 offen offset:2048 sc1\n\t" \
  "s_waitcnt vmcnt(35)\n\t" \
  "v_mfma_f32_16x16x32_bf16 v[144:147], a[56:59], v[28:31], v[144:147]\n\t" \
  "ds_read_b128 v[164:167], v189 offset:9216\n\t" \
  "v_mfma_f32_16x16x32_bf16 v[148:151], a[60:63], v[28:31], v[148:151]\n\t" \
  "s_waitcnt lgkmcnt(2)\n\t" \
  "v_mfma_f32_16x16x32_bf16 v[152:155], v[168:171], v[28:31], v[152:155]\n\t" \
  "buffer_load_dwordx4 v[28:31], v188, s[20:23], s63 offen offset:3072 sc1\n\t" \
  "s_waitcnt vmcnt(35)\n\t" \
  "v_mfma_f32_16x16x32_bf16 v[144:147], a[64:67], v[32:35], v[144:147]\n\t" \
  "ds_read_b128 v[168:171], v189 offset:10240\n\t" \
  "v_mfma_f32_16x16x32_bf16 v[148:151], a[68:71], v[32:35], v[148:151]\n\t" \
  "s_waitcnt lgkmcnt(2)\n\t" \
  "v_mfma_f32_16x16x32_bf16 v[152:155], v[172:175], v[32:35], v[152:155]\n\t" \
  "buffer_load_dwordx4 v[32:35], v188, s[20:23], s64 offen offset:0 sc1\n\t" \
  "s_waitcnt vmcnt(35)\n\t" \
  "v_mfma_f32_16x16x32_bf16 v[144:147], a[72:75], v[36:39], v[144:147]\n\t" \
  "ds_read_b128 v[172:175], v189 offset:11264\n\t" \
  "v_mfma_f32_16x16x32_bf16 v[148:151], a[76:79], v[36:39], v[148:151]\n\t" \
  "s_waitcnt lgkmcnt(2)\n\t" \
  "v_mfma_f32_16x16x32_bf16 v[152:155], v[164:167], v[36:39], v[152:155]\n\t" \
  "buffer_load_dwordx4 v[36:39], v188, s[20:23], s64 offen offset:1024 sc1\n\t" \
  "s_waitcnt vmcnt(35)\n\t" \
  "v_mfma_f32_16x16x32_bf16 v[144:147], a[80:83], v[40:43], v[144:147]\n\t" \
  "ds_read_b128 v[164:167], v189 offset:12288\n\t" \
  "v_mfma_f32_16x16x32_bf16 v[148:151], a[84:87], v[40:43], v[148:151]\n\t" \
  "s_waitcnt lgkmcnt(2)\n\t" \
  "v_mfma_f32_16x16x32_bf16 v[152:155], v[168:171], v[40:43], v[152:155]\n\t" \
  "buffer_load_dwordx4 v[40:43], v188, s[20:23], s64 offen offset:2048 sc1\n\t" \
  "s_waitcnt vmcnt(35)\n\t" \
  "v_mfma_f32_16x16x32_bf16 v[144:147], a[88:91], v[44:47], v[144:147]\n\t" \
  "ds_read_b128 v[168:171], v189 offset:13312\n\t" \
  "v_mfma_f32_16x16x32_bf16 v[148:151], a[92:95], v[44:47], v[148:151]\n\t" \
  "s_waitcnt lgkmcnt(2)\n\t" \
  "v_mfma_f32_16x16x32_bf16 v[152:155], v[172:175], v[44:47], v[152:155]\n\t" \
  "buffer_load_dwordx4 v[44:47], v188, s[20:23], s64 offen offset:3072 sc1\n\t" \
  "s_waitcnt vmcnt(35)\n\t" \
  "v_mfma_f32_16x16x32_bf16 v[144:147], a[96:99], v[48:51], v[144:147]\n\t" \
  "ds_read_b128 v[172:175], v189 offset:14336\n\t" \
  "v_mfma_f32_16x16x32_bf16 v[148:151], a[100:103], v[48:51], v[148:151]\n\t" \
  "s_waitcnt lgkmcnt(2)\n\t" \
  "v_mfma_f32_16x16x32_bf16 v[152:155], v[164:167], v[48:51], v[152:155]\n\t" \
  "buffer_load_dwordx4 v[48:51], v188, s[20:23], s65 offen offset:0 sc1\n\t" \
  "s_waitcnt vmcnt(35)\n\t" \
  "v_mfma_f32_16x16x32_bf16 v[144:147], a[104:107], v[52:55], v[144:147]\n\t" \
  "ds_read_b128 v[164:167], v189 offset:15360\n\t" \
  "v_mfma_f32_16x16x32_bf16 v[148:151], a[108:111], v[52:55], v[148:151]\n\t" \
  "s_waitcnt lgkmcnt(2)\n\t" \
  "v_mfma_f32_16x16x32_bf16 v[152:155], v[168:171], v[52:55], v[152:155]\n\t" \
  "buffer_load_dwordx4 v[52:55], v188, s[20:23], s65 offen offset:1024 sc1\n\t" \
  "s_waitcnt vmcnt(35)\n\t" \
  "v_mfma_f32_16x16x32_bf16 v[144:147], a[112:115], v[56:59], v[144:147]\n\t" \
  "ds_read_b128 v[168:171], v189 offset:16384\n\t" \
  "v_mfma_f32_16x16x32_bf16 v[148:151], a[116:119], v[56:59], v[148:151]\n\t" \
  "s_waitcnt lgkmcnt(2)\n\t" \
  "v_mfma_f32_16x16x32_bf16 v[152:155], v[172:175], v[56:59], v[152:155]\n\t" \
  "buffer_load_dwordx4 v[56:59], v188, s[20:23], s65 offen offset:2048 sc1\n\t" \
  "s_waitcnt vmcnt(35)\n\t" \
  "v_mfma_f32_16x16x32_bf16 v[144:147], a[120:123], v[60:63], v[144:147]\n\t" \
  "ds_read_b128 v[172:175], v189 offset:17408\n\t" \
  "v_mfma_f32_16x16x32_bf16 v[148:151], a[124:127], v[60:63], v[148:151]\n\t" \
  "s_waitcnt lgkmcnt(2)\n\t" \
  "v_mfma_f32_16x16x32_bf16 v[152:155], v[164:167], v[60:63], v[152:155]\n\t" \
  "buffer_load_dwordx4 v[60:63], v188, s[20:23], s65 offen offset:3072 sc1\n\t" \
  "s_waitcnt vmcnt(35)\n\t" \
  "v_mfma_f32_16x16x32_bf16 v[144:147], a[128:131], v[64:67], v[144:147]\n\t" \
  "ds_read_b128 v[164:167], v189 offset:18432\n\t" \
  "v_mfma_f32_16x16x32_bf16 v[148:151], a[132:135], v[64:67], v[148:151]\n\t" \
  "s_waitcnt lgkmcnt(2)\n\t" \
  "v_mfma_f32_16x16x32_bf16 v[152:155], v[168:171], v[64:67], v[152:155]\n\t" \
  "buffer_load_dwordx4 v[64:67], v188, s[20:23], s66 offen offset:0 sc1\n\t" \
  "s_waitcnt vmcnt(35)\n\t" \
  "v_mfma_f32_16x16x32_bf16 v[144:147], a[136:139], v[68:71], v[144:147]\n\t" \
  "ds_read_b128 v[168:171], v189 offset:19456\n\t" \
  "v_mfma_f32_16x16x32_bf16 v[148:151], a[140:143], v[68:71], v[148:151]\n\t" \
  "s_waitcnt lgkmcnt(2)\n\t" \
  "v_mfma_f32_16x16x32_bf16 v[152:155], v[172:175], v[68:71], v[152:155]\n\t" \
  "buffer_load_dwordx4 v[68:71], v188, s[20:23], s66 offen offset:1024 sc1\n\t" \
  "s_waitcnt vmcnt(35)\n\t" \
  "v_mfma_f32_16x16x32_bf16 v[144:147], a[144:147], v[72:75], v[144:147]\n\t" \
  "ds_read_b128 v[172:175], v189 offset:20480\n\t" \
  "v_mfma_f32_16x16x32_bf16 v[148:151], a[148:151], v[72:75], v[148:151]\n\t" \
  "s_waitcnt lgkmcnt(2)\n\t" \
  "v_mfma_f32_16x16x32_bf16 v[152:155], v[164:167], v[72:75], v[152:155]\n\t" \
  "buffer_load_dwordx4 v[72:75], v188, s[20:23], s66 offen offset:2048 sc1\n\t" \
  "s_waitcnt vmcnt(35)\n\t" \
  "v_mfma_f32_16x16x32_bf16 v[144:147], a[152:155], v[76:79], v[144:147]\n\t" \
  "ds_read_b128 v[164:167], v189 offset:21504\n\t" \
  "v_mfma_f32_16x16x32_bf16 v[148:151], a[156:159], v[76:79], v[148:151]\n\t" \
  "s_waitcnt lgkmcnt(2)\n\t" \
  "v_mfma_f32_16x16x32_bf16 v[152:155], v[168:171], v[76:79], v[152:155]\n\t" \
  "buffer_load_dwordx4 v[76:79], v188, s[20:23], s66 offen offset:3072 sc1\n\t" \
  "s_waitcnt vmcnt(35)\n\t" \
  "v_mfma_f32_16x16x32_bf16 v[144:147], a[160:163], v[80:83], v[144:147]\n\t" \
  "ds_read_b128 v[168:171], v189 offset:22528\n\t" \
  "v_mfma_f32_16x16x32_bf16 v[148:151], a[164:167], v[80:83], v[148:151]\n\t" \
  "s_waitcnt lgkmcnt(2)\n\t" \
  "v_mfma_f32_16x16x32_bf16 v[152:155], v[172:175], v[80:83], v[152:155]\n\t" \
  "buffer_load_dwordx4 v[80:83], v188, s[20:23], s67 offen offset:0 sc1\n\t" \
  "s_waitcnt vmcnt(35)\n\t" \
  "v_mfma_f32_16x16x32_bf16 v[144:147], a[168:171], v[84:87], v[144:147]\n\t" \
  "ds_read_b128 v[172:175], v189 offset:23552\n\t" \
  "v_mfma_f32_16x16x32_bf16 v[148:151], a[172:175], v[84:87], v[148:151]\n\t" \
  "s_waitcnt lgkmcnt(2)\n\t" \
  "v_mfma_f32_16x16x32_bf16 v[152:155], v[164:167], v[84:87], v[152:155]\n\t" \
  "buffer_load_dwordx4 v[84:87], v188, s[20:23], s67 offen offset:1024 sc1\n\t" \
  "s_waitcnt vmcnt(35)\n\t" \
  "v_mfma_f32_16x16x32_bf16 v[144:147], a[176:179], v[88:91], v[144:147]\n\t" \
  "ds_read_b128 v[164:167], v189 offset:24576\n\t" \
  "v_mfma_f32_16x16x32_bf16 v[148:151], a[180:183], v[88:91], v[148:151]\n\t" \
  "s_waitcnt lgkmcnt(2)\n\t" \
  "v_mfma_f32_16x16x32_bf16 v[152:155], v[168:171], v[88:91], v[152:155]\n\t" \
  "buffer_load_dwordx4 v[88:91], v188, s[20:23], s67 offen offset:2048 sc1\n\t" \
  "s_waitcnt vmcnt(35)\n\t" \
  "v_mfma_f32_16x16x32_bf16 v[144:147], a[184:187], v[92:95], v[144:147]\n\t" \
  "ds_read_b128 v[168:171], v189 offset:25600\n\t" \
  "v_mfma_f32_16x16x32_bf16 v[148:151], a[188:191], v[92:95], v[148:151]\n\t" \
  "s_waitcnt lgkmcnt(2)\n\t" \
  "v_mfma_f32_16x16x32_bf16 v[152:155], v[172:175], v[92:95], v[152:155]\n\t" \
  "buffer_load_dwordx4 v[92:95], v188, s[20:23], s67 offen offset:3072 sc1\n\t" \
  "s_waitcnt vmcnt(35)\n\t" \
  "v_mfma_f32_16x16x32_bf16 v[144:147], a[192:195], v[96:99], v[144:147]\n\t" \
  "ds_read_b128 v[172:175], v189 offset:26624\n\t" \
  "v_mfma_f32_16x16x32_bf16 v[148:151], a[196:199], v[96:99], v[148:151]\n\t" \
  "s_waitcnt lgkmcnt(2)\n\t" \
  "v_mfma_f32_16x16x32_bf16 v[152:155], v[164:167], v[96:99], v[152:155]\n\t" \
  "buffer_load_dwordx4 v[96:99], v188, s[20:23], s68 offen offset:0 sc1\n\t" \
  "s_waitcnt vmcnt(35)\n\t" \
  "v_mfma_f32_16x16x32_bf16 v[144:147], a[200:203], v[100:103], v[144:147]\n\t" \
  "ds_read_b128 v[164:167], v189 offset:27648\n\t" \
  "v_mfma_f32_16x16x32_bf16 v[148:151], a[204:207], v[100:103], v[148:151]\n\t" \
  "s_waitcnt lgkmcnt(2)\n\t" \
  "v_mfma_f32_16x16x32_bf16 v[152:155], v[168:171], v[100:103], v[152:155]\n\t" \
  "buffer_load_dwordx4 v[100:103], v188, s[20:23], s68 offen offset:1024 sc1\n\t" \
  "s_waitcnt vmcnt(35)\n\t" \
  "v_mfma_f32_16x16x32_bf16 v[144:147], a[208:211], v[104:107], v[144:147]\n\t" \
  "ds_read_b128 v[168:171], v189 offset:28672\n\t" \
  "v_mfma_f32_16x16x32_bf16 v[148:151], a[212:215], v[104:107], v[148:151]\n\t" \
  "s_waitcnt lgkmcnt(2)\n\t" \
  "v_mfma_f32_16x16x32_bf16 v[152:155], v[172:175], v[104:107], v[152:155]\n\t" \
  "buffer_load_dwordx4 v[104:107], v188, s[20:23], s68 offen offset:2048 sc1\n\t" \
  "s_waitcnt vmcnt(35)\n\t" \
  "v_mfma_f32_16x16x32_bf16 v[144:147], a[216:219], v[108:111], v[144:147]\n\t" \
  "ds_read_b128 v[172:175], v189 offset:29696\n\t" \
  "v_mfma_f32_16x16x32_bf16 v[148:151], a[220:223], v[108:111], v[148:151]\n\t" \
  "s_waitcnt lgkmcnt(2)\n\t" \
  "v_mfma_f32_16x16x32_bf16 v[152:155], v[164:167], v[108:111], v[152:155]\n\t" \
  "buffer_load_dwordx4 v[108:111], v188, s[20:23], s68 offen offset:3072 sc1\n\t" \
  "s_waitcnt vmcnt(35)\n\t" \
  "v_mfma_f32_16x16x32_bf16 v[144:147], a[224:227], v[112:115], v[144:147]\n\t" \
  "ds_read_b128 v[164:167], v189 offset:30720\n\t" \
  "v_mfma_f32_16x16x32_bf16 v[148:151], a[228:231], v[112:115], v[148:151]\n\t" \
  "s_waitcnt lgkmcnt(2)\n\t" \
  "v_mfma_f32_16x16x32_bf16 v[152:155], v[168:171], v[112:115], v[152:155]\n\t" \
  "buffer_load_dwordx4 v[112:115], v188, s[20:23], s69 offen offset:0 sc1\n\t" \
  "s_waitcnt vmcnt(35)\n\t" \
  "v_mfma_f32_16x16x32_bf16 v[144:147], a[232:235], v[116:119], v[144:147]\n\t" \
  "ds_read_b128 v[168:171], v189 offset:31744\n\t" \
  "v_mfma_f32_16x16x32_bf16 v[148:151], a[236:239], v[116:119], v[148:151]\n\t" \
  "s_waitcnt lgkmcnt(2)\n\t" \
  "v_mfma_f32_16x16x32_bf16 v[152:155], v[172:175], v[116:119], v[152:155]\n\t" \
  "buffer_load_dwordx4 v[116:119], v188, s[20:23], s69 offen offset:1024 sc1\n\t" \
  "s_waitcnt vmcnt(35)\n\t" \
  "v_mfma_f32_16x16x32_bf16 v[144:147], a[240:243], v[120:123], v[144:147]\n\t" \
  "v_mfma_f32_16x16x32_bf16 v[148:151], a[244:247], v[120:123], v[148:151]\n\t" \
  "s_waitcnt lgkmcnt(1)\n\t" \
  "v_mfma_f32_16x16x32_bf16 v[152:155], v[164:167], v[120:123], v[152:155]\n\t" \
  "buffer_load_dwordx4 v[120:123], v188, s[20:23], s69 offen offset:2048 sc1\n\t" \
  "s_waitcnt vmcnt(35)\n\t" \
  "v_mfma_f32_16x16x32_bf16 v[144:147], a[248:251], v[124:127], v[144:147]\n\t" \
  "v_mfma_f32_16x16x32_bf16 v[148:151], a[252:255], v[124:127], v[148:151]\n\t" \
  "s_waitcnt lgkmcnt(0)\n\t" \
  "v_mfma_f32_16x16x32_bf16 v[152:155], v[168:171], v[124:127], v[152:155]\n\t" \
  "buffer_load_dwordx4 v[124:127], v188, s[20:23], s69 offen offset:3072 sc1\n\t" \
  "s_waitcnt vmcnt(35)\n\t" \
  "v_mfma_f32_16x16x32_bf16 v[144:147], v[224:227], v[128:131], v[144:147]\n\t" \
  "v_mfma_f32_16x16x32_bf16 v[148:151], v[228:231], v[128:131], v[148:151]\n\t" \
  "v_mfma_f32_16x16x32_bf16 v[152:155], v[208:211], v[128:131], v[152:155]\n\t" \
  "buffer_load_dwordx4 v[128:131], v188, s[20:23], s70 offen offset:0 sc1\n\t" \
  "s_waitcnt vmcnt(35)\n\t" \
  "v_mfma_f32_16x16x32_bf16 v[144:147], v[232:235], v[132:135], v[144:147]\n\t" \
  "v_mfma_f32_16x16x32_bf16 v[148:151], v[236:239], v[132:135], v[148:151]\n\t" \
  "v_mfma_f32_16x16x32_bf16 v[152:155], v[212:215], v[132:135], v[152:155]\n\t" \
  "buffer_load_dwordx4 v[132:135], v188, s[20:23], s70 offen offset:1024 sc1\n\t" \
  "s_waitcnt vmcnt(35)\n\t" \
  "v_mfma_f32_16x16x32_bf16 v[144:147], v[240:243], v[136:139], v[144:147]\n\t" \
  "v_mfma_f32_16x16x32_bf16 v[148:151], v[244:247], v[136:139], v[148:151]\n\t" \
  "v_mfma_f32_16x16x32_bf16 v[152:155], v[216:219], v[136:139], v[152:155]\n\t" \
  "buffer_load_dwordx4 v[136:139], v188, s[20:23], s70 offen offset:2048 sc1\n\t" \
  "s_waitcnt vmcnt(35)\n\t" \
  "v_mfma_f32_16x16x32_bf16 v[144:147], v[248:251], v[140:143], v[144:147]\n\t" \
  "v_mfma_f32_16x16x32_bf16 v[148:151], v[252:255], v[140:143], v[148:151]\n\t" \
  "v_mfma_f32_16x16x32_bf16 v[152:155], v[220:223], v[140:143], v[152:155]\n\t" \
  "buffer_load_dwordx4 v[140:143], v188, s[20:23], s70 offen offset:3072 sc1\n\t" \
  "s_waitcnt vmcnt(36)\n\t" \
  "s_branch Lkd_%=\n\t" \
  "Ltl_%=:\n\t" \
  "s_mov_b32 s71, 0\n\t" \
  "Ltm_%=:\n\t" \
  "v_mov_b32 v200, 0\n\t" \
  "s_waitcnt vmcnt(35)\n\t" \
  "v_mfma_f32_16x16x32_bf16 v[144:147], a[0:3], v[0:3], 0\n\t" \
  "ds_read_b128 v[172:175], v189 offset:2048\n\t" \
  "v_mfma_f32_16x16x32_bf16 v[148:151], a[4:7], v[0:3], 0\n\t" \
  "s_waitcnt lgkmcnt(2)\n\t" \
  "v_mfma_f32_16x16x32_bf16 v[152:155], v[164:167], v[0:3], 0\n\t" \
  "v_max3_u32 v200, v200, v0, v1\n\t" \
  "v_max3_u32 v200, v200, v2, v3\n\t" \
  "s_waitcnt vmcnt(34)\n\t" \
  "v_mfma_f32_16x16x32_bf16 v[144:147], a[8:11], v[4:7], v[144:147]\n\t" \
  "ds_read_b128 v[164:167], v189 offset:3072\n\t" \
  "v_mfma_f32_16x16x32_bf16 v[148:151], a[12:15], v[4:7], v[148:151]\n\t" \
  "s_waitcnt lgkmcnt(2)\n\t" \
  "v_mfma_f32_16x16x32_bf16 v[152:155], v[168:171], v[4:7], v[152:155]\n\t" \
  "v_max3_u32 v200, v200, v4, v5\n\t" \
  "v_max3_u32 v200, v200, v6, v7\n\t" \
  "s_waitcnt vmcnt(33)\n\t" \
  "v_mfma_f32_16x16x32_bf16 v[144:147], a[16:19], v[8:11], v[144:147]\n\t" \
  "ds_read_b128 v[168:171], v189 offset:4096\n\t" \
  "v_mfma_f32_16x16x32_bf16 v[148:151], a[20:23], v[8:11], v[148:151]\n\t" \
  "s_waitcnt lgkmcnt(2)\n\t" \
  "v_mfma_f32_16x16x32_bf16 v[152:155], v[172:175], v[8:11], v[152:155]\n\t" \
  "v_max3_u32 v200, v200, v8, v9\n\t" \
  "v_max3_u32 v200, v200, v10, v11\n\t" \
  "s_waitcnt vmcnt(32)\n\t" \
  "v_mfma_f32_16x16x32_bf16 v[144:147], a[24:27], v[12:15], v[144:147]\n\t" \
  "ds_read_b128 v[172:175], v189 offset:5120\n\t" \
  "v_mfma_f32_16x16x32_bf16 v[148:151], a[28:31], v[12:15], v[148:151]\n\t" \
  "s_waitcnt lgkmcnt(2)\n\t" \
  "v_mfma_f32_16x16x32_bf16 v[152:155], v[164:167], v[12:15], v[152:155]\n\t" \
  "v_max3_u32 v200, v200, v12, v13\n\t" \
  "v_max3_u32 v200, v200, v14, v15\n\t" \
  "s_waitcnt vmcnt(31)\n\t" \
  "v_mfma_f32_16x16x32_bf16 v[144:147], a[32:35], v[16:19], v[144:147]\n\t" \
  "ds_read_b128 v[164:167], v189 offset:6144\n\t" \
  "v_mfma_f32_16x16x32_bf16 v[148:151], a[36:39], v[16:19], v[148:151]\n\t" \
  "s_waitcnt lgkmcnt(2)\n\t" \
  "v_mfma_f32_16x16x32_bf16 v[152:155], v[168:171], v[16:19], v[152:155]\n\t" \
  "v_max3_u32 v200, v200, v16, v17\n\t" \
  "v_max3_u32 v200, v200, v18, v19\n\t" \
  "s_waitcnt vmcnt(30)\n\t" \
  "v_mfma_f32_16x16x32_bf16 v[144:147], a[40:43], v[20:23], v[144:147]\n\t" \
  "ds_read_b128 v[168:171], v189 offset:7168\n\t" \
  "v_mfma_f32_16x16x32_bf16 v[148:151], a[44:47], v[20:23], v[148:151]\n\t" \
  "s_waitcnt lgkmcnt(2)\n\t" \
  "v_mfma_f32_16x16x32_bf16 v[152:155], v[172:175], v[20:23], v[152:155]\n\t" \
  "v_max3_u32 v200, v200, v20, v21\n\t" \
  "v_max3_u32 v200, v200, v22, v23\n\t" \
  "s_waitcnt vmcnt(29)\n\t" \
  "v_mfma_f32_16x16x32_bf16 v[144:147], a[48:51], v[24:27], v[144:147]\n\t" \
  "ds_read_b128 v[172:175], v189 offset:8192\n\t" \
  "v_mfma_f32_16x16x32_bf16 v[148:151], a[52:55], v[24:27], v[148:151]\n\t" \
  "s_waitcnt lgkmcnt(2)\n\t" \
  "v_mfma_f32_16x16x32_bf16 v[152:155], v[164:167], v[24:27], v[152:155]\n\t" \
  "v_max3_u32 v200, v200, v24, v25\n\t" \
  "v_max3_u32 v200, v200, v26, v27\n\t" \
  "s_waitcnt vmcnt(28)\n\t" \
  "v_mfma_f32_16x16x32_bf16 v[144:147], a[56:59], v[28:31], v[144:147]\n\t" \
  "ds_read_b128 v[164:167], v189 offset:9216\n\t" \
  "v_mfma_f32_16x16x32_bf16 v[148:151], a[60:63], v[28:31], v[148:151]\n\t" \
  "s_waitcnt lgkmcnt(2)\n\t" \
  "v_mfma_f32_16x16x32_bf16 v[152:155], v[168:171], v[28:31], v[152:155]\n\t" \
  "v_max3_u32 v200, v200, v28, v29\n\t" \
  "v_max3_u32 v200, v200, v30, v31\n\t" \
  "s_waitcnt vmcnt(27)\n\t" \
  "v_mfma_f32_16x16x32_bf16 v[144:147], a[64:67], v[32:35], v[144:147]\n\t" \
  "ds_read_b128 v[168:171], v189 offset:10240\n\t" \
  "v_mfma_f32_16x16x32_bf16 v[148:151], a[68:71], v[32:35], v[148:151]\n\t" \
  "s_waitcnt lgkmcnt(2)\n\t" \
  "v_mfma_f32_16x16x32_bf16 v[152:155], v[172:175], v[32:35], v[152:155]\n\t" \
  "v_max3_u32 v200, v200, v32, v33\n\t" \
  "v_max3_u32 v200, v200, v34, v35\n\t" \
  "s_waitcnt vmcnt(26)\n\t" \
  "v_mfma_f32_16x16x32_bf16 v[144:147], a[72:75], v[36:39], v[144:147]\n\t" \
  "ds_read_b128 v[172:175], v189 offset:11264\n\t" \
  "v_mfma_f32_16x16x32_bf16 v[148:151], a[76:79], v[36:39], v[148:151]\n\t" \
  "s_waitcnt lgkmcnt(2)\n\t" \
  "v_mfma_f32_16x16x32_bf16 v[152:155], v[164:167], v[36:39], v[152:155]\n\t" \
  "v_max3_u32 v200, v200, v36, v37\n\t" \
  "v_max3_u32 v200, v200, v38, v39\n\t" \
  "s_waitcnt vmcnt(25)\n\t" \
  "v_mfma_f32_16x16x32_bf16 v[144:147], a[80:83], v[40:43], v[144:147]\n\t" \
  "ds_read_b128 v[164:167], v189 offset:12288\n\t" \
  "v_mfma_f32_16x16x32_bf16 v[148:151], a[84:87], v[40:43], v[148:151]\n\t" \
  "s_waitcnt lgkmcnt(2)\n\t" \
  "v_mfma_f32_16x16x32_bf16 v[152:155], v[168:171], v[40:43], v[152:155]\n\t" \
  "v_max3_u32 v200, v200, v40, v41\n\t" \
  "v_max3_u32 v200, v200, v42, v43\n\t" \
  "s_waitcnt vmcnt(24)\n\t" \
  "v_mfma_f32_16x16x32_bf16 v[144:147], a[88:91], v[44:47], v[144:147]\n\t" \
  "ds_read_b128 v[168:171], v189 offset:13312\n\t" \
  "v_mfma_f32_16x16x32_bf16 v[148:151], a[92:95], v[44:47], v[148:151]\n\t" \
  "s_waitcnt lgkmcnt(2)\n\t" \
  "v_mfma_f32_16x16x32_bf16 v[152:155], v[172:175], v[44:47], v[152:155]\n\t" \
  "v_max3_u32 v200, v200, v44, v45\n\t" \
  "v_max3_u32 v200, v200, v46, v47\n\t" \
  "s_waitcnt vmcnt(23)\n\t" \
  "v_mfma_f32_16x16x32_bf16 v[144:147], a[96:99], v[48:51], v[144:147]\n\t" \
  "ds_read_b128 v[172:175], v189 offset:14336\n\t" \
  "v_mfma_f32_16x16x32_bf16 v[148:151], a[100:103], v[48:51], v[148:151]\n\t" \
  "s_waitcnt lgkmcnt(2)\n\t" \
  "v_mfma_f32_16x16x32_bf16 v[152:155], v[164:167], v[48:51], v[152:155]\n\t" \
  "v_max3_u32 v200, v200, v48, v49\n\t" \
  "v_max3_u32 v200, v200, v50, v51\n\t" \
  "s_waitcnt vmcnt(22)\n\t" \
  "v_mfma_f32_16x16x32_bf16 v[144:147], a[104:107], v[52:55], v[144:147]\n\t" \
  "ds_read_b128 v[164:167], v189 offset:15360\n\t" \
  "v_mfma_f32_16x16x32_bf16 v[148:151], a[108:111], v[52:55], v[148:151]\n\t" \
  "s_waitcnt lgkmcnt(2)\n\t" \
  "v_mfma_f32_16x16x32_bf16 v[152:155], v[168:171], v[52:55], v[152:155]\n\t" \
  "v_max3_u32 v200, v200, v52, v53\n\t" \
  "v_max3_u32 v200, v200, v54, v55\n\t" \
  "s_waitcnt vmcnt(21)\n\t" \
  "v_mfma_f32_16x16x32_bf16 v[144:147], a[112:115], v[56:59], v[144:147]\n\t" \
  "ds_read_b128 v[168:171], v189 offset:16384\n\t" \
  "v_mfma_f32_16x16x32_bf16 v[148:151], a[116:119], v[56:59], v[148:151]\n\t" \
  "s_waitcnt lgkmcnt(2)\n\t" \
  "v_mfma_f32_16x16x32_bf16 v[152:155], v[172:175], v[56:59], v[152:155]\n\t" \
  "v_max3_u32 v200, v200, v56, v57\n\t" \
  "v_max3_u32 v200, v200, v58, v59\n\t" \
  "s_waitcnt vmcnt(20)\n\t" \
  "v_mfma_f32_16x16x32_bf16 v[144:147], a[120:123], v[60:63], v[144:147]\n\t" \
  "ds_read_b128 v[172:175], v189 offset:17408\n\t" \
  "v_mfma_f32_16x16x32_bf16 v[148:151], a[124:127], v[60:63], v[148:151]\n\t" \
  "s_waitcnt lgkmcnt(2)\n\t" \
  "v_mfma_f32_16x16x32_bf16 v[152:155], v[164:167], v[60:63], v[152:155]\n\t" \
  "v_max3_u32 v200, v200, v60, v61\n\t" \
  "v_max3_u32 v200, v200, v62, v63\n\t" \
  "s_waitcnt vmcnt(19)\n\t" \
  "v_mfma_f32_16x16x32_bf16 v[144:147], a[128:131], v[64:67], v[144:147]\n\t" \
  "ds_read_b128 v[164:167], v189 offset:18432\n\t" \
  "v_mfma_f32_16x16x32_bf16 v[148:151], a[132:135], v[64:67], v[148:151]\n\t" \
  "s_waitcnt lgkmcnt(2)\n\t" \
  "v_mfma_f32_16x16x32_bf16 v[152:155], v[168:171], v[64:67], v[152:155]\n\t" \
  "v_max3_u32 v200, v200, v64, v65\n\t" \
  "v_max3_u32 v200, v200, v66, v67\n\t" \
  "s_waitcnt vmcnt(18)\n\t" \
  "v_mfma_f32_16x16x32_bf16 v[144:147], a[136:139], v[68:71], v[144:147]\n\t" \
  "ds_read_b128 v[168:171], v189 offset:19456\n\t" \
  "v_mfma_f32_16x16x32_bf16 v[148:151], a[140:143], v[68:71], v[148:151]\n\t" \
  "s_waitcnt lgkmcnt(2)\n\t" \
  "v_mfma_f32_16x16x32_bf16 v[152:155], v[172:175], v[68:71], v[152:155]\n\t" \
  "v_max3_u32 v200, v200, v68, v69\n\t" \
  "v_max3_u32 v200, v200, v70, v71\n\t" \
  "s_waitcnt vmcnt(17)\n\t" \
  "v_mfma_f32_16x16x32_bf16 v[144:147], a[144:147], v[72:75], v[144:147]\n\t" \
  "ds_read_b128 v[172:175], v189 offset:20480\n\t" \
  "v_mfma_f32_16x16x32_bf16 v[148:151], a[148:151], v[72:75], v[148:151]\n\t" \
  "s_waitcnt lgkmcnt(2)\n\t" \
  "v_mfma_f32_16x16x32_bf16 v[152:155], v[164:167], v[72:75], v[152:155]\n\t" \
  "v_max3_u32 v200, v200, v72, v73\n\t" \
  "v_max3_u32 v200, v200, v74, v75\n\t" \
  "s_waitcnt vmcnt(16)\n\t" \
  "v_mfma_f32_16x16x32_bf16 v[144:147], a[152:155], v[76:79], v[144:147]\n\t" \
  "ds_read_b128 v[164:167], v189 offset:21504\n\t" \
  "v_mfma_f32_16x16x32_bf16 v[148:151], a[156:159], v[76:79], v[148:151]\n\t" \
  "s_waitcnt lgkmcnt(2)\n\t" \
  "v_mfma_f32_16x16x32_bf16 v[152:155], v[168:171], v[76:79], v[152:155]\n\t" \
  "v_max3_u32 v200, v200, v76, v77\n\t" \
  "v_max3_u32 v200, v200, v78, v79\n\t" \
  "s_waitcnt vmcnt(15)\n\t" \
  "v_mfma_f32_16x16x32_bf16 v[144:147], a[160:163], v[80:83], v[144:147]\n\t" \
  "ds_read_b128 v[168:171], v189 offset:22528\n\t" \
  "v_mfma_f32_16x16x32_bf16 v[148:151], a[164:167], v[80:83], v[148:151]\n\t" \
  "s_waitcnt lgkmcnt(2)\n\t" \
  "v_mfma_f32_16x16x32_bf16 v[152:155], v[172:175], v[80:83], v[152:155]\n\t" \
  "v_max3_u32 v200, v200, v80, v81\n\t" \
  "v_max3_u32 v200, v200, v82, v83\n\t" \
  "s_waitcnt vmcnt(14)\n\t" \
  "v_mfma_f32_16x16x32_bf16 v[144:147], a[168:171], v[84:87], v[144:147]\n\t" \
  "ds_read_b128 v[172:175], v189 offset:23552\n\t" \
  "v_mfma_f32_16x16x32_bf16 v[148:151], a[172:175], v[84:87], v[148:151]\n\t" \
  "s_waitcnt lgkmcnt(2)\n\t" \
  "v_mfma_f32_16x16x32_bf16 v[152:155], v[164:167], v[84:87], v[152:155]\n\t" \
  "v_max3_u32 v200, v200, v84, v85\n\t" \
  "v_max3_u32 v200, v200, v86, v87\n\t" \
  "s_waitcnt vmcnt(13)\n\t" \
  "v_mfma_f32_16x16x32_bf16 v[144:147], a[176:179], v[88:91], v[144:147]\n\t" \
  "ds_read_b128 v[164:167], v189 offset:24576\n\t" \
  "v_mfma_f32_16x16x32_bf16 v[148:151], a[180:183], v[88:91], v[148:151]\n\t" \
  "s_waitcnt lgkmcnt(2)\n\t" \
  "v_mfma_f32_16x16x32_bf16 v[152:155], v[168:171], v[88:91], v[152:155]\n\t" \
  "v_max3_u32 v200, v200, v88, v89\n\t" \
  "v_max3_u32 v200, v200, v90, v91\n\t" \
  "s_waitcnt vmcnt(12)\n\t" \
  "v_mfma_f32_16x16x32_bf16 v[144:147], a[184:187], v[92:95], v[144:147]\n\t" \
  "ds_read_b128 v[168:171], v189 offset:25600\n\t" \
  "v_mfma_f32_16x16x32_bf16 v[148:151], a[188:191], v[92:95], v[148:151]\n\t" \
  "s_waitcnt lgkmcnt(2)\n\t" \
  "v_mfma_f32_16x16x32_bf16 v[152:155], v[172:175], v[92:95], v[152:155]\n\t" \
  "v_max3_u32 v200, v200, v92, v93\n\t" \
  "v_max3_u32 v200, v200, v94, v95\n\t" \
  "s_waitcnt vmcnt(11)\n\t" \
  "v_mfma_f32_16x16x32_bf16 v[144:147], a[192:195], v[96:99], v[144:147]\n\t" \
  "ds_read_b128 v[172:175], v189 offset:26624\n\t" \
  "v_mfma_f32_16x16x32_bf16 v[148:151], a[196:199], v[96:99], v[148:151]\n\t" \
  "s_waitcnt lgkmcnt(2)\n\t" \
  "v_mfma_f32_16x16x32_bf16 v[152:155], v[164:167], v[96:99], v[152:155]\n\t" \
  "v_max3_u32 v200, v200, v96, v97\n\t" \
  "v_max3_u32 v200, v200, v98, v99\n\t" \
  "s_waitcnt vmcnt(10)\n\t" \
  "v_mfma_f32_16x16x32_bf16 v[144:147], a[200:203], v[100:103], v[144:147]\n\t" \
  "ds_read_b128 v[164:167], v189 offset:27648\n\t" \
  "v_mfma_f32_16x16x32_bf16 v[148:151], a[204:207], v[100:103], v[148:151]\n\t" \
  "s_waitcnt lgkmcnt(2)\n\t" \
  "v_mfma_f32_16x16x32_bf16 v[152:155], v[168:171], v[100:103], v[152:155]\n\t" \
  "v_max3_u32 v200, v200, v100, v101\n\t" \
  "v_max3_u32 v200, v200, v102, v103\n\t" \
  "s_waitcnt vmcnt(9)\n\t" \
  "v_mfma_f32_16x16x32_bf16 v[144:147], a[208:211], v[104:107], v[144:147]\n\t" \
  "ds_read_b128 v[168:171], v189 offset:28672\n\t" \
  "v_mfma_f32_16x16x32_bf16 v[148:151], a[212:215], v[104:107], v[148:151]\n\t" \
  "s_waitcnt lgkmcnt(2)\n\t" \
  "v_mfma_f32_16x16x32_bf16 v[152:155], v[172:175], v[104:107], v[152:155]\n\t" \
  "v_max3_u32 v200, v200, v104, v105\n\t" \
  "v_max3_u32 v200, v200, v106, v107\n\t" \
  "s_waitcnt vmcnt(8)\n\t" \
  "v_mfma_f32_16x16x32_bf16 v[144:147], a[216:219], v[108:111], v[144:147]\n\t" \
  "ds_read_b128 v[172:175], v189 offset:29696\n\t" \
  "v_mfma_f32_16x16x32_bf16 v[148:151], a[220:223], v[108:111], v[148:151]\n\t" \
  "s_waitcnt lgkmcnt(2)\n\t" \
  "v_mfma_f32_16x16x32_bf16 v[152:155], v[164:167], v[108:111], v[152:155]\n\t" \
  "v_max3_u32 v200, v200, v108, v109\n\t" \
  "v_max3_u32 v200, v200, v110, v111\n\t" \
  "s_waitcnt vmcnt(7)\n\t" \
  "v_mfma_f32_16x16x32_bf16 v[144:147], a[224:227], v[112:115], v[144:147]\n\t" \
  "ds_read_b128 v[164:167], v189 offset:30720\n\t" \
  "v_mfma_f32_16x16x32_bf16 v[148:151], a[228:231], v[112:115], v[148:151]\n\t" \
  "s_waitcnt lgkmcnt(2)\n\t" \
  "v_mfma_f32_16x16x32_bf16 v[152:155], v[168:171], v[112:115], v[152:155]\n\t" \
  "v_max3_u32 v200, v200, v112, v113\n\t" \
  "v_max3_u32 v200, v200, v114, v115\n\t" \
  "s_waitcnt vmcnt(6)\n\t" \
  "v_mfma_f32_16x16x32_bf16 v[144:147], a[232:235], v[116:119], v[144:147]\n\t" \
  "ds_read_b128 v[168:171], v189 offset:31744\n\t" \
  "v_mfma_f32_16x16x32_bf16 v[148:151], a[236:239], v[116:119], v[148:151]\n\t" \
  "s_waitcnt lgkmcnt(2)\n\t" \
  "v_mfma_f32_16x16x32_bf16 v[152:155], v[172:175], v[116:119], v[152:155]\n\t" \
  "v_max3_u32 v200, v200, v116, v117\n\t" \
  "v_max3_u32 v200, v200, v118, v119\n\t" \
  "s_waitcnt vmcnt(5)\n\t" \
  "v_mfma_f32_16x16x32_bf16 v[144:147], a[240:243], v[120:123], v[144:147]\n\t" \
  "v_mfma_f32_16x16x32_bf16 v[148:151], a[244:247], v[120:123], v[148:151]\n\t" \
  "s_waitcnt lgkmcnt(1)\n\t" \
  "v_mfma_f32_16x16x32_bf16 v[152:155], v[164:167], v[120:123], v[152:155]\n\t" \
  "v_max3_u32 v200, v200, v120, v121\n\t" \
  "v_max3_u32 v200, v200, v122, v123\n\t" \
  "s_waitcnt vmcnt(4)\n\t" \
  "v_mfma_f32_16x16x32_bf16 v[144:147], a[248:251], v[124:127], v[144:147]\n\t" \
  "v_mfma_f32_16x16x32_bf16 v[148:151], a[252:255], v[124:127], v[148:151]\n\t" \
  "s_waitcnt lgkmcnt(0)\n\t" \
  "v_mfma_f32_16x16x32_bf16 v[152:155], v[168:171], v[124:127], v[152:155]\n\t" \
  "v_max3_u32 v200, v200, v124, v125\n\t" \
  "v_max3_u32 v200, v200, v126, v127\n\t" \
  "s_waitcnt vmcnt(3)\n\t" \
  "v_mfma_f32_16x16x32_bf16 v[144:147], v[224:227], v[128:131], v[144:147]\n\t" \
  "v_mfma_f32_16x16x32_bf16 v[148:151], v[228:231], v[128:131], v[148:151]\n\t" \
  "v_mfma_f32_16x16x32_bf16 v[152:155], v[208:211], v[128:131], v[152:155]\n\t" \
  "v_max3_u32 v200, v200, v128, v129\n\t" \
  "v_max3_u32 v200, v200, v130, v131\n\t" \
  "s_waitcnt vmcnt(2)\n\t" \
  "v_mfma_f32_16x16x32_bf16 v[144:147], v[232:235], v[132:135], v[144:147]\n\t" \
  "v_mfma_f32_16x16x32_bf16 v[148:151], v[236:239], v[132:135], v[148:151]\n\t" \
  "v_mfma_f32_16x16x32_bf16 v[152:155], v[212:215], v[132:135], v[152:155]\n\t" \
  "v_max3_u32 v200, v200, v132, v133\n\t" \
  "v_max3_u32 v200, v200, v134, v135\n\t" \
  "s_waitcnt vmcnt(1)\n\t" \
  "v_mfma_f32_16x16x32_bf16 v[144:147], v[240:243], v[136:139], v[144:147]\n\t" \
  "v_mfma_f32_16x16x32_bf16 v[148:151], v[244:247], v[136:139], v[148:151]\n\t" \
  "v_mfma_f32_16x16x32_bf16 v[152:155], v[216:219], v[136:139], v[152:155]\n\t" \
  "v_max3_u32 v200, v200, v136, v137\n\t" \
  "v_max3_u32 v200, v200, v138, v139\n\t" \
  "s_waitcnt vmcnt(0)\n\t" \
  "v_mfma_f32_16x16x32_bf16 v[144:147], v[248:251], v[140:143], v[144:147]\n\t" \
  "v_mfma_f32_16x16x32_bf16 v[148:151], v[252:255], v[140:143], v[148:151]\n\t" \
  "v_mfma_f32_16x16x32_bf16 v[152:155], v[220:223], v[140:143], v[152:155]\n\t" \
  "v_max3_u32 v200, v200, v140, v141\n\t" \
  "v_max3_u32 v200, v200, v142, v143\n\t" \
  "s_waitcnt vmcnt(0)\n\t" \
  "s_cmp_eq_u32 s61, 1\n\t" \
  "s_cbranch_scc0 Ltr_%=\n\t" \
  "v_max3_u32 v200, v200, v156, v157\n\t" \
  "v_max3_u32 v200, v200, v158, v159\n\t" \
  "v_max3_u32 v200, v200, v160, v161\n\t" \
  "Ltr_%=:\n\t" \
  "v_cmp_eq_u32 vcc, -1, v200\n\t" \
  "s_nop 0\n\t" \
  "s_cbranch_vccz Ltn_%=\n\t" \
  "s_add_u32 s71, s71, 1\n\t" \
  "s_cmp_gt_u32 s71, 4096\n\t" \
  "s_cbranch_scc1 Ltn_%=\n\t" \
  "buffer_load_dwordx4 v[0:3], v188, s[20:23], s62 offen offset:0 sc1\n\t" \
  "buffer_load_dwordx4 v[4:7], v188, s[20:23], s62 offen offset:1024 sc1\n\t" \
  "buffer_load_dwordx4 v[8:11], v188, s[20:23], s62 offen offset:2048 sc1\n\t" \
  "buffer_load_dwordx4 v[12:15], v188, s[20:23], s62 offen offset:3072 sc1\n\t" \
  "buffer_load_dwordx4 v[16:19], v188, s[20:23], s63 offen offset:0 sc1\n\t" \
  "buffer_load_dwordx4 v[20:23], v188, s[20:23], s63 offen offset:1024 sc1\n\t" \
  "buffer_load_dwordx4 v[24:27], v188, s[20:23], s63 offen offset:2048 sc1\n\t" \
  "buffer_load_dwordx4 v[28:31], v188, s[20:23], s63 offen offset:3072 sc1\n\t" \
  "buffer_load_dwordx4 v[32:35], v188, s[20:23], s64 offen offset:0 sc1\n\t" \
  "buffer_load_dwordx4 v[36:39], v188, s[20:23], s64 offen offset:1024 sc1\n\t" \
  "buffer_load_dwordx4 v[40:43], v188, s[20:23], s64 offen offset:2048 sc1\n\t" \
  "buffer_load_dwordx4 v[44:47], v188, s[20:23], s64 offen offset:3072 sc1\n\t" \
  "buffer_load_dwordx4 v[48:51], v188, s[20:23], s65 offen offset:0 sc1\n\t" \
  "buffer_load_dwordx4 v[52:55], v188, s[20:23], s65 offen offset:1024 sc1\n\t" \
  "buffer_load_dwordx4 v[56:59], v188, s[20:23], s65 offen offset:2048 sc1\n\t" \
  "buffer_load_dwordx4 v[60:63], v188, s[20:23], s65 offen offset:3072 sc1\n\t" \
  "buffer_load_dwordx4 v[64:67], v188, s[20:23], s66 offen offset:0 sc1\n\t" \
  "buffer_load_dwordx4 v[68:71], v188, s[20:23], s66 offen offset:1024 sc1\n\t" \
  "buffer_load_dwordx4 v[72:75], v188, s[20:23], s66 offen offset:2048 sc1\n\t" \
  "buffer_load_dwordx4 v[76:79], v188, s[20:23], s66 offen offset:3072 sc1\n\t" \
  "buffer_load_dwordx4 v[80:83], v188, s[20:23], s67 offen offset:0 sc1\n\t" \
  "buffer_load_dwordx4 v[84:87], v188, s[20:23], s67 offen offset:1024 sc1\n\t" \
  "buffer_load_dwordx4 v[88:91], v188, s[20:23], s67 offen offset:2048 sc1\n\t" \
  "buffer_load_dwordx4 v[92:95], v188, s[20:23], s67 offen offset:3072 sc1\n\t" \
  "buffer_load_dwordx4 v[96:99], v188, s[20:23], s68 offen offset:0 sc1\n\t" \
  "buffer_load_dwordx4 v[100:103], v188, s[20:23], s68 offen offset:1024 sc1\n\t" \
  "buffer_load_dwordx4 v[104:107], v188, s[20:23], s68 offen offset:2048 sc1\n\t" \
  "buffer_load_dwordx4 v[108:111], v188, s[20:23], s68 offen offset:3072 sc1\n\t" \
  "buffer_load_dwordx4 v[112:115], v188, s[20:23], s69 offen offset:0 sc1\n\t" \
  "buffer_load_dwordx4 v[116:119], v188, s[20:23], s69 offen offset:1024 sc1\n\t" \
  "buffer_load_dwordx4 v[120:123], v188, s[20:23], s69 offen offset:2048 sc1\n\t" \
  "buffer_load_dwordx4 v[124:127], v188, s[20:23], s69 offen offset:3072 sc1\n\t" \
  "buffer_load_dwordx4 v[128:131], v188, s[20:23], s70 offen offset:0 sc1\n\t" \
  "buffer_load_dwordx4 v[132:135], v188, s[20:23], s70 offen offset:1024 sc1\n\t" \
  "buffer_load_dwordx4 v[136:139], v188, s[20:23], s70 offen offset:2048 sc1\n\t" \
  "buffer_load_dwordx4 v[140:143], v188, s[20:23], s70 offen offset:3072 sc1\n\t" \
  "buffer_load_dwordx4 v[156:159], v188, s[20:23], s58 offen sc1\n\t" \
  "buffer_load_dwordx2 v[160:161], v195, s[20:23], s58 offen offset:1024 sc1\n\t" \
  "ds_read_b128 v[164:167], v189 offset:0\n\t" \
  "ds_read_b128 v[168:171], v189 offset:1024\n\t" \
  "s_branch Ltm_%=\n\t" \
  "Ltn_%=:\n\t" \
  "s_mul_i32 s62, s48, 147456\n\t" \
  "s_add_u32 s62, s62, s54\n\t" \
  "s_add_u32 s63, s62, 4096\n\t" \
  "s_add_u32 s64, s62, 8192\n\t" \
  "s_add_u32 s65, s62, 12288\n\t" \
  "s_add_u32 s66, s62, 16384\n\t" \
  "s_add_u32 s67, s62, 20480\n\t" \
  "s_add_u32 s68, s62, 24576\n\t" \
  "s_add_u32 s69, s62, 28672\n\t" \
  "s_add_u32 s70, s62, 32768\n\t" \
  "s_branch Lkd_%=\n\t" \
  "Lnp_%=:\n\t" \
  "s_waitcnt vmcnt(35)\n\t" \
  "v_mfma_f32_16x16x32_bf16 v[144:147], a[0:3], v[0:3], 0\n\t" \
  "ds_read_b128 v[172:175], v189 offset:2048\n\t" \
  "v_mfma_f32_16x16x32_bf16 v[148:151], a[4:7], v[0:3], 0\n\t" \
  "s_waitcnt lgkmcnt(2)\n\t" \
  "v_mfma_f32_16x16x32_bf16 v[152:155], v[164:167], v[0:3], 0\n\t" \
  "s_waitcnt vmcnt(34)\n\t" \
  "v_mfma_f32_16x16x32_bf16 v[144:147], a[8:11], v[4:7], v[144:147]\n\t" \
  "ds_read_b128 v[164:167], v189 offset:3072\n\t" \
  "v_mfma_f32_16x16x32_bf16 v[148:151], a[12:15], v[4:7], v[148:151]\n\t" \
  "s_waitcnt lgkmcnt(2)\n\t" \
  "v_mfma_f32_16x16x32_bf16 v[152:155], v[168:171], v[4:7], v[152:155]\n\t" \
  "s_waitcnt vmcnt(33)\n\t" \
  "v_mfma_f32_16x16x32_bf16 v[144:147], a[16:19], v[8:11], v[144:147]\n\t" \
  "ds_read_b128 v[168:171], v189 offset:4096\n\t" \
  "v_mfma_f32_16x16x32_bf16 v[148:151], a[20:23], v[8:11], v[148:151]\n\t" \
  "s_waitcnt lgkmcnt(2)\n\t" \
  "v_mfma_f32_16x16x32_bf16 v[152:155], v[172:175], v[8:11], v[152:155]\n\t" \
  "s_waitcnt vmcnt(32)\n\t" \
  "v_mfma_f32_16x16x32_bf16 v[144:147], a[24:27], v[12:15], v[144:147]\n\t" \
  "ds_read_b128 v[172:175], v189 offset:5120\n\t" \
  "v_mfma_f32_16x16x32_bf16 v[148:151], a[28:31], v[12:15], v[148:151]\n\t" \
  "s_waitcnt lgkmcnt(2)\n\t" \
  "v_mfma_f32_16x16x32_bf16 v[152:155], v[164:167], v[12:15], v[152:155]\n\t" \
  "s_waitcnt vmcnt(31)\n\t" \
  "v_mfma_f32_16x16x32_bf16 v[144:147], a[32:35], v[16:19], v[144:147]\n\t" \
  "ds_read_b128 v[164:167], v189 offset:6144\n\t" \
  "v_mfma_f32_16x16x32_bf16 v[148:151], a[36:39], v[16:19], v[148:151]\n\t" \
  "s_waitcnt lgkmcnt(2)\n\t" \
  "v_mfma_f32_16x16x32_bf16 v[152:155], v[168:171], v[16:19], v[152:155]\n\t" \
  "s_waitcnt vmcnt(30)\n\t" \
  "v_mfma_f32_16x16x32_bf16 v[144:147], a[40:43], v[20:23], v[144:147]\n\t" \
  "ds_read_b128 v[168:171], v189 offset:7168\n\t" \
  "v_mfma_f32_16x16x32_bf16 v[148:151], a[44:47], v[20:23], v[148:151]\n\t" \
  "s_waitcnt lgkmcnt(2)\n\t" \
  "v_mfma_f32_16x16x32_bf16 v[152:155], v[172:175], v[20:23], v[152:155]\n\t" \
  "s_waitcnt vmcnt(29)\n\t" \
  "v_mfma_f32_16x16x32_bf16 v[144:147], a[48:51], v[24:27], v[144:147]\n\t" \
  "ds_read_b128 v[172:175], v189 offset:8192\n\t" \
  "v_mfma_f32_16x16x32_bf16 v[148:151], a[52:55], v[24:27], v[148:151]\n\t" \
  "s_waitcnt lgkmcnt(2)\n\t" \
  "v_mfma_f32_16x16x32_bf16 v[152:155], v[164:167], v[24:27], v[152:155]\n\t" \
  "s_waitcnt vmcnt(28)\n\t" \
  "v_mfma_f32_16x16x32_bf16 v[144:147], a[56:59], v[28:31], v[144:147]\n\t" \
  "ds_read_b128 v[164:167], v189 offset:9216\n\t" \
  "v_mfma_f32_16x16x32_bf16 v[148:151], a[60:63], v[28:31], v[148:151]\n\t" \
  "s_waitcnt lgkmcnt(2)\n\t" \
  "v_mfma_f32_16x16x32_bf16 v[152:155], v[168:171], v[28:31], v[152:155]\n\t" \
  "s_waitcnt vmcnt(27)\n\t" \
  "v_mfma_f32_16x16x32_bf16 v[144:147], a[64:67], v[32:35], v[144:147]\n\t" \
  "ds_read_b128 v[168:171], v189 offset:10240\n\t" \
  "v_mfma_f32_16x16x32_bf16 v[148:151], a[68:71], v[32:35], v[148:151]\n\t" \
  "s_waitcnt lgkmcnt(2)\n\t" \
  "v_mfma_f32_16x16x32_bf16 v[152:155], v[172:175], v[32:35], v[152:155]\n\t" \
  "s_waitcnt vmcnt(26)\n\t" \
  "v_mfma_f32_16x16x32_bf16 v[144:147], a[72:75], v[36:39], v[144:147]\n\t" \
  "ds_read_b128 v[172:175], v189 offset:11264\n\t" \
  "v_mfma_f32_16x16x32_bf16 v[148:151], a[76:79], v[36:39], v[148:151]\n\t" \
  "s_waitcnt lgkmcnt(2)\n\t" \
  "v_mfma_f32_16x16x32_bf16 v[152:155], v[164:167], v[36:39], v[152:155]\n\t" \
  "s_waitcnt vmcnt(25)\n\t" \
  "v_mfma_f32_16x16x32_bf16 v[144:147], a[80:83], v[40:43], v[144:147]\n\t" \
  "ds_read_b128 v[164:167], v189 offset:12288\n\t" \
  "v_mfma_f32_16x16x32_bf16 v[148:151], a[84:87], v[40:43], v[148:151]\n\t" \
  "s_waitcnt lgkmcnt(2)\n\t" \
  "v_mfma_f32_16x16x32_bf16 v[152:155], v[168:171], v[40:43], v[152:155]\n\t" \
  "s_waitcnt vmcnt(24)\n\t" \
  "v_mfma_f32_16x16x32_bf16 v[144:147], a[88:91], v[44:47], v[144:147]\n\t" \
  "ds_read_b128 v[168:171], v189 offset:13312\n\t" \
  "v_mfma_f32_16x16x32_bf16 v[148:151], a[92:95], v[44:47], v[148:151]\n\t" \
  "s_waitcnt lgkmcnt(2)\n\t" \
  "v_mfma_f32_16x16x32_bf16 v[152:155], v[172:175], v[44:47], v[152:155]\n\t" \
  "s_waitcnt vmcnt(23)\n\t" \
  "v_mfma_f32_16x16x32_bf16 v[144:147], a[96:99], v[48:51], v[144:147]\n\t" \
  "ds_read_b128 v[172:175], v189 offset:14336\n\t" \
  "v_mfma_f32_16x16x32_bf16 v[148:151], a[100:103], v[48:51], v[148:151]\n\t" \
  "s_waitcnt lgkmcnt(2)\n\t" \
  "v_mfma_f32_16x16x32_bf16 v[152:155], v[164:167], v[48:51], v[152:155]\n\t" \
  "s_waitcnt vmcnt(22)\n\t" \
  "v_mfma_f32_16x16x32_bf16 v[144:147], a[104:107], v[52:55], v[144:147]\n\t" \
  "ds_read_b128 v[164:167], v189 offset:15360\n\t" \
  "v_mfma_f32_16x16x32_bf16 v[148:151], a[108:111], v[52:55], v[148:151]\n\t" \
  "s_waitcnt lgkmcnt(2)\n\t" \
  "v_mfma_f32_16x16x32_bf16 v[152:155], v[168:171], v[52:55], v[152:155]\n\t" \
  "s_waitcnt vmcnt(21)\n\t" \
  "v_mfma_f32_16x16x32_bf16 v[144:147], a[112:115], v[56:59], v[144:147]\n\t" \
  "ds_read_b128 v[168:171], v189 offset:16384\n\t" \
  "v_mfma_f32_16x16x32_bf16 v[148:151], a[116:119], v[56:59], v[148:151]\n\t" \
  "s_waitcnt lgkmcnt(2)\n\t" \
  "v_mfma_f32_16x16x32_bf16 v[152:155], v[172:175], v[56:59], v[152:155]\n\t" \
  "s_waitcnt vmcnt(20)\n\t" \
  "v_mfma_f32_16x16x32_bf16 v[144:147], a[120:123], v[60:63], v[144:147]\n\t" \
  "ds_read_b128 v[172:175], v189 offset:17408\n\t" \
  "v_mfma_f32_16x16x32_bf16 v[148:151], a[124:127], v[60:63], v[148:151]\n\t" \
  "s_waitcnt lgkmcnt(2)\n\t" \
  "v_mfma_f32_16x16x32_bf16 v[152:155], v[164:167], v[60:63], v[152:155]\n\t" \
  "s_waitcnt vmcnt(19)\n\t" \
  "v_mfma_f32_16x16x32_bf16 v[144:147], a[128:131], v[64:67], v[144:147]\n\t" \
  "ds_read_b128 v[164:167], v189 offset:18432\n\t" \
  "v_mfma_f32_16x16x32_bf16 v[148:151], a[132:135], v[64:67], v[148:151]\n\t" \
  "s_waitcnt lgkmcnt(2)\n\t" \
  "v_mfma_f32_16x16x32_bf16 v[152:155], v[168:171], v[64:67], v[152:155]\n\t" \
  "s_waitcnt vmcnt(18)\n\t" \
  "v_mfma_f32_16x16x32_bf16 v[144:147], a[136:139], v[68:71], v[144:147]\n\t" \
  "ds_read_b128 v[168:171], v189 offset:19456\n\t" \
  "v_mfma_f32_16x16x32_bf16 v[148:151], a[140:143], v[68:71], v[148:151]\n\t" \
  "s_waitcnt lgkmcnt(2)\n\t" \
  "v_mfma_f32_16x16x32_bf16 v[152:155], v[172:175], v[68:71], v[152:155]\n\t" \
  "s_waitcnt vmcnt(17)\n\t" \
  "v_mfma_f32_16x16x32_bf16 v[144:147], a[144:147], v[72:75], v[144:147]\n\t" \
  "ds_read_b128 v[172:175], v189 offset:20480\n\t" \
  "v_mfma_f32_16x16x32_bf16 v[148:151], a[148:151], v[72:75], v[148:151]\n\t" \
  "s_waitcnt lgkmcnt(2)\n\t" \
  "v_mfma_f32_16x16x32_bf16 v[152:155], v[164:167], v[72:75], v[152:155]\n\t" \
  "s_waitcnt vmcnt(16)\n\t" \
  "v_mfma_f32_16x16x32_bf16 v[144:147], a[152:155], v[76:79], v[144:147]\n\t" \
  "ds_read_b128 v[164:167], v189 offset:21504\n\t" \
  "v_mfma_f32_16x16x32_bf16 v[148:151], a[156:159], v[76:79], v[148:151]\n\t" \
  "s_waitcnt lgkmcnt(2)\n\t" \
  "v_mfma_f32_16x16x32_bf16 v[152:155], v[168:171], v[76:79], v[152:155]\n\t" \
  "s_waitcnt vmcnt(15)\n\t" \
  "v_mfma_f32_16x16x32_bf16 v[144:147], a[160:163], v[80:83], v[144:147]\n\t" \
  "ds_read_b128 v[168:171], v189 offset:22528\n\t" \
  "v_mfma_f32_16x16x32_bf16 v[148:151], a[164:167], v[80:83], v[148:151]\n\t" \
  "s_waitcnt lgkmcnt(2)\n\t" \
  "v_mfma_f32_16x16x32_bf16 v[152:155], v[172:175], v[80:83], v[152:155]\n\t" \
  "s_waitcnt vmcnt(14)\n\t" \
  "v_mfma_f32_16x16x32_bf16 v[144:147], a[168:171], v[84:87], v[144:147]\n\t" \
  "ds_read_b128 v[172:175], v189 offset:23552\n\t" \
  "v_mfma_f32_16x16x32_bf16 v[148:151], a[172:175], v[84:87], v[148:151]\n\t" \
  "s_waitcnt lgkmcnt(2)\n\t" \
  "v_mfma_f32_16x16x32_bf16 v[152:155], v[164:167], v[84:87], v[152:155]\n\t" \
  "s_waitcnt vmcnt(13)\n\t" \
  "v_mfma_f32_16x16x32_bf16 v[144:147], a[176:179], v[88:91], v[144:147]\n\t" \
  "ds_read_b128 v[164:167], v189 offset:24576\n\t" \
  "v_mfma_f32_16x16x32_bf16 v[148:151], a[180:183], v[88:91], v[148:151]\n\t" \
  "s_waitcnt lgkmcnt(2)\n\t" \
  "v_mfma_f32_16x16x32_bf16 v[152:155], v[168:171], v[88:91], v[152:155]\n\t" \
  "s_waitcnt vmcnt(12)\n\t" \
  "v_mfma_f32_16x16x32_bf16 v[144:147], a[184:187], v[92:95], v[144:147]\n\t" \
  "ds_read_b128 v[168:171], v189 offset:25600\n\t" \
  "v_mfma_f32_16x16x32_bf16 v[148:151], a[188:191], v[92:95], v[148:151]\n\t" \
  "s_waitcnt lgkmcnt(2)\n\t" \
  "v_mfma_f32_16x16x32_bf16 v[152:155], v[172:175], v[92:95], v[152:155]\n\t" \
  "s_waitcnt vmcnt(11)\n\t" \
  "v_mfma_f32_16x16x32_bf16 v[144:147], a[192:195], v[96:99], v[144:147]\n\t" \
  "ds_read_b128 v[172:175], v189 offset:26624\n\t" \
  "v_mfma_f32_16x16x32_bf16 v[148:151], a[196:199], v[96:99], v[148:151]\n\t" \
  "s_waitcnt lgkmcnt(2)\n\t" \
  "v_mfma_f32_16x16x32_bf16 v[152:155], v[164:167], v[96:99], v[152:155]\n\t" \
  "s_waitcnt vmcnt(10)\n\t" \
  "v_mfma_f32_16x16x32_bf16 v[144:147], a[200:203], v[100:103], v[144:147]\n\t" \
  "ds_read_b128 v[164:167], v189 offset:27648\n\t" \
  "v_mfma_f32_16x16x32_bf16 v[148:151], a[204:207], v[100:103], v[148:151]\n\t" \
  "s_waitcnt lgkmcnt(2)\n\t" \
  "v_mfma_f32_16x16x32_bf16 v[152:155], v[168:171], v[100:103], v[152:155]\n\t" \
  "s_waitcnt vmcnt(9)\n\t" \
  "v_mfma_f32_16x16x32_bf16 v[144:147], a[208:211], v[104:107], v[144:147]\n\t" \
  "ds_read_b128 v[168:171], v189 offset:28672\n\t" \
  "v_mfma_f32_16x16x32_bf16 v[148:151], a[212:215], v[104:107], v[148:151]\n\t" \
  "s_waitcnt lgkmcnt(2)\n\t" \
  "v_mfma_f32_16x16x32_bf16 v[152:155], v[172:175], v[104:107], v[152:155]\n\t" \
  "s_waitcnt vmcnt(8)\n\t" \
  "v_mfma_f32_16x16x32_bf16 v[144:147], a[216:219], v[108:111], v[144:147]\n\t" \
  "ds_read_b128 v[172:175], v189 offset:29696\n\t" \
  "v_mfma_f32_16x16x32_bf16 v[148:151], a[220:223], v[108:111], v[148:151]\n\t" \
  "s_waitcnt lgkmcnt(2)\n\t" \
  "v_mfma_f32_16x16x32_bf16 v[152:155], v[164:167], v[108:111], v[152:155]\n\t" \
  "s_waitcnt vmcnt(7)\n\t" \
  "v_mfma_f32_16x16x32_bf16 v[144:147], a[224:227], v[112:115], v[144:147]\n\t" \
  "ds_read_b128 v[164:167], v189 offset:30720\n\t" \
  "v_mfma_f32_16x16x32_bf16 v[148:151], a[228:231], v[112:115], v[148:151]\n\t" \
  "s_waitcnt lgkmcnt(2)\n\t" \
  "v_mfma_f32_16x16x32_bf16 v[152:155], v[168:171], v[112:115], v[152:155]\n\t" \
  "s_waitcnt vmcnt(6)\n\t" \
  "v_mfma_f32_16x16x32_bf16 v[144:147], a[232:235], v[116:119], v[144:147]\n\t" \
  "ds_read_b128 v[168:171], v189 offset:31744\n\t" \
  "v_mfma_f32_16x16x32_bf16 v[148:151], a[236:239], v[116:119], v[148:151]\n\t" \
  "s_waitcnt lgkmcnt(2)\n\t" \
  "v_mfma_f32_16x16x32_bf16 v[152:155], v[172:175], v[116:119], v[152:155]\n\t" \
  "s_waitcnt vmcnt(5)\n\t" \
  "v_mfma_f32_16x16x32_bf16 v[144:147], a[240:243], v[120:123], v[144:147]\n\t" \
  "v_mfma_f32_16x16x32_bf16 v[148:151], a[244:247], v[120:123], v[148:151]\n\t" \
  "s_waitcnt lgkmcnt(1)\n\t" \
  "v_mfma_f32_16x16x32_bf16 v[152:155], v[164:167], v[120:123], v[152:155]\n\t" \
  "s_waitcnt vmcnt(4)\n\t" \
  "v_mfma_f32_16x16x32_bf16 v[144:147], a[248:251], v[124:127], v[144:147]\n\t" \
  "v_mfma_f32_16x16x32_bf16 v[148:151], a[252:255], v[124:127], v[148:151]\n\t" \
  "s_waitcnt lgkmcnt(0)\n\t" \
  "v_mfma_f32_16x16x32_bf16 v[152:155], v[168:171], v[124:127], v[152:155]\n\t" \
  "s_waitcnt vmcnt(3)\n\t" \
  "v_mfma_f32_16x16x32_bf16 v[144:147], v[224:227], v[128:131], v[144:147]\n\t" \
  "v_mfma_f32_16x16x32_bf16 v[148:151], v[228:231], v[128:131], v[148:151]\n\t" \
  "v_mfma_f32_16x16x32_bf16 v[152:155], v[208:211], v[128:131], v[152:155]\n\t" \
  "s_waitcnt vmcnt(2)\n\t" \
  "v_mfma_f32_16x16x32_bf16 v[144:147], v[232:235], v[132:135], v[144:147]\n\t" \
  "v_mfma_f32_16x16x32_bf16 v[148:151], v[236:239], v[132:135], v[148:151]\n\t" \
  "v_mfma_f32_16x16x32_bf16 v[152:155], v[212:215], v[132:135], v[152:155]\n\t" \
  "s_waitcnt vmcnt(1)\n\t" \
  "v_mfma_f32_16x16x32_bf16 v[144:147], v[240:243], v[136:139], v[144:147]\n\t" \
  "v_mfma_f32_16x16x32_bf16 v[148:151], v[244:247], v[136:139], v[148:151]\n\t" \
  "v_mfma_f32_16x16x32_bf16 v[152:155], v[216:219], v[136:139], v[152:155]\n\t" \
  "s_waitcnt vmcnt(0)\n\t" \
  "v_mfma_f32_16x16x32_bf16 v[144:147], v[248:251], v[140:143], v[144:147]\n\t" \
  "v_mfma_f32_16x16x32_bf16 v[148:151], v[252:255], v[140:143], v[148:151]\n\t" \
  "v_mfma_f32_16x16x32_bf16 v[152:155], v[220:223], v[140:143], v[152:155]\n\t" \
  "s_waitcnt vmcnt(0)\n\t" \
  "Lkd_%=:\n\t" \
  "s_and_b32 s41, s51, 1\n\t" \
  "s_mul_i32 s41, s41, 12288\n\t" \
  "v_add_u32 v162, s41, v196\n\t" \
  "s_mov_b32 s60, s50\n\t" \
  "s_nop 3\n\t" \
  "ds_write_b128 v162, v[144:147] offset:0\n\t" \
  "ds_write_b128 v162, v[148:151] offset:1024\n\t" \
  "ds_write_b128 v162, v[152:155] offset:2048\n\t" \
  "s_cmp_eq_u32 s59, 1\n\t" \
  "s_cbranch_scc0 Lne_%=\n\t" \
  "s_nop 1\n\t" \
  "v_min_u32_dpp v191, v191, v191 row_ror:1 row_mask:0xf bank_mask:0xf\n\t" \
  "s_nop 1\n\t" \
  "v_min_u32_dpp v191, v191, v191 row_ror:2 row_mask:0xf bank_mask:0xf\n\t" \
  "s_nop 1\n\t" \
  "v_min_u32_dpp v191, v191, v191 row_ror:4 row_mask:0xf bank_mask:0xf\n\t" \
  "s_nop 1\n\t" \
  "v_min_u32_dpp v191, v191, v191 row_ror:8 row_mask:0xf bank_mask:0xf\n\t" \
  "s_nop 1\n\t" \
  "v_min_u32_dpp v191, v191, v191 row_bcast:15 row_mask:0xa bank_mask:0xf\n\t" \
  "s_nop 1\n\t" \
  "v_min_u32_dpp v191, v191, v191 row_bcast:31 row_mask:0xc bank_mask:0xf\n\t" \
  "s_nop 0\n\t" \
  "v_readlane_b32 s44, v191, 63\n\t" \
  "s_nop 1\n\t" \
  "Lne_%=:\n\t" \
  "s_cmp_eq_u32 s50, 1\n\t" \
  "s_cbranch_scc1 Lnb_%=\n\t" \
  "s_cmp_gt_u32 s44, s43\n\t" \
  "s_cbranch_scc0 Lnb_%=\n\t" \
  "buffer_load_dwordx4 v[0:3], v188, s[20:23], s62 offen offset:0 sc1\n\t" \
  "buffer_load_dwordx4 v[4:7], v188, s[20:23], s62 offen offset:1024 sc1\n\t" \
  "buffer_load_dwordx4 v[8:11], v188, s[20:23], s62 offen offset:2048 sc1\n\t" \
  "buffer_load_dwordx4 v[12:15], v188, s[20:23], s62 offen offset:3072 sc1\n\t" \
  "buffer_load_dwordx4 v[16:19], v188, s[20:23], s63 offen offset:0 sc1\n\t" \
  "buffer_load_dwordx4 v[20:23], v188, s[20:23], s63 offen offset:1024 sc1\n\t" \
  "buffer_load_dwordx4 v[24:27], v188, s[20:23], s63 offen offset:2048 sc1\n\t" \
  "buffer_load_dwordx4 v[28:31], v188, s[20:23], s63 offen offset:3072 sc1\n\t" \
  "buffer_load_dwordx4 v[32:35], v188, s[20:23], s64 offen offset:0 sc1\n\t" \
  "buffer_load_dwordx4 v[36:39], v188, s[20:23], s64 offen offset:1024 sc1\n\t" \
  "buffer_load_dwordx4 v[40:43], v188, s[20:23], s64 offen offset:2048 sc1\n\t" \
  "buffer_load_dwordx4 v[44:47], v188, s[20:23], s64 offen offset:3072 sc1\n\t" \
  "buffer_load_dwordx4 v[48:51], v188, s[20:23], s65 offen offset:0 sc1\n\t" \
  "buffer_load_dwordx4 v[52:55], v188, s[20:23], s65 offen offset:1024 sc1\n\t" \
  "buffer_load_dwordx4 v[56:59], v188, s[20:23], s65 offen offset:2048 sc1\n\t" \
  "buffer_load_dwordx4 v[60:63], v188, s[20:23], s65 offen offset:3072 sc1\n\t" \
  "buffer_load_dwordx4 v[64:67], v188, s[20:23], s66 offen offset:0 sc1\n\t" \
  "buffer_load_dwordx4 v[68:71], v188, s[20:23], s66 offen offset:1024 sc1\n\t" \
  "buffer_load_dwordx4 v[72:75], v188, s[20:23], s66 offen offset:2048 sc1\n\t" \
  "buffer_load_dwordx4 v[76:79], v188, s[20:23], s66 offen offset:3072 sc1\n\t" \
  "buffer_load_dwordx4 v[80:83], v188, s[20:23], s67 offen offset:0 sc1\n\t" \
  "buffer_load_dwordx4 v[84:87], v188, s[20:23], s67 offen offset:1024 sc1\n\t" \
  "buffer_load_dwordx4 v[88:91], v188, s[20:23], s67 offen offset:2048 sc1\n\t" \
  "buffer_load_dwordx4 v[92:95], v188, s[20:23], s67 offen offset:3072 sc1\n\t" \
  "buffer_load_dwordx4 v[96:99], v188, s[20:23], s68 offen offset:0 sc1\n\t" \
  "buffer_load_dwordx4 v[100:103], v188, s[20:23], s68 offen offset:1024 sc1\n\t" \
  "buffer_load_dwordx4 v[104:107], v188, s[20:23], s68 offen offset:2048 sc1\n\t" \
  "buffer_load_dwordx4 v[108:111], v188, s[20:23], s68 offen offset:3072 sc1\n\t" \
  "buffer_load_dwordx4 v[112:115], v188, s[20:23], s69 offen offset:0 sc1\n\t" \
  "buffer_load_dwordx4 v[116:119], v188, s[20:23], s69 offen offset:1024 sc1\n\t" \
  "buffer_load_dwordx4 v[120:123], v188, s[20:23], s69 offen offset:2048 sc1\n\t" \
  "buffer_load_dwordx4 v[124:127], v188, s[20:23], s69 offen offset:3072 sc1\n\t" \
  "buffer_load_dwordx4 v[128:131], v188, s[20:23], s70 offen offset:0 sc1\n\t" \
  "buffer_load_dwordx4 v[132:135], v188, s[20:23], s70 offen offset:1024 sc1\n\t" \
  "buffer_load_dwordx4 v[136:139], v188, s[20:23], s70 offen offset:2048 sc1\n\t" \
  "buffer_load_dwordx4 v[140:143], v188, s[20:23], s70 offen offset:3072 sc1\n\t" \
  "s_mov_b32 s60, 1\n\t" \
  "Lnb_%=:\n\t" \
  "s_waitcnt lgkmcnt(0)\n\t" \
  "s_barrier\n\t" \
  "s_cmp_eq_u32 s61, 1\n\t" \
  "s_cbranch_scc0 Lnr_%=\n\t" \
  "s_add_u32 s41, s41, 131072\n\t" \
  "s_add_u32 s40, s45, 1\n\t" \
  "s_and_b32 s40, s40, 3\n\t" \
  "s_mul_i32 s40, s40, 3072\n\t" \
  "s_add_u32 s40, s40, s41\n\t" \
  "v_add_u32 v162, s40, v188\n\t" \
  "ds_read_b128 v[164:167], v162 offset:0\n\t" \
  "ds_read_b128 v[168:171], v162 offset:1024\n\t" \
  "ds_read_b128 v[172:175], v162 offset:2048\n\t" \
  "s_add_u32 s40, s45, 2\n\t" \
  "s_and_b32 s40, s40, 3\n\t" \
  "s_mul_i32 s40, s40, 3072\n\t" \
  "s_add_u32 s40, s40, s41\n\t" \
  "v_add_u32 v162, s40, v188\n\t" \
  "ds_read_b128 v[176:179], v162 offset:0\n\t" \
  "ds_read_b128 v[180:183], v162 offset:1024\n\t" \
  "ds_read_b128 v[184:187], v162 offset:2048\n\t" \
  "s_waitcnt lgkmcnt(3)\n\t" \
  "v_pk_add_f32 v[144:145], v[144:145], v[164:165]\n\t" \
  "v_pk_add_f32 v[146:147], v[146:147], v[166:167]\n\t" \
  "v_pk_add_f32 v[148:149], v[148:149], v[168:169]\n\t" \
  "v_pk_add_f32 v[150:151], v[150:151], v[170:171]\n\t" \
  "v_pk_add_f32 v[152:153], v[152:153], v[172:173]\n\t" \
  "v_pk_add_f32 v[154:155], v[154:155], v[174:175]\n\t" \
  "s_add_u32 s40, s45, 3\n\t" \
  "s_and_b32 s40, s40, 3\n\t" \
  "s_mul_i32 s40, s40, 3072\n\t" \
  "s_add_u32 s40, s40, s41\n\t" \
  "v_add_u32 v162, s40, v188\n\t" \
  "ds_read_b128 v[164:167], v162 offset:0\n\t" \
  "ds_read_b128 v[168:171], v162 offset:1024\n\t" \
  "ds_read_b128 v[172:175], v162 offset:2048\n\t" \
  "s_waitcnt lgkmcnt(3)\n\t" \
  "v_pk_add_f32 v[144:145], v[144:145], v[176:177]\n\t" \
  "v_pk_add_f32 v[146:147], v[146:147], v[178:179]\n\t" \
  "v_pk_add_f32 v[148:149], v[148:149], v[180:181]\n\t" \
  "v_pk_add_f32 v[150:151], v[150:151], v[182:183]\n\t" \
  "v_pk_add_f32 v[152:153], v[152:153], v[184:185]\n\t" \
  "v_pk_add_f32 v[154:155], v[154:155], v[186:187]\n\t" \
  "v_lshlrev_b32 v176, 16, v156\n\t" \
  "v_and_b32 v177, 0xffff0000, v156\n\t" \
  "v_lshlrev_b32 v178, 16, v157\n\t" \
  "v_and_b32 v179, 0xffff0000, v157\n\t" \
  "v_lshlrev_b32 v180, 16, v158\n\t" \
  "v_and_b32 v181, 0xffff0000, v158\n\t" \
  "v_lshlrev_b32 v182, 16, v159\n\t" \
  "v_and_b32 v183, 0xffff0000, v159\n\t" \
  "v_lshlrev_b32 v184, 16, v160\n\t" \
  "v_and_b32 v185, 0xffff0000, v160\n\t" \
  "v_lshlrev_b32 v186, 16, v161\n\t" \
  "v_and_b32 v187, 0xffff0000, v161\n\t" \
  "v_pk_add_f32 v[144:145], v[144:145], v[176:177]\n\t" \
  "v_pk_add_f32 v[146:147], v[146:147], v[178:179]\n\t" \
  "v_pk_add_f32 v[148:149], v[148:149], v[180:181]\n\t" \
  "v_pk_add_f32 v[150:151], v[150:151], v[182:183]\n\t" \
  "v_pk_add_f32 v[152:153], v[152:153], v[184:185]\n\t" \
  "v_pk_add_f32 v[154:155], v[154:155], v[186:187]\n\t" \
  "s_waitcnt lgkmcnt(0)\n\t" \
  "v_pk_add_f32 v[144:145], v[144:145], v[164:165]\n\t" \
  "v_pk_add_f32 v[146:147], v[146:147], v[166:167]\n\t" \
  "v_pk_add_f32 v[148:149], v[148:149], v[168:169]\n\t" \
  "v_pk_add_f32 v[150:151], v[150:151], v[170:171]\n\t" \
  "v_pk_add_f32 v[152:153], v[152:153], v[172:173]\n\t" \
  "v_pk_add_f32 v[154:155], v[154:155], v[174:175]\n\t" \
  "v_cvt_pk_bf16_f32 v156, v144, v145\n\t" \
  "v_cvt_pk_bf16_f32 v157, v146, v147\n\t" \
  "v_cvt_pk_bf16_f32 v158, v148, v149\n\t" \
  "v_cvt_pk_bf16_f32 v159, v150, v151\n\t" \
  "v_cvt_pk_bf16_f32 v160, v152, v153\n\t" \
  "v_cvt_pk_bf16_f32 v161, v154, v155\n\t" \
  "buffer_store_dwordx4 v[156:159], v193, s[36:39], s53 offen\n\t" \
  "buffer_store_dwordx2 v[160:161], v194, s[36:39], s53 offen offset:64\n\t" \
  "Lnr_%=:\n\t" \
  "s_add_u32 s51, s51, 1\n\t" \
  "s_mov_b32 s31, s48\n\t" \
  "s_cmp_eq_u32 s60, 1\n\t" \
  "s_cbranch_scc1 Ltop_%=\n\t" \
  "s_cmp_lt_u32 s48, 128\n\t" \
  "s_cbranch_scc0 Lend_%=\n\t" \
  "Lblk_%=:\n\t" \
  "s_lshr_b32 s43, s48, 1\n\t" \
  "s_mul_i32 s62, s48, 147456\n\t" \
  "s_add_u32 s62, s62, s54\n\t" \
  "s_add_u32 s63, s62, 4096\n\t" \
  "s_add_u32 s64, s62, 8192\n\t" \
  "s_add_u32 s65, s62, 12288\n\t" \
  "s_add_u32 s66, s62, 16384\n\t" \
  "s_add_u32 s67, s62, 20480\n\t" \
  "s_add_u32 s68, s62, 24576\n\t" \
  "s_add_u32 s69, s62, 28672\n\t" \
  "s_add_u32 s70, s62, 32768\n\t" \
  "s_mov_b32 s42, 0\n\t" \
  "La_sp_%=:\n\t" \
  "buffer_load_dword v191, v190, s[20:23], 0 offen sc1\n\t" \
  "s_waitcnt vmcnt(0)\n\t" \
  "s_nop 1\n\t" \
  "v_min_u32_dpp v191, v191, v191 row_ror:1 row_mask:0xf bank_mask:0xf\n\t" \
  "s_nop 1\n\t" \
  "v_min_u32_dpp v191, v191, v191 row_ror:2 row_mask:0xf bank_mask:0xf\n\t" \
  "s_nop 1\n\t" \
  "v_min_u32_dpp v191, v191, v191 row_ror:4 row_mask:0xf bank_mask:0xf\n\t" \
  "s_nop 1\n\t" \
  "v_min_u32_dpp v191, v191, v191 row_ror:8 row_mask:0xf bank_mask:0xf\n\t" \
  "s_nop 1\n\t" \
  "v_min_u32_dpp v191, v191, v191 row_bcast:15 row_mask:0xa bank_mask:0xf\n\t" \
  "s_nop 1\n\t" \
  "v_min_u32_dpp v191, v191, v191 row_bcast:31 row_mask:0xc bank_mask:0xf\n\t" \
  "s_nop 0\n\t" \
  "v_readlane_b32 s44, v191, 63\n\t" \
  "s_nop 1\n\t" \
  "s_cmp_gt_u32 s44, s43\n\t" \
  "s_cbranch_scc1 La_ok_%=\n\t" \
  "s_add_u32 s42, s42, 1\n\t" \
  "s_cmp_gt_u32 s42, 1048576\n\t" \
  "s_cbranch_scc1 Ltmo_%=\n\t" \
  "s_and_b32 s40, s42, 255\n\t" \
  "s_cbranch_scc1 La_sl_%=\n\t" \
  "s_mov_b32 s40, 2157568\n\t" \
  "buffer_load_dword v191, off, s[20:23], s40 sc1\n\t" \
  "s_waitcnt vmcnt(0)\n\t" \
  "v_cmp_ne_u32 vcc, 0, v191\n\t" \
  "s_nop 0\n\t" \
  "s_cbranch_vccnz Lend_%=\n\t" \
  "La_sl_%=:\n\t" \
  "s_sleep 1\n\t" \
  "s_sub_u32 s40, s43, s44\n\t" \
  "s_cmp_lt_u32 s40, 4\n\t" \
  "s_cbranch_scc1 La_sp_%=\n\t" \
  "s_sleep 100\n\t" \
  "s_branch La_sp_%=\n\t" \
  "La_ok_%=:\n\t" \
  "buffer_load_dwordx4 v[0:3], v188, s[20:23], s62 offen offset:0 sc1\n\t" \
  "buffer_load_dwordx4 v[4:7], v188, s[20:23], s62 offen offset:1024 sc1\n\t" \
  "buffer_load_dwordx4 v[8:11], v188, s[20:23], s62 offen offset:2048 sc1\n\t" \
  "buffer_load_dwordx4 v[12:15], v188, s[20:23], s62 offen offset:3072 sc1\n\t" \
  "buffer_load_dwordx4 v[16:19], v188, s[20:23], s63 offen offset:0 sc1\n\t" \
  "buffer_load_dwordx4 v[20:23], v188, s[20:23], s63 offen offset:1024 sc1\n\t" \
  "buffer_load_dwordx4 v[24:27], v188, s[20:23], s63 offen offset:2048 sc1\n\t" \
  "buffer_load_dwordx4 v[28:31], v188, s[20:23], s63 offen offset:3072 sc1\n\t" \
  "buffer_load_dwordx4 v[32:35], v188, s[20:23], s64 offen offset:0 sc1\n\t" \
  "buffer_load_dwordx4 v[36:39], v188, s[20:23], s64 offen offset:1024 sc1\n\t" \
  "buffer_load_dwordx4 v[40:43], v188, s[20:23], s64 offen offset:2048 sc1\n\t" \
  "buffer_load_dwordx4 v[44:47], v188, s[20:23], s64 offen offset:3072 sc1\n\t" \
  "buffer_load_dwordx4 v[48:51], v188, s[20:23], s65 offen offset:0 sc1\n\t" \
  "buffer_load_dwordx4 v[52:55], v188, s[20:23], s65 offen offset:1024 sc1\n\t" \
  "buffer_load_dwordx4 v[56:59], v188, s[20:23], s65 offen offset:2048 sc1\n\t" \
  "buffer_load_dwordx4 v[60:63], v188, s[20:23], s65 offen offset:3072 sc1\n\t" \
  "buffer_load_dwordx4 v[64:67], v188, s[20:23], s66 offen offset:0 sc1\n\t" \
  "buffer_load_dwordx4 v[68:71], v188, s[20:23], s66 offen offset:1024 sc1\n\t" \
  "buffer_load_dwordx4 v[72:75], v188, s[20:23], s66 offen offset:2048 sc1\n\t" \
  "buffer_load_dwordx4 v[76:79], v188, s[20:23], s66 offen offset:3072 sc1\n\t" \
  "buffer_load_dwordx4 v[80:83], v188, s[20:23], s67 offen offset:0 sc1\n\t" \
  "buffer_load_dwordx4 v[84:87], v188, s[20:23], s67 offen offset:1024 sc1\n\t" \
  "buffer_load_dwordx4 v[88:91], v188, s[20:23], s67 offen offset:2048 sc1\n\t" \
  "buffer_load_dwordx4 v[92:95], v188, s[20:23], s67 offen offset:3072 sc1\n\t" \
  "buffer_load_dwordx4 v[96:99], v188, s[20:23], s68 offen offset:0 sc1\n\t" \
  "buffer_load_dwordx4 v[100:103], v188, s[20:23], s68 offen offset:1024 sc1\n\t" \
  "buffer_load_dwordx4 v[104:107], v188, s[20:23], s68 offen offset:2048 sc1\n\t" \
  "buffer_load_dwordx4 v[108:111], v188, s[20:23], s68 offen offset:3072 sc1\n\t" \
  "buffer_load_dwordx4 v[112:115], v188, s[20:23], s69 offen offset:0 sc1\n\t" \
  "buffer_load_dwordx4 v[116:119], v188, s[20:23], s69 offen offset:1024 sc1\n\t" \
  "buffer_load_dwordx4 v[120:123], v188, s[20:23], s69 offen offset:2048 sc1\n\t" \
  "buffer_load_dwordx4 v[124:127], v188, s[20:23], s69 offen offset:3072 sc1\n\t" \
  "buffer_load_dwordx4 v[128:131], v188, s[20:23], s70 offen offset:0 sc1\n\t" \
  "buffer_load_dwordx4 v[132:135], v188, s[20:23], s70 offen offset:1024 sc1\n\t" \
  "buffer_load_dwordx4 v[136:139], v188, s[20:23], s70 offen offset:2048 sc1\n\t" \
  "buffer_load_dwordx4 v[140:143], v188, s[20:23], s70 offen offset:3072 sc1\n\t" \
  "s_branch Ltop_%=\n\t" \
  "s_branch Lend_%=\n\t" \
  "Ltmo_%=:\n\t" \
  "v_mov_b32 v162, 1\n\t" \
  "s_mov_b32 s40, 2157568\n\t" \
  "buffer_store_dword v162, off, s[20:23], s40 sc1\n\t" \
  "Lend_%=:\n\t" \
  "s_waitcnt vmcnt(0) lgkmcnt(0)\n\t"
#define GU_ASM \
  "s_load_dwordx2 s[24:25], %0, 80\n\t" \
  "s_and_b32 s26, %1, 255\n\t" \
  "s_lshr_b32 s27, %1, 16\n\t" \
  "s_mov_b64 exec, -1\n\t" \
  "s_waitcnt lgkmcnt(0)\n\t" \
  "s_mov_b32 s20, s24\n\t" \
  "s_and_b32 s21, s25, 0xffff\n\t" \
  "s_mov_b32 s22, 113246208\n\t" \
  "s_mov_b32 s23, 0x20000\n\t" \
  "v_mbcnt_lo_u32_b32 v165, -1, 0\n\t" \
  "v_mbcnt_hi_u32_b32 v165, -1, v165\n\t" \
  "s_and_b32 s54, s27, 1\n\t" \
  "s_lshr_b32 s55, s27, 1\n\t" \
  "s_cmp_eq_u32 s27, 0\n\t" \
  "s_cselect_b64 s[58:59], 1, 0\n\t" \
  "v_lshrrev_b32 v168, 4, v165\n\t" \
  "v_bfe_u32 v166, v165, 2, 2\n\t" \
  "v_lshl_add_u32 v167, v168, 3, v166\n\t" \
  "v_lshlrev_b32 v167, 8, v167\n\t" \
  "v_and_b32 v170, 1, v168\n\t" \
  "v_lshl_add_u32 v166, v170, 2, v166\n\t" \
  "v_lshlrev_b32 v166, 1, v166\n\t" \
  "v_and_b32 v170, 3, v165\n\t" \
  "s_lshl_b32 s40, s54, 2\n\t" \
  "v_add_u32 v170, s40, v170\n\t" \
  "v_xor_b32 v170, v170, v166\n\t" \
  "v_lshl_add_u32 v0, v170, 4, v167\n\t" \
  "v_xor_b32 v3, 128, v0\n\t" \
  "s_mul_i32 s40, s54, 73728\n\t" \
  "v_add_u32 v0, s40, v0\n\t" \
  "v_add_u32 v3, s40, v3\n\t" \
  "v_xor_b32 v1, 64, v0\n\t" \
  "v_xor_b32 v4, 64, v3\n\t" \
  "v_add_u32 v6, 0x10000, v0\n\t" \
  "v_add_u32 v7, 0x10000, v3\n\t" \
  "v_add_u32 v8, 0x10000, v1\n\t" \
  "v_add_u32 v9, 0x10000, v4\n\t" \
  "v_mov_b32 v184, 0x3f803f80\n\t" \
  "v_mov_b32 v185, 0x3f803f80\n\t" \
  "v_mov_b32 v186, 0x3f803f80\n\t" \
  "v_mov_b32 v187, 0x3f803f80\n\t" \
  "v_lshlrev_b32 v160, 4, v165\n\t" \
  "s_lshl_b32 s40, s27, 12\n\t" \
  "v_add_u32 v174, s40, v160\n\t" \
  "v_xor_b32 v175, 4096, v174\n\t" \
  "ds_read_b64_tr_b16 a[0:1], v0 offset:0\n\t" \
  "ds_read_b64_tr_b16 a[2:3], v0 offset:1024\n\t" \
  "ds_read_b64_tr_b16 a[4:5], v0 offset:8\n\t" \
  "ds_read_b64_tr_b16 a[6:7], v0 offset:1032\n\t" \
  "ds_read_b64_tr_b16 a[8:9], v3 offset:0\n\t" \
  "ds_read_b64_tr_b16 a[10:11], v3 offset:1024\n\t" \
  "ds_read_b64_tr_b16 a[12:13], v3 offset:8\n\t" \
  "ds_read_b64_tr_b16 a[14:15], v3 offset:1032\n\t" \
  "ds_read_b64_tr_b16 a[16:17], v1 offset:0\n\t" \
  "ds_read_b64_tr_b16 a[18:19], v1 offset:1024\n\t" \
  "ds_read_b64_tr_b16 a[20:21], v1 offset:8\n\t" \
  "ds_read_b64_tr_b16 a[22:23], v1 offset:1032\n\t" \
  "ds_read_b64_tr_b16 a[24:25], v4 offset:0\n\t" \
  "ds_read_b64_tr_b16 a[26:27], v4 offset:1024\n\t" \
  "ds_read_b64_tr_b16 a[28:29], v4 offset:8\n\t" \
  "ds_read_b64_tr_b16 a[30:31], v4 offset:1032\n\t" \
  "s_waitcnt lgkmcnt(0)\n\t" \
  "v_mfma_f32_16x16x32_bf16 v[36:39], a[0:3], v[184:187], 0\n\t" \
  "v_mfma_f32_16x16x32_bf16 v[40:43], a[4:7], v[184:187], 0\n\t" \
  "v_mfma_f32_16x16x32_bf16 v[44:47], a[8:11], v[184:187], 0\n\t" \
  "v_mfma_f32_16x16x32_bf16 v[48:51], a[12:15], v[184:187], 0\n\t" \
  "v_mfma_f32_16x16x32_bf16 v[52:55], a[16:19], v[184:187], 0\n\t" \
  "v_mfma_f32_16x16x32_bf16 v[56:59], a[20:23], v[184:187], 0\n\t" \
  "v_mfma_f32_16x16x32_bf16 v[60:63], a[24:27], v[184:187], 0\n\t" \
  "v_mfma_f32_16x16x32_bf16 v[64:67], a[28:31], v[184:187], 0\n\t" \
  "ds_read_b64_tr_b16 a[32:33], v0 offset:8192\n\t" \
  "ds_read_b64_tr_b16 a[34:35], v0 offset:9216\n\t" \
  "ds_read_b64_tr_b16 a[36:37], v0 offset:8200\n\t" \
  "ds_read_b64_tr_b16 a[38:39], v0 offset:9224\n\t" \
  "ds_read_b64_tr_b16 a[40:41], v3 offset:8192\n\t" \
  "ds_read_b64_tr_b16 a[42:43], v3 offset:9216\n\t" \
  "ds_read_b64_tr_b16 a[44:45], v3 offset:8200\n\t" \
  "ds_read_b64_tr_b16 a[46:47], v3 offset:9224\n\t" \
  "ds_read_b64_tr_b16 a[48:49], v1 offset:8192\n\t" \
  "ds_read_b64_tr_b16 a[50:51], v1 offset:9216\n\t" \
  "ds_read_b64_tr_b16 a[52:53], v1 offset:8200\n\t" \
  "ds_read_b64_tr_b16 a[54:55], v1 offset:9224\n\t" \
  "ds_read_b64_tr_b16 a[56:57], v4 offset:8192\n\t" \
  "ds_read_b64_tr_b16 a[58:59], v4 offset:9216\n\t" \
  "ds_read_b64_tr_b16 a[60:61], v4 offset:8200\n\t" \
  "ds_read_b64_tr_b16 a[62:63], v4 offset:9224\n\t" \
  "s_waitcnt lgkmcnt(0)\n\t" \
  "v_mfma_f32_16x16x32_bf16 v[36:39], a[32:35], v[184:187], v[36:39]\n\t" \
  "v_mfma_f32_16x16x32_bf16 v[40:43], a[36:39], v[184:187], v[40:43]\n\t" \
  "v_mfma_f32_16x16x32_bf16 v[44:47], a[40:43], v[184:187], v[44:47]\n\t" \
  "v_mfma_f32_16x16x32_bf16 v[48:51], a[44:47], v[184:187], v[48:51]\n\t" \
  "v_mfma_f32_16x16x32_bf16 v[52:55], a[48:51], v[184:187], v[52:55]\n\t" \
  "v_mfma_f32_16x16x32_bf16 v[56:59], a[52:55], v[184:187], v[56:59]\n\t" \
  "v_mfma_f32_16x16x32_bf16 v[60:63], a[56:59], v[184:187], v[60:63]\n\t" \
  "v_mfma_f32_16x16x32_bf16 v[64:67], a[60:63], v[184:187], v[64:67]\n\t" \
  "ds_read_b64_tr_b16 a[64:65], v0 offset:16384\n\t" \
  "ds_read_b64_tr_b16 a[66:67], v0 offset:17408\n\t" \
  "ds_read_b64_tr_b16 a[68:69], v0 offset:16392\n\t" \
  "ds_read_b64_tr_b16 a[70:71], v0 offset:17416\n\t" \
  "ds_read_b64_tr_b16 a[72:73], v3 offset:16384\n\t" \
  "ds_read_b64_tr_b16 a[74:75], v3 offset:17408\n\t" \
  "ds_read_b64_tr_b16 a[76:77], v3 offset:16392\n\t" \
  "ds_read_b64_tr_b16 a[78:79], v3 offset:17416\n\t" \
  "ds_read_b64_tr_b16 a[80:81], v1 offset:16384\n\t" \
  "ds_read_b64_tr_b16 a[82:83], v1 offset:17408\n\t" \
  "ds_read_b64_tr_b16 a[84:85], v1 offset:16392\n\t" \
  "ds_read_b64_tr_b16 a[86:87], v1 offset:17416\n\t" \
  "ds_read_b64_tr_b16 a[88:89], v4 offset:16384\n\t" \
  "ds_read_b64_tr_b16 a[90:91], v4 offset:17408\n\t" \
  "ds_read_b64_tr_b16 a[92:93], v4 offset:16392\n\t" \
  "ds_read_b64_tr_b16 a[94:95], v4 offset:17416\n\t" \
  "s_waitcnt lgkmcnt(0)\n\t" \
  "v_mfma_f32_16x16x32_bf16 v[36:39], a[64:67], v[184:187], v[36:39]\n\t" \
  "v_mfma_f32_16x16x32_bf16 v[40:43], a[68:71], v[184:187], v[40:43]\n\t" \
  "v_mfma_f32_16x16x32_bf16 v[44:47], a[72:75], v[184:187], v[44:47]\n\t" \
  "v_mfma_f32_16x16x32_bf16 v[48:51], a[76:79], v[184:187], v[48:51]\n\t" \
  "v_mfma_f32_16x16x32_bf16 v[52:55], a[80:83], v[184:187], v[52:55]\n\t" \
  "v_mfma_f32_16x16x32_bf16 v[56:59], a[84:87], v[184:187], v[56:59]\n\t" \
  "v_mfma_f32_16x16x32_bf16 v[60:63], a[88:91], v[184:187], v[60:63]\n\t" \
  "v_mfma_f32_16x16x32_bf16 v[64:67], a[92:95], v[184:187], v[64:67]\n\t" \
  "ds_read_b64_tr_b16 a[96:97], v0 offset:24576\n\t" \
  "ds_read_b64_tr_b16 a[98:99], v0 offset:25600\n\t" \
  "ds_read_b64_tr_b16 a[100:101], v0 offset:24584\n\t" \
  "ds_read_b64_tr_b16 a[102:103], v0 offset:25608\n\t" \
  "ds_read_b64_tr_b16 a[104:105], v3 offset:24576\n\t" \
  "ds_read_b64_tr_b16 a[106:107], v3 offset:25600\n\t" \
  "ds_read_b64_tr_b16 a[108:109], v3 offset:24584\n\t" \
  "ds_read_b64_tr_b16 a[110:111], v3 offset:25608\n\t" \
  "ds_read_b64_tr_b16 a[112:113], v1 offset:24576\n\t" \
  "ds_read_b64_tr_b16 a[114:115], v1 offset:25600\n\t" \
  "ds_read_b64_tr_b16 a[116:117], v1 offset:24584\n\t" \
  "ds_read_b64_tr_b16 a[118:119], v1 offset:25608\n\t" \
  "ds_read_b64_tr_b16 a[120:121], v4 offset:24576\n\t" \
  "ds_read_b64_tr_b16 a[122:123], v4 offset:25600\n\t" \
  "ds_read_b64_tr_b16 a[124:125], v4 offset:24584\n\t" \
  "ds_read_b64_tr_b16 a[126:127], v4 offset:25608\n\t" \
  "s_waitcnt lgkmcnt(0)\n\t" \
  "v_mfma_f32_16x16x32_bf16 v[36:39], a[96:99], v[184:187], v[36:39]\n\t" \
  "v_mfma_f32_16x16x32_bf16 v[40:43], a[100:103], v[184:187], v[40:43]\n\t" \
  "v_mfma_f32_16x16x32_bf16 v[44:47], a[104:107], v[184:187], v[44:47]\n\t" \
  "v_mfma_f32_16x16x32_bf16 v[48:51], a[108:111], v[184:187], v[48:51]\n\t" \
  "v_mfma_f32_16x16x32_bf16 v[52:55], a[112:115], v[184:187], v[52:55]\n\t" \
  "v_mfma_f32_16x16x32_bf16 v[56:59], a[116:119], v[184:187], v[56:59]\n\t" \
  "v_mfma_f32_16x16x32_bf16 v[60:63], a[120:123], v[184:187], v[60:63]\n\t" \
  "v_mfma_f32_16x16x32_bf16 v[64:67], a[124:127], v[184:187], v[64:67]\n\t" \
  "ds_read_b64_tr_b16 a[128:129], v0 offset:32768\n\t" \
  "ds_read_b64_tr_b16 a[130:131], v0 offset:33792\n\t" \
  "ds_read_b64_tr_b16 a[132:133], v0 offset:32776\n\t" \
  "ds_read_b64_tr_b16 a[134:135], v0 offset:33800\n\t" \
  "ds_read_b64_tr_b16 a[136:137], v3 offset:32768\n\t" \
  "ds_read_b64_tr_b16 a[138:139], v3 offset:33792\n\t" \
  "ds_read_b64_tr_b16 a[140:141], v3 offset:32776\n\t" \
  "ds_read_b64_tr_b16 a[142:143], v3 offset:33800\n\t" \
  "ds_read_b64_tr_b16 a[144:145], v1 offset:32768\n\t" \
  "ds_read_b64_tr_b16 a[146:147], v1 offset:33792\n\t" \
  "ds_read_b64_tr_b16 a[148:149], v1 offset:32776\n\t" \
  "ds_read_b64_tr_b16 a[150:151], v1 offset:33800\n\t" \
  "ds_read_b64_tr_b16 a[152:153], v4 offset:32768\n\t" \
  "ds_read_b64_tr_b16 a[154:155], v4 offset:33792\n\t" \
  "ds_read_b64_tr_b16 a[156:157], v4 offset:32776\n\t" \
  "ds_read_b64_tr_b16 a[158:159], v4 offset:33800\n\t" \
  "s_waitcnt lgkmcnt(0)\n\t" \
  "v_mfma_f32_16x16x32_bf16 v[36:39], a[128:131], v[184:187], v[36:39]\n\t" \
  "v_mfma_f32_16x16x32_bf16 v[40:43], a[132:135], v[184:187], v[40:43]\n\t" \
  "v_mfma_f32_16x16x32_bf16 v[44:47], a[136:139], v[184:187], v[44:47]\n\t" \
  "v_mfma_f32_16x16x32_bf16 v[48:51], a[140:143], v[184:187], v[48:51]\n\t" \
  "v_mfma_f32_16x16x32_bf16 v[52:55], a[144:147], v[184:187], v[52:55]\n\t" \
  "v_mfma_f32_16x16x32_bf16 v[56:59], a[148:151], v[184:187], v[56:59]\n\t" \
  "v_mfma_f32_16x16x32_bf16 v[60:63], a[152:155], v[184:187], v[60:63]\n\t" \
  "v_mfma_f32_16x16x32_bf16 v[64:67], a[156:159], v[184:187], v[64:67]\n\t" \
  "ds_read_b64_tr_b16 a[160:161], v0 offset:40960\n\t" \
  "ds_read_b64_tr_b16 a[162:163], v0 offset:41984\n\t" \
  "ds_read_b64_tr_b16 a[164:165], v0 offset:40968\n\t" \
  "ds_read_b64_tr_b16 a[166:167], v0 offset:41992\n\t" \
  "ds_read_b64_tr_b16 a[168:169], v3 offset:40960\n\t" \
  "ds_read_b64_tr_b16 a[170:171], v3 offset:41984\n\t" \
  "ds_read_b64_tr_b16 a[172:173], v3 offset:40968\n\t" \
  "ds_read_b64_tr_b16 a[174:175], v3 offset:41992\n\t" \
  "ds_read_b64_tr_b16 a[176:177], v1 offset:40960\n\t" \
  "ds_read_b64_tr_b16 a[178:179], v1 offset:41984\n\t" \
  "ds_read_b64_tr_b16 a[180:181], v1 offset:40968\n\t" \
  "ds_read_b64_tr_b16 a[182:183], v1 offset:41992\n\t" \
  "ds_read_b64_tr_b16 a[184:185], v4 offset:40960\n\t" \
  "ds_read_b64_tr_b16 a[186:187], v4 offset:41984\n\t" \
  "ds_read_b64_tr_b16 a[188:189], v4 offset:40968\n\t" \
  "ds_read_b64_tr_b16 a[190:191], v4 offset:41992\n\t" \
  "s_waitcnt lgkmcnt(0)\n\t" \
  "v_mfma_f32_16x16x32_bf16 v[36:39], a[160:163], v[184:187], v[36:39]\n\t" \
  "v_mfma_f32_16x16x32_bf16 v[40:43], a[164:167], v[184:187], v[40:43]\n\t" \
  "v_mfma_f32_16x16x32_bf16 v[44:47], a[168:171], v[184:187], v[44:47]\n\t" \
  "v_mfma_f32_16x16x32_bf16 v[48:51], a[172:175], v[184:187], v[48:51]\n\t" \
  "v_mfma_f32_16x16x32_bf16 v[52:55], a[176:179], v[184:187], v[52:55]\n\t" \
  "v_mfma_f32_16x16x32_bf16 v[56:59], a[180:183], v[184:187], v[56:59]\n\t" \
  "v_mfma_f32_16x16x32_bf16 v[60:63], a[184:187], v[184:187], v[60:63]\n\t" \
  "v_mfma_f32_16x16x32_bf16 v[64:67], a[188:191], v[184:187], v[64:67]\n\t" \
  "ds_read_b64_tr_b16 a[192:193], v0 offset:49152\n\t" \
  "ds_read_b64_tr_b16 a[194:195], v0 offset:50176\n\t" \
  "ds_read_b64_tr_b16 a[196:197], v0 offset:49160\n\t" \
  "ds_read_b64_tr_b16 a[198:199], v0 offset:50184\n\t" \
  "ds_read_b64_tr_b16 a[200:201], v3 offset:49152\n\t" \
  "ds_read_b64_tr_b16 a[202:203], v3 offset:50176\n\t" \
  "ds_read_b64_tr_b16 a[204:205], v3 offset:49160\n\t" \
  "ds_read_b64_tr_b16 a[206:207], v3 offset:50184\n\t" \
  "ds_read_b64_tr_b16 a[208:209], v1 offset:49152\n\t" \
  "ds_read_b64_tr_b16 a[210:211], v1 offset:50176\n\t" \
  "ds_read_b64_tr_b16 a[212:213], v1 offset:49160\n\t" \
  "ds_read_b64_tr_b16 a[214:215], v1 offset:50184\n\t" \
  "ds_read_b64_tr_b16 a[216:217], v4 offset:49152\n\t" \
  "ds_read_b64_tr_b16 a[218:219], v4 offset:50176\n\t" \
  "ds_read_b64_tr_b16 a[220:221], v4 offset:49160\n\t" \
  "ds_read_b64_tr_b16 a[222:223], v4 offset:50184\n\t" \
  "s_waitcnt lgkmcnt(0)\n\t" \
  "v_mfma_f32_16x16x32_bf16 v[36:39], a[192:195], v[184:187], v[36:39]\n\t" \
  "v_mfma_f32_16x16x32_bf16 v[40:43], a[196:199], v[184:187], v[40:43]\n\t" \
  "v_mfma_f32_16x16x32_bf16 v[44:47], a[200:203], v[184:187], v[44:47]\n\t" \
  "v_mfma_f32_16x16x32_bf16 v[48:51], a[204:207], v[184:187], v[48:51]\n\t" \
  "v_mfma_f32_16x16x32_bf16 v[52:55], a[208:211], v[184:187], v[52:55]\n\t" \
  "v_mfma_f32_16x16x32_bf16 v[56:59], a[212:215], v[184:187], v[56:59]\n\t" \
  "v_mfma_f32_16x16x32_bf16 v[60:63], a[216:219], v[184:187], v[60:63]\n\t" \
  "v_mfma_f32_16x16x32_bf16 v[64:67], a[220:223], v[184:187], v[64:67]\n\t" \
  "ds_read_b64_tr_b16 a[224:225], v0 offset:57344\n\t" \
  "ds_read_b64_tr_b16 a[226:227], v0 offset:58368\n\t" \
  "ds_read_b64_tr_b16 a[228:229], v0 offset:57352\n\t" \
  "ds_read_b64_tr_b16 a[230:231], v0 offset:58376\n\t" \
  "ds_read_b64_tr_b16 a[232:233], v3 offset:57344\n\t" \
  "ds_read_b64_tr_b16 a[234:235], v3 offset:58368\n\t" \
  "ds_read_b64_tr_b16 a[236:237], v3 offset:57352\n\t" \
  "ds_read_b64_tr_b16 a[238:239], v3 offset:58376\n\t" \
  "ds_read_b64_tr_b16 a[240:241], v1 offset:57344\n\t" \
  "ds_read_b64_tr_b16 a[242:243], v1 offset:58368\n\t" \
  "ds_read_b64_tr_b16 a[244:245], v1 offset:57352\n\t" \
  "ds_read_b64_tr_b16 a[246:247], v1 offset:58376\n\t" \
  "ds_read_b64_tr_b16 a[248:249], v4 offset:57344\n\t" \
  "ds_read_b64_tr_b16 a[250:251], v4 offset:58368\n\t" \
  "ds_read_b64_tr_b16 a[252:253], v4 offset:57352\n\t" \
  "ds_read_b64_tr_b16 a[254:255], v4 offset:58376\n\t" \
  "s_waitcnt lgkmcnt(0)\n\t" \
  "v_mfma_f32_16x16x32_bf16 v[36:39], a[224:227], v[184:187], v[36:39]\n\t" \
  "v_mfma_f32_16x16x32_bf16 v[40:43], a[228:231], v[184:187], v[40:43]\n\t" \
  "v_mfma_f32_16x16x32_bf16 v[44:47], a[232:235], v[184:187], v[44:47]\n\t" \
  "v_mfma_f32_16x16x32_bf16 v[48:51], a[236:239], v[184:187], v[48:51]\n\t" \
  "v_mfma_f32_16x16x32_bf16 v[52:55], a[240:243], v[184:187], v[52:55]\n\t" \
  "v_mfma_f32_16x16x32_bf16 v[56:59], a[244:247], v[184:187], v[56:59]\n\t" \
  "v_mfma_f32_16x16x32_bf16 v[60:63], a[248:251], v[184:187], v[60:63]\n\t" \
  "v_mfma_f32_16x16x32_bf16 v[64:67], a[252:255], v[184:187], v[64:67]\n\t" \
  "ds_read_b64_tr_b16 v[224:225], v6 offset:0\n\t" \
  "ds_read_b64_tr_b16 v[226:227], v6 offset:1024\n\t" \
  "ds_read_b64_tr_b16 v[228:229], v6 offset:8\n\t" \
  "ds_read_b64_tr_b16 v[230:231], v6 offset:1032\n\t" \
  "ds_read_b64_tr_b16 v[232:233], v7 offset:0\n\t" \
  "ds_read_b64_tr_b16 v[234:235], v7 offset:1024\n\t" \
  "ds_read_b64_tr_b16 v[236:237], v7 offset:8\n\t" \
  "ds_read_b64_tr_b16 v[238:239], v7 offset:1032\n\t" \
  "ds_read_b64_tr_b16 v[240:241], v8 offset:0\n\t" \
  "ds_read_b64_tr_b16 v[242:243], v8 offset:1024\n\t" \
  "ds_read_b64_tr_b16 v[244:245], v8 offset:8\n\t" \
  "ds_read_b64_tr_b16 v[246:247], v8 offset:1032\n\t" \
  "ds_read_b64_tr_b16 v[248:249], v9 offset:0\n\t" \
  "ds_read_b64_tr_b16 v[250:251], v9 offset:1024\n\t" \
  "ds_read_b64_tr_b16 v[252:253], v9 offset:8\n\t" \
  "ds_read_b64_tr_b16 v[254:255], v9 offset:1032\n\t" \
  "s_waitcnt lgkmcnt(0)\n\t" \
  "v_mfma_f32_16x16x32_bf16 v[36:39], v[224:227], v[184:187], v[36:39]\n\t" \
  "v_mfma_f32_16x16x32_bf16 v[40:43], v[228:231], v[184:187], v[40:43]\n\t" \
  "v_mfma_f32_16x16x32_bf16 v[44:47], v[232:235], v[184:187], v[44:47]\n\t" \
  "v_mfma_f32_16x16x32_bf16 v[48:51], v[236:239], v[184:187], v[48:51]\n\t" \
  "v_mfma_f32_16x16x32_bf16 v[52:55], v[240:243], v[184:187], v[52:55]\n\t" \
  "v_mfma_f32_16x16x32_bf16 v[56:59], v[244:247], v[184:187], v[56:59]\n\t" \
  "v_mfma_f32_16x16x32_bf16 v[60:63], v[248:251], v[184:187], v[60:63]\n\t" \
  "v_mfma_f32_16x16x32_bf16 v[64:67], v[252:255], v[184:187], v[64:67]\n\t" \
  "s_barrier\n\t" \
  "s_nop 7\n\t" \
  "ds_write_b128 v174, v[52:55] offset:0\n\t" \
  "ds_write_b128 v174, v[56:59] offset:1024\n\t" \
  "ds_write_b128 v174, v[60:63] offset:2048\n\t" \
  "ds_write_b128 v174, v[64:67] offset:3072\n\t" \
  "s_waitcnt lgkmcnt(0)\n\t" \
  "s_barrier\n\t" \
  "ds_read_b128 v[68:71], v175 offset:0\n\t" \
  "ds_read_b128 v[72:75], v175 offset:1024\n\t" \
  "ds_read_b128 v[76:79], v175 offset:2048\n\t" \
  "ds_read_b128 v[80:83], v175 offset:3072\n\t" \
  "s_waitcnt lgkmcnt(3)\n\t" \
  "v_pk_add_f32 v[36:37], v[36:37], v[68:69]\n\t" \
  "v_pk_add_f32 v[38:39], v[38:39], v[70:71]\n\t" \
  "s_waitcnt lgkmcnt(2)\n\t" \
  "v_pk_add_f32 v[40:41], v[40:41], v[72:73]\n\t" \
  "v_pk_add_f32 v[42:43], v[42:43], v[74:75]\n\t" \
  "s_waitcnt lgkmcnt(1)\n\t" \
  "v_pk_add_f32 v[44:45], v[44:45], v[76:77]\n\t" \
  "v_pk_add_f32 v[46:47], v[46:47], v[78:79]\n\t" \
  "s_waitcnt lgkmcnt(0)\n\t" \
  "v_pk_add_f32 v[48:49], v[48:49], v[80:81]\n\t" \
  "v_pk_add_f32 v[50:51], v[50:51], v[82:83]\n\t" \
  "v_mov_b32 v88, v36\n\t" \
  "v_mov_b32 v89, v37\n\t" \
  "v_mov_b32 v90, v38\n\t" \
  "v_mov_b32 v91, v39\n\t" \
  "v_mov_b32 v92, v40\n\t" \
  "v_mov_b32 v93, v41\n\t" \
  "v_mov_b32 v94, v42\n\t" \
  "v_mov_b32 v95, v43\n\t" \
  "v_mov_b32 v96, v44\n\t" \
  "v_mov_b32 v97, v45\n\t" \
  "v_mov_b32 v98, v46\n\t" \
  "v_mov_b32 v99, v47\n\t" \
  "v_mov_b32 v100, v48\n\t" \
  "v_mov_b32 v101, v49\n\t" \
  "v_mov_b32 v102, v50\n\t" \
  "v_mov_b32 v103, v51\n\t" \
  "s_barrier\n\t" \
  "v_lshlrev_b32 v161, 3, v165\n\t" \
  "v_min_u32 v166, 47, v165\n\t" \
  "v_and_b32 v164, 3, v166\n\t" \
  "v_cmp_eq_u32 vcc, 3, v164\n\t" \
  "v_mov_b32 v167, 115\n\t" \
  "s_nop 0\n\t" \
  "v_cndmask_b32 v164, v164, v167, vcc\n\t" \
  "v_lshlrev_b32 v166, 7, v166\n\t" \
  "v_add_u32 v162, 2142208, v166\n\t" \
  "s_lshl_b32 s52, s26, 7\n\t" \
  "s_add_u32 s52, s52, 2148352\n\t" \
  "s_lshl_b32 s53, s26, 1\n\t" \
  "s_add_u32 s53, s53, s54\n\t" \
  "s_mul_i32 s63, s54, 9216\n\t" \
  "s_add_u32 s63, s63, 6291456\n\t" \
  "s_mov_b32 s48, -1\n\t" \
  "s_mov_b32 s46, 0\n\t" \
  "s_mov_b32 s45, 0\n\t" \
  "s_mov_b32 s44, 0\n\t" \
  "s_branch Lblk_%=\n\t" \
  "Ltop_%=:\n\t" \
  "s_add_u32 s46, s45, 1\n\t" \
  "s_cmp_ge_u32 s45, 56\n\t" \
  "s_cbranch_scc1 Ltl_%=\n\t" \
  "s_lshl_b32 s40, s46, 1\n\t" \
  "s_or_b32 s43, s40, 1\n\t" \
  "s_add_u32 s40, s40, s55\n\t" \
  "s_mul_i32 s60, s40, 18432\n\t" \
  "s_add_u32 s60, s60, s63\n\t" \
  "s_mul_i32 s50, s40, 1536\n\t" \
  "s_add_u32 s50, s50, 9437184\n\t" \
  "s_add_u32 s61, s60, 4096\n\t" \
  "s_add_u32 s62, s60, 8192\n\t" \
  "s_cmp_gt_u32 s44, s43\n\t" \
  "s_cselect_b32 s47, 1, 0\n\t" \
  "s_cmp_lt_u32 s46, 56\n\t" \
  "s_cselect_b32 s47, s47, 0\n\t" \
  "s_add_u32 s41, s43, 2\n\t" \
  "s_min_u32 s41, s41, 127\n\t" \
  "s_cmp_le_u32 s44, s41\n\t" \
  "s_cselect_b32 s56, s58, 0\n\t" \
  "s_waitcnt vmcnt(9)\n\t" \
  "v_pk_add_f32 v[116:117], v[104:105], v[106:107]\n\t" \
  "s_cmp_eq_u32 s56, 1\n\t" \
  "v_pk_add_f32 v[116:117], v[116:117], v[108:109]\n\t" \
  "s_cbranch_scc0 Lnn_%=\n\t" \
  "buffer_load_dword v163, v162, s[20:23], 0 offen sc1\n\t" \
  "Lnn_%=:\n\t" \
  "s_cmp_eq_u32 s47, 1\n\t" \
  "s_cbranch_scc0 Lnp_%=\n\t" \
  "buffer_load_dwordx2 v[104:105], v161, s[20:23], s50 offen offset:0 sc1\n\t" \
  "buffer_load_dwordx2 v[106:107], v161, s[20:23], s50 offen offset:512 sc1\n\t" \
  "buffer_load_dwordx2 v[108:109], v161, s[20:23], s50 offen offset:1024 sc1\n\t" \
  "s_waitcnt vmcnt(11)\n\t" \
  "v_mfma_f32_16x16x32_bf16 v[36:39], a[0:3], v[0:3], 0\n\t" \
  "v_mfma_f32_16x16x32_bf16 v[40:43], a[4:7], v[0:3], 0\n\t" \
  "v_mfma_f32_16x16x32_bf16 v[44:47], a[8:11], v[0:3], 0\n\t" \
  "v_mfma_f32_16x16x32_bf16 v[48:51], a[12:15], v[0:3], 0\n\t" \
  "v_mfma_f32_16x16x32_bf16 v[52:55], a[16:19], v[0:3], 0\n\t" \
  "v_mfma_f32_16x16x32_bf16 v[56:59], a[20:23], v[0:3], 0\n\t" \
  "v_mfma_f32_16x16x32_bf16 v[60:63], a[24:27], v[0:3], 0\n\t" \
  "v_mfma_f32_16x16x32_bf16 v[64:67], a[28:31], v[0:3], 0\n\t" \
  "buffer_load_dwordx4 v[0:3], v160, s[20:23], s60 offen offset:0 sc1\n\t" \
  "s_waitcnt vmcnt(11)\n\t" \
  "v_mfma_f32_16x16x32_bf16 v[36:39], a[32:35], v[4:7], v[36:39]\n\t" \
  "v_mov_b32 v118, v116\n\t" \
  "v_mfma_f32_16x16x32_bf16 v[40:43], a[36:39], v[4:7], v[40:43]\n\t" \
  "v_mov_b32 v119, v117\n\t" \
  "v_mfma_f32_16x16x32_bf16 v[44:47], a[40:43], v[4:7], v[44:47]\n\t" \
  "s_nop 0\n\t" \
  "v_mfma_f32_16x16x32_bf16 v[48:51], a[44:47], v[4:7], v[48:51]\n\t" \
  "v_permlane16_swap_b32 v116, v118\n\t" \
  "v_mfma_f32_16x16x32_bf16 v[52:55], a[48:51], v[4:7], v[52:55]\n\t" \
  "v_permlane16_swap_b32 v117, v119\n\t" \
  "v_mfma_f32_16x16x32_bf16 v[56:59], a[52:55], v[4:7], v[56:59]\n\t" \
  "v_add_f32 v116, v116, v118\n\t" \
  "v_mfma_f32_16x16x32_bf16 v[60:63], a[56:59], v[4:7], v[60:63]\n\t" \
  "v_add_f32 v117, v117, v119\n\t" \
  "v_mfma_f32_16x16x32_bf16 v[64:67], a[60:63], v[4:7], v[64:67]\n\t" \
  "v_mov_b32 v118, v116\n\t" \
  "buffer_load_dwordx4 v[4:7], v160, s[20:23], s60 offen offset:1024 sc1\n\t" \
  "s_waitcnt vmcnt(11)\n\t" \
  "v_mfma_f32_16x16x32_bf16 v[36:39], a[64:67], v[8:11], v[36:39]\n\t" \
  "v_mov_b32 v119, v117\n\t" \
  "v_mfma_f32_16x16x32_bf16 v[40:43], a[68:71], v[8:11], v[40:43]\n\t" \
  "s_nop 0\n\t" \
  "v_mfma_f32_16x16x32_bf16 v[44:47], a[72:75], v[8:11], v[44:47]\n\t" \
  "v_permlane32_swap_b32 v116, v118\n\t" \
  "v_mfma_f32_16x16x32_bf16 v[48:51], a[76:79], v[8:11], v[48:51]\n\t" \
  "v_permlane32_swap_b32 v117, v119\n\t" \
  "v_mfma_f32_16x16x32_bf16 v[52:55], a[80:83], v[8:11], v[52:55]\n\t" \
  "v_add_f32 v116, v116, v118\n\t" \
  "v_mfma_f32_16x16x32_bf16 v[56:59], a[84:87], v[8:11], v[56:59]\n\t" \
  "v_add_f32 v117, v117, v119\n\t" \
  "v_mfma_f32_16x16x32_bf16 v[60:63], a[88:91], v[8:11], v[60:63]\n\t" \
  "v_mul_f32 v170, 0x3ae38e39, v116\n\t" \
  "v_mfma_f32_16x16x32_bf16 v[64:67], a[92:95], v[8:11], v[64:67]\n\t" \
  "v_mul_f32 v117, 0x3ae38e39, v117\n\t" \
  "buffer_load_dwordx4 v[8:11], v160, s[20:23], s60 offen offset:2048 sc1\n\t" \
  "s_waitcnt vmcnt(11)\n\t" \
  "v_mfma_f32_16x16x32_bf16 v[36:39], a[96:99], v[12:15], v[36:39]\n\t" \
  "v_fma_f32 v117, -v170, v170, v117\n\t" \
  "v_mfma_f32_16x16x32_bf16 v[40:43], a[100:103], v[12:15], v[40:43]\n\t" \
  "v_max_f32 v117, 0, v117\n\t" \
  "v_mfma_f32_16x16x32_bf16 v[44:47], a[104:107], v[12:15], v[44:47]\n\t" \
  "v_add_f32 v117, 0x3727c5ac, v117\n\t" \
  "v_mfma_f32_16x16x32_bf16 v[48:51], a[108:111], v[12:15], v[48:51]\n\t" \
  "v_rsq_f32 v171, v117\n\t" \
  "v_mfma_f32_16x16x32_bf16 v[52:55], a[112:115], v[12:15], v[52:55]\n\t" \
  "s_nop 0\n\t" \
  "v_mfma_f32_16x16x32_bf16 v[56:59], a[116:119], v[12:15], v[56:59]\n\t" \
  "v_mul_f32 v172, v171, v171\n\t" \
  "v_mfma_f32_16x16x32_bf16 v[60:63], a[120:123], v[12:15], v[60:63]\n\t" \
  "v_mul_f32 v173, 0xbfb8aa3b, v171\n\t" \
  "v_mfma_f32_16x16x32_bf16 v[64:67], a[124:127], v[12:15], v[64:67]\n\t" \
  "buffer_load_dwordx4 v[12:15], v160, s[20:23], s60 offen offset:3072 sc1\n\t" \
  "s_waitcnt vmcnt(11)\n\t" \
  "v_mfma_f32_16x16x32_bf16 v[36:39], a[128:131], v[16:19], v[36:39]\n\t" \
  "v_mfma_f32_16x16x32_bf16 v[40:43], a[132:135], v[16:19], v[40:43]\n\t" \
  "v_mfma_f32_16x16x32_bf16 v[44:47], a[136:139], v[16:19], v[44:47]\n\t" \
  "v_mfma_f32_16x16x32_bf16 v[48:51], a[140:143], v[16:19], v[48:51]\n\t" \
  "v_mfma_f32_16x16x32_bf16 v[52:55], a[144:147], v[16:19], v[52:55]\n\t" \
  "v_mfma_f32_16x16x32_bf16 v[56:59], a[148:151], v[16:19], v[56:59]\n\t" \
  "v_mfma_f32_16x16x32_bf16 v[60:63], a[152:155], v[16:19], v[60:63]\n\t" \
  "v_mfma_f32_16x16x32_bf16 v[64:67], a[156:159], v[16:19], v[64:67]\n\t" \
  "buffer_load_dwordx4 v[16:19], v160, s[20:23], s61 offen offset:0 sc1\n\t" \
  "s_waitcnt vmcnt(11)\n\t" \
  "v_mfma_f32_16x16x32_bf16 v[36:39], a[160:163], v[20:23], v[36:39]\n\t" \
  "v_mfma_f32_16x16x32_bf16 v[40:43], a[164:167], v[20:23], v[40:43]\n\t" \
  "v_mfma_f32_16x16x32_bf16 v[44:47], a[168:171], v[20:23], v[44:47]\n\t" \
  "v_mfma_f32_16x16x32_bf16 v[48:51], a[172:175], v[20:23], v[48:51]\n\t" \
  "v_mfma_f32_16x16x32_bf16 v[52:55], a[176:179], v[20:23], v[52:55]\n\t" \
  "v_mfma_f32_16x16x32_bf16 v[56:59], a[180:183], v[20:23], v[56:59]\n\t" \
  "v_mfma_f32_16x16x32_bf16 v[60:63], a[184:187], v[20:23], v[60:63]\n\t" \
  "v_mfma_f32_16x16x32_bf16 v[64:67], a[188:191], v[20:23], v[64:67]\n\t" \
  "buffer_load_dwordx4 v[20:23], v160, s[20:23], s61 offen offset:1024 sc1\n\t" \
  "s_waitcnt vmcnt(11)\n\t" \
  "v_mfma_f32_16x16x32_bf16 v[36:39], a[192:195], v[24:27], v[36:39]\n\t" \
  "v_mfma_f32_16x16x32_bf16 v[40:43], a[196:199], v[24:27], v[40:43]\n\t" \
  "v_mfma_f32_16x16x32_bf16 v[44:47], a[200:203], v[24:27], v[44:47]\n\t" \
  "v_mfma_f32_16x16x32_bf16 v[48:51], a[204:207], v[24:27], v[48:51]\n\t" \
  "v_mfma_f32_16x16x32_bf16 v[52:55], a[208:211], v[24:27], v[52:55]\n\t" \
  "v_mfma_f32_16x16x32_bf16 v[56:59], a[212:215], v[24:27], v[56:59]\n\t" \
  "v_mfma_f32_16x16x32_bf16 v[60:63], a[216:219], v[24:27], v[60:63]\n\t" \
  "v_mfma_f32_16x16x32_bf16 v[64:67], a[220:223], v[24:27], v[64:67]\n\t" \
  "buffer_load_dwordx4 v[24:27], v160, s[20:23], s61 offen offset:2048 sc1\n\t" \
  "s_waitcnt vmcnt(11)\n\t" \
  "v_mfma_f32_16x16x32_bf16 v[36:39], a[224:227], v[28:31], v[36:39]\n\t" \
  "v_mfma_f32_16x16x32_bf16 v[40:43], a[228:231], v[28:31], v[40:43]\n\t" \
  "v_mfma_f32_16x16x32_bf16 v[44:47], a[232:235], v[28:31], v[44:47]\n\t" \
  "v_mfma_f32_16x16x32_bf16 v[48:51], a[236:239], v[28:31], v[48:51]\n\t" \
  "v_mfma_f32_16x16x32_bf16 v[52:55], a[240:243], v[28:31], v[52:55]\n\t" \
  "v_mfma_f32_16x16x32_bf16 v[56:59], a[244:247], v[28:31], v[56:59]\n\t" \
  "v_mfma_f32_16x16x32_bf16 v[60:63], a[248:251], v[28:31], v[60:63]\n\t" \
  "v_mfma_f32_16x16x32_bf16 v[64:67], a[252:255], v[28:31], v[64:67]\n\t" \
  "buffer_load_dwordx4 v[28:31], v160, s[20:23], s61 offen offset:3072 sc1\n\t" \
  "s_waitcnt vmcnt(11)\n\t" \
  "v_mfma_f32_16x16x32_bf16 v[36:39], v[224:227], v[32:35], v[36:39]\n\t" \
  "v_mfma_f32_16x16x32_bf16 v[40:43], v[228:231], v[32:35], v[40:43]\n\t" \
  "v_mfma_f32_16x16x32_bf16 v[44:47], v[232:235], v[32:35], v[44:47]\n\t" \
  "v_mfma_f32_16x16x32_bf16 v[48:51], v[236:239], v[32:35], v[48:51]\n\t" \
  "v_mfma_f32_16x16x32_bf16 v[52:55], v[240:243], v[32:35], v[52:55]\n\t" \
  "v_mfma_f32_16x16x32_bf16 v[56:59], v[244:247], v[32:35], v[56:59]\n\t" \
  "v_mfma_f32_16x16x32_bf16 v[60:63], v[248:251], v[32:35], v[60:63]\n\t" \
  "v_mfma_f32_16x16x32_bf16 v[64:67], v[252:255], v[32:35], v[64:67]\n\t" \
  "buffer_load_dwordx4 v[32:35], v160, s[20:23], s62 offen offset:0 sc1\n\t" \
  "s_waitcnt vmcnt(12)\n\t" \
  "s_branch Lkd_%=\n\t" \
  "Lnp_%=:\n\t" \
  "s_waitcnt vmcnt(8)\n\t" \
  "v_mfma_f32_16x16x32_bf16 v[36:39], a[0:3], v[0:3], 0\n\t" \
  "v_mfma_f32_16x16x32_bf16 v[40:43], a[4:7], v[0:3], 0\n\t" \
  "v_mfma_f32_16x16x32_bf16 v[44:47], a[8:11], v[0:3], 0\n\t" \
  "v_mfma_f32_16x16x32_bf16 v[48:51], a[12:15], v[0:3], 0\n\t" \
  "v_mfma_f32_16x16x32_bf16 v[52:55], a[16:19], v[0:3], 0\n\t" \
  "v_mfma_f32_16x16x32_bf16 v[56:59], a[20:23], v[0:3], 0\n\t" \
  "v_mfma_f32_16x16x32_bf16 v[60:63], a[24:27], v[0:3], 0\n\t" \
  "v_mfma_f32_16x16x32_bf16 v[64:67], a[28:31], v[0:3], 0\n\t" \
  "s_waitcnt vmcnt(7)\n\t" \
  "v_mfma_f32_16x16x32_bf16 v[36:39], a[32:35], v[4:7], v[36:39]\n\t" \
  "v_mov_b32 v118, v116\n\t" \
  "v_mfma_f32_16x16x32_bf16 v[40:43], a[36:39], v[4:7], v[40:43]\n\t" \
  "v_mov_b32 v119, v117\n\t" \
  "v_mfma_f32_16x16x32_bf16 v[44:47], a[40:43], v[4:7], v[44:47]\n\t" \
  "s_nop 0\n\t" \
  "v_mfma_f32_16x16x32_bf16 v[48:51], a[44:47], v[4:7], v[48:51]\n\t" \
  "v_permlane16_swap_b32 v116, v118\n\t" \
  "v_mfma_f32_16x16x32_bf16 v[52:55], a[48:51], v[4:7], v[52:55]\n\t" \
  "v_permlane16_swap_b32 v117, v119\n\t" \
  "v_mfma_f32_16x16x32_bf16 v[56:59], a[52:55], v[4:7], v[56:59]\n\t" \
  "v_add_f32 v116, v116, v118\n\t" \
  "v_mfma_f32_16x16x32_bf16 v[60:63], a[56:59], v[4:7], v[60:63]\n\t" \
  "v_add_f32 v117, v117, v119\n\t" \
  "v_mfma_f32_16x16x32_bf16 v[64:67], a[60:63], v[4:7], v[64:67]\n\t" \
  "v_mov_b32 v118, v116\n\t" \
  "s_waitcnt vmcnt(6)\n\t" \
  "v_mfma_f32_16x16x32_bf16 v[36:39], a[64:67], v[8:11], v[36:39]\n\t" \
  "v_mov_b32 v119, v117\n\t" \
  "v_mfma_f32_16x16x32_bf16 v[40:43], a[68:71], v[8:11], v[40:43]\n\t" \
  "s_nop 0\n\t" \
  "v_mfma_f32_16x16x32_bf16 v[44:47], a[72:75], v[8:11], v[44:47]\n\t" \
  "v_permlane32_swap_b32 v116, v118\n\t" \
  "v_mfma_f32_16x16x32_bf16 v[48:51], a[76:79], v[8:11], v[48:51]\n\t" \
  "v_permlane32_swap_b32 v117, v119\n\t" \
  "v_mfma_f32_16x16x32_bf16 v[52:55], a[80:83], v[8:11], v[52:55]\n\t" \
  "v_add_f32 v116, v116, v118\n\t" \
  "v_mfma_f32_16x16x32_bf16 v[56:59], a[84:87], v[8:11], v[56:59]\n\t" \
  "v_add_f32 v117, v117, v119\n\t" \
  "v_mfma_f32_16x16x32_bf16 v[60:63], a[88:91], v[8:11], v[60:63]\n\t" \
  "v_mul_f32 v170, 0x3ae38e39, v116\n\t" \
  "v_mfma_f32_16x16x32_bf16 v[64:67], a[92:95], v[8:11], v[64:67]\n\t" \
  "v_mul_f32 v117, 0x3ae38e39, v117\n\t" \
  "s_waitcnt vmcnt(5)\n\t" \
  "v_mfma_f32_16x16x32_bf16 v[36:39], a[96:99], v[12:15], v[36:39]\n\t" \
  "v_fma_f32 v117, -v170, v170, v117\n\t" \
  "v_mfma_f32_16x16x32_bf16 v[40:43], a[100:103], v[12:15], v[40:43]\n\t" \
  "v_max_f32 v117, 0, v117\n\t" \
  "v_mfma_f32_16x16x32_bf16 v[44:47], a[104:107], v[12:15], v[44:47]\n\t" \
  "v_add_f32 v117, 0x3727c5ac, v117\n\t" \
  "v_mfma_f32_16x16x32_bf16 v[48:51], a[108:111], v[12:15], v[48:51]\n\t" \
  "v_rsq_f32 v171, v117\n\t" \
  "v_mfma_f32_16x16x32_bf16 v[52:55], a[112:115], v[12:15], v[52:55]\n\t" \
  "s_nop 0\n\t" \
  "v_mfma_f32_16x16x32_bf16 v[56:59], a[116:119], v[12:15], v[56:59]\n\t" \
  "v_mul_f32 v172, v171, v171\n\t" \
  "v_mfma_f32_16x16x32_bf16 v[60:63], a[120:123], v[12:15], v[60:63]\n\t" \
  "v_mul_f32 v173, 0xbfb8aa3b, v171\n\t" \
  "v_mfma_f32_16x16x32_bf16 v[64:67], a[124:127], v[12:15], v[64:67]\n\t" \
  "s_waitcnt vmcnt(4)\n\t" \
  "v_mfma_f32_16x16x32_bf16 v[36:39], a[128:131], v[16:19], v[36:39]\n\t" \
  "v_mfma_f32_16x16x32_bf16 v[40:43], a[132:135], v[16:19], v[40:43]\n\t" \
  "v_mfma_f32_16x16x32_bf16 v[44:47], a[136:139], v[16:19], v[44:47]\n\t" \
  "v_mfma_f32_16x16x32_bf16 v[48:51], a[140:143], v[16:19], v[48:51]\n\t" \
  "v_mfma_f32_16x16x32_bf16 v[52:55], a[144:147], v[16:19], v[52:55]\n\t" \
  "v_mfma_f32_16x16x32_bf16 v[56:59], a[148:151], v[16:19], v[56:59]\n\t" \
  "v_mfma_f32_16x16x32_bf16 v[60:63], a[152:155], v[16:19], v[60:63]\n\t" \
  "v_mfma_f32_16x16x32_bf16 v[64:67], a[156:159], v[16:19], v[64:67]\n\t" \
  "s_waitcnt vmcnt(3)\n\t" \
  "v_mfma_f32_16x16x32_bf16 v[36:39], a[160:163], v[20:23], v[36:39]\n\t" \
  "v_mfma_f32_16x16x32_bf16 v[40:43], a[164:167], v[20:23], v[40:43]\n\t" \
  "v_mfma_f32_16x16x32_bf16 v[44:47], a[168:171], v[20:23], v[44:47]\n\t" \
  "v_mfma_f32_16x16x32_bf16 v[48:51], a[172:175], v[20:23], v[48:51]\n\t" \
  "v_mfma_f32_16x16x32_bf16 v[52:55], a[176:179], v[20:23], v[52:55]\n\t" \
  "v_mfma_f32_16x16x32_bf16 v[56:59], a[180:183], v[20:23], v[56:59]\n\t" \
  "v_mfma_f32_16x16x32_bf16 v[60:63], a[184:187], v[20:23], v[60:63]\n\t" \
  "v_mfma_f32_16x16x32_bf16 v[64:67], a[188:191], v[20:23], v[64:67]\n\t" \
  "s_waitcnt vmcnt(2)\n\t" \
  "v_mfma_f32_16x16x32_bf16 v[36:39], a[192:195], v[24:27], v[36:39]\n\t" \
  "v_mfma_f32_16x16x32_bf16 v[40:43], a[196:199], v[24:27], v[40:43]\n\t" \
  "v_mfma_f32_16x16x32_bf16 v[44:47], a[200:203], v[24:27], v[44:47]\n\t" \
  "v_mfma_f32_16x16x32_bf16 v[48:51], a[204:207], v[24:27], v[48:51]\n\t" \
  "v_mfma_f32_16x16x32_bf16 v[52:55], a[208:211], v[24:27], v[52:55]\n\t" \
  "v_mfma_f32_16x16x32_bf16 v[56:59], a[212:215], v[24:27], v[56:59]\n\t" \
  "v_mfma_f32_16x16x32_bf16 v[60:63], a[216:219], v[24:27], v[60:63]\n\t" \
  "v_mfma_f32_16x16x32_bf16 v[64:67], a[220:223], v[24:27], v[64:67]\n\t" \
  "s_waitcnt vmcnt(1)\n\t" \
  "v_mfma_f32_16x16x32_bf16 v[36:39], a[224:227], v[28:31], v[36:39]\n\t" \
  "v_mfma_f32_16x16x32_bf16 v[40:43], a[228:231], v[28:31], v[40:43]\n\t" \
  "v_mfma_f32_16x16x32_bf16 v[44:47], a[232:235], v[28:31], v[44:47]\n\t" \
  "v_mfma_f32_16x16x32_bf16 v[48:51], a[236:239], v[28:31], v[48:51]\n\t" \
  "v_mfma_f32_16x16x32_bf16 v[52:55], a[240:243], v[28:31], v[52:55]\n\t" \
  "v_mfma_f32_16x16x32_bf16 v[56:59], a[244:247], v[28:31], v[56:59]\n\t" \
  "v_mfma_f32_16x16x32_bf16 v[60:63], a[248:251], v[28:31], v[60:63]\n\t" \
  "v_mfma_f32_16x16x32_bf16 v[64:67], a[252:255], v[28:31], v[64:67]\n\t" \
  "s_waitcnt vmcnt(0)\n\t" \
  "v_mfma_f32_16x16x32_bf16 v[36:39], v[224:227], v[32:35], v[36:39]\n\t" \
  "v_mfma_f32_16x16x32_bf16 v[40:43], v[228:231], v[32:35], v[40:43]\n\t" \
  "v_mfma_f32_16x16x32_bf16 v[44:47], v[232:235], v[32:35], v[44:47]\n\t" \
  "v_mfma_f32_16x16x32_bf16 v[48:51], v[236:239], v[32:35], v[48:51]\n\t" \
  "v_mfma_f32_16x16x32_bf16 v[52:55], v[240:243], v[32:35], v[52:55]\n\t" \
  "v_mfma_f32_16x16x32_bf16 v[56:59], v[244:247], v[32:35], v[56:59]\n\t" \
  "v_mfma_f32_16x16x32_bf16 v[60:63], v[248:251], v[32:35], v[60:63]\n\t" \
  "v_mfma_f32_16x16x32_bf16 v[64:67], v[252:255], v[32:35], v[64:67]\n\t" \
  "s_waitcnt vmcnt(0)\n\t" \
  "Lkd_%=:\n\t" \
  "s_lshl_b32 s51, s45, 1\n\t" \
  "s_add_u32 s51, s51, s55\n\t" \
  "s_mul_i32 s51, s51, 144\n\t" \
  "s_add_u32 s51, s51, s53\n\t" \
  "s_lshl_b32 s51, s51, 10\n\t" \
  "s_add_u32 s51, s51, 94371840\n\t" \
  "s_and_b32 s64, s45, 1\n\t" \
  "s_lshl_b32 s64, s64, 14\n\t" \
  "v_add_u32 v166, s64, v174\n\t" \
  "v_add_u32 v167, s64, v175\n\t" \
  "ds_write_b128 v166, v[52:55] offset:0\n\t" \
  "ds_write_b128 v166, v[56:59] offset:1024\n\t" \
  "ds_write_b128 v166, v[60:63] offset:2048\n\t" \
  "ds_write_b128 v166, v[64:67] offset:3072\n\t" \
  "s_waitcnt lgkmcnt(0)\n\t" \
  "s_barrier\n\t" \
  "ds_read_b128 v[68:71], v167 offset:0\n\t" \
  "ds_read_b128 v[72:75], v167 offset:1024\n\t" \
  "ds_read_b128 v[76:79], v167 offset:2048\n\t" \
  "ds_read_b128 v[80:83], v167 offset:3072\n\t" \
  "s_waitcnt lgkmcnt(3)\n\t" \
  "v_pk_add_f32 v[36:37], v[36:37], v[68:69]\n\t" \
  "v_pk_add_f32 v[38:39], v[38:39], v[70:71]\n\t" \
  "s_waitcnt lgkmcnt(2)\n\t" \
  "v_pk_add_f32 v[40:41], v[40:41], v[72:73]\n\t" \
  "v_pk_add_f32 v[42:43], v[42:43], v[74:75]\n\t" \
  "s_waitcnt lgkmcnt(1)\n\t" \
  "v_pk_add_f32 v[44:45], v[44:45], v[76:77]\n\t" \
  "v_pk_add_f32 v[46:47], v[46:47], v[78:79]\n\t" \
  "s_waitcnt lgkmcnt(0)\n\t" \
  "v_pk_add_f32 v[48:49], v[48:49], v[80:81]\n\t" \
  "v_pk_add_f32 v[50:51], v[50:51], v[82:83]\n\t" \
  "v_pk_fma_f32 v[36:37], v[88:89], v[170:171], v[36:37] op_sel_hi:[1,0,1] neg_lo:[0,1,0] neg_hi:[0,1,0]\n\t" \
  "v_pk_fma_f32 v[38:39], v[90:91], v[170:171], v[38:39] op_sel_hi:[1,0,1] neg_lo:[0,1,0] neg_hi:[0,1,0]\n\t" \
  "v_pk_fma_f32 v[40:41], v[92:93], v[170:171], v[40:41] op_sel_hi:[1,0,1] neg_lo:[0,1,0] neg_hi:[0,1,0]\n\t" \
  "v_pk_fma_f32 v[42:43], v[94:95], v[170:171], v[42:43] op_sel_hi:[1,0,1] neg_lo:[0,1,0] neg_hi:[0,1,0]\n\t" \
  "v_pk_fma_f32 v[44:45], v[96:97], v[170:171], v[44:45] op_sel_hi:[1,0,1] neg_lo:[0,1,0] neg_hi:[0,1,0]\n\t" \
  "v_pk_fma_f32 v[46:47], v[98:99], v[170:171], v[46:47] op_sel_hi:[1,0,1] neg_lo:[0,1,0] neg_hi:[0,1,0]\n\t" \
  "v_pk_fma_f32 v[48:49], v[100:101], v[170:171], v[48:49] op_sel_hi:[1,0,1] neg_lo:[0,1,0] neg_hi:[0,1,0]\n\t" \
  "v_pk_fma_f32 v[50:51], v[102:103], v[170:171], v[50:51] op_sel_hi:[1,0,1] neg_lo:[0,1,0] neg_hi:[0,1,0]\n\t" \
  "v_mul_f32 v116, v173, v36\n\t" \
  "v_mul_f32 v117, v173, v37\n\t" \
  "v_mul_f32 v118, v173, v38\n\t" \
  "v_mul_f32 v119, v173, v39\n\t" \
  "v_mul_f32 v120, v173, v40\n\t" \
  "v_mul_f32 v121, v173, v41\n\t" \
  "v_mul_f32 v122, v173, v42\n\t" \
  "v_mul_f32 v123, v173, v43\n\t" \
  "v_exp_f32 v116, v116\n\t" \
  "v_exp_f32 v117, v117\n\t" \
  "v_exp_f32 v118, v118\n\t" \
  "v_exp_f32 v119, v119\n\t" \
  "v_exp_f32 v120, v120\n\t" \
  "v_exp_f32 v121, v121\n\t" \
  "v_exp_f32 v122, v122\n\t" \
  "v_exp_f32 v123, v123\n\t" \
  "v_pk_mul_f32 v[36:37], v[36:37], v[44:45]\n\t" \
  "v_pk_mul_f32 v[38:39], v[38:39], v[46:47]\n\t" \
  "v_pk_mul_f32 v[40:41], v[40:41], v[48:49]\n\t" \
  "v_pk_mul_f32 v[42:43], v[42:43], v[50:51]\n\t" \
  "v_add_f32 v116, 1.0, v116\n\t" \
  "v_add_f32 v117, 1.0, v117\n\t" \
  "v_add_f32 v118, 1.0, v118\n\t" \
  "v_add_f32 v119, 1.0, v119\n\t" \
  "v_add_f32 v120, 1.0, v120\n\t" \
  "v_add_f32 v121, 1.0, v121\n\t" \
  "v_add_f32 v122, 1.0, v122\n\t" \
  "v_add_f32 v123, 1.0, v123\n\t" \
  "v_rcp_f32 v116, v116\n\t" \
  "v_rcp_f32 v117, v117\n\t" \
  "v_rcp_f32 v118, v118\n\t" \
  "v_rcp_f32 v119, v119\n\t" \
  "v_rcp_f32 v120, v120\n\t" \
  "v_rcp_f32 v121, v121\n\t" \
  "v_rcp_f32 v122, v122\n\t" \
  "v_rcp_f32 v123, v123\n\t" \
  "v_pk_mul_f32 v[36:37], v[36:37], v[172:173] op_sel_hi:[1,0]\n\t" \
  "v_pk_mul_f32 v[38:39], v[38:39], v[172:173] op_sel_hi:[1,0]\n\t" \
  "v_pk_mul_f32 v[40:41], v[40:41], v[172:173] op_sel_hi:[1,0]\n\t" \
  "v_pk_mul_f32 v[42:43], v[42:43], v[172:173] op_sel_hi:[1,0]\n\t" \
  "v_pk_mul_f32 v[36:37], v[36:37], v[116:117]\n\t" \
  "v_pk_mul_f32 v[38:39], v[38:39], v[118:119]\n\t" \
  "v_pk_mul_f32 v[40:41], v[40:41], v[120:121]\n\t" \
  "v_pk_mul_f32 v[42:43], v[42:43], v[122:123]\n\t" \
  "v_cvt_pk_bf16_f32 v124, v36, v37\n\t" \
  "v_cvt_pk_bf16_f32 v125, v38, v39\n\t" \
  "v_cvt_pk_bf16_f32 v126, v40, v41\n\t" \
  "v_cvt_pk_bf16_f32 v127, v42, v43\n\t" \
  "buffer_store_dwordx4 v[124:127], v160, s[20:23], s51 offen sc1\n\t" \
  "s_and_b32 s41, s45, 1\n\t" \
  "s_lshl_b32 s41, s41, 2\n\t" \
  "s_add_u32 s41, s41, 150000\n\t" \
  "s_cmp_eq_u32 s56, 1\n\t" \
  "s_cbranch_scc0 Lne_%=\n\t" \
  "v_max_u32 v163, v163, v164\n\t" \
  "s_nop 1\n\t" \
  "v_min_u32_dpp v163, v163, v163 row_ror:1 row_mask:0xf bank_mask:0xf\n\t" \
  "s_nop 1\n\t" \
  "v_min_u32_dpp v163, v163, v163 row_ror:2 row_mask:0xf bank_mask:0xf\n\t" \
  "s_nop 1\n\t" \
  "v_min_u32_dpp v163, v163, v163 row_ror:4 row_mask:0xf bank_mask:0xf\n\t" \
  "s_nop 1\n\t" \
  "v_min_u32_dpp v163, v163, v163 row_ror:8 row_mask:0xf bank_mask:0xf\n\t" \
  "s_nop 1\n\t" \
  "v_min_u32_dpp v163, v163, v163 row_bcast:15 row_mask:0xa bank_mask:0xf\n\t" \
  "s_nop 1\n\t" \
  "v_min_u32_dpp v163, v163, v163 row_bcast:31 row_mask:0xc bank_mask:0xf\n\t" \
  "s_nop 0\n\t" \
  "v_readlane_b32 s44, v163, 63\n\t" \
  "s_nop 1\n\t" \
  "Lne_%=:\n\t" \
  "v_mov_b32 v169, s41\n\t" \
  "v_mov_b32 v166, s44\n\t" \
  "s_mov_b64 exec, s[58:59]\n\t" \
  "ds_write_b32 v169, v166\n\t" \
  "s_mov_b64 exec, -1\n\t" \
  "s_waitcnt lgkmcnt(0)\n\t" \
  "s_barrier\n\t" \
  "ds_read_b32 v166, v169\n\t" \
  "s_waitcnt lgkmcnt(0)\n\t" \
  "v_readfirstlane_b32 s44, v166\n\t" \
  "s_nop 1\n\t" \
  "s_cmp_lt_i32 s48, 0\n\t" \
  "s_cbranch_scc1 Lnf_%=\n\t" \
  "s_and_b32 s40, s45, s47\n\t" \
  "s_cbranch_scc1 Lnf_%=\n\t" \
  "v_mov_b32 v166, s48\n\t" \
  "s_mov_b64 exec, s[58:59]\n\t" \
  "buffer_store_dword v166, off, s[20:23], s52 sc1\n\t" \
  "s_mov_b64 exec, -1\n\t" \
  "Lnf_%=:\n\t" \
  "s_mov_b32 s45, s46\n\t" \
  "s_mov_b32 s48, s46\n\t" \
  "s_cmp_eq_u32 s47, 1\n\t" \
  "s_cbranch_scc1 Ltop_%=\n\t" \
  "s_cmp_eq_u32 s46, 56\n\t" \
  "s_cbranch_scc1 Ldr_%=\n\t" \
  "s_cmp_gt_u32 s44, s43\n\t" \
  "s_cbranch_scc1 Lbu_%=\n\t" \
  "Ldr_%=:\n\t" \
  "s_waitcnt vmcnt(0)\n\t" \
  "s_barrier\n\t" \
  "v_mov_b32 v166, s48\n\t" \
  "s_mov_b64 exec, s[58:59]\n\t" \
  "buffer_store_dword v166, off, s[20:23], s52 sc1\n\t" \
  "s_mov_b64 exec, -1\n\t" \
  "s_mov_b32 s48, -1\n\t" \
  "s_cmp_lt_u32 s46, 64\n\t" \
  "s_cbranch_scc0 Lend_%=\n\t" \
  "Lblk_%=:\n\t" \
  "s_lshl_b32 s40, s46, 1\n\t" \
  "s_or_b32 s43, s40, 1\n\t" \
  "s_add_u32 s40, s40, s55\n\t" \
  "s_mul_i32 s60, s40, 18432\n\t" \
  "s_add_u32 s60, s60, s63\n\t" \
  "s_mul_i32 s50, s40, 1536\n\t" \
  "s_add_u32 s50, s50, 9437184\n\t" \
  "s_add_u32 s61, s60, 4096\n\t" \
  "s_add_u32 s62, s60, 8192\n\t" \
  "v_mov_b32 v169, 150008\n\t" \
  "s_cmp_eq_u32 s27, 0\n\t" \
  "s_cbranch_scc0 Lsw_%=\n\t" \
  "s_mov_b32 s42, 0\n\t" \
  "La_sp_%=:\n\t" \
  "buffer_load_dword v163, v162, s[20:23], 0 offen sc1\n\t" \
  "s_waitcnt vmcnt(0)\n\t" \
  "v_max_u32 v163, v163, v164\n\t" \
  "s_nop 1\n\t" \
  "v_min_u32_dpp v163, v163, v163 row_ror:1 row_mask:0xf bank_mask:0xf\n\t" \
  "s_nop 1\n\t" \
  "v_min_u32_dpp v163, v163, v163 row_ror:2 row_mask:0xf bank_mask:0xf\n\t" \
  "s_nop 1\n\t" \
  "v_min_u32_dpp v163, v163, v163 row_ror:4 row_mask:0xf bank_mask:0xf\n\t" \
  "s_nop 1\n\t" \
  "v_min_u32_dpp v163, v163, v163 row_ror:8 row_mask:0xf bank_mask:0xf\n\t" \
  "s_nop 1\n\t" \
  "v_min_u32_dpp v163, v163, v163 row_bcast:15 row_mask:0xa bank_mask:0xf\n\t" \
  "s_nop 1\n\t" \
  "v_min_u32_dpp v163, v163, v163 row_bcast:31 row_mask:0xc bank_mask:0xf\n\t" \
  "s_nop 0\n\t" \
  "v_readlane_b32 s44, v163, 63\n\t" \
  "s_nop 1\n\t" \
  "s_cmp_gt_u32 s44, s43\n\t" \
  "s_cbranch_scc1 La_ok_%=\n\t" \
  "s_add_u32 s42, s42, 1\n\t" \
  "s_cmp_gt_u32 s42, 1048576\n\t" \
  "s_cbranch_scc1 Ltmo_%=\n\t" \
  "s_and_b32 s40, s42, 255\n\t" \
  "s_cbranch_scc1 La_sl_%=\n\t" \
  "s_mov_b32 s40, 2157568\n\t" \
  "buffer_load_dword v163, off, s[20:23], s40 sc1\n\t" \
  "s_waitcnt vmcnt(0)\n\t" \
  "v_cmp_ne_u32 vcc, 0, v163\n\t" \
  "s_nop 0\n\t" \
  "s_cbranch_vccnz Lend_%=\n\t" \
  "La_sl_%=:\n\t" \
  "s_sleep 1\n\t" \
  "s_sub_u32 s40, s43, s44\n\t" \
  "s_cmp_lt_u32 s40, 4\n\t" \
  "s_cbranch_scc1 La_sp_%=\n\t" \
  "s_sleep 100\n\t" \
  "s_branch La_sp_%=\n\t" \
  "La_ok_%=:\n\t" \
  "v_mov_b32 v166, s44\n\t" \
  "ds_write_b32 v169, v166\n\t" \
  "s_waitcnt lgkmcnt(0)\n\t" \
  "Lsw_%=:\n\t" \
  "s_barrier\n\t" \
  "buffer_load_dwordx2 v[104:105], v161, s[20:23], s50 offen offset:0 sc1\n\t" \
  "buffer_load_dwordx2 v[106:107], v161, s[20:23], s50 offen offset:512 sc1\n\t" \
  "buffer_load_dwordx2 v[108:109], v161, s[20:23], s50 offen offset:1024 sc1\n\t" \
  "buffer_load_dwordx4 v[0:3], v160, s[20:23], s60 offen offset:0 sc1\n\t" \
  "buffer_load_dwordx4 v[4:7], v160, s[20:23], s60 offen offset:1024 sc1\n\t" \
  "buffer_load_dwordx4 v[8:11], v160, s[20:23], s60 offen offset:2048 sc1\n\t" \
  "buffer_load_dwordx4 v[12:15], v160, s[20:23], s60 offen offset:3072 sc1\n\t" \
  "buffer_load_dwordx4 v[16:19], v160, s[20:23], s61 offen offset:0 sc1\n\t" \
  "buffer_load_dwordx4 v[20:23], v160, s[20:23], s61 offen offset:1024 sc1\n\t" \
  "buffer_load_dwordx4 v[24:27], v160, s[20:23], s61 offen offset:2048 sc1\n\t" \
  "buffer_load_dwordx4 v[28:31], v160, s[20:23], s61 offen offset:3072 sc1\n\t" \
  "buffer_load_dwordx4 v[32:35], v160, s[20:23], s62 offen offset:0 sc1\n\t" \
  "s_cmp_ge_u32 s46, 56\n\t" \
  "s_cbranch_scc0 Lbh1_%=\n\t" \
  "s_add_u32 s40, s46, 1\n\t" \
  "v_mov_b32 v166, s40\n\t" \
  "s_mov_b64 exec, s[58:59]\n\t" \
  "buffer_store_dword v166, off, s[20:23], s52 sc1\n\t" \
  "s_mov_b64 exec, -1\n\t" \
  "Lbh1_%=:\n\t" \
  "ds_read_b32 v166, v169\n\t" \
  "s_waitcnt lgkmcnt(0)\n\t" \
  "v_readfirstlane_b32 s44, v166\n\t" \
  "s_nop 1\n\t" \
  "s_branch Ltop_%=\n\t" \
  "Lbu_%=:\n\t" \
  "buffer_load_dwordx2 v[104:105], v161, s[20:23], s50 offen offset:0 sc1\n\t" \
  "buffer_load_dwordx2 v[106:107], v161, s[20:23], s50 offen offset:512 sc1\n\t" \
  "buffer_load_dwordx2 v[108:109], v161, s[20:23], s50 offen offset:1024 sc1\n\t" \
  "buffer_load_dwordx4 v[0:3], v160, s[20:23], s60 offen offset:0 sc1\n\t" \
  "buffer_load_dwordx4 v[4:7], v160, s[20:23], s60 offen offset:1024 sc1\n\t" \
  "buffer_load_dwordx4 v[8:11], v160, s[20:23], s60 offen offset:2048 sc1\n\t" \
  "buffer_load_dwordx4 v[12:15], v160, s[20:23], s60 offen offset:3072 sc1\n\t" \
  "buffer_load_dwordx4 v[16:19], v160, s[20:23], s61 offen offset:0 sc1\n\t" \
  "buffer_load_dwordx4 v[20:23], v160, s[20:23], s61 offen offset:1024 sc1\n\t" \
  "buffer_load_dwordx4 v[24:27], v160, s[20:23], s61 offen offset:2048 sc1\n\t" \
  "buffer_load_dwordx4 v[28:31], v160, s[20:23], s61 offen offset:3072 sc1\n\t" \
  "buffer_load_dwordx4 v[32:35], v160, s[20:23], s62 offen offset:0 sc1\n\t" \
  "s_waitcnt vmcnt(12)\n\t" \
  "s_barrier\n\t" \
  "v_mov_b32 v166, s48\n\t" \
  "s_mov_b64 exec, s[58:59]\n\t" \
  "buffer_store_dword v166, off, s[20:23], s52 sc1\n\t" \
  "s_mov_b64 exec, -1\n\t" \
  "s_mov_b32 s48, -1\n\t" \
  "s_branch Ltop_%=\n\t" \
  "Ltl_%=:\n\t" \
  "s_mov_b32 s57, 0\n\t" \
  "s_cmp_eq_u32 s27, 0\n\t" \
  "s_cbranch_scc0 Lto_%=\n\t" \
  "buffer_load_dword v163, v162, s[20:23], 0 offen sc1\n\t" \
  "Lto_%=:\n\t" \
  "Ltm_%=:\n\t" \
  "s_waitcnt vmcnt(9)\n\t" \
  "v_max3_u32 v168, v104, v105, v106\n\t" \
  "v_max3_u32 v168, v168, v107, v108\n\t" \
  "v_max_u32 v168, v168, v109\n\t" \
  "v_pk_add_f32 v[116:117], v[104:105], v[106:107]\n\t" \
  "v_pk_add_f32 v[116:117], v[116:117], v[108:109]\n\t" \
  "s_waitcnt vmcnt(8)\n\t" \
  "v_mfma_f32_16x16x32_bf16 v[36:39], a[0:3], v[0:3], 0\n\t" \
  "v_mfma_f32_16x16x32_bf16 v[40:43], a[4:7], v[0:3], 0\n\t" \
  "v_mfma_f32_16x16x32_bf16 v[44:47], a[8:11], v[0:3], 0\n\t" \
  "v_mfma_f32_16x16x32_bf16 v[48:51], a[12:15], v[0:3], 0\n\t" \
  "v_mfma_f32_16x16x32_bf16 v[52:55], a[16:19], v[0:3], 0\n\t" \
  "v_mfma_f32_16x16x32_bf16 v[56:59], a[20:23], v[0:3], 0\n\t" \
  "v_mfma_f32_16x16x32_bf16 v[60:63], a[24:27], v[0:3], 0\n\t" \
  "v_mfma_f32_16x16x32_bf16 v[64:67], a[28:31], v[0:3], 0\n\t" \
  "v_max3_u32 v168, v168, v0, v1\n\t" \
  "v_max3_u32 v168, v168, v2, v3\n\t" \
  "s_waitcnt vmcnt(7)\n\t" \
  "v_mfma_f32_16x16x32_bf16 v[36:39], a[32:35], v[4:7], v[36:39]\n\t" \
  "v_mov_b32 v118, v116\n\t" \
  "v_mfma_f32_16x16x32_bf16 v[40:43], a[36:39], v[4:7], v[40:43]\n\t" \
  "v_mov_b32 v119, v117\n\t" \
  "v_mfma_f32_16x16x32_bf16 v[44:47], a[40:43], v[4:7], v[44:47]\n\t" \
  "s_nop 0\n\t" \
  "v_mfma_f32_16x16x32_bf16 v[48:51], a[44:47], v[4:7], v[48:51]\n\t" \
  "v_permlane16_swap_b32 v116, v118\n\t" \
  "v_mfma_f32_16x16x32_bf16 v[52:55], a[48:51], v[4:7], v[52:55]\n\t" \
  "v_permlane16_swap_b32 v117, v119\n\t" \
  "v_mfma_f32_16x16x32_bf16 v[56:59], a[52:55], v[4:7], v[56:59]\n\t" \
  "v_add_f32 v116, v116, v118\n\t" \
  "v_mfma_f32_16x16x32_bf16 v[60:63], a[56:59], v[4:7], v[60:63]\n\t" \
  "v_add_f32 v117, v117, v119\n\t" \
  "v_mfma_f32_16x16x32_bf16 v[64:67], a[60:63], v[4:7], v[64:67]\n\t" \
  "v_mov_b32 v118, v116\n\t" \
  "v_max3_u32 v168, v168, v4, v5\n\t" \
  "v_max3_u32 v168, v168, v6, v7\n\t" \
  "s_waitcnt vmcnt(6)\n\t" \
  "v_mfma_f32_16x16x32_bf16 v[36:39], a[64:67], v[8:11], v[36:39]\n\t" \
  "v_mov_b32 v119, v117\n\t" \
  "v_mfma_f32_16x16x32_bf16 v[40:43], a[68:71], v[8:11], v[40:43]\n\t" \
  "s_nop 0\n\t" \
  "v_mfma_f32_16x16x32_bf16 v[44:47], a[72:75], v[8:11], v[44:47]\n\t" \
  "v_permlane32_swap_b32 v116, v118\n\t" \
  "v_mfma_f32_16x16x32_bf16 v[48:51], a[76:79], v[8:11], v[48:51]\n\t" \
  "v_permlane32_swap_b32 v117, v119\n\t" \
  "v_mfma_f32_16x16x32_bf16 v[52:55], a[80:83], v[8:11], v[52:55]\n\t" \
  "v_add_f32 v116, v116, v118\n\t" \
  "v_mfma_f32_16x16x32_bf16 v[56:59], a[84:87], v[8:11], v[56:59]\n\t" \
  "v_add_f32 v117, v117, v119\n\t" \
  "v_mfma_f32_16x16x32_bf16 v[60:63], a[88:91], v[8:11], v[60:63]\n\t" \
  "v_mul_f32 v170, 0x3ae38e39, v116\n\t" \
  "v_mfma_f32_16x16x32_bf16 v[64:67], a[92:95], v[8:11], v[64:67]\n\t" \
  "v_mul_f32 v117, 0x3ae38e39, v117\n\t" \
  "v_max3_u32 v168, v168, v8, v9\n\t" \
  "v_max3_u32 v168, v168, v10, v11\n\t" \
  "s_waitcnt vmcnt(5)\n\t" \
  "v_mfma_f32_16x16x32_bf16 v[36:39], a[96:99], v[12:15], v[36:39]\n\t" \
  "v_fma_f32 v117, -v170, v170, v117\n\t" \
  "v_mfma_f32_16x16x32_bf16 v[40:43], a[100:103], v[12:15], v[40:43]\n\t" \
  "v_max_f32 v117, 0, v117\n\t" \
  "v_mfma_f32_16x16x32_bf16 v[44:47], a[104:107], v[12:15], v[44:47]\n\t" \
  "v_add_f32 v117, 0x3727c5ac, v117\n\t" \
  "v_mfma_f32_16x16x32_bf16 v[48:51], a[108:111], v[12:15], v[48:51]\n\t" \
  "v_rsq_f32 v171, v117\n\t" \
  "v_mfma_f32_16x16x32_bf16 v[52:55], a[112:115], v[12:15], v[52:55]\n\t" \
  "s_nop 0\n\t" \
  "v_mfma_f32_16x16x32_bf16 v[56:59], a[116:119], v[12:15], v[56:59]\n\t" \
  "v_mul_f32 v172, v171, v171\n\t" \
  "v_mfma_f32_16x16x32_bf16 v[60:63], a[120:123], v[12:15], v[60:63]\n\t" \
  "v_mul_f32 v173, 0xbfb8aa3b, v171\n\t" \
  "v_mfma_f32_16x16x32_bf16 v[64:67], a[124:127], v[12:15], v[64:67]\n\t" \
  "v_max3_u32 v168, v168, v12, v13\n\t" \
  "v_max3_u32 v168, v168, v14, v15\n\t" \
  "s_waitcnt vmcnt(4)\n\t" \
  "v_mfma_f32_16x16x32_bf16 v[36:39], a[128:131], v[16:19], v[36:39]\n\t" \
  "v_mfma_f32_16x16x32_bf16 v[40:43], a[132:135], v[16:19], v[40:43]\n\t" \
  "v_mfma_f32_16x16x32_bf16 v[44:47], a[136:139], v[16:19], v[44:47]\n\t" \
  "v_mfma_f32_16x16x32_bf16 v[48:51], a[140:143], v[16:19], v[48:51]\n\t" \
  "v_mfma_f32_16x16x32_bf16 v[52:55], a[144:147], v[16:19], v[52:55]\n\t" \
  "v_mfma_f32_16x16x32_bf16 v[56:59], a[148:151], v[16:19], v[56:59]\n\t" \
  "v_mfma_f32_16x16x32_bf16 v[60:63], a[152:155], v[16:19], v[60:63]\n\t" \
  "v_mfma_f32_16x16x32_bf16 v[64:67], a[156:159], v[16:19], v[64:67]\n\t" \
  "v_max3_u32 v168, v168, v16, v17\n\t" \
  "v_max3_u32 v168, v168, v18, v19\n\t" \
  "s_waitcnt vmcnt(3)\n\t" \
  "v_mfma_f32_16x16x32_bf16 v[36:39], a[160:163], v[20:23], v[36:39]\n\t" \
  "v_mfma_f32_16x16x32_bf16 v[40:43], a[164:167], v[20:23], v[40:43]\n\t" \
  "v_mfma_f32_16x16x32_bf16 v[44:47], a[168:171], v[20:23], v[44:47]\n\t" \
  "v_mfma_f32_16x16x32_bf16 v[48:51], a[172:175], v[20:23], v[48:51]\n\t" \
  "v_mfma_f32_16x16x32_bf16 v[52:55], a[176:179], v[20:23], v[52:55]\n\t" \
  "v_mfma_f32_16x16x32_bf16 v[56:59], a[180:183], v[20:23], v[56:59]\n\t" \
  "v_mfma_f32_16x16x32_bf16 v[60:63], a[184:187], v[20:23], v[60:63]\n\t" \
  "v_mfma_f32_16x16x32_bf16 v[64:67], a[188:191], v[20:23], v[64:67]\n\t" \
  "v_max3_u32 v168, v168, v20, v21\n\t" \
  "v_max3_u32 v168, v168, v22, v23\n\t" \
  "s_waitcnt vmcnt(2)\n\t" \
  "v_mfma_f32_16x16x32_bf16 v[36:39], a[192:195], v[24:27], v[36:39]\n\t" \
  "v_mfma_f32_16x16x32_bf16 v[40:43], a[196:199], v[24:27], v[40:43]\n\t" \
  "v_mfma_f32_16x16x32_bf16 v[44:47], a[200:203], v[24:27], v[44:47]\n\t" \
  "v_mfma_f32_16x16x32_bf16 v[48:51], a[204:207], v[24:27], v[48:51]\n\t" \
  "v_mfma_f32_16x16x32_bf16 v[52:55], a[208:211], v[24:27], v[52:55]\n\t" \
  "v_mfma_f32_16x16x32_bf16 v[56:59], a[212:215], v[24:27], v[56:59]\n\t" \
  "v_mfma_f32_16x16x32_bf16 v[60:63], a[216:219], v[24:27], v[60:63]\n\t" \
  "v_mfma_f32_16x16x32_bf16 v[64:67], a[220:223], v[24:27], v[64:67]\n\t" \
  "v_max3_u32 v168, v168, v24, v25\n\t" \
  "v_max3_u32 v168, v168, v26, v27\n\t" \
  "s_waitcnt vmcnt(1)\n\t" \
  "v_mfma_f32_16x16x32_bf16 v[36:39], a[224:227], v[28:31], v[36:39]\n\t" \
  "v_mfma_f32_16x16x32_bf16 v[40:43], a[228:231], v[28:31], v[40:43]\n\t" \
  "v_mfma_f32_16x16x32_bf16 v[44:47], a[232:235], v[28:31], v[44:47]\n\t" \
  "v_mfma_f32_16x16x32_bf16 v[48:51], a[236:239], v[28:31], v[48:51]\n\t" \
  "v_mfma_f32_16x16x32_bf16 v[52:55], a[240:243], v[28:31], v[52:55]\n\t" \
  "v_mfma_f32_16x16x32_bf16 v[56:59], a[244:247], v[28:31], v[56:59]\n\t" \
  "v_mfma_f32_16x16x32_bf16 v[60:63], a[248:251], v[28:31], v[60:63]\n\t" \
  "v_mfma_f32_16x16x32_bf16 v[64:67], a[252:255], v[28:31], v[64:67]\n\t" \
  "v_max3_u32 v168, v168, v28, v29\n\t" \
  "v_max3_u32 v168, v168, v30, v31\n\t" \
  "s_waitcnt vmcnt(0)\n\t" \
  "v_mfma_f32_16x16x32_bf16 v[36:39], v[224:227], v[32:35], v[36:39]\n\t" \
  "v_mfma_f32_16x16x32_bf16 v[40:43], v[228:231], v[32:35], v[40:43]\n\t" \
  "v_mfma_f32_16x16x32_bf16 v[44:47], v[232:235], v[32:35], v[44:47]\n\t" \
  "v_mfma_f32_16x16x32_bf16 v[48:51], v[236:239], v[32:35], v[48:51]\n\t" \
  "v_mfma_f32_16x16x32_bf16 v[52:55], v[240:243], v[32:35], v[52:55]\n\t" \
  "v_mfma_f32_16x16x32_bf16 v[56:59], v[244:247], v[32:35], v[56:59]\n\t" \
  "v_mfma_f32_16x16x32_bf16 v[60:63], v[248:251], v[32:35], v[60:63]\n\t" \
  "v_mfma_f32_16x16x32_bf16 v[64:67], v[252:255], v[32:35], v[64:67]\n\t" \
  "v_max3_u32 v168, v168, v32, v33\n\t" \
  "v_max3_u32 v168, v168, v34, v35\n\t" \
  "v_cmp_eq_u32 vcc, -1, v168\n\t" \
  "s_waitcnt vmcnt(0)\n\t" \
  "s_cbranch_vccz Ltn_%=\n\t" \
  "s_add_u32 s57, s57, 1\n\t" \
  "s_cmp_gt_u32 s57, 4096\n\t" \
  "s_cbranch_scc1 Ltn_%=\n\t" \
  "s_nop 7\n\t" \
  "buffer_load_dwordx2 v[104:105], v161, s[20:23], s50 offen offset:0 sc1\n\t" \
  "buffer_load_dwordx2 v[106:107], v161, s[20:23], s50 offen offset:512 sc1\n\t" \
  "buffer_load_dwordx2 v[108:109], v161, s[20:23], s50 offen offset:1024 sc1\n\t" \
  "buffer_load_dwordx4 v[0:3], v160, s[20:23], s60 offen offset:0 sc1\n\t" \
  "buffer_load_dwordx4 v[4:7], v160, s[20:23], s60 offen offset:1024 sc1\n\t" \
  "buffer_load_dwordx4 v[8:11], v160, s[20:23], s60 offen offset:2048 sc1\n\t" \
  "buffer_load_dwordx4 v[12:15], v160, s[20:23], s60 offen offset:3072 sc1\n\t" \
  "buffer_load_dwordx4 v[16:19], v160, s[20:23], s61 offen offset:0 sc1\n\t" \
  "buffer_load_dwordx4 v[20:23], v160, s[20:23], s61 offen offset:1024 sc1\n\t" \
  "buffer_load_dwordx4 v[24:27], v160, s[20:23], s61 offen offset:2048 sc1\n\t" \
  "buffer_load_dwordx4 v[28:31], v160, s[20:23], s61 offen offset:3072 sc1\n\t" \
  "buffer_load_dwordx4 v[32:35], v160, s[20:23], s62 offen offset:0 sc1\n\t" \
  "s_branch Ltm_%=\n\t" \
  "Ltn_%=:\n\t" \
  "s_lshl_b32 s51, s45, 1\n\t" \
  "s_add_u32 s51, s51, s55\n\t" \
  "s_mul_i32 s51, s51, 144\n\t" \
  "s_add_u32 s51, s51, s53\n\t" \
  "s_lshl_b32 s51, s51, 10\n\t" \
  "s_add_u32 s51, s51, 94371840\n\t" \
  "s_lshl_b32 s40, s46, 1\n\t" \
  "s_or_b32 s43, s40, 1\n\t" \
  "s_add_u32 s40, s40, s55\n\t" \
  "s_mul_i32 s60, s40, 18432\n\t" \
  "s_add_u32 s60, s60, s63\n\t" \
  "s_mul_i32 s50, s40, 1536\n\t" \
  "s_add_u32 s50, s50, 9437184\n\t" \
  "s_add_u32 s61, s60, 4096\n\t" \
  "s_add_u32 s62, s60, 8192\n\t" \
  "s_cmp_eq_u32 s45, 63\n\t" \
  "s_cbranch_scc1 Lts_%=\n\t" \
  "s_and_b32 s41, s45, 1\n\t" \
  "s_lshl_b32 s41, s41, 2\n\t" \
  "s_add_u32 s41, s41, 150000\n\t" \
  "s_cmp_eq_u32 s27, 0\n\t" \
  "s_cbranch_scc0 Ltf_%=\n\t" \
  "v_max_u32 v163, v163, v164\n\t" \
  "s_nop 1\n\t" \
  "v_min_u32_dpp v163, v163, v163 row_ror:1 row_mask:0xf bank_mask:0xf\n\t" \
  "s_nop 1\n\t" \
  "v_min_u32_dpp v163, v163, v163 row_ror:2 row_mask:0xf bank_mask:0xf\n\t" \
  "s_nop 1\n\t" \
  "v_min_u32_dpp v163, v163, v163 row_ror:4 row_mask:0xf bank_mask:0xf\n\t" \
  "s_nop 1\n\t" \
  "v_min_u32_dpp v163, v163, v163 row_ror:8 row_mask:0xf bank_mask:0xf\n\t" \
  "s_nop 1\n\t" \
  "v_min_u32_dpp v163, v163, v163 row_bcast:15 row_mask:0xa bank_mask:0xf\n\t" \
  "s_nop 1\n\t" \
  "v_min_u32_dpp v163, v163, v163 row_bcast:31 row_mask:0xc bank_mask:0xf\n\t" \
  "s_nop 0\n\t" \
  "v_readlane_b32 s44, v163, 63\n\t" \
  "s_nop 1\n\t" \
  "Ltf_%=:\n\t" \
  "v_mov_b32 v169, s41\n\t" \
  "v_mov_b32 v166, s44\n\t" \
  "s_mov_b64 exec, s[58:59]\n\t" \
  "ds_write_b32 v169, v166\n\t" \
  "s_mov_b64 exec, -1\n\t" \
  "s_waitcnt lgkmcnt(0)\n\t" \
  "s_barrier\n\t" \
  "ds_read_b32 v166, v169\n\t" \
  "s_waitcnt lgkmcnt(0)\n\t" \
  "v_readfirstlane_b32 s44, v166\n\t" \
  "s_nop 1\n\t" \
  "s_cmp_gt_u32 s44, s43\n\t" \
  "s_cselect_b32 s47, 1, 0\n\t" \
  "s_cbranch_scc0 Lts_%=\n\t" \
  "buffer_load_dwordx2 v[104:105], v161, s[20:23], s50 offen offset:0 sc1\n\t" \
  "buffer_load_dwordx2 v[106:107], v161, s[20:23], s50 offen offset:512 sc1\n\t" \
  "buffer_load_dwordx2 v[108:109], v161, s[20:23], s50 offen offset:1024 sc1\n\t" \
  "buffer_load_dwordx4 v[0:3], v160, s[20:23], s60 offen offset:0 sc1\n\t" \
  "buffer_load_dwordx4 v[4:7], v160, s[20:23], s60 offen offset:1024 sc1\n\t" \
  "buffer_load_dwordx4 v[8:11], v160, s[20:23], s60 offen offset:2048 sc1\n\t" \
  "buffer_load_dwordx4 v[12:15], v160, s[20:23], s60 offen offset:3072 sc1\n\t" \
  "buffer_load_dwordx4 v[16:19], v160, s[20:23], s61 offen offset:0 sc1\n\t" \
  "buffer_load_dwordx4 v[20:23], v160, s[20:23], s61 offen offset:1024 sc1\n\t" \
  "buffer_load_dwordx4 v[24:27], v160, s[20:23], s61 offen offset:2048 sc1\n\t" \
  "buffer_load_dwordx4 v[28:31], v160, s[20:23], s61 offen offset:3072 sc1\n\t" \
  "buffer_load_dwordx4 v[32:35], v160, s[20:23], s62 offen offset:0 sc1\n\t" \
  "s_cmp_ge_u32 s46, 56\n\t" \
  "s_cbranch_scc0 Lbh2_%=\n\t" \
  "s_add_u32 s40, s46, 1\n\t" \
  "v_mov_b32 v166, s40\n\t" \
  "s_mov_b64 exec, s[58:59]\n\t" \
  "buffer_store_dword v166, off, s[20:23], s52 sc1\n\t" \
  "s_mov_b64 exec, -1\n\t" \
  "Lbh2_%=:\n\t" \
  "Lts_%=:\n\t" \
  "s_and_b32 s64, s45, 1\n\t" \
  "s_lshl_b32 s64, s64, 14\n\t" \
  "v_add_u32 v166, s64, v174\n\t" \
  "v_add_u32 v167, s64, v175\n\t" \
  "ds_write_b128 v166, v[52:55] offset:0\n\t" \
  "ds_write_b128 v166, v[56:59] offset:1024\n\t" \
  "ds_write_b128 v166, v[60:63] offset:2048\n\t" \
  "ds_write_b128 v166, v[64:67] offset:3072\n\t" \
  "s_waitcnt lgkmcnt(0)\n\t" \
  "s_barrier\n\t" \
  "ds_read_b128 v[68:71], v167 offset:0\n\t" \
  "ds_read_b128 v[72:75], v167 offset:1024\n\t" \
  "ds_read_b128 v[76:79], v167 offset:2048\n\t" \
  "ds_read_b128 v[80:83], v167 offset:3072\n\t" \
  "s_waitcnt lgkmcnt(3)\n\t" \
  "v_pk_add_f32 v[36:37], v[36:37], v[68:69]\n\t" \
  "v_pk_add_f32 v[38:39], v[38:39], v[70:71]\n\t" \
  "s_waitcnt lgkmcnt(2)\n\t" \
  "v_pk_add_f32 v[40:41], v[40:41], v[72:73]\n\t" \
  "v_pk_add_f32 v[42:43], v[42:43], v[74:75]\n\t" \
  "s_waitcnt lgkmcnt(1)\n\t" \
  "v_pk_add_f32 v[44:45], v[44:45], v[76:77]\n\t" \
  "v_pk_add_f32 v[46:47], v[46:47], v[78:79]\n\t" \
  "s_waitcnt lgkmcnt(0)\n\t" \
  "v_pk_add_f32 v[48:49], v[48:49], v[80:81]\n\t" \
  "v_pk_add_f32 v[50:51], v[50:51], v[82:83]\n\t" \
  "v_pk_fma_f32 v[36:37], v[88:89], v[170:171], v[36:37] op_sel_hi:[1,0,1] neg_lo:[0,1,0] neg_hi:[0,1,0]\n\t" \
  "v_pk_fma_f32 v[38:39], v[90:91], v[170:171], v[38:39] op_sel_hi:[1,0,1] neg_lo:[0,1,0] neg_hi:[0,1,0]\n\t" \
  "v_pk_fma_f32 v[40:41], v[92:93], v[170:171], v[40:41] op_sel_hi:[1,0,1] neg_lo:[0,1,0] neg_hi:[0,1,0]\n\t" \
  "v_pk_fma_f32 v[42:43], v[94:95], v[170:171], v[42:43] op_sel_hi:[1,0,1] neg_lo:[0,1,0] neg_hi:[0,1,0]\n\t" \
  "v_pk_fma_f32 v[44:45], v[96:97], v[170:171], v[44:45] op_sel_hi:[1,0,1] neg_lo:[0,1,0] neg_hi:[0,1,0]\n\t" \
  "v_pk_fma_f32 v[46:47], v[98:99], v[170:171], v[46:47] op_sel_hi:[1,0,1] neg_lo:[0,1,0] neg_hi:[0,1,0]\n\t" \
  "v_pk_fma_f32 v[48:49], v[100:101], v[170:171], v[48:49] op_sel_hi:[1,0,1] neg_lo:[0,1,0] neg_hi:[0,1,0]\n\t" \
  "v_pk_fma_f32 v[50:51], v[102:103], v[170:171], v[50:51] op_sel_hi:[1,0,1] neg_lo:[0,1,0] neg_hi:[0,1,0]\n\t" \
  "v_mul_f32 v116, v173, v36\n\t" \
  "v_mul_f32 v117, v173, v37\n\t" \
  "v_mul_f32 v118, v173, v38\n\t" \
  "v_mul_f32 v119, v173, v39\n\t" \
  "v_mul_f32 v120, v173, v40\n\t" \
  "v_mul_f32 v121, v173, v41\n\t" \
  "v_mul_f32 v122, v173, v42\n\t" \
  "v_mul_f32 v123, v173, v43\n\t" \
  "v_exp_f32 v116, v116\n\t" \
  "v_exp_f32 v117, v117\n\t" \
  "v_exp_f32 v118, v118\n\t" \
  "v_exp_f32 v119, v119\n\t" \
  "v_exp_f32 v120, v120\n\t" \
  "v_exp_f32 v121, v121\n\t" \
  "v_exp_f32 v122, v122\n\t" \
  "v_exp_f32 v123, v123\n\t" \
  "v_pk_mul_f32 v[36:37], v[36:37], v[44:45]\n\t" \
  "v_pk_mul_f32 v[38:39], v[38:39], v[46:47]\n\t" \
  "v_pk_mul_f32 v[40:41], v[40:41], v[48:49]\n\t" \
  "v_pk_mul_f32 v[42:43], v[42:43], v[50:51]\n\t" \
  "v_add_f32 v116, 1.0, v116\n\t" \
  "v_add_f32 v117, 1.0, v117\n\t" \
  "v_add_f32 v118, 1.0, v118\n\t" \
  "v_add_f32 v119, 1.0, v119\n\t" \
  "v_add_f32 v120, 1.0, v120\n\t" \
  "v_add_f32 v121, 1.0, v121\n\t" \
  "v_add_f32 v122, 1.0, v122\n\t" \
  "v_add_f32 v123, 1.0, v123\n\t" \
  "v_rcp_f32 v116, v116\n\t" \
  "v_rcp_f32 v117, v117\n\t" \
  "v_rcp_f32 v118, v118\n\t" \
  "v_rcp_f32 v119, v119\n\t" \
  "v_rcp_f32 v120, v120\n\t" \
  "v_rcp_f32 v121, v121\n\t" \
  "v_rcp_f32 v122, v122\n\t" \
  "v_rcp_f32 v123, v123\n\t" \
  "v_pk_mul_f32 v[36:37], v[36:37], v[172:173] op_sel_hi:[1,0]\n\t" \
  "v_pk_mul_f32 v[38:39], v[38:39], v[172:173] op_sel_hi:[1,0]\n\t" \
  "v_pk_mul_f32 v[40:41], v[40:41], v[172:173] op_sel_hi:[1,0]\n\t" \
  "v_pk_mul_f32 v[42:43], v[42:43], v[172:173] op_sel_hi:[1,0]\n\t" \
  "v_pk_mul_f32 v[36:37], v[36:37], v[116:117]\n\t" \
  "v_pk_mul_f32 v[38:39], v[38:39], v[118:119]\n\t" \
  "v_pk_mul_f32 v[40:41], v[40:41], v[120:121]\n\t" \
  "v_pk_mul_f32 v[42:43], v[42:43], v[122:123]\n\t" \
  "v_cvt_pk_bf16_f32 v124, v36, v37\n\t" \
  "v_cvt_pk_bf16_f32 v125, v38, v39\n\t" \
  "v_cvt_pk_bf16_f32 v126, v40, v41\n\t" \
  "v_cvt_pk_bf16_f32 v127, v42, v43\n\t" \
  "buffer_store_dwordx4 v[124:127], v160, s[20:23], s51 offen sc1\n\t" \
  "s_cmp_eq_u32 s45, 63\n\t" \
  "s_cbranch_scc1 Lend_%=\n\t" \
  "s_mov_b32 s45, s46\n\t" \
  "s_cmp_eq_u32 s47, 1\n\t" \
  "s_cbranch_scc1 Ltop_%=\n\t" \
  "s_branch Lblk_%=\n\t" \
  "s_branch Lend_%=\n\t" \
  "Ltmo_%=:\n\t" \
  "v_mov_b32 v166, 1\n\t" \
  "s_mov_b32 s40, 2157568\n\t" \
  "buffer_store_dword v166, off, s[20:23], s40 sc1\n\t" \
  "Lend_%=:\n\t" \
  "s_waitcnt vmcnt(0) lgkmcnt(0)\n\t"
#define QK_ASM \
  "s_mov_b32 s59, m0\n\t" \
  "s_load_dwordx2 s[24:25], %0, 80\n\t" \
  "s_and_b32 s26, %1, 3\n\t" \
  "s_bfe_u32 s27, %1, 0x50008\n\t" \
  "s_bfe_u32 s28, %1, 0x10010\n\t" \
  "s_bfe_u32 s29, %1, 0x20014\n\t" \
  "s_lshr_b32 s52, %1, 24\n\t" \
  "s_lshl_b32 s52, s52, 7\n\t" \
  "s_add_u32 s52, s52, 2752512\n\t" \
  "s_mov_b64 exec, -1\n\t" \
  "s_waitcnt lgkmcnt(0)\n\t" \
  "s_mov_b32 s20, s24\n\t" \
  "s_and_b32 s21, s25, 0xffff\n\t" \
  "s_mov_b32 s22, 113246208\n\t" \
  "s_mov_b32 s23, 0x20000\n\t" \
  "v_mbcnt_lo_u32_b32 v76, -1, 0\n\t" \
  "v_mbcnt_hi_u32_b32 v76, -1, v76\n\t" \
  "s_lshl_b32 s40, s26, 3\n\t" \
  "s_add_u32 s40, s40, 16\n\t" \
  "s_load_dwordx2 s[46:47], %0, s40\n\t" \
  "s_cmp_eq_u32 s29, 0\n\t" \
  "s_cselect_b64 s[54:55], 1, 0\n\t" \
  "s_mov_b32 s56, 0\n\t" \
  "s_mov_b32 s57, 0x80000000\n\t" \
  "v_lshrrev_b32 v0, 4, v76\n\t" \
  "v_and_b32 v1, 15, v76\n\t" \
  "v_lshlrev_b32 v2, 1, v0\n\t" \
  "v_xor_b32 v1, v1, v2\n\t" \
  "v_lshlrev_b32 v0, 13, v0\n\t" \
  "v_lshl_add_u32 v0, v1, 4, v0\n\t" \
  "v_xor_b32 v1, 128, v0\n\t" \
  "v_add_u32 v1, 65536, v1\n\t" \
  "v_add_u32 v2, 32768, v0\n\t" \
  "v_add_u32 v3, 32768, v1\n\t" \
  "v_min_u32 v75, 62, v76\n\t" \
  "v_lshlrev_b32 v75, 2, v75\n\t" \
  "v_add_u32 v75, 2158848, v75\n\t" \
  "s_mul_i32 s40, s29, 1179648\n\t" \
  "s_lshl_b32 s41, s27, 8\n\t" \
  "s_add_u32 s40, s40, s41\n\t" \
  "s_mul_i32 s58, s29, 36864\n\t" \
  "s_waitcnt lgkmcnt(0)\n\t" \
  "s_add_u32 s46, s46, s40\n\t" \
  "s_addc_u32 s47, s47, 0\n\t" \
  "s_add_u32 m0, s58, 0\n\t" \
  "s_nop 0\n\t" \
  "global_load_lds_dwordx4 v0, s[46:47]\n\t" \
  "s_add_u32 m0, s58, 1024\n\t" \
  "s_nop 0\n\t" \
  "global_load_lds_dwordx4 v2, s[46:47]\n\t" \
  "s_add_u32 m0, s58, 2048\n\t" \
  "s_nop 0\n\t" \
  "global_load_lds_dwordx4 v1, s[46:47]\n\t" \
  "s_add_u32 m0, s58, 3072\n\t" \
  "s_nop 0\n\t" \
  "global_load_lds_dwordx4 v3, s[46:47]\n\t" \
  "s_add_u32 s46, s46, 131072\n\t" \
  "s_addc_u32 s47, s47, 0\n\t" \
  "s_add_u32 m0, s58, 4096\n\t" \
  "s_nop 0\n\t" \
  "global_load_lds_dwordx4 v0, s[46:47]\n\t" \
  "s_add_u32 m0, s58, 5120\n\t" \
  "s_nop 0\n\t" \
  "global_load_lds_dwordx4 v2, s[46:47]\n\t" \
  "s_add_u32 m0, s58, 6144\n\t" \
  "s_nop 0\n\t" \
  "global_load_lds_dwordx4 v1, s[46:47]\n\t" \
  "s_add_u32 m0, s58, 7168\n\t" \
  "s_nop 0\n\t" \
  "global_load_lds_dwordx4 v3, s[46:47]\n\t" \
  "s_add_u32 s46, s46, 131072\n\t" \
  "s_addc_u32 s47, s47, 0\n\t" \
  "s_add_u32 m0, s58, 8192\n\t" \
  "s_nop 0\n\t" \
  "global_load_lds_dwordx4 v0, s[46:47]\n\t" \
  "s_add_u32 m0, s58, 9216\n\t" \
  "s_nop 0\n\t" \
  "global_load_lds_dwordx4 v2, s[46:47]\n\t" \
  "s_add_u32 m0, s58, 10240\n\t" \
  "s_nop 0\n\t" \
  "global_load_lds_dwordx4 v1, s[46:47]\n\t" \
  "s_add_u32 m0, s58, 11264\n\t" \
  "s_nop 0\n\t" \
  "global_load_lds_dwordx4 v3, s[46:47]\n\t" \
  "s_add_u32 s46, s46, 131072\n\t" \
  "s_addc_u32 s47, s47, 0\n\t" \
  "s_add_u32 m0, s58, 12288\n\t" \
  "s_nop 0\n\t" \
  "global_load_lds_dwordx4 v0, s[46:47]\n\t" \
  "s_add_u32 m0, s58, 13312\n\t" \
  "s_nop 0\n\t" \
  "global_load_lds_dwordx4 v2, s[46:47]\n\t" \
  "s_add_u32 m0, s58, 14336\n\t" \
  "s_nop 0\n\t" \
  "global_load_lds_dwordx4 v1, s[46:47]\n\t" \
  "s_add_u32 m0, s58, 15360\n\t" \
  "s_nop 0\n\t" \
  "global_load_lds_dwordx4 v3, s[46:47]\n\t" \
  "s_add_u32 s46, s46, 131072\n\t" \
  "s_addc_u32 s47, s47, 0\n\t" \
  "s_or_b32 s40, s29, 0\n\t" \
  "s_cbranch_scc1 Lh0_%=\n\t" \
  "s_waitcnt vmcnt(16)\n\t" \
  "buffer_load_dword v84, off, s[20:23], s52 sc1\n\t" \
  "Lh0_%=:\n\t" \
  "s_add_u32 m0, s58, 16384\n\t" \
  "s_nop 0\n\t" \
  "global_load_lds_dwordx4 v0, s[46:47]\n\t" \
  "s_add_u32 m0, s58, 17408\n\t" \
  "s_nop 0\n\t" \
  "global_load_lds_dwordx4 v2, s[46:47]\n\t" \
  "s_add_u32 m0, s58, 18432\n\t" \
  "s_nop 0\n\t" \
  "global_load_lds_dwordx4 v1, s[46:47]\n\t" \
  "s_add_u32 m0, s58, 19456\n\t" \
  "s_nop 0\n\t" \
  "global_load_lds_dwordx4 v3, s[46:47]\n\t" \
  "s_add_u32 s46, s46, 131072\n\t" \
  "s_addc_u32 s47, s47, 0\n\t" \
  "s_add_u32 m0, s58, 20480\n\t" \
  "s_nop 0\n\t" \
  "global_load_lds_dwordx4 v0, s[46:47]\n\t" \
  "s_add_u32 m0, s58, 21504\n\t" \
  "s_nop 0\n\t" \
  "global_load_lds_dwordx4 v2, s[46:47]\n\t" \
  "s_add_u32 m0, s58, 22528\n\t" \
  "s_nop 0\n\t" \
  "global_load_lds_dwordx4 v1, s[46:47]\n\t" \
  "s_add_u32 m0, s58, 23552\n\t" \
  "s_nop 0\n\t" \
  "global_load_lds_dwordx4 v3, s[46:47]\n\t" \
  "s_add_u32 s46, s46, 131072\n\t" \
  "s_addc_u32 s47, s47, 0\n\t" \
  "s_add_u32 m0, s58, 24576\n\t" \
  "s_nop 0\n\t" \
  "global_load_lds_dwordx4 v0, s[46:47]\n\t" \
  "s_add_u32 m0, s58, 25600\n\t" \
  "s_nop 0\n\t" \
  "global_load_lds_dwordx4 v2, s[46:47]\n\t" \
  "s_add_u32 m0, s58, 26624\n\t" \
  "s_nop 0\n\t" \
  "global_load_lds_dwordx4 v1, s[46:47]\n\t" \
  "s_add_u32 m0, s58, 27648\n\t" \
  "s_nop 0\n\t" \
  "global_load_lds_dwordx4 v3, s[46:47]\n\t" \
  "s_add_u32 s46, s46, 131072\n\t" \
  "s_addc_u32 s47, s47, 0\n\t" \
  "s_add_u32 m0, s58, 28672\n\t" \
  "s_nop 0\n\t" \
  "global_load_lds_dwordx4 v0, s[46:47]\n\t" \
  "s_add_u32 m0, s58, 29696\n\t" \
  "s_nop 0\n\t" \
  "global_load_lds_dwordx4 v2, s[46:47]\n\t" \
  "s_add_u32 m0, s58, 30720\n\t" \
  "s_nop 0\n\t" \
  "global_load_lds_dwordx4 v1, s[46:47]\n\t" \
  "s_add_u32 m0, s58, 31744\n\t" \
  "s_nop 0\n\t" \
  "global_load_lds_dwordx4 v3, s[46:47]\n\t" \
  "s_add_u32 s46, s46, 131072\n\t" \
  "s_addc_u32 s47, s47, 0\n\t" \
  "s_add_u32 m0, s58, 32768\n\t" \
  "s_nop 0\n\t" \
  "global_load_lds_dwordx4 v0, s[46:47]\n\t" \
  "s_add_u32 m0, s58, 33792\n\t" \
  "s_nop 0\n\t" \
  "global_load_lds_dwordx4 v2, s[46:47]\n\t" \
  "s_add_u32 m0, s58, 34816\n\t" \
  "s_nop 0\n\t" \
  "global_load_lds_dwordx4 v1, s[46:47]\n\t" \
  "s_add_u32 m0, s58, 35840\n\t" \
  "s_nop 0\n\t" \
  "global_load_lds_dwordx4 v3, s[46:47]\n\t" \
  "s_add_u32 s46, s46, 131072\n\t" \
  "s_addc_u32 s47, s47, 0\n\t" \
  "s_or_b32 s40, s29, 0\n\t" \
  "s_cbranch_scc1 Lh3_%=\n\t" \
  "s_cmp_eq_u32 s26, 0\n\t" \
  "s_cbranch_scc1 Lh2_%=\n\t" \
  "s_mov_b32 s42, 0\n\t" \
  "s_waitcnt vmcnt(20)\n\t" \
  "Lhs0_%=:\n\t" \
  "v_cmp_eq_u32 vcc, 0, v84\n\t" \
  "s_nop 0\n\t" \
  "s_cbranch_vccz Lhs1_%=\n\t" \
  "s_add_u32 s42, s42, 1\n\t" \
  "s_cmp_gt_u32 s42, 524288\n\t" \
  "s_cbranch_scc1 Lquit_%=\n\t" \
  "s_sleep 2\n\t" \
  "buffer_load_dword v84, off, s[20:23], s52 sc1\n\t" \
  "s_waitcnt vmcnt(0)\n\t" \
  "s_branch Lhs0_%=\n\t" \
  "Lhs1_%=:\n\t" \
  "s_sub_u32 s52, s52, 32768\n\t" \
  "s_mov_b64 exec, 1\n\t" \
  "buffer_store_dword v84, off, s[20:23], s52 sc1\n\t" \
  "s_mov_b64 exec, -1\n\t" \
  "Lh2_%=:\n\t" \
  "s_waitcnt vmcnt(21)\n\t" \
  "buffer_load_dword v84, v75, s[20:23], 0 offen sc1\n\t" \
  "s_mov_b32 s42, 0\n\t" \
  "s_waitcnt vmcnt(0)\n\t" \
  "Lxf0_%=:\n\t" \
  "v_cmp_eq_u32 vcc, 0, v84\n\t" \
  "s_nop 0\n\t" \
  "s_cbranch_vccz Lxf1_%=\n\t" \
  "s_add_u32 s42, s42, 1\n\t" \
  "s_cmp_gt_u32 s42, 524288\n\t" \
  "s_cbranch_scc1 Ltmo_%=\n\t" \
  "s_sleep 2\n\t" \
  "buffer_load_dword v84, v75, s[20:23], 0 offen sc1\n\t" \
  "s_waitcnt vmcnt(0)\n\t" \
  "s_branch Lxf0_%=\n\t" \
  "Lxf1_%=:\n\t" \
  "Lh3_%=:\n\t" \
  "s_waitcnt vmcnt(0)\n\t" \
  "s_barrier\n\t" \
  "v_lshrrev_b32 v0, 4, v76\n\t" \
  "v_bfe_u32 v77, v76, 2, 2\n\t" \
  "v_lshl_add_u32 v78, v0, 3, v77\n\t" \
  "v_lshlrev_b32 v78, 8, v78\n\t" \
  "v_and_b32 v0, 1, v0\n\t" \
  "v_lshl_add_u32 v77, v0, 2, v77\n\t" \
  "v_lshlrev_b32 v77, 1, v77\n\t" \
  "s_lshl_b32 s40, s29, 2\n\t" \
  "v_and_b32 v1, 3, v76\n\t" \
  "v_add_u32 v1, s40, v1\n\t" \
  "v_xor_b32 v1, v1, v77\n\t" \
  "v_lshl_add_u32 v2, v1, 4, v78\n\t" \
  "v_add_u32 v5, 8, v2\n\t" \
  "v_add_u32 v3, 65536, v2\n\t" \
  "v_add_u32 v4, 131072, v2\n\t" \
  "v_add_u32 v6, 65536, v5\n\t" \
  "v_add_u32 v7, 131072, v5\n\t" \
  "v_lshlrev_b32 v72, 4, v76\n\t" \
  "s_mul_i32 s40, s28, 36864\n\t" \
  "s_mul_i32 s58, s29, 9216\n\t" \
  "s_add_u32 s58, s58, 4096\n\t" \
  "s_add_u32 s40, s40, s58\n\t" \
  "s_add_u32 s40, s40, 3145728\n\t" \
  "s_add_u32 s46, s24, s40\n\t" \
  "s_addc_u32 s47, s25, 0\n\t" \
  "s_lshl_b32 s50, s26, 24\n\t" \
  "s_lshl_b32 s40, s27, 13\n\t" \
  "s_add_u32 s50, s50, s40\n\t" \
  "s_lshl_b32 s40, s28, 18\n\t" \
  "s_add_u32 s50, s50, s40\n\t" \
  "s_lshl_b32 s40, s29, 10\n\t" \
  "s_add_u32 s50, s50, s40\n\t" \
  "s_add_u32 s50, s50, 10485760\n\t" \
  "s_sub_u32 s51, 2, s26\n\t" \
  "s_lshl_b32 s51, s51, 7\n\t" \
  "s_lshl_b32 s40, s27, 2\n\t" \
  "s_add_u32 s51, s51, s40\n\t" \
  "s_lshl_b32 s40, s28, 9\n\t" \
  "s_add_u32 s51, s51, s40\n\t" \
  "s_add_u32 s51, s51, 2097152\n\t" \
  "v_add_u32 v75, 0, v75\n\t" \
  "s_lshl_b32 s40, s29, 2\n\t" \
  "s_add_u32 s40, s40, 147456\n\t" \
  "v_mov_b32 v80, s40\n\t" \
  "v_add_u32 v78, 4096, v72\n\t" \
  "ds_read_b64_tr_b16 a[0:1], v2 offset:0\n\t" \
  "ds_read_b64_tr_b16 a[2:3], v2 offset:1024\n\t" \
  "ds_read_b64_tr_b16 a[4:5], v5 offset:0\n\t" \
  "ds_read_b64_tr_b16 a[6:7], v5 offset:1024\n\t" \
  "ds_read_b64_tr_b16 a[8:9], v2 offset:8192\n\t" \
  "ds_read_b64_tr_b16 a[10:11], v2 offset:9216\n\t" \
  "ds_read_b64_tr_b16 a[12:13], v5 offset:8192\n\t" \
  "ds_read_b64_tr_b16 a[14:15], v5 offset:9216\n\t" \
  "ds_read_b64_tr_b16 a[16:17], v2 offset:16384\n\t" \
  "ds_read_b64_tr_b16 a[18:19], v2 offset:17408\n\t" \
  "ds_read_b64_tr_b16 a[20:21], v5 offset:16384\n\t" \
  "ds_read_b64_tr_b16 a[22:23], v5 offset:17408\n\t" \
  "ds_read_b64_tr_b16 a[24:25], v2 offset:24576\n\t" \
  "ds_read_b64_tr_b16 a[26:27], v2 offset:25600\n\t" \
  "ds_read_b64_tr_b16 a[28:29], v5 offset:24576\n\t" \
  "ds_read_b64_tr_b16 a[30:31], v5 offset:25600\n\t" \
  "ds_read_b64_tr_b16 a[32:33], v2 offset:32768\n\t" \
  "ds_read_b64_tr_b16 a[34:35], v2 offset:33792\n\t" \
  "ds_read_b64_tr_b16 a[36:37], v5 offset:32768\n\t" \
  "ds_read_b64_tr_b16 a[38:39], v5 offset:33792\n\t" \
  "s_waitcnt lgkmcnt(0)\n\t" \
  "s_barrier\n\t" \
  "s_add_u32 s48, s58, 0\n\t" \
  "s_mov_b32 m0, s48\n\t" \
  "s_nop 0\n\t" \
  "global_load_lds_dwordx4 v72, s[46:47] offset:-4096 sc1\n\t" \
  "global_load_lds_dwordx4 v72, s[46:47] offset:-3072 sc1\n\t" \
  "global_load_lds_dwordx4 v72, s[46:47] offset:-2048 sc1\n\t" \
  "global_load_lds_dwordx4 v72, s[46:47] offset:-1024 sc1\n\t" \
  "global_load_lds_dwordx4 v72, s[46:47] offset:0 sc1\n\t" \
  "global_load_lds_dwordx4 v72, s[46:47] offset:1024 sc1\n\t" \
  "global_load_lds_dwordx4 v72, s[46:47] offset:2048 sc1\n\t" \
  "global_load_lds_dwordx4 v72, s[46:47] offset:3072 sc1\n\t" \
  "s_add_u32 m0, s48, 4096\n\t" \
  "s_nop 0\n\t" \
  "global_load_lds_dwordx4 v78, s[46:47] sc1\n\t" \
  "s_add_u32 s46, s46, 73728\n\t" \
  "s_addc_u32 s47, s47, 0\n\t" \
  "ds_read_b64_tr_b16 a[40:41], v2 offset:40960\n\t" \
  "ds_read_b64_tr_b16 a[42:43], v2 offset:41984\n\t" \
  "ds_read_b64_tr_b16 a[44:45], v5 offset:40960\n\t" \
  "ds_read_b64_tr_b16 a[46:47], v5 offset:41984\n\t" \
  "ds_read_b64_tr_b16 a[48:49], v2 offset:49152\n\t" \
  "ds_read_b64_tr_b16 a[50:51], v2 offset:50176\n\t" \
  "ds_read_b64_tr_b16 a[52:53], v5 offset:49152\n\t" \
  "ds_read_b64_tr_b16 a[54:55], v5 offset:50176\n\t" \
  "ds_read_b64_tr_b16 a[56:57], v2 offset:57344\n\t" \
  "ds_read_b64_tr_b16 a[58:59], v2 offset:58368\n\t" \
  "ds_read_b64_tr_b16 a[60:61], v5 offset:57344\n\t" \
  "ds_read_b64_tr_b16 a[62:63], v5 offset:58368\n\t" \
  "ds_read_b64_tr_b16 a[64:65], v3 offset:0\n\t" \
  "ds_read_b64_tr_b16 a[66:67], v3 offset:1024\n\t" \
  "ds_read_b64_tr_b16 a[68:69], v6 offset:0\n\t" \
  "ds_read_b64_tr_b16 a[70:71], v6 offset:1024\n\t" \
  "s_waitcnt lgkmcnt(0)\n\t" \
  "s_barrier\n\t" \
  "s_add_u32 s48, s58, 36864\n\t" \
  "s_mov_b32 m0, s48\n\t" \
  "s_nop 0\n\t" \
  "global_load_lds_dwordx4 v72, s[46:47] offset:-4096 sc1\n\t" \
  "global_load_lds_dwordx4 v72, s[46:47] offset:-3072 sc1\n\t" \
  "global_load_lds_dwordx4 v72, s[46:47] offset:-2048 sc1\n\t" \
  "global_load_lds_dwordx4 v72, s[46:47] offset:-1024 sc1\n\t" \
  "global_load_lds_dwordx4 v72, s[46:47] offset:0 sc1\n\t" \
  "global_load_lds_dwordx4 v72, s[46:47] offset:1024 sc1\n\t" \
  "global_load_lds_dwordx4 v72, s[46:47] offset:2048 sc1\n\t" \
  "global_load_lds_dwordx4 v72, s[46:47] offset:3072 sc1\n\t" \
  "s_add_u32 m0, s48, 4096\n\t" \
  "s_nop 0\n\t" \
  "global_load_lds_dwordx4 v78, s[46:47] sc1\n\t" \
  "s_add_u32 s46, s46, 73728\n\t" \
  "s_addc_u32 s47, s47, 0\n\t" \
  "ds_read_b64_tr_b16 a[72:73], v3 offset:8192\n\t" \
  "ds_read_b64_tr_b16 a[74:75], v3 offset:9216\n\t" \
  "ds_read_b64_tr_b16 a[76:77], v6 offset:8192\n\t" \
  "ds_read_b64_tr_b16 a[78:79], v6 offset:9216\n\t" \
  "ds_read_b64_tr_b16 a[80:81], v3 offset:16384\n\t" \
  "ds_read_b64_tr_b16 a[82:83], v3 offset:17408\n\t" \
  "ds_read_b64_tr_b16 a[84:85], v6 offset:16384\n\t" \
  "ds_read_b64_tr_b16 a[86:87], v6 offset:17408\n\t" \
  "ds_read_b64_tr_b16 a[88:89], v3 offset:24576\n\t" \
  "ds_read_b64_tr_b16 a[90:91], v3 offset:25600\n\t" \
  "ds_read_b64_tr_b16 a[92:93], v6 offset:24576\n\t" \
  "ds_read_b64_tr_b16 a[94:95], v6 offset:25600\n\t" \
  "ds_read_b64_tr_b16 a[96:97], v3 offset:32768\n\t" \
  "ds_read_b64_tr_b16 a[98:99], v3 offset:33792\n\t" \
  "ds_read_b64_tr_b16 a[100:101], v6 offset:32768\n\t" \
  "ds_read_b64_tr_b16 a[102:103], v6 offset:33792\n\t" \
  "ds_read_b64_tr_b16 a[104:105], v3 offset:40960\n\t" \
  "ds_read_b64_tr_b16 a[106:107], v3 offset:41984\n\t" \
  "ds_read_b64_tr_b16 a[108:109], v6 offset:40960\n\t" \
  "ds_read_b64_tr_b16 a[110:111], v6 offset:41984\n\t" \
  "s_waitcnt lgkmcnt(0)\n\t" \
  "s_barrier\n\t" \
  "s_add_u32 s48, s58, 73728\n\t" \
  "s_mov_b32 m0, s48\n\t" \
  "s_nop 0\n\t" \
  "global_load_lds_dwordx4 v72, s[46:47] offset:-4096 sc1\n\t" \
  "global_load_lds_dwordx4 v72, s[46:47] offset:-3072 sc1\n\t" \
  "global_load_lds_dwordx4 v72, s[46:47] offset:-2048 sc1\n\t" \
  "global_load_lds_dwordx4 v72, s[46:47] offset:-1024 sc1\n\t" \
  "global_load_lds_dwordx4 v72, s[46:47] offset:0 sc1\n\t" \
  "global_load_lds_dwordx4 v72, s[46:47] offset:1024 sc1\n\t" \
  "global_load_lds_dwordx4 v72, s[46:47] offset:2048 sc1\n\t" \
  "global_load_lds_dwordx4 v72, s[46:47] offset:3072 sc1\n\t" \
  "s_add_u32 m0, s48, 4096\n\t" \
  "s_nop 0\n\t" \
  "global_load_lds_dwordx4 v78, s[46:47] sc1\n\t" \
  "s_add_u32 s46, s46, 73728\n\t" \
  "s_addc_u32 s47, s47, 0\n\t" \
  "ds_read_b64_tr_b16 a[112:113], v3 offset:49152\n\t" \
  "ds_read_b64_tr_b16 a[114:115], v3 offset:50176\n\t" \
  "ds_read_b64_tr_b16 a[116:117], v6 offset:49152\n\t" \
  "ds_read_b64_tr_b16 a[118:119], v6 offset:50176\n\t" \
  "ds_read_b64_tr_b16 a[120:121], v3 offset:57344\n\t" \
  "ds_read_b64_tr_b16 a[122:123], v3 offset:58368\n\t" \
  "ds_read_b64_tr_b16 a[124:125], v6 offset:57344\n\t" \
  "ds_read_b64_tr_b16 a[126:127], v6 offset:58368\n\t" \
  "ds_read_b64_tr_b16 a[128:129], v4 offset:0\n\t" \
  "ds_read_b64_tr_b16 a[130:131], v4 offset:1024\n\t" \
  "ds_read_b64_tr_b16 a[132:133], v7 offset:0\n\t" \
  "ds_read_b64_tr_b16 a[134:135], v7 offset:1024\n\t" \
  "ds_read_b64_tr_b16 a[136:137], v4 offset:8192\n\t" \
  "ds_read_b64_tr_b16 a[138:139], v4 offset:9216\n\t" \
  "ds_read_b64_tr_b16 a[140:141], v7 offset:8192\n\t" \
  "ds_read_b64_tr_b16 a[142:143], v7 offset:9216\n\t" \
  "s_waitcnt vmcnt(18) lgkmcnt(0)\n\t" \
  "s_barrier\n\t" \
  "v_mov_b32 v73, v72\n\t" \
  "ds_read_b128 v[0:3], v73 offset:0\n\t" \
  "ds_read_b128 v[4:7], v73 offset:18432\n\t" \
  "ds_read_b128 v[8:11], v73 offset:1024\n\t" \
  "ds_read_b128 v[12:15], v73 offset:19456\n\t" \
  "ds_read_b128 v[16:19], v73 offset:2048\n\t" \
  "ds_read_b128 v[20:23], v73 offset:20480\n\t" \
  "v_add_u32 v74, 36864, v72\n\t" \
  "s_waitcnt lgkmcnt(4)\n\t" \
  "v_mfma_f32_16x16x32_bf16 v[32:35], a[0:3], v[0:3], 0\n\t" \
  "ds_read_b128 v[24:27], v73 offset:3072\n\t" \
  "v_mfma_f32_16x16x32_bf16 v[36:39], a[4:7], v[0:3], 0\n\t" \
  "ds_read_b128 v[28:31], v73 offset:21504\n\t" \
  "v_mfma_f32_16x16x32_bf16 v[40:43], a[0:3], v[4:7], 0\n\t" \
  "v_mfma_f32_16x16x32_bf16 v[44:47], a[4:7], v[4:7], 0\n\t" \
  "s_waitcnt lgkmcnt(4)\n\t" \
  "v_mfma_f32_16x16x32_bf16 v[32:35], a[8:11], v[8:11], v[32:35]\n\t" \
  "ds_read_b128 v[0:3], v73 offset:4096\n\t" \
  "v_mfma_f32_16x16x32_bf16 v[36:39], a[12:15], v[8:11], v[36:39]\n\t" \
  "ds_read_b128 v[4:7], v73 offset:22528\n\t" \
  "v_mfma_f32_16x16x32_bf16 v[40:43], a[8:11], v[12:15], v[40:43]\n\t" \
  "v_mfma_f32_16x16x32_bf16 v[44:47], a[12:15], v[12:15], v[44:47]\n\t" \
  "s_waitcnt lgkmcnt(4)\n\t" \
  "v_mfma_f32_16x16x32_bf16 v[32:35], a[16:19], v[16:19], v[32:35]\n\t" \
  "ds_read_b128 v[8:11], v73 offset:5120\n\t" \
  "v_mfma_f32_16x16x32_bf16 v[36:39], a[20:23], v[16:19], v[36:39]\n\t" \
  "ds_read_b128 v[12:15], v73 offset:23552\n\t" \
  "v_mfma_f32_16x16x32_bf16 v[40:43], a[16:19], v[20:23], v[40:43]\n\t" \
  "v_mfma_f32_16x16x32_bf16 v[44:47], a[20:23], v[20:23], v[44:47]\n\t" \
  "s_waitcnt lgkmcnt(4)\n\t" \
  "v_mfma_f32_16x16x32_bf16 v[32:35], a[24:27], v[24:27], v[32:35]\n\t" \
  "ds_read_b128 v[16:19], v73 offset:6144\n\t" \
  "v_mfma_f32_16x16x32_bf16 v[36:39], a[28:31], v[24:27], v[36:39]\n\t" \
  "ds_read_b128 v[20:23], v73 offset:24576\n\t" \
  "v_mfma_f32_16x16x32_bf16 v[40:43], a[24:27], v[28:31], v[40:43]\n\t" \
  "v_mfma_f32_16x16x32_bf16 v[44:47], a[28:31], v[28:31], v[44:47]\n\t" \
  "s_waitcnt lgkmcnt(4)\n\t" \
  "v_mfma_f32_16x16x32_bf16 v[32:35], a[32:35], v[0:3], v[32:35]\n\t" \
  "ds_read_b128 v[24:27], v73 offset:7168\n\t" \
  "v_mfma_f32_16x16x32_bf16 v[36:39], a[36:39], v[0:3], v[36:39]\n\t" \
  "ds_read_b128 v[28:31], v73 offset:25600\n\t" \
  "v_mfma_f32_16x16x32_bf16 v[40:43], a[32:35], v[4:7], v[40:43]\n\t" \
  "v_mfma_f32_16x16x32_bf16 v[44:47], a[36:39], v[4:7], v[44:47]\n\t" \
  "s_waitcnt lgkmcnt(4)\n\t" \
  "v_mfma_f32_16x16x32_bf16 v[32:35], a[40:43], v[8:11], v[32:35]\n\t" \
  "ds_read_b128 v[0:3], v73 offset:8192\n\t" \
  "v_mfma_f32_16x16x32_bf16 v[36:39], a[44:47], v[8:11], v[36:39]\n\t" \
  "ds_read_b128 v[4:7], v73 offset:26624\n\t" \
  "v_mfma_f32_16x16x32_bf16 v[40:43], a[40:43], v[12:15], v[40:43]\n\t" \
  "v_mfma_f32_16x16x32_bf16 v[44:47], a[44:47], v[12:15], v[44:47]\n\t" \
  "s_waitcnt lgkmcnt(4)\n\t" \
  "v_mfma_f32_16x16x32_bf16 v[32:35], a[48:51], v[16:19], v[32:35]\n\t" \
  "ds_read_b128 v[8:11], v73 offset:9216\n\t" \
  "v_mfma_f32_16x16x32_bf16 v[36:39], a[52:55], v[16:19], v[36:39]\n\t" \
  "ds_read_b128 v[12:15], v73 offset:27648\n\t" \
  "v_mfma_f32_16x16x32_bf16 v[40:43], a[48:51], v[20:23], v[40:43]\n\t" \
  "v_mfma_f32_16x16x32_bf16 v[44:47], a[52:55], v[20:23], v[44:47]\n\t" \
  "s_waitcnt lgkmcnt(4)\n\t" \
  "v_mfma_f32_16x16x32_bf16 v[32:35], a[56:59], v[24:27], v[32:35]\n\t" \
  "ds_read_b128 v[16:19], v73 offset:10240\n\t" \
  "v_mfma_f32_16x16x32_bf16 v[36:39], a[60:63], v[24:27], v[36:39]\n\t" \
  "ds_read_b128 v[20:23], v73 offset:28672\n\t" \
  "v_mfma_f32_16x16x32_bf16 v[40:43], a[56:59], v[28:31], v[40:43]\n\t" \
  "v_mfma_f32_16x16x32_bf16 v[44:47], a[60:63], v[28:31], v[44:47]\n\t" \
  "s_waitcnt lgkmcnt(4)\n\t" \
  "v_mfma_f32_16x16x32_bf16 v[32:35], a[64:67], v[0:3], v[32:35]\n\t" \
  "ds_read_b128 v[24:27], v73 offset:11264\n\t" \
  "v_mfma_f32_16x16x32_bf16 v[36:39], a[68:71], v[0:3], v[36:39]\n\t" \
  "ds_read_b128 v[28:31], v73 offset:29696\n\t" \
  "v_mfma_f32_16x16x32_bf16 v[40:43], a[64:67], v[4:7], v[40:43]\n\t" \
  "v_mfma_f32_16x16x32_bf16 v[44:47], a[68:71], v[4:7], v[44:47]\n\t" \
  "s_waitcnt vmcnt(9)\n\t" \
  "s_barrier\n\t" \
  "s_waitcnt lgkmcnt(4)\n\t" \
  "v_mfma_f32_16x16x32_bf16 v[32:35], a[72:75], v[8:11], v[32:35]\n\t" \
  "ds_read_b128 v[0:3], v73 offset:12288\n\t" \
  "s_add_u32 s48, s58, 110592\n\t" \
  "v_mfma_f32_16x16x32_bf16 v[36:39], a[76:79], v[8:11], v[36:39]\n\t" \
  "ds_read_b128 v[4:7], v73 offset:30720\n\t" \
  "s_mov_b32 m0, s48\n\t" \
  "v_mfma_f32_16x16x32_bf16 v[40:43], a[72:75], v[12:15], v[40:43]\n\t" \
  "s_nop 0\n\t" \
  "global_load_lds_dwordx4 v72, s[46:47] offset:-4096 sc1\n\t" \
  "v_mfma_f32_16x16x32_bf16 v[44:47], a[76:79], v[12:15], v[44:47]\n\t" \
  "global_load_lds_dwordx4 v72, s[46:47] offset:-3072 sc1\n\t" \
  "global_load_lds_dwordx4 v72, s[46:47] offset:-2048 sc1\n\t" \
  "s_waitcnt lgkmcnt(4)\n\t" \
  "v_mfma_f32_16x16x32_bf16 v[32:35], a[80:83], v[16:19], v[32:35]\n\t" \
  "ds_read_b128 v[8:11], v73 offset:13312\n\t" \
  "global_load_lds_dwordx4 v72, s[46:47] offset:-1024 sc1\n\t" \
  "v_mfma_f32_16x16x32_bf16 v[36:39], a[84:87], v[16:19], v[36:39]\n\t" \
  "ds_read_b128 v[12:15], v73 offset:31744\n\t" \
  "global_load_lds_dwordx4 v72, s[46:47] offset:0 sc1\n\t" \
  "v_mfma_f32_16x16x32_bf16 v[40:43], a[80:83], v[20:23], v[40:43]\n\t" \
  "global_load_lds_dwordx4 v72, s[46:47] offset:1024 sc1\n\t" \
  "global_load_lds_dwordx4 v72, s[46:47] offset:2048 sc1\n\t" \
  "v_mfma_f32_16x16x32_bf16 v[44:47], a[84:87], v[20:23], v[44:47]\n\t" \
  "global_load_lds_dwordx4 v72, s[46:47] offset:3072 sc1\n\t" \
  "s_add_u32 m0, s48, 4096\n\t" \
  "s_waitcnt lgkmcnt(4)\n\t" \
  "v_mfma_f32_16x16x32_bf16 v[32:35], a[88:91], v[24:27], v[32:35]\n\t" \
  "ds_read_b128 v[16:19], v73 offset:14336\n\t" \
  "s_nop 0\n\t" \
  "v_mfma_f32_16x16x32_bf16 v[36:39], a[92:95], v[24:27], v[36:39]\n\t" \
  "ds_read_b128 v[20:23], v73 offset:32768\n\t" \
  "global_load_lds_dwordx4 v78, s[46:47] sc1\n\t" \
  "v_mfma_f32_16x16x32_bf16 v[40:43], a[88:91], v[28:31], v[40:43]\n\t" \
  "s_add_u32 s46, s46, 73728\n\t" \
  "s_addc_u32 s47, s47, 0\n\t" \
  "v_mfma_f32_16x16x32_bf16 v[44:47], a[92:95], v[28:31], v[44:47]\n\t" \
  "s_waitcnt lgkmcnt(4)\n\t" \
  "v_mfma_f32_16x16x32_bf16 v[32:35], a[96:99], v[0:3], v[32:35]\n\t" \
  "ds_read_b128 v[24:27], v73 offset:15360\n\t" \
  "v_mfma_f32_16x16x32_bf16 v[36:39], a[100:103], v[0:3], v[36:39]\n\t" \
  "ds_read_b128 v[28:31], v73 offset:33792\n\t" \
  "v_mfma_f32_16x16x32_bf16 v[40:43], a[96:99], v[4:7], v[40:43]\n\t" \
  "v_mfma_f32_16x16x32_bf16 v[44:47], a[100:103], v[4:7], v[44:47]\n\t" \
  "s_waitcnt lgkmcnt(4)\n\t" \
  "v_mfma_f32_16x16x32_bf16 v[32:35], a[104:107], v[8:11], v[32:35]\n\t" \
  "ds_read_b128 v[0:3], v73 offset:16384\n\t" \
  "v_mfma_f32_16x16x32_bf16 v[36:39], a[108:111], v[8:11], v[36:39]\n\t" \
  "ds_read_b128 v[4:7], v73 offset:34816\n\t" \
  "v_mfma_f32_16x16x32_bf16 v[40:43], a[104:107], v[12:15], v[40:43]\n\t" \
  "v_mfma_f32_16x16x32_bf16 v[44:47], a[108:111], v[12:15], v[44:47]\n\t" \
  "s_waitcnt lgkmcnt(4)\n\t" \
  "v_mfma_f32_16x16x32_bf16 v[32:35], a[112:115], v[16:19], v[32:35]\n\t" \
  "ds_read_b128 v[8:11], v73 offset:17408\n\t" \
  "v_mfma_f32_16x16x32_bf16 v[36:39], a[116:119], v[16:19], v[36:39]\n\t" \
  "ds_read_b128 v[12:15], v73 offset:35840\n\t" \
  "v_mfma_f32_16x16x32_bf16 v[40:43], a[112:115], v[20:23], v[40:43]\n\t" \
  "v_mfma_f32_16x16x32_bf16 v[44:47], a[116:119], v[20:23], v[44:47]\n\t" \
  "s_waitcnt lgkmcnt(4)\n\t" \
  "v_mfma_f32_16x16x32_bf16 v[32:35], a[120:123], v[24:27], v[32:35]\n\t" \
  "ds_read_b128 v[16:19], v74 offset:0\n\t" \
  "v_mfma_f32_16x16x32_bf16 v[36:39], a[124:127], v[24:27], v[36:39]\n\t" \
  "ds_read_b128 v[20:23], v74 offset:18432\n\t" \
  "v_mfma_f32_16x16x32_bf16 v[40:43], a[120:123], v[28:31], v[40:43]\n\t" \
  "v_mfma_f32_16x16x32_bf16 v[44:47], a[124:127], v[28:31], v[44:47]\n\t" \
  "s_waitcnt lgkmcnt(4)\n\t" \
  "v_mfma_f32_16x16x32_bf16 v[32:35], a[128:131], v[0:3], v[32:35]\n\t" \
  "ds_read_b128 v[24:27], v74 offset:1024\n\t" \
  "v_mfma_f32_16x16x32_bf16 v[36:39], a[132:135], v[0:3], v[36:39]\n\t" \
  "ds_read_b128 v[28:31], v74 offset:19456\n\t" \
  "v_mfma_f32_16x16x32_bf16 v[40:43], a[128:131], v[4:7], v[40:43]\n\t" \
  "v_mfma_f32_16x16x32_bf16 v[44:47], a[132:135], v[4:7], v[44:47]\n\t" \
  "s_waitcnt lgkmcnt(4)\n\t" \
  "v_mfma_f32_16x16x32_bf16 v[32:35], a[136:139], v[8:11], v[32:35]\n\t" \
  "ds_read_b128 v[0:3], v74 offset:2048\n\t" \
  "v_mfma_f32_16x16x32_bf16 v[36:39], a[140:143], v[8:11], v[36:39]\n\t" \
  "ds_read_b128 v[4:7], v74 offset:20480\n\t" \
  "v_mfma_f32_16x16x32_bf16 v[40:43], a[136:139], v[12:15], v[40:43]\n\t" \
  "v_mfma_f32_16x16x32_bf16 v[44:47], a[140:143], v[12:15], v[44:47]\n\t" \
  "v_mov_b32 v73, v74\n\t" \
  "s_nop 7\n\t" \
  "v_cvt_pk_bf16_f32 v64, v32, v33\n\t" \
  "v_cvt_pk_bf16_f32 v65, v34, v35\n\t" \
  "v_cvt_pk_bf16_f32 v66, v36, v37\n\t" \
  "v_cvt_pk_bf16_f32 v67, v38, v39\n\t" \
  "buffer_store_dwordx4 v[64:67], v72, s[20:23], s50 offen offset:0 sc1\n\t" \
  "v_cvt_pk_bf16_f32 v68, v40, v41\n\t" \
  "v_cvt_pk_bf16_f32 v69, v42, v43\n\t" \
  "v_cvt_pk_bf16_f32 v70, v44, v45\n\t" \
  "v_cvt_pk_bf16_f32 v71, v46, v47\n\t" \
  "buffer_store_dwordx4 v[68:71], v78, s[20:23], s50 offen offset:0 sc1\n\t" \
  "s_add_u32 s50, s50, 524288\n\t" \
  "v_add_u32 v74, 73728, v72\n\t" \
  "s_waitcnt lgkmcnt(4)\n\t" \
  "v_mfma_f32_16x16x32_bf16 v[48:51], a[0:3], v[16:19], 0\n\t" \
  "ds_read_b128 v[8:11], v73 offset:3072\n\t" \
  "v_mfma_f32_16x16x32_bf16 v[52:55], a[4:7], v[16:19], 0\n\t" \
  "ds_read_b128 v[12:15], v73 offset:21504\n\t" \
  "v_mfma_f32_16x16x32_bf16 v[56:59], a[0:3], v[20:23], 0\n\t" \
  "v_mfma_f32_16x16x32_bf16 v[60:63], a[4:7], v[20:23], 0\n\t" \
  "s_waitcnt lgkmcnt(4)\n\t" \
  "v_mfma_f32_16x16x32_bf16 v[48:51], a[8:11], v[24:27], v[48:51]\n\t" \
  "ds_read_b128 v[16:19], v73 offset:4096\n\t" \
  "v_mfma_f32_16x16x32_bf16 v[52:55], a[12:15], v[24:27], v[52:55]\n\t" \
  "ds_read_b128 v[20:23], v73 offset:22528\n\t" \
  "v_mfma_f32_16x16x32_bf16 v[56:59], a[8:11], v[28:31], v[56:59]\n\t" \
  "v_mfma_f32_16x16x32_bf16 v[60:63], a[12:15], v[28:31], v[60:63]\n\t" \
  "s_waitcnt lgkmcnt(4)\n\t" \
  "v_mfma_f32_16x16x32_bf16 v[48:51], a[16:19], v[0:3], v[48:51]\n\t" \
  "ds_read_b128 v[24:27], v73 offset:5120\n\t" \
  "v_mfma_f32_16x16x32_bf16 v[52:55], a[20:23], v[0:3], v[52:55]\n\t" \
  "ds_read_b128 v[28:31], v73 offset:23552\n\t" \
  "v_mfma_f32_16x16x32_bf16 v[56:59], a[16:19], v[4:7], v[56:59]\n\t" \
  "v_mfma_f32_16x16x32_bf16 v[60:63], a[20:23], v[4:7], v[60:63]\n\t" \
  "s_waitcnt lgkmcnt(4)\n\t" \
  "v_mfma_f32_16x16x32_bf16 v[48:51], a[24:27], v[8:11], v[48:51]\n\t" \
  "ds_read_b128 v[0:3], v73 offset:6144\n\t" \
  "v_mfma_f32_16x16x32_bf16 v[52:55], a[28:31], v[8:11], v[52:55]\n\t" \
  "ds_read_b128 v[4:7], v73 offset:24576\n\t" \
  "v_mfma_f32_16x16x32_bf16 v[56:59], a[24:27], v[12:15], v[56:59]\n\t" \
  "v_mfma_f32_16x16x32_bf16 v[60:63], a[28:31], v[12:15], v[60:63]\n\t" \
  "s_waitcnt lgkmcnt(4)\n\t" \
  "v_mfma_f32_16x16x32_bf16 v[48:51], a[32:35], v[16:19], v[48:51]\n\t" \
  "ds_read_b128 v[8:11], v73 offset:7168\n\t" \
  "v_mfma_f32_16x16x32_bf16 v[52:55], a[36:39], v[16:19], v[52:55]\n\t" \
  "ds_read_b128 v[12:15], v73 offset:25600\n\t" \
  "v_mfma_f32_16x16x32_bf16 v[56:59], a[32:35], v[20:23], v[56:59]\n\t" \
  "v_mfma_f32_16x16x32_bf16 v[60:63], a[36:39], v[20:23], v[60:63]\n\t" \
  "s_waitcnt lgkmcnt(4)\n\t" \
  "v_mfma_f32_16x16x32_bf16 v[48:51], a[40:43], v[24:27], v[48:51]\n\t" \
  "ds_read_b128 v[16:19], v73 offset:8192\n\t" \
  "v_mfma_f32_16x16x32_bf16 v[52:55], a[44:47], v[24:27], v[52:55]\n\t" \
  "ds_read_b128 v[20:23], v73 offset:26624\n\t" \
  "v_mfma_f32_16x16x32_bf16 v[56:59], a[40:43], v[28:31], v[56:59]\n\t" \
  "v_mfma_f32_16x16x32_bf16 v[60:63], a[44:47], v[28:31], v[60:63]\n\t" \
  "s_waitcnt lgkmcnt(4)\n\t" \
  "v_mfma_f32_16x16x32_bf16 v[48:51], a[48:51], v[0:3], v[48:51]\n\t" \
  "ds_read_b128 v[24:27], v73 offset:9216\n\t" \
  "v_mfma_f32_16x16x32_bf16 v[52:55], a[52:55], v[0:3], v[52:55]\n\t" \
  "ds_read_b128 v[28:31], v73 offset:27648\n\t" \
  "v_mfma_f32_16x16x32_bf16 v[56:59], a[48:51], v[4:7], v[56:59]\n\t" \
  "v_mfma_f32_16x16x32_bf16 v[60:63], a[52:55], v[4:7], v[60:63]\n\t" \
  "s_waitcnt lgkmcnt(4)\n\t" \
  "v_mfma_f32_16x16x32_bf16 v[48:51], a[56:59], v[8:11], v[48:51]\n\t" \
  "ds_read_b128 v[0:3], v73 offset:10240\n\t" \
  "v_mfma_f32_16x16x32_bf16 v[52:55], a[60:63], v[8:11], v[52:55]\n\t" \
  "ds_read_b128 v[4:7], v73 offset:28672\n\t" \
  "v_mfma_f32_16x16x32_bf16 v[56:59], a[56:59], v[12:15], v[56:59]\n\t" \
  "v_mfma_f32_16x16x32_bf16 v[60:63], a[60:63], v[12:15], v[60:63]\n\t" \
  "s_waitcnt lgkmcnt(4)\n\t" \
  "v_mfma_f32_16x16x32_bf16 v[48:51], a[64:67], v[16:19], v[48:51]\n\t" \
  "ds_read_b128 v[8:11], v73 offset:11264\n\t" \
  "v_mfma_f32_16x16x32_bf16 v[52:55], a[68:71], v[16:19], v[52:55]\n\t" \
  "ds_read_b128 v[12:15], v73 offset:29696\n\t" \
  "v_mfma_f32_16x16x32_bf16 v[56:59], a[64:67], v[20:23], v[56:59]\n\t" \
  "v_mfma_f32_16x16x32_bf16 v[60:63], a[68:71], v[20:23], v[60:63]\n\t" \
  "s_waitcnt vmcnt(0) lgkmcnt(0)\n\t" \
  "s_barrier\n\t" \
  "s_waitcnt lgkmcnt(4)\n\t" \
  "v_mfma_f32_16x16x32_bf16 v[48:51], a[72:75], v[24:27], v[48:51]\n\t" \
  "ds_read_b128 v[16:19], v73 offset:12288\n\t" \
  "v_mov_b32 v77, 1\n\t" \
  "v_mfma_f32_16x16x32_bf16 v[52:55], a[76:79], v[24:27], v[52:55]\n\t" \
  "ds_read_b128 v[20:23], v73 offset:30720\n\t" \
  "s_mov_b64 exec, s[54:55]\n\t" \
  "buffer_store_dword v77, off, s[20:23], s51 sc1\n\t" \
  "s_mov_b64 exec, -1\n\t" \
  "v_mfma_f32_16x16x32_bf16 v[56:59], a[72:75], v[28:31], v[56:59]\n\t" \
  "s_add_u32 s51, s51, 1024\n\t" \
  "s_add_u32 s48, s58, 0\n\t" \
  "v_mfma_f32_16x16x32_bf16 v[60:63], a[76:79], v[28:31], v[60:63]\n\t" \
  "s_mov_b32 m0, s48\n\t" \
  "s_nop 0\n\t" \
  "s_waitcnt lgkmcnt(4)\n\t" \
  "v_mfma_f32_16x16x32_bf16 v[48:51], a[80:83], v[0:3], v[48:51]\n\t" \
  "ds_read_b128 v[24:27], v73 offset:13312\n\t" \
  "global_load_lds_dwordx4 v72, s[46:47] offset:-4096 sc1\n\t" \
  "v_mfma_f32_16x16x32_bf16 v[52:55], a[84:87], v[0:3], v[52:55]\n\t" \
  "ds_read_b128 v[28:31], v73 offset:31744\n\t" \
  "global_load_lds_dwordx4 v72, s[46:47] offset:-3072 sc1\n\t" \
  "v_mfma_f32_16x16x32_bf16 v[56:59], a[80:83], v[4:7], v[56:59]\n\t" \
  "global_load_lds_dwordx4 v72, s[46:47] offset:-2048 sc1\n\t" \
  "global_load_lds_dwordx4 v72, s[46:47] offset:-1024 sc1\n\t" \
  "v_mfma_f32_16x16x32_bf16 v[60:63], a[84:87], v[4:7], v[60:63]\n\t" \
  "global_load_lds_dwordx4 v72, s[46:47] offset:0 sc1\n\t" \
  "global_load_lds_dwordx4 v72, s[46:47] offset:1024 sc1\n\t" \
  "s_waitcnt lgkmcnt(4)\n\t" \
  "v_mfma_f32_16x16x32_bf16 v[48:51], a[88:91], v[8:11], v[48:51]\n\t" \
  "ds_read_b128 v[0:3], v73 offset:14336\n\t" \
  "global_load_lds_dwordx4 v72, s[46:47] offset:2048 sc1\n\t" \
  "v_mfma_f32_16x16x32_bf16 v[52:55], a[92:95], v[8:11], v[52:55]\n\t" \
  "ds_read_b128 v[4:7], v73 offset:32768\n\t" \
  "global_load_lds_dwordx4 v72, s[46:47] offset:3072 sc1\n\t" \
  "v_mfma_f32_16x16x32_bf16 v[56:59], a[88:91], v[12:15], v[56:59]\n\t" \
  "s_add_u32 m0, s48, 4096\n\t" \
  "s_nop 0\n\t" \
  "v_mfma_f32_16x16x32_bf16 v[60:63], a[92:95], v[12:15], v[60:63]\n\t" \
  "global_load_lds_dwordx4 v78, s[46:47] sc1\n\t" \
  "s_add_u32 s46, s46, 73728\n\t" \
  "s_waitcnt lgkmcnt(4)\n\t" \
  "v_mfma_f32_16x16x32_bf16 v[48:51], a[96:99], v[16:19], v[48:51]\n\t" \
  "ds_read_b128 v[8:11], v73 offset:15360\n\t" \
  "s_addc_u32 s47, s47, 0\n\t" \
  "v_mfma_f32_16x16x32_bf16 v[52:55], a[100:103], v[16:19], v[52:55]\n\t" \
  "ds_read_b128 v[12:15], v73 offset:33792\n\t" \
  "v_mfma_f32_16x16x32_bf16 v[56:59], a[96:99], v[20:23], v[56:59]\n\t" \
  "v_mfma_f32_16x16x32_bf16 v[60:63], a[100:103], v[20:23], v[60:63]\n\t" \
  "s_waitcnt lgkmcnt(4)\n\t" \
  "v_mfma_f32_16x16x32_bf16 v[48:51], a[104:107], v[24:27], v[48:51]\n\t" \
  "ds_read_b128 v[16:19], v73 offset:16384\n\t" \
  "v_mfma_f32_16x16x32_bf16 v[52:55], a[108:111], v[24:27], v[52:55]\n\t" \
  "ds_read_b128 v[20:23], v73 offset:34816\n\t" \
  "v_mfma_f32_16x16x32_bf16 v[56:59], a[104:107], v[28:31], v[56:59]\n\t" \
  "v_mfma_f32_16x16x32_bf16 v[60:63], a[108:111], v[28:31], v[60:63]\n\t" \
  "s_waitcnt lgkmcnt(4)\n\t" \
  "v_mfma_f32_16x16x32_bf16 v[48:51], a[112:115], v[0:3], v[48:51]\n\t" \
  "ds_read_b128 v[24:27], v73 offset:17408\n\t" \
  "v_mfma_f32_16x16x32_bf16 v[52:55], a[116:119], v[0:3], v[52:55]\n\t" \
  "ds_read_b128 v[28:31], v73 offset:35840\n\t" \
  "v_mfma_f32_16x16x32_bf16 v[56:59], a[112:115], v[4:7], v[56:59]\n\t" \
  "v_mfma_f32_16x16x32_bf16 v[60:63], a[116:119], v[4:7], v[60:63]\n\t" \
  "s_waitcnt lgkmcnt(4)\n\t" \
  "v_mfma_f32_16x16x32_bf16 v[48:51], a[120:123], v[8:11], v[48:51]\n\t" \
  "ds_read_b128 v[0:3], v74 offset:0\n\t" \
  "v_mfma_f32_16x16x32_bf16 v[52:55], a[124:127], v[8:11], v[52:55]\n\t" \
  "ds_read_b128 v[4:7], v74 offset:18432\n\t" \
  "v_mfma_f32_16x16x32_bf16 v[56:59], a[120:123], v[12:15], v[56:59]\n\t" \
  "v_mfma_f32_16x16x32_bf16 v[60:63], a[124:127], v[12:15], v[60:63]\n\t" \
  "s_waitcnt lgkmcnt(4)\n\t" \
  "v_mfma_f32_16x16x32_bf16 v[48:51], a[128:131], v[16:19], v[48:51]\n\t" \
  "ds_read_b128 v[8:11], v74 offset:1024\n\t" \
  "v_mfma_f32_16x16x32_bf16 v[52:55], a[132:135], v[16:19], v[52:55]\n\t" \
  "ds_read_b128 v[12:15], v74 offset:19456\n\t" \
  "v_mfma_f32_16x16x32_bf16 v[56:59], a[128:131], v[20:23], v[56:59]\n\t" \
  "v_mfma_f32_16x16x32_bf16 v[60:63], a[132:135], v[20:23], v[60:63]\n\t" \
  "s_waitcnt lgkmcnt(4)\n\t" \
  "v_mfma_f32_16x16x32_bf16 v[48:51], a[136:139], v[24:27], v[48:51]\n\t" \
  "ds_read_b128 v[16:19], v74 offset:2048\n\t" \
  "v_mfma_f32_16x16x32_bf16 v[52:55], a[140:143], v[24:27], v[52:55]\n\t" \
  "ds_read_b128 v[20:23], v74 offset:20480\n\t" \
  "v_mfma_f32_16x16x32_bf16 v[56:59], a[136:139], v[28:31], v[56:59]\n\t" \
  "v_mfma_f32_16x16x32_bf16 v[60:63], a[140:143], v[28:31], v[60:63]\n\t" \
  "v_mov_b32 v73, v74\n\t" \
  "v_add_u32 v74, 110592, v72\n\t" \
  "s_waitcnt lgkmcnt(4)\n\t" \
  "v_mfma_f32_16x16x32_bf16 v[32:35], a[0:3], v[0:3], 0\n\t" \
  "ds_read_b128 v[24:27], v73 offset:3072\n\t" \
  "v_mfma_f32_16x16x32_bf16 v[36:39], a[4:7], v[0:3], 0\n\t" \
  "ds_read_b128 v[28:31], v73 offset:21504\n\t" \
  "v_mfma_f32_16x16x32_bf16 v[40:43], a[0:3], v[4:7], 0\n\t" \
  "v_mfma_f32_16x16x32_bf16 v[44:47], a[4:7], v[4:7], 0\n\t" \
  "s_waitcnt lgkmcnt(4)\n\t" \
  "v_mfma_f32_16x16x32_bf16 v[32:35], a[8:11], v[8:11], v[32:35]\n\t" \
  "ds_read_b128 v[0:3], v73 offset:4096\n\t" \
  "v_cvt_pk_bf16_f32 v64, v48, v49\n\t" \
  "v_mfma_f32_16x16x32_bf16 v[36:39], a[12:15], v[8:11], v[36:39]\n\t" \
  "ds_read_b128 v[4:7], v73 offset:22528\n\t" \
  "v_cvt_pk_bf16_f32 v65, v50, v51\n\t" \
  "v_mfma_f32_16x16x32_bf16 v[40:43], a[8:11], v[12:15], v[40:43]\n\t" \
  "v_cvt_pk_bf16_f32 v66, v52, v53\n\t" \
  "v_cvt_pk_bf16_f32 v67, v54, v55\n\t" \
  "v_mfma_f32_16x16x32_bf16 v[44:47], a[12:15], v[12:15], v[44:47]\n\t" \
  "buffer_store_dwordx4 v[64:67], v72, s[20:23], s50 offen offset:0 sc1\n\t" \
  "v_cvt_pk_bf16_f32 v68, v56, v57\n\t" \
  "s_waitcnt lgkmcnt(4)\n\t" \
  "v_mfma_f32_16x16x32_bf16 v[32:35], a[16:19], v[16:19], v[32:35]\n\t" \
  "ds_read_b128 v[8:11], v73 offset:5120\n\t" \
  "v_cvt_pk_bf16_f32 v69, v58, v59\n\t" \
  "v_mfma_f32_16x16x32_bf16 v[36:39], a[20:23], v[16:19], v[36:39]\n\t" \
  "ds_read_b128 v[12:15], v73 offset:23552\n\t" \
  "v_cvt_pk_bf16_f32 v70, v60, v61\n\t" \
  "v_mfma_f32_16x16x32_bf16 v[40:43], a[16:19], v[20:23], v[40:43]\n\t" \
  "v_cvt_pk_bf16_f32 v71, v62, v63\n\t" \
  "buffer_store_dwordx4 v[68:71], v78, s[20:23], s50 offen offset:0 sc1\n\t" \
  "v_mfma_f32_16x16x32_bf16 v[44:47], a[20:23], v[20:23], v[44:47]\n\t" \
  "s_add_u32 s50, s50, 524288\n\t" \
  "s_waitcnt lgkmcnt(4)\n\t" \
  "v_mfma_f32_16x16x32_bf16 v[32:35], a[24:27], v[24:27], v[32:35]\n\t" \
  "ds_read_b128 v[16:19], v73 offset:6144\n\t" \
  "v_mfma_f32_16x16x32_bf16 v[36:39], a[28:31], v[24:27], v[36:39]\n\t" \
  "ds_read_b128 v[20:23], v73 offset:24576\n\t" \
  "v_mfma_f32_16x16x32_bf16 v[40:43], a[24:27], v[28:31], v[40:43]\n\t" \
  "v_mfma_f32_16x16x32_bf16 v[44:47], a[28:31], v[28:31], v[44:47]\n\t" \
  "s_waitcnt lgkmcnt(4)\n\t" \
  "v_mfma_f32_16x16x32_bf16 v[32:35], a[32:35], v[0:3], v[32:35]\n\t" \
  "ds_read_b128 v[24:27], v73 offset:7168\n\t" \
  "v_mfma_f32_16x16x32_bf16 v[36:39], a[36:39], v[0:3], v[36:39]\n\t" \
  "ds_read_b128 v[28:31], v73 offset:25600\n\t" \
  "v_mfma_f32_16x16x32_bf16 v[40:43], a[32:35], v[4:7], v[40:43]\n\t" \
  "v_mfma_f32_16x16x32_bf16 v[44:47], a[36:39], v[4:7], v[44:47]\n\t" \
  "s_waitcnt lgkmcnt(4)\n\t" \
  "v_mfma_f32_16x16x32_bf16 v[32:35], a[40:43], v[8:11], v[32:35]\n\t" \
  "ds_read_b128 v[0:3], v73 offset:8192\n\t" \
  "v_mfma_f32_16x16x32_bf16 v[36:39], a[44:47], v[8:11], v[36:39]\n\t" \
  "ds_read_b128 v[4:7], v73 offset:26624\n\t" \
  "v_mfma_f32_16x16x32_bf16 v[40:43], a[40:43], v[12:15], v[40:43]\n\t" \
  "v_mfma_f32_16x16x32_bf16 v[44:47], a[44:47], v[12:15], v[44:47]\n\t" \
  "s_waitcnt lgkmcnt(4)\n\t" \
  "v_mfma_f32_16x16x32_bf16 v[32:35], a[48:51], v[16:19], v[32:35]\n\t" \
  "ds_read_b128 v[8:11], v73 offset:9216\n\t" \
  "v_mfma_f32_16x16x32_bf16 v[36:39], a[52:55], v[16:19], v[36:39]\n\t" \
  "ds_read_b128 v[12:15], v73 offset:27648\n\t" \
  "v_mfma_f32_16x16x32_bf16 v[40:43], a[48:51], v[20:23], v[40:43]\n\t" \
  "v_mfma_f32_16x16x32_bf16 v[44:47], a[52:55], v[20:23], v[44:47]\n\t" \
  "s_waitcnt lgkmcnt(4)\n\t" \
  "v_mfma_f32_16x16x32_bf16 v[32:35], a[56:59], v[24:27], v[32:35]\n\t" \
  "ds_read_b128 v[16:19], v73 offset:10240\n\t" \
  "v_mfma_f32_16x16x32_bf16 v[36:39], a[60:63], v[24:27], v[36:39]\n\t" \
  "ds_read_b128 v[20:23], v73 offset:28672\n\t" \
  "v_mfma_f32_16x16x32_bf16 v[40:43], a[56:59], v[28:31], v[40:43]\n\t" \
  "v_mfma_f32_16x16x32_bf16 v[44:47], a[60:63], v[28:31], v[44:47]\n\t" \
  "s_waitcnt lgkmcnt(4)\n\t" \
  "v_mfma_f32_16x16x32_bf16 v[32:35], a[64:67], v[0:3], v[32:35]\n\t" \
  "ds_read_b128 v[24:27], v73 offset:11264\n\t" \
  "v_mfma_f32_16x16x32_bf16 v[36:39], a[68:71], v[0:3], v[36:39]\n\t" \
  "ds_read_b128 v[28:31], v73 offset:29696\n\t" \
  "v_mfma_f32_16x16x32_bf16 v[40:43], a[64:67], v[4:7], v[40:43]\n\t" \
  "v_mfma_f32_16x16x32_bf16 v[44:47], a[68:71], v[4:7], v[44:47]\n\t" \
  "s_waitcnt vmcnt(11)\n\t" \
  "s_barrier\n\t" \
  "s_waitcnt lgkmcnt(4)\n\t" \
  "v_mfma_f32_16x16x32_bf16 v[32:35], a[72:75], v[8:11], v[32:35]\n\t" \
  "ds_read_b128 v[0:3], v73 offset:12288\n\t" \
  "s_add_u32 s48, s58, 36864\n\t" \
  "v_mfma_f32_16x16x32_bf16 v[36:39], a[76:79], v[8:11], v[36:39]\n\t" \
  "ds_read_b128 v[4:7], v73 offset:30720\n\t" \
  "s_mov_b32 m0, s48\n\t" \
  "v_mfma_f32_16x16x32_bf16 v[40:43], a[72:75], v[12:15], v[40:43]\n\t" \
  "s_nop 0\n\t" \
  "global_load_lds_dwordx4 v72, s[46:47] offset:-4096 sc1\n\t" \
  "v_mfma_f32_16x16x32_bf16 v[44:47], a[76:79], v[12:15], v[44:47]\n\t" \
  "global_load_lds_dwordx4 v72, s[46:47] offset:-3072 sc1\n\t" \
  "global_load_lds_dwordx4 v72, s[46:47] offset:-2048 sc1\n\t" \
  "s_waitcnt lgkmcnt(4)\n\t" \
  "v_mfma_f32_16x16x32_bf16 v[32:35], a[80:83], v[16:19], v[32:35]\n\t" \
  "ds_read_b128 v[8:11], v73 offset:13312\n\t" \
  "global_load_lds_dwordx4 v72, s[46:47] offset:-1024 sc1\n\t" \
  "v_mfma_f32_16x16x32_bf16 v[36:39], a[84:87], v[16:19], v[36:39]\n\t" \
  "ds_read_b128 v[12:15], v73 offset:31744\n\t" \
  "global_load_lds_dwordx4 v72, s[46:47] offset:0 sc1\n\t" \
  "v_mfma_f32_16x16x32_bf16 v[40:43], a[80:83], v[20:23], v[40:43]\n\t" \
  "global_load_lds_dwordx4 v72, s[46:47] offset:1024 sc1\n\t" \
  "global_load_lds_dwordx4 v72, s[46:47] offset:2048 sc1\n\t" \
  "v_mfma_f32_16x16x32_bf16 v[44:47], a[84:87], v[20:23], v[44:47]\n\t" \
  "global_load_lds_dwordx4 v72, s[46:47] offset:3072 sc1\n\t" \
  "s_add_u32 m0, s48, 4096\n\t" \
  "s_waitcnt lgkmcnt(4)\n\t" \
  "v_mfma_f32_16x16x32_bf16 v[32:35], a[88:91], v[24:27], v[32:35]\n\t" \
  "ds_read_b128 v[16:19], v73 offset:14336\n\t" \
  "s_nop 0\n\t" \
  "v_mfma_f32_16x16x32_bf16 v[36:39], a[92:95], v[24:27], v[36:39]\n\t" \
  "ds_read_b128 v[20:23], v73 offset:32768\n\t" \
  "global_load_lds_dwordx4 v78, s[46:47] sc1\n\t" \
  "v_mfma_f32_16x16x32_bf16 v[40:43], a[88:91], v[28:31], v[40:43]\n\t" \
  "s_add_u32 s46, s46, 73728\n\t" \
  "s_addc_u32 s47, s47, 0\n\t" \
  "v_mfma_f32_16x16x32_bf16 v[44:47], a[92:95], v[28:31], v[44:47]\n\t" \
  "s_waitcnt lgkmcnt(4)\n\t" \
  "v_mfma_f32_16x16x32_bf16 v[32:35], a[96:99], v[0:3], v[32:35]\n\t" \
  "ds_read_b128 v[24:27], v73 offset:15360\n\t" \
  "v_mfma_f32_16x16x32_bf16 v[36:39], a[100:103], v[0:3], v[36:39]\n\t" \
  "ds_read_b128 v[28:31], v73 offset:33792\n\t" \
  "v_mfma_f32_16x16x32_bf16 v[40:43], a[96:99], v[4:7], v[40:43]\n\t" \
  "v_mfma_f32_16x16x32_bf16 v[44:47], a[100:103], v[4:7], v[44:47]\n\t" \
  "s_waitcnt lgkmcnt(4)\n\t" \
  "v_mfma_f32_16x16x32_bf16 v[32:35], a[104:107], v[8:11], v[32:35]\n\t" \
  "ds_read_b128 v[0:3], v73 offset:16384\n\t" \
  "v_mfma_f32_16x16x32_bf16 v[36:39], a[108:111], v[8:11], v[36:39]\n\t" \
  "ds_read_b128 v[4:7], v73 offset:34816\n\t" \
  "v_mfma_f32_16x16x32_bf16 v[40:43], a[104:107], v[12:15], v[40:43]\n\t" \
  "v_mfma_f32_16x16x32_bf16 v[44:47], a[108:111], v[12:15], v[44:47]\n\t" \
  "s_waitcnt lgkmcnt(4)\n\t" \
  "v_mfma_f32_16x16x32_bf16 v[32:35], a[112:115], v[16:19], v[32:35]\n\t" \
  "ds_read_b128 v[8:11], v73 offset:17408\n\t" \
  "v_mfma_f32_16x16x32_bf16 v[36:39], a[116:119], v[16:19], v[36:39]\n\t" \
  "ds_read_b128 v[12:15], v73 offset:35840\n\t" \
  "v_mfma_f32_16x16x32_bf16 v[40:43], a[112:115], v[20:23], v[40:43]\n\t" \
  "v_mfma_f32_16x16x32_bf16 v[44:47], a[116:119], v[20:23], v[44:47]\n\t" \
  "s_waitcnt lgkmcnt(4)\n\t" \
  "v_mfma_f32_16x16x32_bf16 v[32:35], a[120:123], v[24:27], v[32:35]\n\t" \
  "ds_read_b128 v[16:19], v74 offset:0\n\t" \
  "v_mfma_f32_16x16x32_bf16 v[36:39], a[124:127], v[24:27], v[36:39]\n\t" \
  "ds_read_b128 v[20:23], v74 offset:18432\n\t" \
  "v_mfma_f32_16x16x32_bf16 v[40:43], a[120:123], v[28:31], v[40:43]\n\t" \
  "v_mfma_f32_16x16x32_bf16 v[44:47], a[124:127], v[28:31], v[44:47]\n\t" \
  "s_waitcnt lgkmcnt(4)\n\t" \
  "v_mfma_f32_16x16x32_bf16 v[32:35], a[128:131], v[0:3], v[32:35]\n\t" \
  "ds_read_b128 v[24:27], v74 offset:1024\n\t" \
  "v_mfma_f32_16x16x32_bf16 v[36:39], a[132:135], v[0:3], v[36:39]\n\t" \
  "ds_read_b128 v[28:31], v74 offset:19456\n\t" \
  "v_mfma_f32_16x16x32_bf16 v[40:43], a[128:131], v[4:7], v[40:43]\n\t" \
  "v_mfma_f32_16x16x32_bf16 v[44:47], a[132:135], v[4:7], v[44:47]\n\t" \
  "s_waitcnt lgkmcnt(4)\n\t" \
  "v_mfma_f32_16x16x32_bf16 v[32:35], a[136:139], v[8:11], v[32:35]\n\t" \
  "ds_read_b128 v[0:3], v74 offset:2048\n\t" \
  "v_mfma_f32_16x16x32_bf16 v[36:39], a[140:143], v[8:11], v[36:39]\n\t" \
  "ds_read_b128 v[4:7], v74 offset:20480\n\t" \
  "v_mfma_f32_16x16x32_bf16 v[40:43], a[136:139], v[12:15], v[40:43]\n\t" \
  "v_mfma_f32_16x16x32_bf16 v[44:47], a[140:143], v[12:15], v[44:47]\n\t" \
  "v_mov_b32 v73, v74\n\t" \
  "v_add_u32 v74, 0, v72\n\t" \
  "s_waitcnt lgkmcnt(4)\n\t" \
  "v_mfma_f32_16x16x32_bf16 v[48:51], a[0:3], v[16:19], 0\n\t" \
  "ds_read_b128 v[8:11], v73 offset:3072\n\t" \
  "v_mfma_f32_16x16x32_bf16 v[52:55], a[4:7], v[16:19], 0\n\t" \
  "ds_read_b128 v[12:15], v73 offset:21504\n\t" \
  "v_mfma_f32_16x16x32_bf16 v[56:59], a[0:3], v[20:23], 0\n\t" \
  "v_mfma_f32_16x16x32_bf16 v[60:63], a[4:7], v[20:23], 0\n\t" \
  "s_waitcnt lgkmcnt(4)\n\t" \
  "v_mfma_f32_16x16x32_bf16 v[48:51], a[8:11], v[24:27], v[48:51]\n\t" \
  "ds_read_b128 v[16:19], v73 offset:4096\n\t" \
  "v_cvt_pk_bf16_f32 v64, v32, v33\n\t" \
  "v_mfma_f32_16x16x32_bf16 v[52:55], a[12:15], v[24:27], v[52:55]\n\t" \
  "ds_read_b128 v[20:23], v73 offset:22528\n\t" \
  "v_cvt_pk_bf16_f32 v65, v34, v35\n\t" \
  "v_mfma_f32_16x16x32_bf16 v[56:59], a[8:11], v[28:31], v[56:59]\n\t" \
  "v_cvt_pk_bf16_f32 v66, v36, v37\n\t" \
  "v_cvt_pk_bf16_f32 v67, v38, v39\n\t" \
  "v_mfma_f32_16x16x32_bf16 v[60:63], a[12:15], v[28:31], v[60:63]\n\t" \
  "buffer_store_dwordx4 v[64:67], v72, s[20:23], s50 offen offset:0 sc1\n\t" \
  "v_cvt_pk_bf16_f32 v68, v40, v41\n\t" \
  "s_waitcnt lgkmcnt(4)\n\t" \
  "v_mfma_f32_16x16x32_bf16 v[48:51], a[16:19], v[0:3], v[48:51]\n\t" \
  "ds_read_b128 v[24:27], v73 offset:5120\n\t" \
  "v_cvt_pk_bf16_f32 v69, v42, v43\n\t" \
  "v_mfma_f32_16x16x32_bf16 v[52:55], a[20:23], v[0:3], v[52:55]\n\t" \
  "ds_read_b128 v[28:31], v73 offset:23552\n\t" \
  "v_cvt_pk_bf16_f32 v70, v44, v45\n\t" \
  "v_mfma_f32_16x16x32_bf16 v[56:59], a[16:19], v[4:7], v[56:59]\n\t" \
  "v_cvt_pk_bf16_f32 v71, v46, v47\n\t" \
  "buffer_store_dwordx4 v[68:71], v78, s[20:23], s50 offen offset:0 sc1\n\t" \
  "v_mfma_f32_16x16x32_bf16 v[60:63], a[20:23], v[4:7], v[60:63]\n\t" \
  "s_add_u32 s50, s50, 524288\n\t" \
  "s_waitcnt lgkmcnt(4)\n\t" \
  "v_mfma_f32_16x16x32_bf16 v[48:51], a[24:27], v[8:11], v[48:51]\n\t" \
  "ds_read_b128 v[0:3], v73 offset:6144\n\t" \
  "v_mfma_f32_16x16x32_bf16 v[52:55], a[28:31], v[8:11], v[52:55]\n\t" \
  "ds_read_b128 v[4:7], v73 offset:24576\n\t" \
  "v_mfma_f32_16x16x32_bf16 v[56:59], a[24:27], v[12:15], v[56:59]\n\t" \
  "v_mfma_f32_16x16x32_bf16 v[60:63], a[28:31], v[12:15], v[60:63]\n\t" \
  "s_waitcnt lgkmcnt(4)\n\t" \
  "v_mfma_f32_16x16x32_bf16 v[48:51], a[32:35], v[16:19], v[48:51]\n\t" \
  "ds_read_b128 v[8:11], v73 offset:7168\n\t" \
  "v_mfma_f32_16x16x32_bf16 v[52:55], a[36:39], v[16:19], v[52:55]\n\t" \
  "ds_read_b128 v[12:15], v73 offset:25600\n\t" \
  "v_mfma_f32_16x16x32_bf16 v[56:59], a[32:35], v[20:23], v[56:59]\n\t" \
  "v_mfma_f32_16x16x32_bf16 v[60:63], a[36:39], v[20:23], v[60:63]\n\t" \
  "s_waitcnt lgkmcnt(4)\n\t" \
  "v_mfma_f32_16x16x32_bf16 v[48:51], a[40:43], v[24:27], v[48:51]\n\t" \
  "ds_read_b128 v[16:19], v73 offset:8192\n\t" \
  "v_mfma_f32_16x16x32_bf16 v[52:55], a[44:47], v[24:27], v[52:55]\n\t" \
  "ds_read_b128 v[20:23], v73 offset:26624\n\t" \
  "v_mfma_f32_16x16x32_bf16 v[56:59], a[40:43], v[28:31], v[56:59]\n\t" \
  "v_mfma_f32_16x16x32_bf16 v[60:63], a[44:47], v[28:31], v[60:63]\n\t" \
  "s_waitcnt lgkmcnt(4)\n\t" \
  "v_mfma_f32_16x16x32_bf16 v[48:51], a[48:51], v[0:3], v[48:51]\n\t" \
  "ds_read_b128 v[24:27], v73 offset:9216\n\t" \
  "v_mfma_f32_16x16x32_bf16 v[52:55], a[52:55], v[0:3], v[52:55]\n\t" \
  "ds_read_b128 v[28:31], v73 offset:27648\n\t" \
  "v_mfma_f32_16x16x32_bf16 v[56:59], a[48:51], v[4:7], v[56:59]\n\t" \
  "v_mfma_f32_16x16x32_bf16 v[60:63], a[52:55], v[4:7], v[60:63]\n\t" \
  "s_waitcnt lgkmcnt(4)\n\t" \
  "v_mfma_f32_16x16x32_bf16 v[48:51], a[56:59], v[8:11], v[48:51]\n\t" \
  "ds_read_b128 v[0:3], v73 offset:10240\n\t" \
  "v_mfma_f32_16x16x32_bf16 v[52:55], a[60:63], v[8:11], v[52:55]\n\t" \
  "ds_read_b128 v[4:7], v73 offset:28672\n\t" \
  "v_mfma_f32_16x16x32_bf16 v[56:59], a[56:59], v[12:15], v[56:59]\n\t" \
  "v_mfma_f32_16x16x32_bf16 v[60:63], a[60:63], v[12:15], v[60:63]\n\t" \
  "s_waitcnt lgkmcnt(4)\n\t" \
  "v_mfma_f32_16x16x32_bf16 v[48:51], a[64:67], v[16:19], v[48:51]\n\t" \
  "ds_read_b128 v[8:11], v73 offset:11264\n\t" \
  "v_mfma_f32_16x16x32_bf16 v[52:55], a[68:71], v[16:19], v[52:55]\n\t" \
  "ds_read_b128 v[12:15], v73 offset:29696\n\t" \
  "v_mfma_f32_16x16x32_bf16 v[56:59], a[64:67], v[20:23], v[56:59]\n\t" \
  "v_mfma_f32_16x16x32_bf16 v[60:63], a[68:71], v[20:23], v[60:63]\n\t" \
  "s_waitcnt vmcnt(11)\n\t" \
  "s_barrier\n\t" \
  "s_waitcnt lgkmcnt(4)\n\t" \
  "v_mfma_f32_16x16x32_bf16 v[48:51], a[72:75], v[24:27], v[48:51]\n\t" \
  "ds_read_b128 v[16:19], v73 offset:12288\n\t" \
  "v_mov_b32 v77, 1\n\t" \
  "v_mfma_f32_16x16x32_bf16 v[52:55], a[76:79], v[24:27], v[52:55]\n\t" \
  "ds_read_b128 v[20:23], v73 offset:30720\n\t" \
  "s_mov_b64 exec, s[54:55]\n\t" \
  "buffer_store_dword v77, off, s[20:23], s51 sc1\n\t" \
  "s_mov_b64 exec, -1\n\t" \
  "v_mfma_f32_16x16x32_bf16 v[56:59], a[72:75], v[28:31], v[56:59]\n\t" \
  "s_add_u32 s51, s51, 1024\n\t" \
  "s_add_u32 s48, s58, 73728\n\t" \
  "v_mfma_f32_16x16x32_bf16 v[60:63], a[76:79], v[28:31], v[60:63]\n\t" \
  "s_mov_b32 m0, s48\n\t" \
  "s_nop 0\n\t" \
  "s_waitcnt lgkmcnt(4)\n\t" \
  "v_mfma_f32_16x16x32_bf16 v[48:51], a[80:83], v[0:3], v[48:51]\n\t" \
  "ds_read_b128 v[24:27], v73 offset:13312\n\t" \
  "global_load_lds_dwordx4 v72, s[46:47] offset:-4096 sc1\n\t" \
  "v_mfma_f32_16x16x32_bf16 v[52:55], a[84:87], v[0:3], v[52:55]\n\t" \
  "ds_read_b128 v[28:31], v73 offset:31744\n\t" \
  "global_load_lds_dwordx4 v72, s[46:47] offset:-3072 sc1\n\t" \
  "v_mfma_f32_16x16x32_bf16 v[56:59], a[80:83], v[4:7], v[56:59]\n\t" \
  "global_load_lds_dwordx4 v72, s[46:47] offset:-2048 sc1\n\t" \
  "global_load_lds_dwordx4 v72, s[46:47] offset:-1024 sc1\n\t" \
  "v_mfma_f32_16x16x32_bf16 v[60:63], a[84:87], v[4:7], v[60:63]\n\t" \
  "global_load_lds_dwordx4 v72, s[46:47] offset:0 sc1\n\t" \
  "global_load_lds_dwordx4 v72, s[46:47] offset:1024 sc1\n\t" \
  "s_waitcnt lgkmcnt(4)\n\t" \
  "v_mfma_f32_16x16x32_bf16 v[48:51], a[88:91], v[8:11], v[48:51]\n\t" \
  "ds_read_b128 v[0:3], v73 offset:14336\n\t" \
  "global_load_lds_dwordx4 v72, s[46:47] offset:2048 sc1\n\t" \
  "v_mfma_f32_16x16x32_bf16 v[52:55], a[92:95], v[8:11], v[52:55]\n\t" \
  "ds_read_b128 v[4:7], v73 offset:32768\n\t" \
  "global_load_lds_dwordx4 v72, s[46:47] offset:3072 sc1\n\t" \
  "v_mfma_f32_16x16x32_bf16 v[56:59], a[88:91], v[12:15], v[56:59]\n\t" \
  "s_add_u32 m0, s48, 4096\n\t" \
  "s_nop 0\n\t" \
  "v_mfma_f32_16x16x32_bf16 v[60:63], a[92:95], v[12:15], v[60:63]\n\t" \
  "global_load_lds_dwordx4 v78, s[46:47] sc1\n\t" \
  "s_add_u32 s46, s46, 73728\n\t" \
  "s_waitcnt lgkmcnt(4)\n\t" \
  "v_mfma_f32_16x16x32_bf16 v[48:51], a[96:99], v[16:19], v[48:51]\n\t" \
  "ds_read_b128 v[8:11], v73 offset:15360\n\t" \
  "s_addc_u32 s47, s47, 0\n\t" \
  "v_mfma_f32_16x16x32_bf16 v[52:55], a[100:103], v[16:19], v[52:55]\n\t" \
  "ds_read_b128 v[12:15], v73 offset:33792\n\t" \
  "v_mfma_f32_16x16x32_bf16 v[56:59], a[96:99], v[20:23], v[56:59]\n\t" \
  "v_mfma_f32_16x16x32_bf16 v[60:63], a[100:103], v[20:23], v[60:63]\n\t" \
  "s_waitcnt lgkmcnt(4)\n\t" \
  "v_mfma_f32_16x16x32_bf16 v[48:51], a[104:107], v[24:27], v[48:51]\n\t" \
  "ds_read_b128 v[16:19], v73 offset:16384\n\t" \
  "v_mfma_f32_16x16x32_bf16 v[52:55], a[108:111], v[24:27], v[52:55]\n\t" \
  "ds_read_b128 v[20:23], v73 offset:34816\n\t" \
  "v_mfma_f32_16x16x32_bf16 v[56:59], a[104:107], v[28:31], v[56:59]\n\t" \
  "v_mfma_f32_16x16x32_bf16 v[60:63], a[108:111], v[28:31], v[60:63]\n\t" \
  "s_waitcnt lgkmcnt(4)\n\t" \
  "v_mfma_f32_16x16x32_bf16 v[48:51], a[112:115], v[0:3], v[48:51]\n\t" \
  "ds_read_b128 v[24:27], v73 offset:17408\n\t" \
  "v_mfma_f32_16x16x32_bf16 v[52:55], a[116:119], v[0:3], v[52:55]\n\t" \
  "ds_read_b128 v[28:31], v73 offset:35840\n\t" \
  "v_mfma_f32_16x16x32_bf16 v[56:59], a[112:115], v[4:7], v[56:59]\n\t" \
  "v_mfma_f32_16x16x32_bf16 v[60:63], a[116:119], v[4:7], v[60:63]\n\t" \
  "s_waitcnt lgkmcnt(4)\n\t" \
  "v_mfma_f32_16x16x32_bf16 v[48:51], a[120:123], v[8:11], v[48:51]\n\t" \
  "ds_read_b128 v[0:3], v74 offset:0\n\t" \
  "v_mfma_f32_16x16x32_bf16 v[52:55], a[124:127], v[8:11], v[52:55]\n\t" \
  "ds_read_b128 v[4:7], v74 offset:18432\n\t" \
  "v_mfma_f32_16x16x32_bf16 v[56:59], a[120:123], v[12:15], v[56:59]\n\t" \
  "v_mfma_f32_16x16x32_bf16 v[60:63], a[124:127], v[12:15], v[60:63]\n\t" \
  "s_waitcnt lgkmcnt(4)\n\t" \
  "v_mfma_f32_16x16x32_bf16 v[48:51], a[128:131], v[16:19], v[48:51]\n\t" \
  "ds_read_b128 v[8:11], v74 offset:1024\n\t" \
  "v_mfma_f32_16x16x32_bf16 v[52:55], a[132:135], v[16:19], v[52:55]\n\t" \
  "ds_read_b128 v[12:15], v74 offset:19456\n\t" \
  "v_mfma_f32_16x16x32_bf16 v[56:59], a[128:131], v[20:23], v[56:59]\n\t" \
  "v_mfma_f32_16x16x32_bf16 v[60:63], a[132:135], v[20:23], v[60:63]\n\t" \
  "s_waitcnt lgkmcnt(4)\n\t" \
  "v_mfma_f32_16x16x32_bf16 v[48:51], a[136:139], v[24:27], v[48:51]\n\t" \
  "ds_read_b128 v[16:19], v74 offset:2048\n\t" \
  "v_mfma_f32_16x16x32_bf16 v[52:55], a[140:143], v[24:27], v[52:55]\n\t" \
  "ds_read_b128 v[20:23], v74 offset:20480\n\t" \
  "v_mfma_f32_16x16x32_bf16 v[56:59], a[136:139], v[28:31], v[56:59]\n\t" \
  "v_mfma_f32_16x16x32_bf16 v[60:63], a[140:143], v[28:31], v[60:63]\n\t" \
  "v_mov_b32 v73, v74\n\t" \
  "s_mov_b32 s45, 6\n\t" \
  "Lq_%=:\n\t" \
  "v_add_u32 v74, 36864, v72\n\t" \
  "s_waitcnt lgkmcnt(4)\n\t" \
  "v_mfma_f32_16x16x32_bf16 v[32:35], a[0:3], v[0:3], 0\n\t" \
  "ds_read_b128 v[24:27], v73 offset:3072\n\t" \
  "v_mfma_f32_16x16x32_bf16 v[36:39], a[4:7], v[0:3], 0\n\t" \
  "ds_read_b128 v[28:31], v73 offset:21504\n\t" \
  "v_mfma_f32_16x16x32_bf16 v[40:43], a[0:3], v[4:7], 0\n\t" \
  "v_mfma_f32_16x16x32_bf16 v[44:47], a[4:7], v[4:7], 0\n\t" \
  "s_waitcnt lgkmcnt(4)\n\t" \
  "v_mfma_f32_16x16x32_bf16 v[32:35], a[8:11], v[8:11], v[32:35]\n\t" \
  "ds_read_b128 v[0:3], v73 offset:4096\n\t" \
  "v_cvt_pk_bf16_f32 v64, v48, v49\n\t" \
  "v_mfma_f32_16x16x32_bf16 v[36:39], a[12:15], v[8:11], v[36:39]\n\t" \
  "ds_read_b128 v[4:7], v73 offset:22528\n\t" \
  "v_cvt_pk_bf16_f32 v65, v50, v51\n\t" \
  "v_mfma_f32_16x16x32_bf16 v[40:43], a[8:11], v[12:15], v[40:43]\n\t" \
  "v_cvt_pk_bf16_f32 v66, v52, v53\n\t" \
  "v_cvt_pk_bf16_f32 v67, v54, v55\n\t" \
  "v_mfma_f32_16x16x32_bf16 v[44:47], a[12:15], v[12:15], v[44:47]\n\t" \
  "buffer_store_dwordx4 v[64:67], v72, s[20:23], s50 offen offset:0 sc1\n\t" \
  "v_cvt_pk_bf16_f32 v68, v56, v57\n\t" \
  "s_waitcnt lgkmcnt(4)\n\t" \
  "v_mfma_f32_16x16x32_bf16 v[32:35], a[16:19], v[16:19], v[32:35]\n\t" \
  "ds_read_b128 v[8:11], v73 offset:5120\n\t" \
  "v_cvt_pk_bf16_f32 v69, v58, v59\n\t" \
  "v_mfma_f32_16x16x32_bf16 v[36:39], a[20:23], v[16:19], v[36:39]\n\t" \
  "ds_read_b128 v[12:15], v73 offset:23552\n\t" \
  "v_cvt_pk_bf16_f32 v70, v60, v61\n\t" \
  "v_mfma_f32_16x16x32_bf16 v[40:43], a[16:19], v[20:23], v[40:43]\n\t" \
  "v_cvt_pk_bf16_f32 v71, v62, v63\n\t" \
  "buffer_store_dwordx4 v[68:71], v78, s[20:23], s50 offen offset:0 sc1\n\t" \
  "v_mfma_f32_16x16x32_bf16 v[44:47], a[20:23], v[20:23], v[44:47]\n\t" \
  "s_add_u32 s50, s50, 524288\n\t" \
  "s_waitcnt lgkmcnt(4)\n\t" \
  "v_mfma_f32_16x16x32_bf16 v[32:35], a[24:27], v[24:27], v[32:35]\n\t" \
  "ds_read_b128 v[16:19], v73 offset:6144\n\t" \
  "v_mfma_f32_16x16x32_bf16 v[36:39], a[28:31], v[24:27], v[36:39]\n\t" \
  "ds_read_b128 v[20:23], v73 offset:24576\n\t" \
  "v_mfma_f32_16x16x32_bf16 v[40:43], a[24:27], v[28:31], v[40:43]\n\t" \
  "v_mfma_f32_16x16x32_bf16 v[44:47], a[28:31], v[28:31], v[44:47]\n\t" \
  "s_waitcnt lgkmcnt(4)\n\t" \
  "v_mfma_f32_16x16x32_bf16 v[32:35], a[32:35], v[0:3], v[32:35]\n\t" \
  "ds_read_b128 v[24:27], v73 offset:7168\n\t" \
  "v_mfma_f32_16x16x32_bf16 v[36:39], a[36:39], v[0:3], v[36:39]\n\t" \
  "ds_read_b128 v[28:31], v73 offset:25600\n\t" \
  "v_mfma_f32_16x16x32_bf16 v[40:43], a[32:35], v[4:7], v[40:43]\n\t" \
  "v_mfma_f32_16x16x32_bf16 v[44:47], a[36:39], v[4:7], v[44:47]\n\t" \
  "s_waitcnt lgkmcnt(4)\n\t" \
  "v_mfma_f32_16x16x32_bf16 v[32:35], a[40:43], v[8:11], v[32:35]\n\t" \
  "ds_read_b128 v[0:3], v73 offset:8192\n\t" \
  "v_mfma_f32_16x16x32_bf16 v[36:39], a[44:47], v[8:11], v[36:39]\n\t" \
  "ds_read_b128 v[4:7], v73 offset:26624\n\t" \
  "v_mfma_f32_16x16x32_bf16 v[40:43], a[40:43], v[12:15], v[40:43]\n\t" \
  "v_mfma_f32_16x16x32_bf16 v[44:47], a[44:47], v[12:15], v[44:47]\n\t" \
  "s_waitcnt lgkmcnt(4)\n\t" \
  "v_mfma_f32_16x16x32_bf16 v[32:35], a[48:51], v[16:19], v[32:35]\n\t" \
  "ds_read_b128 v[8:11], v73 offset:9216\n\t" \
  "v_mfma_f32_16x16x32_bf16 v[36:39], a[52:55], v[16:19], v[36:39]\n\t" \
  "ds_read_b128 v[12:15], v73 offset:27648\n\t" \
  "v_mfma_f32_16x16x32_bf16 v[40:43], a[48:51], v[20:23], v[40:43]\n\t" \
  "v_mfma_f32_16x16x32_bf16 v[44:47], a[52:55], v[20:23], v[44:47]\n\t" \
  "s_waitcnt lgkmcnt(4)\n\t" \
  "v_mfma_f32_16x16x32_bf16 v[32:35], a[56:59], v[24:27], v[32:35]\n\t" \
  "ds_read_b128 v[16:19], v73 offset:10240\n\t" \
  "v_mfma_f32_16x16x32_bf16 v[36:39], a[60:63], v[24:27], v[36:39]\n\t" \
  "ds_read_b128 v[20:23], v73 offset:28672\n\t" \
  "v_mfma_f32_16x16x32_bf16 v[40:43], a[56:59], v[28:31], v[40:43]\n\t" \
  "v_mfma_f32_16x16x32_bf16 v[44:47], a[60:63], v[28:31], v[44:47]\n\t" \
  "s_waitcnt lgkmcnt(4)\n\t" \
  "v_mfma_f32_16x16x32_bf16 v[32:35], a[64:67], v[0:3], v[32:35]\n\t" \
  "ds_read_b128 v[24:27], v73 offset:11264\n\t" \
  "v_mfma_f32_16x16x32_bf16 v[36:39], a[68:71], v[0:3], v[36:39]\n\t" \
  "ds_read_b128 v[28:31], v73 offset:29696\n\t" \
  "v_mfma_f32_16x16x32_bf16 v[40:43], a[64:67], v[4:7], v[40:43]\n\t" \
  "v_mfma_f32_16x16x32_bf16 v[44:47], a[68:71], v[4:7], v[44:47]\n\t" \
  "s_waitcnt vmcnt(11)\n\t" \
  "s_barrier\n\t" \
  "s_waitcnt lgkmcnt(4)\n\t" \
  "v_mfma_f32_16x16x32_bf16 v[32:35], a[72:75], v[8:11], v[32:35]\n\t" \
  "ds_read_b128 v[0:3], v73 offset:12288\n\t" \
  "v_mov_b32 v77, 1\n\t" \
  "v_mfma_f32_16x16x32_bf16 v[36:39], a[76:79], v[8:11], v[36:39]\n\t" \
  "ds_read_b128 v[4:7], v73 offset:30720\n\t" \
  "s_mov_b64 exec, s[54:55]\n\t" \
  "buffer_store_dword v77, off, s[20:23], s51 sc1\n\t" \
  "s_mov_b64 exec, -1\n\t" \
  "v_mfma_f32_16x16x32_bf16 v[40:43], a[72:75], v[12:15], v[40:43]\n\t" \
  "s_add_u32 s51, s51, 1024\n\t" \
  "s_add_u32 s48, s58, 110592\n\t" \
  "v_mfma_f32_16x16x32_bf16 v[44:47], a[76:79], v[12:15], v[44:47]\n\t" \
  "s_mov_b32 m0, s48\n\t" \
  "s_nop 0\n\t" \
  "s_waitcnt lgkmcnt(4)\n\t" \
  "v_mfma_f32_16x16x32_bf16 v[32:35], a[80:83], v[16:19], v[32:35]\n\t" \
  "ds_read_b128 v[8:11], v73 offset:13312\n\t" \
  "global_load_lds_dwordx4 v72, s[46:47] offset:-4096 sc1\n\t" \
  "v_mfma_f32_16x16x32_bf16 v[36:39], a[84:87], v[16:19], v[36:39]\n\t" \
  "ds_read_b128 v[12:15], v73 offset:31744\n\t" \
  "global_load_lds_dwordx4 v72, s[46:47] offset:-3072 sc1\n\t" \
  "v_mfma_f32_16x16x32_bf16 v[40:43], a[80:83], v[20:23], v[40:43]\n\t" \
  "global_load_lds_dwordx4 v72, s[46:47] offset:-2048 sc1\n\t" \
  "global_load_lds_dwordx4 v72, s[46:47] offset:-1024 sc1\n\t" \
  "v_mfma_f32_16x16x32_bf16 v[44:47], a[84:87], v[20:23], v[44:47]\n\t" \
  "global_load_lds_dwordx4 v72, s[46:47] offset:0 sc1\n\t" \
  "global_load_lds_dwordx4 v72, s[46:47] offset:1024 sc1\n\t" \
  "s_waitcnt lgkmcnt(4)\n\t" \
  "v_mfma_f32_16x16x32_bf16 v[32:35], a[88:91], v[24:27], v[32:35]\n\t" \
  "ds_read_b128 v[16:19], v73 offset:14336\n\t" \
  "global_load_lds_dwordx4 v72, s[46:47] offset:2048 sc1\n\t" \
  "v_mfma_f32_16x16x32_bf16 v[36:39], a[92:95], v[24:27], v[36:39]\n\t" \
  "ds_read_b128 v[20:23], v73 offset:32768\n\t" \
  "global_load_lds_dwordx4 v72, s[46:47] offset:3072 sc1\n\t" \
  "v_mfma_f32_16x16x32_bf16 v[40:43], a[88:91], v[28:31], v[40:43]\n\t" \
  "s_add_u32 m0, s48, 4096\n\t" \
  "s_nop 0\n\t" \
  "v_mfma_f32_16x16x32_bf16 v[44:47], a[92:95], v[28:31], v[44:47]\n\t" \
  "global_load_lds_dwordx4 v78, s[46:47] sc1\n\t" \
  "s_add_u32 s46, s46, 73728\n\t" \
  "s_waitcnt lgkmcnt(4)\n\t" \
  "v_mfma_f32_16x16x32_bf16 v[32:35], a[96:99], v[0:3], v[32:35]\n\t" \
  "ds_read_b128 v[24:27], v73 offset:15360\n\t" \
  "s_addc_u32 s47, s47, 0\n\t" \
  "v_mfma_f32_16x16x32_bf16 v[36:39], a[100:103], v[0:3], v[36:39]\n\t" \
  "ds_read_b128 v[28:31], v73 offset:33792\n\t" \
  "v_mfma_f32_16x16x32_bf16 v[40:43], a[96:99], v[4:7], v[40:43]\n\t" \
  "v_mfma_f32_16x16x32_bf16 v[44:47], a[100:103], v[4:7], v[44:47]\n\t" \
  "s_waitcnt lgkmcnt(4)\n\t" \
  "v_mfma_f32_16x16x32_bf16 v[32:35], a[104:107], v[8:11], v[32:35]\n\t" \
  "ds_read_b128 v[0:3], v73 offset:16384\n\t" \
  "v_mfma_f32_16x16x32_bf16 v[36:39], a[108:111], v[8:11], v[36:39]\n\t" \
  "ds_read_b128 v[4:7], v73 offset:34816\n\t" \
  "v_mfma_f32_16x16x32_bf16 v[40:43], a[104:107], v[12:15], v[40:43]\n\t" \
  "v_mfma_f32_16x16x32_bf16 v[44:47], a[108:111], v[12:15], v[44:47]\n\t" \
  "s_waitcnt lgkmcnt(4)\n\t" \
  "v_mfma_f32_16x16x32_bf16 v[32:35], a[112:115], v[16:19], v[32:35]\n\t" \
  "ds_read_b128 v[8:11], v73 offset:17408\n\t" \
  "v_mfma_f32_16x16x32_bf16 v[36:39], a[116:119], v[16:19], v[36:39]\n\t" \
  "ds_read_b128 v[12:15], v73 offset:35840\n\t" \
  "v_mfma_f32_16x16x32_bf16 v[40:43], a[112:115], v[20:23], v[40:43]\n\t" \
  "v_mfma_f32_16x16x32_bf16 v[44:47], a[116:119], v[20:23], v[44:47]\n\t" \
  "s_waitcnt lgkmcnt(4)\n\t" \
  "v_mfma_f32_16x16x32_bf16 v[32:35], a[120:123], v[24:27], v[32:35]\n\t" \
  "ds_read_b128 v[16:19], v74 offset:0\n\t" \
  "v_mfma_f32_16x16x32_bf16 v[36:39], a[124:127], v[24:27], v[36:39]\n\t" \
  "ds_read_b128 v[20:23], v74 offset:18432\n\t" \
  "v_mfma_f32_16x16x32_bf16 v[40:43], a[120:123], v[28:31], v[40:43]\n\t" \
  "v_mfma_f32_16x16x32_bf16 v[44:47], a[124:127], v[28:31], v[44:47]\n\t" \
  "s_waitcnt lgkmcnt(4)\n\t" \
  "v_mfma_f32_16x16x32_bf16 v[32:35], a[128:131], v[0:3], v[32:35]\n\t" \
  "ds_read_b128 v[24:27], v74 offset:1024\n\t" \
  "v_mfma_f32_16x16x32_bf16 v[36:39], a[132:135], v[0:3], v[36:39]\n\t" \
  "ds_read_b128 v[28:31], v74 offset:19456\n\t" \
  "v_mfma_f32_16x16x32_bf16 v[40:43], a[128:131], v[4:7], v[40:43]\n\t" \
  "v_mfma_f32_16x16x32_bf16 v[44:47], a[132:135], v[4:7], v[44:47]\n\t" \
  "s_waitcnt lgkmcnt(4)\n\t" \
  "v_mfma_f32_16x16x32_bf16 v[32:35], a[136:139], v[8:11], v[32:35]\n\t" \
  "ds_read_b128 v[0:3], v74 offset:2048\n\t" \
  "v_mfma_f32_16x16x32_bf16 v[36:39], a[140:143], v[8:11], v[36:39]\n\t" \
  "ds_read_b128 v[4:7], v74 offset:20480\n\t" \
  "v_mfma_f32_16x16x32_bf16 v[40:43], a[136:139], v[12:15], v[40:43]\n\t" \
  "v_mfma_f32_16x16x32_bf16 v[44:47], a[140:143], v[12:15], v[44:47]\n\t" \
  "v_mov_b32 v73, v74\n\t" \
  "v_add_u32 v74, 73728, v72\n\t" \
  "s_waitcnt lgkmcnt(4)\n\t" \
  "v_mfma_f32_16x16x32_bf16 v[48:51], a[0:3], v[16:19], 0\n\t" \
  "ds_read_b128 v[8:11], v73 offset:3072\n\t" \
  "v_mfma_f32_16x16x32_bf16 v[52:55], a[4:7], v[16:19], 0\n\t" \
  "ds_read_b128 v[12:15], v73 offset:21504\n\t" \
  "v_mfma_f32_16x16x32_bf16 v[56:59], a[0:3], v[20:23], 0\n\t" \
  "v_mfma_f32_16x16x32_bf16 v[60:63], a[4:7], v[20:23], 0\n\t" \
  "s_waitcnt lgkmcnt(4)\n\t" \
  "v_mfma_f32_16x16x32_bf16 v[48:51], a[8:11], v[24:27], v[48:51]\n\t" \
  "ds_read_b128 v[16:19], v73 offset:4096\n\t" \
  "v_cvt_pk_bf16_f32 v64, v32, v33\n\t" \
  "v_mfma_f32_16x16x32_bf16 v[52:55], a[12:15], v[24:27], v[52:55]\n\t" \
  "ds_read_b128 v[20:23], v73 offset:22528\n\t" \
  "v_cvt_pk_bf16_f32 v65, v34, v35\n\t" \
  "v_mfma_f32_16x16x32_bf16 v[56:59], a[8:11], v[28:31], v[56:59]\n\t" \
  "v_cvt_pk_bf16_f32 v66, v36, v37\n\t" \
  "v_cvt_pk_bf16_f32 v67, v38, v39\n\t" \
  "v_mfma_f32_16x16x32_bf16 v[60:63], a[12:15], v[28:31], v[60:63]\n\t" \
  "buffer_store_dwordx4 v[64:67], v72, s[20:23], s50 offen offset:0 sc1\n\t" \
  "v_cvt_pk_bf16_f32 v68, v40, v41\n\t" \
  "s_waitcnt lgkmcnt(4)\n\t" \
  "v_mfma_f32_16x16x32_bf16 v[48:51], a[16:19], v[0:3], v[48:51]\n\t" \
  "ds_read_b128 v[24:27], v73 offset:5120\n\t" \
  "v_cvt_pk_bf16_f32 v69, v42, v43\n\t" \
  "v_mfma_f32_16x16x32_bf16 v[52:55], a[20:23], v[0:3], v[52:55]\n\t" \
  "ds_read_b128 v[28:31], v73 offset:23552\n\t" \
  "v_cvt_pk_bf16_f32 v70, v44, v45\n\t" \
  "v_mfma_f32_16x16x32_bf16 v[56:59], a[16:19], v[4:7], v[56:59]\n\t" \
  "v_cvt_pk_bf16_f32 v71, v46, v47\n\t" \
  "buffer_store_dwordx4 v[68:71], v78, s[20:23], s50 offen offset:0 sc1\n\t" \
  "v_mfma_f32_16x16x32_bf16 v[60:63], a[20:23], v[4:7], v[60:63]\n\t" \
  "s_add_u32 s50, s50, 524288\n\t" \
  "s_waitcnt lgkmcnt(4)\n\t" \
  "v_mfma_f32_16x16x32_bf16 v[48:51], a[24:27], v[8:11], v[48:51]\n\t" \
  "ds_read_b128 v[0:3], v73 offset:6144\n\t" \
  "v_mfma_f32_16x16x32_bf16 v[52:55], a[28:31], v[8:11], v[52:55]\n\t" \
  "ds_read_b128 v[4:7], v73 offset:24576\n\t" \
  "v_mfma_f32_16x16x32_bf16 v[56:59], a[24:27], v[12:15], v[56:59]\n\t" \
  "v_mfma_f32_16x16x32_bf16 v[60:63], a[28:31], v[12:15], v[60:63]\n\t" \
  "s_waitcnt lgkmcnt(4)\n\t" \
  "v_mfma_f32_16x16x32_bf16 v[48:51], a[32:35], v[16:19], v[48:51]\n\t" \
  "ds_read_b128 v[8:11], v73 offset:7168\n\t" \
  "v_mfma_f32_16x16x32_bf16 v[52:55], a[36:39], v[16:19], v[52:55]\n\t" \
  "ds_read_b128 v[12:15], v73 offset:25600\n\t" \
  "v_mfma_f32_16x16x32_bf16 v[56:59], a[32:35], v[20:23], v[56:59]\n\t" \
  "v_mfma_f32_16x16x32_bf16 v[60:63], a[36:39], v[20:23], v[60:63]\n\t" \
  "s_waitcnt lgkmcnt(4)\n\t" \
  "v_mfma_f32_16x16x32_bf16 v[48:51], a[40:43], v[24:27], v[48:51]\n\t" \
  "ds_read_b128 v[16:19], v73 offset:8192\n\t" \
  "v_mfma_f32_16x16x32_bf16 v[52:55], a[44:47], v[24:27], v[52:55]\n\t" \
  "ds_read_b128 v[20:23], v73 offset:26624\n\t" \
  "v_mfma_f32_16x16x32_bf16 v[56:59], a[40:43], v[28:31], v[56:59]\n\t" \
  "v_mfma_f32_16x16x32_bf16 v[60:63], a[44:47], v[28:31], v[60:63]\n\t" \
  "s_waitcnt lgkmcnt(4)\n\t" \
  "v_mfma_f32_16x16x32_bf16 v[48:51], a[48:51], v[0:3], v[48:51]\n\t" \
  "ds_read_b128 v[24:27], v73 offset:9216\n\t" \
  "v_mfma_f32_16x16x32_bf16 v[52:55], a[52:55], v[0:3], v[52:55]\n\t" \
  "ds_read_b128 v[28:31], v73 offset:27648\n\t" \
  "v_mfma_f32_16x16x32_bf16 v[56:59], a[48:51], v[4:7], v[56:59]\n\t" \
  "v_mfma_f32_16x16x32_bf16 v[60:63], a[52:55], v[4:7], v[60:63]\n\t" \
  "s_waitcnt lgkmcnt(4)\n\t" \
  "v_mfma_f32_16x16x32_bf16 v[48:51], a[56:59], v[8:11], v[48:51]\n\t" \
  "ds_read_b128 v[0:3], v73 offset:10240\n\t" \
  "v_mfma_f32_16x16x32_bf16 v[52:55], a[60:63], v[8:11], v[52:55]\n\t" \
  "ds_read_b128 v[4:7], v73 offset:28672\n\t" \
  "v_mfma_f32_16x16x32_bf16 v[56:59], a[56:59], v[12:15], v[56:59]\n\t" \
  "v_mfma_f32_16x16x32_bf16 v[60:63], a[60:63], v[12:15], v[60:63]\n\t" \
  "s_waitcnt lgkmcnt(4)\n\t" \
  "v_mfma_f32_16x16x32_bf16 v[48:51], a[64:67], v[16:19], v[48:51]\n\t" \
  "ds_read_b128 v[8:11], v73 offset:11264\n\t" \
  "v_mfma_f32_16x16x32_bf16 v[52:55], a[68:71], v[16:19], v[52:55]\n\t" \
  "ds_read_b128 v[12:15], v73 offset:29696\n\t" \
  "v_mfma_f32_16x16x32_bf16 v[56:59], a[64:67], v[20:23], v[56:59]\n\t" \
  "v_mfma_f32_16x16x32_bf16 v[60:63], a[68:71], v[20:23], v[60:63]\n\t" \
  "s_waitcnt vmcnt(11)\n\t" \
  "s_barrier\n\t" \
  "s_waitcnt lgkmcnt(4)\n\t" \
  "v_mfma_f32_16x16x32_bf16 v[48:51], a[72:75], v[24:27], v[48:51]\n\t" \
  "ds_read_b128 v[16:19], v73 offset:12288\n\t" \
  "v_mov_b32 v77, 1\n\t" \
  "v_mfma_f32_16x16x32_bf16 v[52:55], a[76:79], v[24:27], v[52:55]\n\t" \
  "ds_read_b128 v[20:23], v73 offset:30720\n\t" \
  "s_mov_b64 exec, s[54:55]\n\t" \
  "buffer_store_dword v77, off, s[20:23], s51 sc1\n\t" \
  "s_mov_b64 exec, -1\n\t" \
  "v_mfma_f32_16x16x32_bf16 v[56:59], a[72:75], v[28:31], v[56:59]\n\t" \
  "s_add_u32 s51, s51, 1024\n\t" \
  "s_add_u32 s48, s58, 0\n\t" \
  "v_mfma_f32_16x16x32_bf16 v[60:63], a[76:79], v[28:31], v[60:63]\n\t" \
  "s_mov_b32 m0, s48\n\t" \
  "s_nop 0\n\t" \
  "s_waitcnt lgkmcnt(4)\n\t" \
  "v_mfma_f32_16x16x32_bf16 v[48:51], a[80:83], v[0:3], v[48:51]\n\t" \
  "ds_read_b128 v[24:27], v73 offset:13312\n\t" \
  "global_load_lds_dwordx4 v72, s[46:47] offset:-4096 sc1\n\t" \
  "v_mfma_f32_16x16x32_bf16 v[52:55], a[84:87], v[0:3], v[52:55]\n\t" \
  "ds_read_b128 v[28:31], v73 offset:31744\n\t" \
  "global_load_lds_dwordx4 v72, s[46:47] offset:-3072 sc1\n\t" \
  "v_mfma_f32_16x16x32_bf16 v[56:59], a[80:83], v[4:7], v[56:59]\n\t" \
  "global_load_lds_dwordx4 v72, s[46:47] offset:-2048 sc1\n\t" \
  "global_load_lds_dwordx4 v72, s[46:47] offset:-1024 sc1\n\t" \
  "v_mfma_f32_16x16x32_bf16 v[60:63], a[84:87], v[4:7], v[60:63]\n\t" \
  "global_load_lds_dwordx4 v72, s[46:47] offset:0 sc1\n\t" \
  "global_load_lds_dwordx4 v72, s[46:47] offset:1024 sc1\n\t" \
  "s_waitcnt lgkmcnt(4)\n\t" \
  "v_mfma_f32_16x16x32_bf16 v[48:51], a[88:91], v[8:11], v[48:51]\n\t" \
  "ds_read_b128 v[0:3], v73 offset:14336\n\t" \
  "global_load_lds_dwordx4 v72, s[46:47] offset:2048 sc1\n\t" \
  "v_mfma_f32_16x16x32_bf16 v[52:55], a[92:95], v[8:11], v[52:55]\n\t" \
  "ds_read_b128 v[4:7], v73 offset:32768\n\t" \
  "global_load_lds_dwordx4 v72, s[46:47] offset:3072 sc1\n\t" \
  "v_mfma_f32_16x16x32_bf16 v[56:59], a[88:91], v[12:15], v[56:59]\n\t" \
  "s_add_u32 m0, s48, 4096\n\t" \
  "s_nop 0\n\t" \
  "v_mfma_f32_16x16x32_bf16 v[60:63], a[92:95], v[12:15], v[60:63]\n\t" \
  "global_load_lds_dwordx4 v78, s[46:47] sc1\n\t" \
  "s_add_u32 s46, s46, 73728\n\t" \
  "s_waitcnt lgkmcnt(4)\n\t" \
  "v_mfma_f32_16x16x32_bf16 v[48:51], a[96:99], v[16:19], v[48:51]\n\t" \
  "ds_read_b128 v[8:11], v73 offset:15360\n\t" \
  "s_addc_u32 s47, s47, 0\n\t" \
  "v_mfma_f32_16x16x32_bf16 v[52:55], a[100:103], v[16:19], v[52:55]\n\t" \
  "ds_read_b128 v[12:15], v73 offset:33792\n\t" \
  "v_mfma_f32_16x16x32_bf16 v[56:59], a[96:99], v[20:23], v[56:59]\n\t" \
  "v_mfma_f32_16x16x32_bf16 v[60:63], a[100:103], v[20:23], v[60:63]\n\t" \
  "s_waitcnt lgkmcnt(4)\n\t" \
  "v_mfma_f32_16x16x32_bf16 v[48:51], a[104:107], v[24:27], v[48:51]\n\t" \
  "ds_read_b128 v[16:19], v73 offset:16384\n\t" \
  "v_mfma_f32_16x16x32_bf16 v[52:55], a[108:111], v[24:27], v[52:55]\n\t" \
  "ds_read_b128 v[20:23], v73 offset:34816\n\t" \
  "v_mfma_f32_16x16x32_bf16 v[56:59], a[104:107], v[28:31], v[56:59]\n\t" \
  "v_mfma_f32_16x16x32_bf16 v[60:63], a[108:111], v[28:31], v[60:63]\n\t" \
  "s_waitcnt lgkmcnt(4)\n\t" \
  "v_mfma_f32_16x16x32_bf16 v[48:51], a[112:115], v[0:3], v[48:51]\n\t" \
  "ds_read_b128 v[24:27], v73 offset:17408\n\t" \
  "v_mfma_f32_16x16x32_bf16 v[52:55], a[116:119], v[0:3], v[52:55]\n\t" \
  "ds_read_b128 v[28:31], v73 offset:35840\n\t" \
  "v_mfma_f32_16x16x32_bf16 v[56:59], a[112:115], v[4:7], v[56:59]\n\t" \
  "v_mfma_f32_16x16x32_bf16 v[60:63], a[116:119], v[4:7], v[60:63]\n\t" \
  "s_waitcnt lgkmcnt(4)\n\t" \
  "v_mfma_f32_16x16x32_bf16 v[48:51], a[120:123], v[8:11], v[48:51]\n\t" \
  "ds_read_b128 v[0:3], v74 offset:0\n\t" \
  "v_mfma_f32_16x16x32_bf16 v[52:55], a[124:127], v[8:11], v[52:55]\n\t" \
  "ds_read_b128 v[4:7], v74 offset:18432\n\t" \
  "v_mfma_f32_16x16x32_bf16 v[56:59], a[120:123], v[12:15], v[56:59]\n\t" \
  "v_mfma_f32_16x16x32_bf16 v[60:63], a[124:127], v[12:15], v[60:63]\n\t" \
  "s_waitcnt lgkmcnt(4)\n\t" \
  "v_mfma_f32_16x16x32_bf16 v[48:51], a[128:131], v[16:19], v[48:51]\n\t" \
  "ds_read_b128 v[8:11], v74 offset:1024\n\t" \
  "v_mfma_f32_16x16x32_bf16 v[52:55], a[132:135], v[16:19], v[52:55]\n\t" \
  "ds_read_b128 v[12:15], v74 offset:19456\n\t" \
  "v_mfma_f32_16x16x32_bf16 v[56:59], a[128:131], v[20:23], v[56:59]\n\t" \
  "v_mfma_f32_16x16x32_bf16 v[60:63], a[132:135], v[20:23], v[60:63]\n\t" \
  "s_waitcnt lgkmcnt(4)\n\t" \
  "v_mfma_f32_16x16x32_bf16 v[48:51], a[136:139], v[24:27], v[48:51]\n\t" \
  "ds_read_b128 v[16:19], v74 offset:2048\n\t" \
  "v_mfma_f32_16x16x32_bf16 v[52:55], a[140:143], v[24:27], v[52:55]\n\t" \
  "ds_read_b128 v[20:23], v74 offset:20480\n\t" \
  "v_mfma_f32_16x16x32_bf16 v[56:59], a[136:139], v[28:31], v[56:59]\n\t" \
  "v_mfma_f32_16x16x32_bf16 v[60:63], a[140:143], v[28:31], v[60:63]\n\t" \
  "v_mov_b32 v73, v74\n\t" \
  "v_add_u32 v74, 110592, v72\n\t" \
  "s_waitcnt lgkmcnt(4)\n\t" \
  "v_mfma_f32_16x16x32_bf16 v[32:35], a[0:3], v[0:3], 0\n\t" \
  "ds_read_b128 v[24:27], v73 offset:3072\n\t" \
  "v_mfma_f32_16x16x32_bf16 v[36:39], a[4:7], v[0:3], 0\n\t" \
  "ds_read_b128 v[28:31], v73 offset:21504\n\t" \
  "v_mfma_f32_16x16x32_bf16 v[40:43], a[0:3], v[4:7], 0\n\t" \
  "v_mfma_f32_16x16x32_bf16 v[44:47], a[4:7], v[4:7], 0\n\t" \
  "s_waitcnt lgkmcnt(4)\n\t" \
  "v_mfma_f32_16x16x32_bf16 v[32:35], a[8:11], v[8:11], v[32:35]\n\t" \
  "ds_read_b128 v[0:3], v73 offset:4096\n\t" \
  "v_cvt_pk_bf16_f32 v64, v48, v49\n\t" \
  "v_mfma_f32_16x16x32_bf16 v[36:39], a[12:15], v[8:11], v[36:39]\n\t" \
  "ds_read_b128 v[4:7], v73 offset:22528\n\t" \
  "v_cvt_pk_bf16_f32 v65, v50, v51\n\t" \
  "v_mfma_f32_16x16x32_bf16 v[40:43], a[8:11], v[12:15], v[40:43]\n\t" \
  "v_cvt_pk_bf16_f32 v66, v52, v53\n\t" \
  "v_cvt_pk_bf16_f32 v67, v54, v55\n\t" \
  "v_mfma_f32_16x16x32_bf16 v[44:47], a[12:15], v[12:15], v[44:47]\n\t" \
  "buffer_store_dwordx4 v[64:67], v72, s[20:23], s50 offen offset:0 sc1\n\t" \
  "v_cvt_pk_bf16_f32 v68, v56, v57\n\t" \
  "s_waitcnt lgkmcnt(4)\n\t" \
  "v_mfma_f32_16x16x32_bf16 v[32:35], a[16:19], v[16:19], v[32:35]\n\t" \
  "ds_read_b128 v[8:11], v73 offset:5120\n\t" \
  "v_cvt_pk_bf16_f32 v69, v58, v59\n\t" \
  "v_mfma_f32_16x16x32_bf16 v[36:39], a[20:23], v[16:19], v[36:39]\n\t" \
  "ds_read_b128 v[12:15], v73 offset:23552\n\t" \
  "v_cvt_pk_bf16_f32 v70, v60, v61\n\t" \
  "v_mfma_f32_16x16x32_bf16 v[40:43], a[16:19], v[20:23], v[40:43]\n\t" \
  "v_cvt_pk_bf16_f32 v71, v62, v63\n\t" \
  "buffer_store_dwordx4 v[68:71], v78, s[20:23], s50 offen offset:0 sc1\n\t" \
  "v_mfma_f32_16x16x32_bf16 v[44:47], a[20:23], v[20:23], v[44:47]\n\t" \
  "s_add_u32 s50, s50, 524288\n\t" \
  "s_waitcnt lgkmcnt(4)\n\t" \
  "v_mfma_f32_16x16x32_bf16 v[32:35], a[24:27], v[24:27], v[32:35]\n\t" \
  "ds_read_b128 v[16:19], v73 offset:6144\n\t" \
  "v_mfma_f32_16x16x32_bf16 v[36:39], a[28:31], v[24:27], v[36:39]\n\t" \
  "ds_read_b128 v[20:23], v73 offset:24576\n\t" \
  "v_mfma_f32_16x16x32_bf16 v[40:43], a[24:27], v[28:31], v[40:43]\n\t" \
  "v_mfma_f32_16x16x32_bf16 v[44:47], a[28:31], v[28:31], v[44:47]\n\t" \
  "s_waitcnt lgkmcnt(4)\n\t" \
  "v_mfma_f32_16x16x32_bf16 v[32:35], a[32:35], v[0:3], v[32:35]\n\t" \
  "ds_read_b128 v[24:27], v73 offset:7168\n\t" \
  "v_mfma_f32_16x16x32_bf16 v[36:39], a[36:39], v[0:3], v[36:39]\n\t" \
  "ds_read_b128 v[28:31], v73 offset:25600\n\t" \
  "v_mfma_f32_16x16x32_bf16 v[40:43], a[32:35], v[4:7], v[40:43]\n\t" \
  "v_mfma_f32_16x16x32_bf16 v[44:47], a[36:39], v[4:7], v[44:47]\n\t" \
  "s_waitcnt lgkmcnt(4)\n\t" \
  "v_mfma_f32_16x16x32_bf16 v[32:35], a[40:43], v[8:11], v[32:35]\n\t" \
  "ds_read_b128 v[0:3], v73 offset:8192\n\t" \
  "v_mfma_f32_16x16x32_bf16 v[36:39], a[44:47], v[8:11], v[36:39]\n\t" \
  "ds_read_b128 v[4:7], v73 offset:26624\n\t" \
  "v_mfma_f32_16x16x32_bf16 v[40:43], a[40:43], v[12:15], v[40:43]\n\t" \
  "v_mfma_f32_16x16x32_bf16 v[44:47], a[44:47], v[12:15], v[44:47]\n\t" \
  "s_waitcnt lgkmcnt(4)\n\t" \
  "v_mfma_f32_16x16x32_bf16 v[32:35], a[48:51], v[16:19], v[32:35]\n\t" \
  "ds_read_b128 v[8:11], v73 offset:9216\n\t" \
  "v_mfma_f32_16x16x32_bf16 v[36:39], a[52:55], v[16:19], v[36:39]\n\t" \
  "ds_read_b128 v[12:15], v73 offset:27648\n\t" \
  "v_mfma_f32_16x16x32_bf16 v[40:43], a[48:51], v[20:23], v[40:43]\n\t" \
  "v_mfma_f32_16x16x32_bf16 v[44:47], a[52:55], v[20:23], v[44:47]\n\t" \
  "s_waitcnt lgkmcnt(4)\n\t" \
  "v_mfma_f32_16x16x32_bf16 v[32:35], a[56:59], v[24:27], v[32:35]\n\t" \
  "ds_read_b128 v[16:19], v73 offset:10240\n\t" \
  "v_mfma_f32_16x16x32_bf16 v[36:39], a[60:63], v[24:27], v[36:39]\n\t" \
  "ds_read_b128 v[20:23], v73 offset:28672\n\t" \
  "v_mfma_f32_16x16x32_bf16 v[40:43], a[56:59], v[28:31], v[40:43]\n\t" \
  "v_mfma_f32_16x16x32_bf16 v[44:47], a[60:63], v[28:31], v[44:47]\n\t" \
  "s_waitcnt lgkmcnt(4)\n\t" \
  "v_mfma_f32_16x16x32_bf16 v[32:35], a[64:67], v[0:3], v[32:35]\n\t" \
  "ds_read_b128 v[24:27], v73 offset:11264\n\t" \
  "v_mfma_f32_16x16x32_bf16 v[36:39], a[68:71], v[0:3], v[36:39]\n\t" \
  "ds_read_b128 v[28:31], v73 offset:29696\n\t" \
  "v_mfma_f32_16x16x32_bf16 v[40:43], a[64:67], v[4:7], v[40:43]\n\t" \
  "v_mfma_f32_16x16x32_bf16 v[44:47], a[68:71], v[4:7], v[44:47]\n\t" \
  "s_waitcnt vmcnt(11)\n\t" \
  "s_barrier\n\t" \
  "s_waitcnt lgkmcnt(4)\n\t" \
  "v_mfma_f32_16x16x32_bf16 v[32:35], a[72:75], v[8:11], v[32:35]\n\t" \
  "ds_read_b128 v[0:3], v73 offset:12288\n\t" \
  "v_mov_b32 v77, 1\n\t" \
  "v_mfma_f32_16x16x32_bf16 v[36:39], a[76:79], v[8:11], v[36:39]\n\t" \
  "ds_read_b128 v[4:7], v73 offset:30720\n\t" \
  "s_mov_b64 exec, s[54:55]\n\t" \
  "buffer_store_dword v77, off, s[20:23], s51 sc1\n\t" \
  "s_mov_b64 exec, -1\n\t" \
  "v_mfma_f32_16x16x32_bf16 v[40:43], a[72:75], v[12:15], v[40:43]\n\t" \
  "s_add_u32 s51, s51, 1024\n\t" \
  "s_add_u32 s48, s58, 36864\n\t" \
  "v_mfma_f32_16x16x32_bf16 v[44:47], a[76:79], v[12:15], v[44:47]\n\t" \
  "s_mov_b32 m0, s48\n\t" \
  "s_nop 0\n\t" \
  "s_waitcnt lgkmcnt(4)\n\t" \
  "v_mfma_f32_16x16x32_bf16 v[32:35], a[80:83], v[16:19], v[32:35]\n\t" \
  "ds_read_b128 v[8:11], v73 offset:13312\n\t" \
  "global_load_lds_dwordx4 v72, s[46:47] offset:-4096 sc1\n\t" \
  "v_mfma_f32_16x16x32_bf16 v[36:39], a[84:87], v[16:19], v[36:39]\n\t" \
  "ds_read_b128 v[12:15], v73 offset:31744\n\t" \
  "global_load_lds_dwordx4 v72, s[46:47] offset:-3072 sc1\n\t" \
  "v_mfma_f32_16x16x32_bf16 v[40:43], a[80:83], v[20:23], v[40:43]\n\t" \
  "global_load_lds_dwordx4 v72, s[46:47] offset:-2048 sc1\n\t" \
  "global_load_lds_dwordx4 v72, s[46:47] offset:-1024 sc1\n\t" \
  "v_mfma_f32_16x16x32_bf16 v[44:47], a[84:87], v[20:23], v[44:47]\n\t" \
  "global_load_lds_dwordx4 v72, s[46:47] offset:0 sc1\n\t" \
  "global_load_lds_dwordx4 v72, s[46:47] offset:1024 sc1\n\t" \
  "s_waitcnt lgkmcnt(4)\n\t" \
  "v_mfma_f32_16x16x32_bf16 v[32:35], a[88:91], v[24:27], v[32:35]\n\t" \
  "ds_read_b128 v[16:19], v73 offset:14336\n\t" \
  "global_load_lds_dwordx4 v72, s[46:47] offset:2048 sc1\n\t" \
  "v_mfma_f32_16x16x32_bf16 v[36:39], a[92:95], v[24:27], v[36:39]\n\t" \
  "ds_read_b128 v[20:23], v73 offset:32768\n\t" \
  "global_load_lds_dwordx4 v72, s[46:47] offset:3072 sc1\n\t" \
  "v_mfma_f32_16x16x32_bf16 v[40:43], a[88:91], v[28:31], v[40:43]\n\t" \
  "s_add_u32 m0, s48, 4096\n\t" \
  "s_nop 0\n\t" \
  "v_mfma_f32_16x16x32_bf16 v[44:47], a[92:95], v[28:31], v[44:47]\n\t" \
  "global_load_lds_dwordx4 v78, s[46:47] sc1\n\t" \
  "s_add_u32 s46, s46, 73728\n\t" \
  "s_waitcnt lgkmcnt(4)\n\t" \
  "v_mfma_f32_16x16x32_bf16 v[32:35], a[96:99], v[0:3], v[32:35]\n\t" \
  "ds_read_b128 v[24:27], v73 offset:15360\n\t" \
  "s_addc_u32 s47, s47, 0\n\t" \
  "v_mfma_f32_16x16x32_bf16 v[36:39], a[100:103], v[0:3], v[36:39]\n\t" \
  "ds_read_b128 v[28:31], v73 offset:33792\n\t" \
  "v_mfma_f32_16x16x32_bf16 v[40:43], a[96:99], v[4:7], v[40:43]\n\t" \
  "v_mfma_f32_16x16x32_bf16 v[44:47], a[100:103], v[4:7], v[44:47]\n\t" \
  "s_waitcnt lgkmcnt(4)\n\t" \
  "v_mfma_f32_16x16x32_bf16 v[32:35], a[104:107], v[8:11], v[32:35]\n\t" \
  "ds_read_b128 v[0:3], v73 offset:16384\n\t" \
  "v_mfma_f32_16x16x32_bf16 v[36:39], a[108:111], v[8:11], v[36:39]\n\t" \
  "ds_read_b128 v[4:7], v73 offset:34816\n\t" \
  "v_mfma_f32_16x16x32_bf16 v[40:43], a[104:107], v[12:15], v[40:43]\n\t" \
  "v_mfma_f32_16x16x32_bf16 v[44:47], a[108:111], v[12:15], v[44:47]\n\t" \
  "s_waitcnt lgkmcnt(4)\n\t" \
  "v_mfma_f32_16x16x32_bf16 v[32:35], a[112:115], v[16:19], v[32:35]\n\t" \
  "ds_read_b128 v[8:11], v73 offset:17408\n\t" \
  "v_mfma_f32_16x16x32_bf16 v[36:39], a[116:119], v[16:19], v[36:39]\n\t" \
  "ds_read_b128 v[12:15], v73 offset:35840\n\t" \
  "v_mfma_f32_16x16x32_bf16 v[40:43], a[112:115], v[20:23], v[40:43]\n\t" \
  "v_mfma_f32_16x16x32_bf16 v[44:47], a[116:119], v[20:23], v[44:47]\n\t" \
  "s_waitcnt lgkmcnt(4)\n\t" \
  "v_mfma_f32_16x16x32_bf16 v[32:35], a[120:123], v[24:27], v[32:35]\n\t" \
  "ds_read_b128 v[16:19], v74 offset:0\n\t" \
  "v_mfma_f32_16x16x32_bf16 v[36:39], a[124:127], v[24:27], v[36:39]\n\t" \
  "ds_read_b128 v[20:23], v74 offset:18432\n\t" \
  "v_mfma_f32_16x16x32_bf16 v[40:43], a[120:123], v[28:31], v[40:43]\n\t" \
  "v_mfma_f32_16x16x32_bf16 v[44:47], a[124:127], v[28:31], v[44:47]\n\t" \
  "s_waitcnt lgkmcnt(4)\n\t" \
  "v_mfma_f32_16x16x32_bf16 v[32:35], a[128:131], v[0:3], v[32:35]\n\t" \
  "ds_read_b128 v[24:27], v74 offset:1024\n\t" \
  "v_mfma_f32_16x16x32_bf16 v[36:39], a[132:135], v[0:3], v[36:39]\n\t" \
  "ds_read_b128 v[28:31], v74 offset:19456\n\t" \
  "v_mfma_f32_16x16x32_bf16 v[40:43], a[128:131], v[4:7], v[40:43]\n\t" \
  "v_mfma_f32_16x16x32_bf16 v[44:47], a[132:135], v[4:7], v[44:47]\n\t" \
  "s_waitcnt lgkmcnt(4)\n\t" \
  "v_mfma_f32_16x16x32_bf16 v[32:35], a[136:139], v[8:11], v[32:35]\n\t" \
  "ds_read_b128 v[0:3], v74 offset:2048\n\t" \
  "v_mfma_f32_16x16x32_bf16 v[36:39], a[140:143], v[8:11], v[36:39]\n\t" \
  "ds_read_b128 v[4:7], v74 offset:20480\n\t" \
  "v_mfma_f32_16x16x32_bf16 v[40:43], a[136:139], v[12:15], v[40:43]\n\t" \
  "v_mfma_f32_16x16x32_bf16 v[44:47], a[140:143], v[12:15], v[44:47]\n\t" \
  "v_mov_b32 v73, v74\n\t" \
  "v_add_u32 v74, 0, v72\n\t" \
  "s_waitcnt lgkmcnt(4)\n\t" \
  "v_mfma_f32_16x16x32_bf16 v[48:51], a[0:3], v[16:19], 0\n\t" \
  "ds_read_b128 v[8:11], v73 offset:3072\n\t" \
  "v_mfma_f32_16x16x32_bf16 v[52:55], a[4:7], v[16:19], 0\n\t" \
  "ds_read_b128 v[12:15], v73 offset:21504\n\t" \
  "v_mfma_f32_16x16x32_bf16 v[56:59], a[0:3], v[20:23], 0\n\t" \
  "v_mfma_f32_16x16x32_bf16 v[60:63], a[4:7], v[20:23], 0\n\t" \
  "s_waitcnt lgkmcnt(4)\n\t" \
  "v_mfma_f32_16x16x32_bf16 v[48:51], a[8:11], v[24:27], v[48:51]\n\t" \
  "ds_read_b128 v[16:19], v73 offset:4096\n\t" \
  "v_cvt_pk_bf16_f32 v64, v32, v33\n\t" \
  "v_mfma_f32_16x16x32_bf16 v[52:55], a[12:15], v[24:27], v[52:55]\n\t" \
  "ds_read_b128 v[20:23], v73 offset:22528\n\t" \
  "v_cvt_pk_bf16_f32 v65, v34, v35\n\t" \
  "v_mfma_f32_16x16x32_bf16 v[56:59], a[8:11], v[28:31], v[56:59]\n\t" \
  "v_cvt_pk_bf16_f32 v66, v36, v37\n\t" \
  "v_cvt_pk_bf16_f32 v67, v38, v39\n\t" \
  "v_mfma_f32_16x16x32_bf16 v[60:63], a[12:15], v[28:31], v[60:63]\n\t" \
  "buffer_store_dwordx4 v[64:67], v72, s[20:23], s50 offen offset:0 sc1\n\t" \
  "v_cvt_pk_bf16_f32 v68, v40, v41\n\t" \
  "s_waitcnt lgkmcnt(4)\n\t" \
  "v_mfma_f32_16x16x32_bf16 v[48:51], a[16:19], v[0:3], v[48:51]\n\t" \
  "ds_read_b128 v[24:27], v73 offset:5120\n\t" \
  "v_cvt_pk_bf16_f32 v69, v42, v43\n\t" \
  "v_mfma_f32_16x16x32_bf16 v[52:55], a[20:23], v[0:3], v[52:55]\n\t" \
  "ds_read_b128 v[28:31], v73 offset:23552\n\t" \
  "v_cvt_pk_bf16_f32 v70, v44, v45\n\t" \
  "v_mfma_f32_16x16x32_bf16 v[56:59], a[16:19], v[4:7], v[56:59]\n\t" \
  "v_cvt_pk_bf16_f32 v71, v46, v47\n\t" \
  "buffer_store_dwordx4 v[68:71], v78, s[20:23], s50 offen offset:0 sc1\n\t" \
  "v_mfma_f32_16x16x32_bf16 v[60:63], a[20:23], v[4:7], v[60:63]\n\t" \
  "s_add_u32 s50, s50, 524288\n\t" \
  "s_waitcnt lgkmcnt(4)\n\t" \
  "v_mfma_f32_16x16x32_bf16 v[48:51], a[24:27], v[8:11], v[48:51]\n\t" \
  "ds_read_b128 v[0:3], v73 offset:6144\n\t" \
  "v_mfma_f32_16x16x32_bf16 v[52:55], a[28:31], v[8:11], v[52:55]\n\t" \
  "ds_read_b128 v[4:7], v73 offset:24576\n\t" \
  "v_mfma_f32_16x16x32_bf16 v[56:59], a[24:27], v[12:15], v[56:59]\n\t" \
  "v_mfma_f32_16x16x32_bf16 v[60:63], a[28:31], v[12:15], v[60:63]\n\t" \
  "s_waitcnt lgkmcnt(4)\n\t" \
  "v_mfma_f32_16x16x32_bf16 v[48:51], a[32:35], v[16:19], v[48:51]\n\t" \
  "ds_read_b128 v[8:11], v73 offset:7168\n\t" \
  "v_mfma_f32_16x16x32_bf16 v[52:55], a[36:39], v[16:19], v[52:55]\n\t" \
  "ds_read_b128 v[12:15], v73 offset:25600\n\t" \
  "v_mfma_f32_16x16x32_bf16 v[56:59], a[32:35], v[20:23], v[56:59]\n\t" \
  "v_mfma_f32_16x16x32_bf16 v[60:63], a[36:39], v[20:23], v[60:63]\n\t" \
  "s_waitcnt lgkmcnt(4)\n\t" \
  "v_mfma_f32_16x16x32_bf16 v[48:51], a[40:43], v[24:27], v[48:51]\n\t" \
  "ds_read_b128 v[16:19], v73 offset:8192\n\t" \
  "v_mfma_f32_16x16x32_bf16 v[52:55], a[44:47], v[24:27], v[52:55]\n\t" \
  "ds_read_b128 v[20:23], v73 offset:26624\n\t" \
  "v_mfma_f32_16x16x32_bf16 v[56:59], a[40:43], v[28:31], v[56:59]\n\t" \
  "v_mfma_f32_16x16x32_bf16 v[60:63], a[44:47], v[28:31], v[60:63]\n\t" \
  "s_waitcnt lgkmcnt(4)\n\t" \
  "v_mfma_f32_16x16x32_bf16 v[48:51], a[48:51], v[0:3], v[48:51]\n\t" \
  "ds_read_b128 v[24:27], v73 offset:9216\n\t" \
  "v_mfma_f32_16x16x32_bf16 v[52:55], a[52:55], v[0:3], v[52:55]\n\t" \
  "ds_read_b128 v[28:31], v73 offset:27648\n\t" \
  "v_mfma_f32_16x16x32_bf16 v[56:59], a[48:51], v[4:7], v[56:59]\n\t" \
  "v_mfma_f32_16x16x32_bf16 v[60:63], a[52:55], v[4:7], v[60:63]\n\t" \
  "s_waitcnt lgkmcnt(4)\n\t" \
  "v_mfma_f32_16x16x32_bf16 v[48:51], a[56:59], v[8:11], v[48:51]\n\t" \
  "ds_read_b128 v[0:3], v73 offset:10240\n\t" \
  "v_mfma_f32_16x16x32_bf16 v[52:55], a[60:63], v[8:11], v[52:55]\n\t" \
  "ds_read_b128 v[4:7], v73 offset:28672\n\t" \
  "v_mfma_f32_16x16x32_bf16 v[56:59], a[56:59], v[12:15], v[56:59]\n\t" \
  "v_mfma_f32_16x16x32_bf16 v[60:63], a[60:63], v[12:15], v[60:63]\n\t" \
  "s_waitcnt lgkmcnt(4)\n\t" \
  "v_mfma_f32_16x16x32_bf16 v[48:51], a[64:67], v[16:19], v[48:51]\n\t" \
  "ds_read_b128 v[8:11], v73 offset:11264\n\t" \
  "v_mfma_f32_16x16x32_bf16 v[52:55], a[68:71], v[16:19], v[52:55]\n\t" \
  "ds_read_b128 v[12:15], v73 offset:29696\n\t" \
  "v_mfma_f32_16x16x32_bf16 v[56:59], a[64:67], v[20:23], v[56:59]\n\t" \
  "v_mfma_f32_16x16x32_bf16 v[60:63], a[68:71], v[20:23], v[60:63]\n\t" \
  "s_waitcnt vmcnt(11)\n\t" \
  "s_barrier\n\t" \
  "s_waitcnt lgkmcnt(4)\n\t" \
  "v_mfma_f32_16x16x32_bf16 v[48:51], a[72:75], v[24:27], v[48:51]\n\t" \
  "ds_read_b128 v[16:19], v73 offset:12288\n\t" \
  "v_mov_b32 v77, 1\n\t" \
  "v_mfma_f32_16x16x32_bf16 v[52:55], a[76:79], v[24:27], v[52:55]\n\t" \
  "ds_read_b128 v[20:23], v73 offset:30720\n\t" \
  "s_mov_b64 exec, s[54:55]\n\t" \
  "buffer_store_dword v77, off, s[20:23], s51 sc1\n\t" \
  "s_mov_b64 exec, -1\n\t" \
  "v_mfma_f32_16x16x32_bf16 v[56:59], a[72:75], v[28:31], v[56:59]\n\t" \
  "s_add_u32 s51, s51, 1024\n\t" \
  "s_add_u32 s48, s58, 73728\n\t" \
  "v_mfma_f32_16x16x32_bf16 v[60:63], a[76:79], v[28:31], v[60:63]\n\t" \
  "s_mov_b32 m0, s48\n\t" \
  "s_nop 0\n\t" \
  "s_waitcnt lgkmcnt(4)\n\t" \
  "v_mfma_f32_16x16x32_bf16 v[48:51], a[80:83], v[0:3], v[48:51]\n\t" \
  "ds_read_b128 v[24:27], v73 offset:13312\n\t" \
  "global_load_lds_dwordx4 v72, s[46:47] offset:-4096 sc1\n\t" \
  "v_mfma_f32_16x16x32_bf16 v[52:55], a[84:87], v[0:3], v[52:55]\n\t" \
  "ds_read_b128 v[28:31], v73 offset:31744\n\t" \
  "global_load_lds_dwordx4 v72, s[46:47] offset:-3072 sc1\n\t" \
  "v_mfma_f32_16x16x32_bf16 v[56:59], a[80:83], v[4:7], v[56:59]\n\t" \
  "global_load_lds_dwordx4 v72, s[46:47] offset:-2048 sc1\n\t" \
  "global_load_lds_dwordx4 v72, s[46:47] offset:-1024 sc1\n\t" \
  "v_mfma_f32_16x16x32_bf16 v[60:63], a[84:87], v[4:7], v[60:63]\n\t" \
  "global_load_lds_dwordx4 v72, s[46:47] offset:0 sc1\n\t" \
  "global_load_lds_dwordx4 v72, s[46:47] offset:1024 sc1\n\t" \
  "s_waitcnt lgkmcnt(4)\n\t" \
  "v_mfma_f32_16x16x32_bf16 v[48:51], a[88:91], v[8:11], v[48:51]\n\t" \
  "ds_read_b128 v[0:3], v73 offset:14336\n\t" \
  "global_load_lds_dwordx4 v72, s[46:47] offset:2048 sc1\n\t" \
  "v_mfma_f32_16x16x32_bf16 v[52:55], a[92:95], v[8:11], v[52:55]\n\t" \
  "ds_read_b128 v[4:7], v73 offset:32768\n\t" \
  "global_load_lds_dwordx4 v72, s[46:47] offset:3072 sc1\n\t" \
  "v_mfma_f32_16x16x32_bf16 v[56:59], a[88:91], v[12:15], v[56:59]\n\t" \
  "s_add_u32 m0, s48, 4096\n\t" \
  "s_nop 0\n\t" \
  "v_mfma_f32_16x16x32_bf16 v[60:63], a[92:95], v[12:15], v[60:63]\n\t" \
  "global_load_lds_dwordx4 v78, s[46:47] sc1\n\t" \
  "s_add_u32 s46, s46, 73728\n\t" \
  "s_waitcnt lgkmcnt(4)\n\t" \
  "v_mfma_f32_16x16x32_bf16 v[48:51], a[96:99], v[16:19], v[48:51]\n\t" \
  "ds_read_b128 v[8:11], v73 offset:15360\n\t" \
  "s_addc_u32 s47, s47, 0\n\t" \
  "v_mfma_f32_16x16x32_bf16 v[52:55], a[100:103], v[16:19], v[52:55]\n\t" \
  "ds_read_b128 v[12:15], v73 offset:33792\n\t" \
  "v_mfma_f32_16x16x32_bf16 v[56:59], a[96:99], v[20:23], v[56:59]\n\t" \
  "v_mfma_f32_16x16x32_bf16 v[60:63], a[100:103], v[20:23], v[60:63]\n\t" \
  "s_waitcnt lgkmcnt(4)\n\t" \
  "v_mfma_f32_16x16x32_bf16 v[48:51], a[104:107], v[24:27], v[48:51]\n\t" \
  "ds_read_b128 v[16:19], v73 offset:16384\n\t" \
  "v_mfma_f32_16x16x32_bf16 v[52:55], a[108:111], v[24:27], v[52:55]\n\t" \
  "ds_read_b128 v[20:23], v73 offset:34816\n\t" \
  "v_mfma_f32_16x16x32_bf16 v[56:59], a[104:107], v[28:31], v[56:59]\n\t" \
  "v_mfma_f32_16x16x32_bf16 v[60:63], a[108:111], v[28:31], v[60:63]\n\t" \
  "s_waitcnt lgkmcnt(4)\n\t" \
  "v_mfma_f32_16x16x32_bf16 v[48:51], a[112:115], v[0:3], v[48:51]\n\t" \
  "ds_read_b128 v[24:27], v73 offset:17408\n\t" \
  "v_mfma_f32_16x16x32_bf16 v[52:55], a[116:119], v[0:3], v[52:55]\n\t" \
  "ds_read_b128 v[28:31], v73 offset:35840\n\t" \
  "v_mfma_f32_16x16x32_bf16 v[56:59], a[112:115], v[4:7], v[56:59]\n\t" \
  "v_mfma_f32_16x16x32_bf16 v[60:63], a[116:119], v[4:7], v[60:63]\n\t" \
  "s_waitcnt lgkmcnt(4)\n\t" \
  "v_mfma_f32_16x16x32_bf16 v[48:51], a[120:123], v[8:11], v[48:51]\n\t" \
  "ds_read_b128 v[0:3], v74 offset:0\n\t" \
  "v_mfma_f32_16x16x32_bf16 v[52:55], a[124:127], v[8:11], v[52:55]\n\t" \
  "ds_read_b128 v[4:7], v74 offset:18432\n\t" \
  "v_mfma_f32_16x16x32_bf16 v[56:59], a[120:123], v[12:15], v[56:59]\n\t" \
  "v_mfma_f32_16x16x32_bf16 v[60:63], a[124:127], v[12:15], v[60:63]\n\t" \
  "s_waitcnt lgkmcnt(4)\n\t" \
  "v_mfma_f32_16x16x32_bf16 v[48:51], a[128:131], v[16:19], v[48:51]\n\t" \
  "ds_read_b128 v[8:11], v74 offset:1024\n\t" \
  "v_mfma_f32_16x16x32_bf16 v[52:55], a[132:135], v[16:19], v[52:55]\n\t" \
  "ds_read_b128 v[12:15], v74 offset:19456\n\t" \
  "v_mfma_f32_16x16x32_bf16 v[56:59], a[128:131], v[20:23], v[56:59]\n\t" \
  "v_mfma_f32_16x16x32_bf16 v[60:63], a[132:135], v[20:23], v[60:63]\n\t" \
  "s_waitcnt lgkmcnt(4)\n\t" \
  "v_mfma_f32_16x16x32_bf16 v[48:51], a[136:139], v[24:27], v[48:51]\n\t" \
  "ds_read_b128 v[16:19], v74 offset:2048\n\t" \
  "v_mfma_f32_16x16x32_bf16 v[52:55], a[140:143], v[24:27], v[52:55]\n\t" \
  "ds_read_b128 v[20:23], v74 offset:20480\n\t" \
  "v_mfma_f32_16x16x32_bf16 v[56:59], a[136:139], v[28:31], v[56:59]\n\t" \
  "v_mfma_f32_16x16x32_bf16 v[60:63], a[140:143], v[28:31], v[60:63]\n\t" \
  "v_mov_b32 v73, v74\n\t" \
  "s_sub_u32 s45, s45, 1\n\t" \
  "s_cmp_lg_u32 s45, 0\n\t" \
  "s_cbranch_scc1 Lq_%=\n\t" \
  "s_cmp_eq_u32 s28, 0\n\t" \
  "s_cbranch_scc1 Lx3_%=\n\t" \
  "v_mov_b32 v75, 2159100\n\t" \
  "buffer_load_dword v84, v75, s[20:23], 0 offen sc1\n\t" \
  "s_mov_b32 s42, 0\n\t" \
  "s_waitcnt vmcnt(0)\n\t" \
  "Lxl0_%=:\n\t" \
  "v_cmp_eq_u32 vcc, 0, v84\n\t" \
  "s_nop 0\n\t" \
  "s_cbranch_vccz Lxl1_%=\n\t" \
  "s_add_u32 s42, s42, 1\n\t" \
  "s_cmp_gt_u32 s42, 524288\n\t" \
  "s_cbranch_scc1 Ltmo_%=\n\t" \
  "s_sleep 2\n\t" \
  "buffer_load_dword v84, v75, s[20:23], 0 offen sc1\n\t" \
  "s_waitcnt vmcnt(0)\n\t" \
  "s_branch Lxl0_%=\n\t" \
  "Lxl1_%=:\n\t" \
  "Lx3_%=:\n\t" \
  "v_add_u32 v74, 36864, v72\n\t" \
  "s_waitcnt lgkmcnt(4)\n\t" \
  "v_mfma_f32_16x16x32_bf16 v[32:35], a[0:3], v[0:3], 0\n\t" \
  "ds_read_b128 v[24:27], v73 offset:3072\n\t" \
  "v_mfma_f32_16x16x32_bf16 v[36:39], a[4:7], v[0:3], 0\n\t" \
  "ds_read_b128 v[28:31], v73 offset:21504\n\t" \
  "v_mfma_f32_16x16x32_bf16 v[40:43], a[0:3], v[4:7], 0\n\t" \
  "v_mfma_f32_16x16x32_bf16 v[44:47], a[4:7], v[4:7], 0\n\t" \
  "s_waitcnt lgkmcnt(4)\n\t" \
  "v_mfma_f32_16x16x32_bf16 v[32:35], a[8:11], v[8:11], v[32:35]\n\t" \
  "ds_read_b128 v[0:3], v73 offset:4096\n\t" \
  "v_cvt_pk_bf16_f32 v64, v48, v49\n\t" \
  "v_mfma_f32_16x16x32_bf16 v[36:39], a[12:15], v[8:11], v[36:39]\n\t" \
  "ds_read_b128 v[4:7], v73 offset:22528\n\t" \
  "v_cvt_pk_bf16_f32 v65, v50, v51\n\t" \
  "v_mfma_f32_16x16x32_bf16 v[40:43], a[8:11], v[12:15], v[40:43]\n\t" \
  "v_cvt_pk_bf16_f32 v66, v52, v53\n\t" \
  "v_cvt_pk_bf16_f32 v67, v54, v55\n\t" \
  "v_mfma_f32_16x16x32_bf16 v[44:47], a[12:15], v[12:15], v[44:47]\n\t" \
  "buffer_store_dwordx4 v[64:67], v72, s[20:23], s50 offen offset:0 sc1\n\t" \
  "v_cvt_pk_bf16_f32 v68, v56, v57\n\t" \
  "s_waitcnt lgkmcnt(4)\n\t" \
  "v_mfma_f32_16x16x32_bf16 v[32:35], a[16:19], v[16:19], v[32:35]\n\t" \
  "ds_read_b128 v[8:11], v73 offset:5120\n\t" \
  "v_cvt_pk_bf16_f32 v69, v58, v59\n\t" \
  "v_mfma_f32_16x16x32_bf16 v[36:39], a[20:23], v[16:19], v[36:39]\n\t" \
  "ds_read_b128 v[12:15], v73 offset:23552\n\t" \
  "v_cvt_pk_bf16_f32 v70, v60, v61\n\t" \
  "v_mfma_f32_16x16x32_bf16 v[40:43], a[16:19], v[20:23], v[40:43]\n\t" \
  "v_cvt_pk_bf16_f32 v71, v62, v63\n\t" \
  "buffer_store_dwordx4 v[68:71], v78, s[20:23], s50 offen offset:0 sc1\n\t" \
  "v_mfma_f32_16x16x32_bf16 v[44:47], a[20:23], v[20:23], v[44:47]\n\t" \
  "s_add_u32 s50, s50, 524288\n\t" \
  "s_waitcnt lgkmcnt(4)\n\t" \
  "v_mfma_f32_16x16x32_bf16 v[32:35], a[24:27], v[24:27], v[32:35]\n\t" \
  "ds_read_b128 v[16:19], v73 offset:6144\n\t" \
  "v_mfma_f32_16x16x32_bf16 v[36:39], a[28:31], v[24:27], v[36:39]\n\t" \
  "ds_read_b128 v[20:23], v73 offset:24576\n\t" \
  "v_mfma_f32_16x16x32_bf16 v[40:43], a[24:27], v[28:31], v[40:43]\n\t" \
  "v_mfma_f32_16x16x32_bf16 v[44:47], a[28:31], v[28:31], v[44:47]\n\t" \
  "s_waitcnt lgkmcnt(4)\n\t" \
  "v_mfma_f32_16x16x32_bf16 v[32:35], a[32:35], v[0:3], v[32:35]\n\t" \
  "ds_read_b128 v[24:27], v73 offset:7168\n\t" \
  "v_mfma_f32_16x16x32_bf16 v[36:39], a[36:39], v[0:3], v[36:39]\n\t" \
  "ds_read_b128 v[28:31], v73 offset:25600\n\t" \
  "v_mfma_f32_16x16x32_bf16 v[40:43], a[32:35], v[4:7], v[40:43]\n\t" \
  "v_mfma_f32_16x16x32_bf16 v[44:47], a[36:39], v[4:7], v[44:47]\n\t" \
  "s_waitcnt lgkmcnt(4)\n\t" \
  "v_mfma_f32_16x16x32_bf16 v[32:35], a[40:43], v[8:11], v[32:35]\n\t" \
  "ds_read_b128 v[0:3], v73 offset:8192\n\t" \
  "v_mfma_f32_16x16x32_bf16 v[36:39], a[44:47], v[8:11], v[36:39]\n\t" \
  "ds_read_b128 v[4:7], v73 offset:26624\n\t" \
  "v_mfma_f32_16x16x32_bf16 v[40:43], a[40:43], v[12:15], v[40:43]\n\t" \
  "v_mfma_f32_16x16x32_bf16 v[44:47], a[44:47], v[12:15], v[44:47]\n\t" \
  "s_waitcnt lgkmcnt(4)\n\t" \
  "v_mfma_f32_16x16x32_bf16 v[32:35], a[48:51], v[16:19], v[32:35]\n\t" \
  "ds_read_b128 v[8:11], v73 offset:9216\n\t" \
  "v_mfma_f32_16x16x32_bf16 v[36:39], a[52:55], v[16:19], v[36:39]\n\t" \
  "ds_read_b128 v[12:15], v73 offset:27648\n\t" \
  "v_mfma_f32_16x16x32_bf16 v[40:43], a[48:51], v[20:23], v[40:43]\n\t" \
  "v_mfma_f32_16x16x32_bf16 v[44:47], a[52:55], v[20:23], v[44:47]\n\t" \
  "s_waitcnt lgkmcnt(4)\n\t" \
  "v_mfma_f32_16x16x32_bf16 v[32:35], a[56:59], v[24:27], v[32:35]\n\t" \
  "ds_read_b128 v[16:19], v73 offset:10240\n\t" \
  "v_mfma_f32_16x16x32_bf16 v[36:39], a[60:63], v[24:27], v[36:39]\n\t" \
  "ds_read_b128 v[20:23], v73 offset:28672\n\t" \
  "v_mfma_f32_16x16x32_bf16 v[40:43], a[56:59], v[28:31], v[40:43]\n\t" \
  "v_mfma_f32_16x16x32_bf16 v[44:47], a[60:63], v[28:31], v[44:47]\n\t" \
  "s_waitcnt lgkmcnt(4)\n\t" \
  "v_mfma_f32_16x16x32_bf16 v[32:35], a[64:67], v[0:3], v[32:35]\n\t" \
  "ds_read_b128 v[24:27], v73 offset:11264\n\t" \
  "v_mfma_f32_16x16x32_bf16 v[36:39], a[68:71], v[0:3], v[36:39]\n\t" \
  "ds_read_b128 v[28:31], v73 offset:29696\n\t" \
  "v_mfma_f32_16x16x32_bf16 v[40:43], a[64:67], v[4:7], v[40:43]\n\t" \
  "v_mfma_f32_16x16x32_bf16 v[44:47], a[68:71], v[4:7], v[44:47]\n\t" \
  "s_waitcnt vmcnt(11)\n\t" \
  "s_barrier\n\t" \
  "s_waitcnt lgkmcnt(4)\n\t" \
  "v_mfma_f32_16x16x32_bf16 v[32:35], a[72:75], v[8:11], v[32:35]\n\t" \
  "ds_read_b128 v[0:3], v73 offset:12288\n\t" \
  "v_mov_b32 v77, 1\n\t" \
  "v_mfma_f32_16x16x32_bf16 v[36:39], a[76:79], v[8:11], v[36:39]\n\t" \
  "ds_read_b128 v[4:7], v73 offset:30720\n\t" \
  "s_mov_b64 exec, s[54:55]\n\t" \
  "buffer_store_dword v77, off, s[20:23], s51 sc1\n\t" \
  "s_mov_b64 exec, -1\n\t" \
  "v_mfma_f32_16x16x32_bf16 v[40:43], a[72:75], v[12:15], v[40:43]\n\t" \
  "s_add_u32 s51, s51, 1024\n\t" \
  "s_add_u32 s48, s58, 110592\n\t" \
  "v_mfma_f32_16x16x32_bf16 v[44:47], a[76:79], v[12:15], v[44:47]\n\t" \
  "s_mov_b32 m0, s48\n\t" \
  "s_nop 0\n\t" \
  "s_waitcnt lgkmcnt(4)\n\t" \
  "v_mfma_f32_16x16x32_bf16 v[32:35], a[80:83], v[16:19], v[32:35]\n\t" \
  "ds_read_b128 v[8:11], v73 offset:13312\n\t" \
  "global_load_lds_dwordx4 v72, s[46:47] offset:-4096 sc1\n\t" \
  "v_mfma_f32_16x16x32_bf16 v[36:39], a[84:87], v[16:19], v[36:39]\n\t" \
  "ds_read_b128 v[12:15], v73 offset:31744\n\t" \
  "global_load_lds_dwordx4 v72, s[46:47] offset:-3072 sc1\n\t" \
  "v_mfma_f32_16x16x32_bf16 v[40:43], a[80:83], v[20:23], v[40:43]\n\t" \
  "global_load_lds_dwordx4 v72, s[46:47] offset:-2048 sc1\n\t" \
  "global_load_lds_dwordx4 v72, s[46:47] offset:-1024 sc1\n\t" \
  "v_mfma_f32_16x16x32_bf16 v[44:47], a[84:87], v[20:23], v[44:47]\n\t" \
  "global_load_lds_dwordx4 v72, s[46:47] offset:0 sc1\n\t" \
  "global_load_lds_dwordx4 v72, s[46:47] offset:1024 sc1\n\t" \
  "s_waitcnt lgkmcnt(4)\n\t" \
  "v_mfma_f32_16x16x32_bf16 v[32:35], a[88:91], v[24:27], v[32:35]\n\t" \
  "ds_read_b128 v[16:19], v73 offset:14336\n\t" \
  "global_load_lds_dwordx4 v72, s[46:47] offset:2048 sc1\n\t" \
  "v_mfma_f32_16x16x32_bf16 v[36:39], a[92:95], v[24:27], v[36:39]\n\t" \
  "ds_read_b128 v[20:23], v73 offset:32768\n\t" \
  "global_load_lds_dwordx4 v72, s[46:47] offset:3072 sc1\n\t" \
  "v_mfma_f32_16x16x32_bf16 v[40:43], a[88:91], v[28:31], v[40:43]\n\t" \
  "s_add_u32 m0, s48, 4096\n\t" \
  "s_nop 0\n\t" \
  "v_mfma_f32_16x16x32_bf16 v[44:47], a[92:95], v[28:31], v[44:47]\n\t" \
  "global_load_lds_dwordx4 v78, s[46:47] sc1\n\t" \
  "s_add_u32 s46, s46, 73728\n\t" \
  "s_waitcnt lgkmcnt(4)\n\t" \
  "v_mfma_f32_16x16x32_bf16 v[32:35], a[96:99], v[0:3], v[32:35]\n\t" \
  "ds_read_b128 v[24:27], v73 offset:15360\n\t" \
  "s_addc_u32 s47, s47, 0\n\t" \
  "v_mfma_f32_16x16x32_bf16 v[36:39], a[100:103], v[0:3], v[36:39]\n\t" \
  "ds_read_b128 v[28:31], v73 offset:33792\n\t" \
  "v_mfma_f32_16x16x32_bf16 v[40:43], a[96:99], v[4:7], v[40:43]\n\t" \
  "v_mfma_f32_16x16x32_bf16 v[44:47], a[100:103], v[4:7], v[44:47]\n\t" \
  "s_waitcnt lgkmcnt(4)\n\t" \
  "v_mfma_f32_16x16x32_bf16 v[32:35], a[104:107], v[8:11], v[32:35]\n\t" \
  "ds_read_b128 v[0:3], v73 offset:16384\n\t" \
  "v_mfma_f32_16x16x32_bf16 v[36:39], a[108:111], v[8:11], v[36:39]\n\t" \
  "ds_read_b128 v[4:7], v73 offset:34816\n\t" \
  "v_mfma_f32_16x16x32_bf16 v[40:43], a[104:107], v[12:15], v[40:43]\n\t" \
  "v_mfma_f32_16x16x32_bf16 v[44:47], a[108:111], v[12:15], v[44:47]\n\t" \
  "s_waitcnt lgkmcnt(4)\n\t" \
  "v_mfma_f32_16x16x32_bf16 v[32:35], a[112:115], v[16:19], v[32:35]\n\t" \
  "ds_read_b128 v[8:11], v73 offset:17408\n\t" \
  "v_mfma_f32_16x16x32_bf16 v[36:39], a[116:119], v[16:19], v[36:39]\n\t" \
  "ds_read_b128 v[12:15], v73 offset:35840\n\t" \
  "v_mfma_f32_16x16x32_bf16 v[40:43], a[112:115], v[20:23], v[40:43]\n\t" \
  "v_mfma_f32_16x16x32_bf16 v[44:47], a[116:119], v[20:23], v[44:47]\n\t" \
  "s_waitcnt lgkmcnt(4)\n\t" \
  "v_mfma_f32_16x16x32_bf16 v[32:35], a[120:123], v[24:27], v[32:35]\n\t" \
  "ds_read_b128 v[16:19], v74 offset:0\n\t" \
  "v_mfma_f32_16x16x32_bf16 v[36:39], a[124:127], v[24:27], v[36:39]\n\t" \
  "ds_read_b128 v[20:23], v74 offset:18432\n\t" \
  "v_mfma_f32_16x16x32_bf16 v[40:43], a[120:123], v[28:31], v[40:43]\n\t" \
  "v_mfma_f32_16x16x32_bf16 v[44:47], a[124:127], v[28:31], v[44:47]\n\t" \
  "s_waitcnt lgkmcnt(4)\n\t" \
  "v_mfma_f32_16x16x32_bf16 v[32:35], a[128:131], v[0:3], v[32:35]\n\t" \
  "ds_read_b128 v[24:27], v74 offset:1024\n\t" \
  "v_mfma_f32_16x16x32_bf16 v[36:39], a[132:135], v[0:3], v[36:39]\n\t" \
  "ds_read_b128 v[28:31], v74 offset:19456\n\t" \
  "v_mfma_f32_16x16x32_bf16 v[40:43], a[128:131], v[4:7], v[40:43]\n\t" \
  "v_mfma_f32_16x16x32_bf16 v[44:47], a[132:135], v[4:7], v[44:47]\n\t" \
  "s_waitcnt lgkmcnt(4)\n\t" \
  "v_mfma_f32_16x16x32_bf16 v[32:35], a[136:139], v[8:11], v[32:35]\n\t" \
  "ds_read_b128 v[0:3], v74 offset:2048\n\t" \
  "v_mfma_f32_16x16x32_bf16 v[36:39], a[140:143], v[8:11], v[36:39]\n\t" \
  "ds_read_b128 v[4:7], v74 offset:20480\n\t" \
  "v_mfma_f32_16x16x32_bf16 v[40:43], a[136:139], v[12:15], v[40:43]\n\t" \
  "v_mfma_f32_16x16x32_bf16 v[44:47], a[140:143], v[12:15], v[44:47]\n\t" \
  "v_mov_b32 v73, v74\n\t" \
  "v_add_u32 v74, 73728, v72\n\t" \
  "s_waitcnt lgkmcnt(4)\n\t" \
  "v_mfma_f32_16x16x32_bf16 v[48:51], a[0:3], v[16:19], 0\n\t" \
  "ds_read_b128 v[8:11], v73 offset:3072\n\t" \
  "v_mfma_f32_16x16x32_bf16 v[52:55], a[4:7], v[16:19], 0\n\t" \
  "ds_read_b128 v[12:15], v73 offset:21504\n\t" \
  "v_mfma_f32_16x16x32_bf16 v[56:59], a[0:3], v[20:23], 0\n\t" \
  "v_mfma_f32_16x16x32_bf16 v[60:63], a[4:7], v[20:23], 0\n\t" \
  "s_waitcnt lgkmcnt(4)\n\t" \
  "v_mfma_f32_16x16x32_bf16 v[48:51], a[8:11], v[24:27], v[48:51]\n\t" \
  "ds_read_b128 v[16:19], v73 offset:4096\n\t" \
  "v_cvt_pk_bf16_f32 v64, v32, v33\n\t" \
  "v_mfma_f32_16x16x32_bf16 v[52:55], a[12:15], v[24:27], v[52:55]\n\t" \
  "ds_read_b128 v[20:23], v73 offset:22528\n\t" \
  "v_cvt_pk_bf16_f32 v65, v34, v35\n\t" \
  "v_mfma_f32_16x16x32_bf16 v[56:59], a[8:11], v[28:31], v[56:59]\n\t" \
  "v_cvt_pk_bf16_f32 v66, v36, v37\n\t" \
  "v_cvt_pk_bf16_f32 v67, v38, v39\n\t" \
  "v_mfma_f32_16x16x32_bf16 v[60:63], a[12:15], v[28:31], v[60:63]\n\t" \
  "buffer_store_dwordx4 v[64:67], v72, s[20:23], s50 offen offset:0 sc1\n\t" \
  "v_cvt_pk_bf16_f32 v68, v40, v41\n\t" \
  "s_waitcnt lgkmcnt(4)\n\t" \
  "v_mfma_f32_16x16x32_bf16 v[48:51], a[16:19], v[0:3], v[48:51]\n\t" \
  "ds_read_b128 v[24:27], v73 offset:5120\n\t" \
  "v_cvt_pk_bf16_f32 v69, v42, v43\n\t" \
  "v_mfma_f32_16x16x32_bf16 v[52:55], a[20:23], v[0:3], v[52:55]\n\t" \
  "ds_read_b128 v[28:31], v73 offset:23552\n\t" \
  "v_cvt_pk_bf16_f32 v70, v44, v45\n\t" \
  "v_mfma_f32_16x16x32_bf16 v[56:59], a[16:19], v[4:7], v[56:59]\n\t" \
  "v_cvt_pk_bf16_f32 v71, v46, v47\n\t" \
  "buffer_store_dwordx4 v[68:71], v78, s[20:23], s50 offen offset:0 sc1\n\t" \
  "v_mfma_f32_16x16x32_bf16 v[60:63], a[20:23], v[4:7], v[60:63]\n\t" \
  "s_add_u32 s50, s50, 524288\n\t" \
  "s_waitcnt lgkmcnt(4)\n\t" \
  "v_mfma_f32_16x16x32_bf16 v[48:51], a[24:27], v[8:11], v[48:51]\n\t" \
  "ds_read_b128 v[0:3], v73 offset:6144\n\t" \
  "v_mfma_f32_16x16x32_bf16 v[52:55], a[28:31], v[8:11], v[52:55]\n\t" \
  "ds_read_b128 v[4:7], v73 offset:24576\n\t" \
  "v_mfma_f32_16x16x32_bf16 v[56:59], a[24:27], v[12:15], v[56:59]\n\t" \
  "v_mfma_f32_16x16x32_bf16 v[60:63], a[28:31], v[12:15], v[60:63]\n\t" \
  "s_waitcnt lgkmcnt(4)\n\t" \
  "v_mfma_f32_16x16x32_bf16 v[48:51], a[32:35], v[16:19], v[48:51]\n\t" \
  "ds_read_b128 v[8:11], v73 offset:7168\n\t" \
  "v_mfma_f32_16x16x32_bf16 v[52:55], a[36:39], v[16:19], v[52:55]\n\t" \
  "ds_read_b128 v[12:15], v73 offset:25600\n\t" \
  "v_mfma_f32_16x16x32_bf16 v[56:59], a[32:35], v[20:23], v[56:59]\n\t" \
  "v_mfma_f32_16x16x32_bf16 v[60:63], a[36:39], v[20:23], v[60:63]\n\t" \
  "s_waitcnt lgkmcnt(4)\n\t" \
  "v_mfma_f32_16x16x32_bf16 v[48:51], a[40:43], v[24:27], v[48:51]\n\t" \
  "ds_read_b128 v[16:19], v73 offset:8192\n\t" \
  "v_mfma_f32_16x16x32_bf16 v[52:55], a[44:47], v[24:27], v[52:55]\n\t" \
  "ds_read_b128 v[20:23], v73 offset:26624\n\t" \
  "v_mfma_f32_16x16x32_bf16 v[56:59], a[40:43], v[28:31], v[56:59]\n\t" \
  "v_mfma_f32_16x16x32_bf16 v[60:63], a[44:47], v[28:31], v[60:63]\n\t" \
  "s_waitcnt lgkmcnt(4)\n\t" \
  "v_mfma_f32_16x16x32_bf16 v[48:51], a[48:51], v[0:3], v[48:51]\n\t" \
  "ds_read_b128 v[24:27], v73 offset:9216\n\t" \
  "v_mfma_f32_16x16x32_bf16 v[52:55], a[52:55], v[0:3], v[52:55]\n\t" \
  "ds_read_b128 v[28:31], v73 offset:27648\n\t" \
  "v_mfma_f32_16x16x32_bf16 v[56:59], a[48:51], v[4:7], v[56:59]\n\t" \
  "v_mfma_f32_16x16x32_bf16 v[60:63], a[52:55], v[4:7], v[60:63]\n\t" \
  "s_waitcnt lgkmcnt(4)\n\t" \
  "v_mfma_f32_16x16x32_bf16 v[48:51], a[56:59], v[8:11], v[48:51]\n\t" \
  "ds_read_b128 v[0:3], v73 offset:10240\n\t" \
  "v_mfma_f32_16x16x32_bf16 v[52:55], a[60:63], v[8:11], v[52:55]\n\t" \
  "ds_read_b128 v[4:7], v73 offset:28672\n\t" \
  "v_mfma_f32_16x16x32_bf16 v[56:59], a[56:59], v[12:15], v[56:59]\n\t" \
  "v_mfma_f32_16x16x32_bf16 v[60:63], a[60:63], v[12:15], v[60:63]\n\t" \
  "s_waitcnt lgkmcnt(4)\n\t" \
  "v_mfma_f32_16x16x32_bf16 v[48:51], a[64:67], v[16:19], v[48:51]\n\t" \
  "ds_read_b128 v[8:11], v73 offset:11264\n\t" \
  "v_mfma_f32_16x16x32_bf16 v[52:55], a[68:71], v[16:19], v[52:55]\n\t" \
  "ds_read_b128 v[12:15], v73 offset:29696\n\t" \
  "v_mfma_f32_16x16x32_bf16 v[56:59], a[64:67], v[20:23], v[56:59]\n\t" \
  "v_mfma_f32_16x16x32_bf16 v[60:63], a[68:71], v[20:23], v[60:63]\n\t" \
  "s_waitcnt vmcnt(11)\n\t" \
  "s_barrier\n\t" \
  "s_waitcnt lgkmcnt(4)\n\t" \
  "v_mfma_f32_16x16x32_bf16 v[48:51], a[72:75], v[24:27], v[48:51]\n\t" \
  "ds_read_b128 v[16:19], v73 offset:12288\n\t" \
  "v_mov_b32 v77, 1\n\t" \
  "v_mfma_f32_16x16x32_bf16 v[52:55], a[76:79], v[24:27], v[52:55]\n\t" \
  "ds_read_b128 v[20:23], v73 offset:30720\n\t" \
  "s_mov_b64 exec, s[54:55]\n\t" \
  "buffer_store_dword v77, off, s[20:23], s51 sc1\n\t" \
  "s_mov_b64 exec, -1\n\t" \
  "v_mfma_f32_16x16x32_bf16 v[56:59], a[72:75], v[28:31], v[56:59]\n\t" \
  "s_add_u32 s51, s51, 1024\n\t" \
  "v_mfma_f32_16x16x32_bf16 v[60:63], a[76:79], v[28:31], v[60:63]\n\t" \
  "s_waitcnt lgkmcnt(4)\n\t" \
  "v_mfma_f32_16x16x32_bf16 v[48:51], a[80:83], v[0:3], v[48:51]\n\t" \
  "ds_read_b128 v[24:27], v73 offset:13312\n\t" \
  "v_mfma_f32_16x16x32_bf16 v[52:55], a[84:87], v[0:3], v[52:55]\n\t" \
  "ds_read_b128 v[28:31], v73 offset:31744\n\t" \
  "v_mfma_f32_16x16x32_bf16 v[56:59], a[80:83], v[4:7], v[56:59]\n\t" \
  "v_mfma_f32_16x16x32_bf16 v[60:63], a[84:87], v[4:7], v[60:63]\n\t" \
  "s_waitcnt lgkmcnt(4)\n\t" \
  "v_mfma_f32_16x16x32_bf16 v[48:51], a[88:91], v[8:11], v[48:51]\n\t" \
  "ds_read_b128 v[0:3], v73 offset:14336\n\t" \
  "v_mfma_f32_16x16x32_bf16 v[52:55], a[92:95], v[8:11], v[52:55]\n\t" \
  "ds_read_b128 v[4:7], v73 offset:32768\n\t" \
  "v_mfma_f32_16x16x32_bf16 v[56:59], a[88:91], v[12:15], v[56:59]\n\t" \
  "v_mfma_f32_16x16x32_bf16 v[60:63], a[92:95], v[12:15], v[60:63]\n\t" \
  "s_waitcnt lgkmcnt(4)\n\t" \
  "v_mfma_f32_16x16x32_bf16 v[48:51], a[96:99], v[16:19], v[48:51]\n\t" \
  "ds_read_b128 v[8:11], v73 offset:15360\n\t" \
  "v_mfma_f32_16x16x32_bf16 v[52:55], a[100:103], v[16:19], v[52:55]\n\t" \
  "ds_read_b128 v[12:15], v73 offset:33792\n\t" \
  "v_mfma_f32_16x16x32_bf16 v[56:59], a[96:99], v[20:23], v[56:59]\n\t" \
  "v_mfma_f32_16x16x32_bf16 v[60:63], a[100:103], v[20:23], v[60:63]\n\t" \
  "s_waitcnt lgkmcnt(4)\n\t" \
  "v_mfma_f32_16x16x32_bf16 v[48:51], a[104:107], v[24:27], v[48:51]\n\t" \
  "ds_read_b128 v[16:19], v73 offset:16384\n\t" \
  "v_mfma_f32_16x16x32_bf16 v[52:55], a[108:111], v[24:27], v[52:55]\n\t" \
  "ds_read_b128 v[20:23], v73 offset:34816\n\t" \
  "v_mfma_f32_16x16x32_bf16 v[56:59], a[104:107], v[28:31], v[56:59]\n\t" \
  "v_mfma_f32_16x16x32_bf16 v[60:63], a[108:111], v[28:31], v[60:63]\n\t" \
  "s_waitcnt lgkmcnt(4)\n\t" \
  "v_mfma_f32_16x16x32_bf16 v[48:51], a[112:115], v[0:3], v[48:51]\n\t" \
  "ds_read_b128 v[24:27], v73 offset:17408\n\t" \
  "v_mfma_f32_16x16x32_bf16 v[52:55], a[116:119], v[0:3], v[52:55]\n\t" \
  "ds_read_b128 v[28:31], v73 offset:35840\n\t" \
  "v_mfma_f32_16x16x32_bf16 v[56:59], a[112:115], v[4:7], v[56:59]\n\t" \
  "v_mfma_f32_16x16x32_bf16 v[60:63], a[116:119], v[4:7], v[60:63]\n\t" \
  "s_waitcnt lgkmcnt(4)\n\t" \
  "v_mfma_f32_16x16x32_bf16 v[48:51], a[120:123], v[8:11], v[48:51]\n\t" \
  "ds_read_b128 v[0:3], v74 offset:0\n\t" \
  "v_mfma_f32_16x16x32_bf16 v[52:55], a[124:127], v[8:11], v[52:55]\n\t" \
  "ds_read_b128 v[4:7], v74 offset:18432\n\t" \
  "v_mfma_f32_16x16x32_bf16 v[56:59], a[120:123], v[12:15], v[56:59]\n\t" \
  "v_mfma_f32_16x16x32_bf16 v[60:63], a[124:127], v[12:15], v[60:63]\n\t" \
  "s_waitcnt lgkmcnt(4)\n\t" \
  "v_mfma_f32_16x16x32_bf16 v[48:51], a[128:131], v[16:19], v[48:51]\n\t" \
  "ds_read_b128 v[8:11], v74 offset:1024\n\t" \
  "v_mfma_f32_16x16x32_bf16 v[52:55], a[132:135], v[16:19], v[52:55]\n\t" \
  "ds_read_b128 v[12:15], v74 offset:19456\n\t" \
  "v_mfma_f32_16x16x32_bf16 v[56:59], a[128:131], v[20:23], v[56:59]\n\t" \
  "v_mfma_f32_16x16x32_bf16 v[60:63], a[132:135], v[20:23], v[60:63]\n\t" \
  "s_waitcnt lgkmcnt(4)\n\t" \
  "v_mfma_f32_16x16x32_bf16 v[48:51], a[136:139], v[24:27], v[48:51]\n\t" \
  "ds_read_b128 v[16:19], v74 offset:2048\n\t" \
  "v_mfma_f32_16x16x32_bf16 v[52:55], a[140:143], v[24:27], v[52:55]\n\t" \
  "ds_read_b128 v[20:23], v74 offset:20480\n\t" \
  "v_mfma_f32_16x16x32_bf16 v[56:59], a[136:139], v[28:31], v[56:59]\n\t" \
  "v_mfma_f32_16x16x32_bf16 v[60:63], a[140:143], v[28:31], v[60:63]\n\t" \
  "v_mov_b32 v73, v74\n\t" \
  "v_add_u32 v74, 110592, v72\n\t" \
  "s_waitcnt lgkmcnt(4)\n\t" \
  "v_mfma_f32_16x16x32_bf16 v[32:35], a[0:3], v[0:3], 0\n\t" \
  "ds_read_b128 v[24:27], v73 offset:3072\n\t" \
  "v_mfma_f32_16x16x32_bf16 v[36:39], a[4:7], v[0:3], 0\n\t" \
  "ds_read_b128 v[28:31], v73 offset:21504\n\t" \
  "v_mfma_f32_16x16x32_bf16 v[40:43], a[0:3], v[4:7], 0\n\t" \
  "v_mfma_f32_16x16x32_bf16 v[44:47], a[4:7], v[4:7], 0\n\t" \
  "s_waitcnt lgkmcnt(4)\n\t" \
  "v_mfma_f32_16x16x32_bf16 v[32:35], a[8:11], v[8:11], v[32:35]\n\t" \
  "ds_read_b128 v[0:3], v73 offset:4096\n\t" \
  "v_cvt_pk_bf16_f32 v64, v48, v49\n\t" \
  "v_mfma_f32_16x16x32_bf16 v[36:39], a[12:15], v[8:11], v[36:39]\n\t" \
  "ds_read_b128 v[4:7], v73 offset:22528\n\t" \
  "v_cvt_pk_bf16_f32 v65, v50, v51\n\t" \
  "v_mfma_f32_16x16x32_bf16 v[40:43], a[8:11], v[12:15], v[40:43]\n\t" \
  "v_cvt_pk_bf16_f32 v66, v52, v53\n\t" \
  "v_cvt_pk_bf16_f32 v67, v54, v55\n\t" \
  "v_mfma_f32_16x16x32_bf16 v[44:47], a[12:15], v[12:15], v[44:47]\n\t" \
  "buffer_store_dwordx4 v[64:67], v72, s[20:23], s50 offen offset:0 sc1\n\t" \
  "v_cvt_pk_bf16_f32 v68, v56, v57\n\t" \
  "s_waitcnt lgkmcnt(4)\n\t" \
  "v_mfma_f32_16x16x32_bf16 v[32:35], a[16:19], v[16:19], v[32:35]\n\t" \
  "ds_read_b128 v[8:11], v73 offset:5120\n\t" \
  "v_cvt_pk_bf16_f32 v69, v58, v59\n\t" \
  "v_mfma_f32_16x16x32_bf16 v[36:39], a[20:23], v[16:19], v[36:39]\n\t" \
  "ds_read_b128 v[12:15], v73 offset:23552\n\t" \
  "v_cvt_pk_bf16_f32 v70, v60, v61\n\t" \
  "v_mfma_f32_16x16x32_bf16 v[40:43], a[16:19], v[20:23], v[40:43]\n\t" \
  "v_cvt_pk_bf16_f32 v71, v62, v63\n\t" \
  "buffer_store_dwordx4 v[68:71], v78, s[20:23], s50 offen offset:0 sc1\n\t" \
  "v_mfma_f32_16x16x32_bf16 v[44:47], a[20:23], v[20:23], v[44:47]\n\t" \
  "s_add_u32 s50, s50, 524288\n\t" \
  "s_waitcnt lgkmcnt(4)\n\t" \
  "v_mfma_f32_16x16x32_bf16 v[32:35], a[24:27], v[24:27], v[32:35]\n\t" \
  "ds_read_b128 v[16:19], v73 offset:6144\n\t" \
  "v_mfma_f32_16x16x32_bf16 v[36:39], a[28:31], v[24:27], v[36:39]\n\t" \
  "ds_read_b128 v[20:23], v73 offset:24576\n\t" \
  "v_mfma_f32_16x16x32_bf16 v[40:43], a[24:27], v[28:31], v[40:43]\n\t" \
  "v_mfma_f32_16x16x32_bf16 v[44:47], a[28:31], v[28:31], v[44:47]\n\t" \
  "s_waitcnt lgkmcnt(4)\n\t" \
  "v_mfma_f32_16x16x32_bf16 v[32:35], a[32:35], v[0:3], v[32:35]\n\t" \
  "ds_read_b128 v[24:27], v73 offset:7168\n\t" \
  "v_mfma_f32_16x16x32_bf16 v[36:39], a[36:39], v[0:3], v[36:39]\n\t" \
  "ds_read_b128 v[28:31], v73 offset:25600\n\t" \
  "v_mfma_f32_16x16x32_bf16 v[40:43], a[32:35], v[4:7], v[40:43]\n\t" \
  "v_mfma_f32_16x16x32_bf16 v[44:47], a[36:39], v[4:7], v[44:47]\n\t" \
  "s_waitcnt lgkmcnt(4)\n\t" \
  "v_mfma_f32_16x16x32_bf16 v[32:35], a[40:43], v[8:11], v[32:35]\n\t" \
  "ds_read_b128 v[0:3], v73 offset:8192\n\t" \
  "v_mfma_f32_16x16x32_bf16 v[36:39], a[44:47], v[8:11], v[36:39]\n\t" \
  "ds_read_b128 v[4:7], v73 offset:26624\n\t" \
  "v_mfma_f32_16x16x32_bf16 v[40:43], a[40:43], v[12:15], v[40:43]\n\t" \
  "v_mfma_f32_16x16x32_bf16 v[44:47], a[44:47], v[12:15], v[44:47]\n\t" \
  "s_waitcnt lgkmcnt(4)\n\t" \
  "v_mfma_f32_16x16x32_bf16 v[32:35], a[48:51], v[16:19], v[32:35]\n\t" \
  "ds_read_b128 v[8:11], v73 offset:9216\n\t" \
  "v_mfma_f32_16x16x32_bf16 v[36:39], a[52:55], v[16:19], v[36:39]\n\t" \
  "ds_read_b128 v[12:15], v73 offset:27648\n\t" \
  "v_mfma_f32_16x16x32_bf16 v[40:43], a[48:51], v[20:23], v[40:43]\n\t" \
  "v_mfma_f32_16x16x32_bf16 v[44:47], a[52:55], v[20:23], v[44:47]\n\t" \
  "s_waitcnt lgkmcnt(4)\n\t" \
  "v_mfma_f32_16x16x32_bf16 v[32:35], a[56:59], v[24:27], v[32:35]\n\t" \
  "ds_read_b128 v[16:19], v73 offset:10240\n\t" \
  "v_mfma_f32_16x16x32_bf16 v[36:39], a[60:63], v[24:27], v[36:39]\n\t" \
  "ds_read_b128 v[20:23], v73 offset:28672\n\t" \
  "v_mfma_f32_16x16x32_bf16 v[40:43], a[56:59], v[28:31], v[40:43]\n\t" \
  "v_mfma_f32_16x16x32_bf16 v[44:47], a[60:63], v[28:31], v[44:47]\n\t" \
  "s_waitcnt lgkmcnt(4)\n\t" \
  "v_mfma_f32_16x16x32_bf16 v[32:35], a[64:67], v[0:3], v[32:35]\n\t" \
  "ds_read_b128 v[24:27], v73 offset:11264\n\t" \
  "v_mfma_f32_16x16x32_bf16 v[36:39], a[68:71], v[0:3], v[36:39]\n\t" \
  "ds_read_b128 v[28:31], v73 offset:29696\n\t" \
  "v_mfma_f32_16x16x32_bf16 v[40:43], a[64:67], v[4:7], v[40:43]\n\t" \
  "v_mfma_f32_16x16x32_bf16 v[44:47], a[68:71], v[4:7], v[44:47]\n\t" \
  "s_waitcnt vmcnt(2)\n\t" \
  "s_barrier\n\t" \
  "s_waitcnt lgkmcnt(4)\n\t" \
  "v_mfma_f32_16x16x32_bf16 v[32:35], a[72:75], v[8:11], v[32:35]\n\t" \
  "ds_read_b128 v[0:3], v73 offset:12288\n\t" \
  "v_mov_b32 v77, 1\n\t" \
  "v_mfma_f32_16x16x32_bf16 v[36:39], a[76:79], v[8:11], v[36:39]\n\t" \
  "ds_read_b128 v[4:7], v73 offset:30720\n\t" \
  "s_mov_b64 exec, s[54:55]\n\t" \
  "buffer_store_dword v77, off, s[20:23], s51 sc1\n\t" \
  "s_mov_b64 exec, -1\n\t" \
  "v_mfma_f32_16x16x32_bf16 v[40:43], a[72:75], v[12:15], v[40:43]\n\t" \
  "s_add_u32 s51, s51, 1024\n\t" \
  "v_mfma_f32_16x16x32_bf16 v[44:47], a[76:79], v[12:15], v[44:47]\n\t" \
  "s_waitcnt lgkmcnt(4)\n\t" \
  "v_mfma_f32_16x16x32_bf16 v[32:35], a[80:83], v[16:19], v[32:35]\n\t" \
  "ds_read_b128 v[8:11], v73 offset:13312\n\t" \
  "v_mfma_f32_16x16x32_bf16 v[36:39], a[84:87], v[16:19], v[36:39]\n\t" \
  "ds_read_b128 v[12:15], v73 offset:31744\n\t" \
  "v_mfma_f32_16x16x32_bf16 v[40:43], a[80:83], v[20:23], v[40:43]\n\t" \
  "v_mfma_f32_16x16x32_bf16 v[44:47], a[84:87], v[20:23], v[44:47]\n\t" \
  "s_waitcnt lgkmcnt(4)\n\t" \
  "v_mfma_f32_16x16x32_bf16 v[32:35], a[88:91], v[24:27], v[32:35]\n\t" \
  "ds_read_b128 v[16:19], v73 offset:14336\n\t" \
  "v_mfma_f32_16x16x32_bf16 v[36:39], a[92:95], v[24:27], v[36:39]\n\t" \
  "ds_read_b128 v[20:23], v73 offset:32768\n\t" \
  "v_mfma_f32_16x16x32_bf16 v[40:43], a[88:91], v[28:31], v[40:43]\n\t" \
  "v_mfma_f32_16x16x32_bf16 v[44:47], a[92:95], v[28:31], v[44:47]\n\t" \
  "s_waitcnt lgkmcnt(4)\n\t" \
  "v_mfma_f32_16x16x32_bf16 v[32:35], a[96:99], v[0:3], v[32:35]\n\t" \
  "ds_read_b128 v[24:27], v73 offset:15360\n\t" \
  "v_mfma_f32_16x16x32_bf16 v[36:39], a[100:103], v[0:3], v[36:39]\n\t" \
  "ds_read_b128 v[28:31], v73 offset:33792\n\t" \
  "v_mfma_f32_16x16x32_bf16 v[40:43], a[96:99], v[4:7], v[40:43]\n\t" \
  "v_mfma_f32_16x16x32_bf16 v[44:47], a[100:103], v[4:7], v[44:47]\n\t" \
  "s_waitcnt lgkmcnt(4)\n\t" \
  "v_mfma_f32_16x16x32_bf16 v[32:35], a[104:107], v[8:11], v[32:35]\n\t" \
  "ds_read_b128 v[0:3], v73 offset:16384\n\t" \
  "v_mfma_f32_16x16x32_bf16 v[36:39], a[108:111], v[8:11], v[36:39]\n\t" \
  "ds_read_b128 v[4:7], v73 offset:34816\n\t" \
  "v_mfma_f32_16x16x32_bf16 v[40:43], a[104:107], v[12:15], v[40:43]\n\t" \
  "v_mfma_f32_16x16x32_bf16 v[44:47], a[108:111], v[12:15], v[44:47]\n\t" \
  "s_waitcnt lgkmcnt(4)\n\t" \
  "v_mfma_f32_16x16x32_bf16 v[32:35], a[112:115], v[16:19], v[32:35]\n\t" \
  "ds_read_b128 v[8:11], v73 offset:17408\n\t" \
  "v_mfma_f32_16x16x32_bf16 v[36:39], a[116:119], v[16:19], v[36:39]\n\t" \
  "ds_read_b128 v[12:15], v73 offset:35840\n\t" \
  "v_mfma_f32_16x16x32_bf16 v[40:43], a[112:115], v[20:23], v[40:43]\n\t" \
  "v_mfma_f32_16x16x32_bf16 v[44:47], a[116:119], v[20:23], v[44:47]\n\t" \
  "s_waitcnt lgkmcnt(4)\n\t" \
  "v_mfma_f32_16x16x32_bf16 v[32:35], a[120:123], v[24:27], v[32:35]\n\t" \
  "ds_read_b128 v[16:19], v74 offset:0\n\t" \
  "v_mfma_f32_16x16x32_bf16 v[36:39], a[124:127], v[24:27], v[36:39]\n\t" \
  "ds_read_b128 v[20:23], v74 offset:18432\n\t" \
  "v_mfma_f32_16x16x32_bf16 v[40:43], a[120:123], v[28:31], v[40:43]\n\t" \
  "v_mfma_f32_16x16x32_bf16 v[44:47], a[124:127], v[28:31], v[44:47]\n\t" \
  "s_waitcnt lgkmcnt(4)\n\t" \
  "v_mfma_f32_16x16x32_bf16 v[32:35], a[128:131], v[0:3], v[32:35]\n\t" \
  "ds_read_b128 v[24:27], v74 offset:1024\n\t" \
  "v_mfma_f32_16x16x32_bf16 v[36:39], a[132:135], v[0:3], v[36:39]\n\t" \
  "ds_read_b128 v[28:31], v74 offset:19456\n\t" \
  "v_mfma_f32_16x16x32_bf16 v[40:43], a[128:131], v[4:7], v[40:43]\n\t" \
  "v_mfma_f32_16x16x32_bf16 v[44:47], a[132:135], v[4:7], v[44:47]\n\t" \
  "s_waitcnt lgkmcnt(4)\n\t" \
  "v_mfma_f32_16x16x32_bf16 v[32:35], a[136:139], v[8:11], v[32:35]\n\t" \
  "ds_read_b128 v[0:3], v74 offset:2048\n\t" \
  "v_mfma_f32_16x16x32_bf16 v[36:39], a[140:143], v[8:11], v[36:39]\n\t" \
  "ds_read_b128 v[4:7], v74 offset:20480\n\t" \
  "v_mfma_f32_16x16x32_bf16 v[40:43], a[136:139], v[12:15], v[40:43]\n\t" \
  "v_mfma_f32_16x16x32_bf16 v[44:47], a[140:143], v[12:15], v[44:47]\n\t" \
  "v_mov_b32 v73, v74\n\t" \
  "s_waitcnt lgkmcnt(4)\n\t" \
  "v_mfma_f32_16x16x32_bf16 v[48:51], a[0:3], v[16:19], 0\n\t" \
  "ds_read_b128 v[8:11], v73 offset:3072\n\t" \
  "v_mfma_f32_16x16x32_bf16 v[52:55], a[4:7], v[16:19], 0\n\t" \
  "ds_read_b128 v[12:15], v73 offset:21504\n\t" \
  "v_mfma_f32_16x16x32_bf16 v[56:59], a[0:3], v[20:23], 0\n\t" \
  "v_mfma_f32_16x16x32_bf16 v[60:63], a[4:7], v[20:23], 0\n\t" \
  "s_waitcnt lgkmcnt(4)\n\t" \
  "v_mfma_f32_16x16x32_bf16 v[48:51], a[8:11], v[24:27], v[48:51]\n\t" \
  "ds_read_b128 v[16:19], v73 offset:4096\n\t" \
  "v_cvt_pk_bf16_f32 v64, v32, v33\n\t" \
  "v_mfma_f32_16x16x32_bf16 v[52:55], a[12:15], v[24:27], v[52:55]\n\t" \
  "ds_read_b128 v[20:23], v73 offset:22528\n\t" \
  "v_cvt_pk_bf16_f32 v65, v34, v35\n\t" \
  "v_mfma_f32_16x16x32_bf16 v[56:59], a[8:11], v[28:31], v[56:59]\n\t" \
  "v_cvt_pk_bf16_f32 v66, v36, v37\n\t" \
  "v_cvt_pk_bf16_f32 v67, v38, v39\n\t" \
  "v_mfma_f32_16x16x32_bf16 v[60:63], a[12:15], v[28:31], v[60:63]\n\t" \
  "buffer_store_dwordx4 v[64:67], v72, s[20:23], s50 offen offset:0 sc1\n\t" \
  "v_cvt_pk_bf16_f32 v68, v40, v41\n\t" \
  "s_waitcnt lgkmcnt(4)\n\t" \
  "v_mfma_f32_16x16x32_bf16 v[48:51], a[16:19], v[0:3], v[48:51]\n\t" \
  "ds_read_b128 v[24:27], v73 offset:5120\n\t" \
  "v_cvt_pk_bf16_f32 v69, v42, v43\n\t" \
  "v_mfma_f32_16x16x32_bf16 v[52:55], a[20:23], v[0:3], v[52:55]\n\t" \
  "ds_read_b128 v[28:31], v73 offset:23552\n\t" \
  "v_cvt_pk_bf16_f32 v70, v44, v45\n\t" \
  "v_mfma_f32_16x16x32_bf16 v[56:59], a[16:19], v[4:7], v[56:59]\n\t" \
  "v_cvt_pk_bf16_f32 v71, v46, v47\n\t" \
  "buffer_store_dwordx4 v[68:71], v78, s[20:23], s50 offen offset:0 sc1\n\t" \
  "v_mfma_f32_16x16x32_bf16 v[60:63], a[20:23], v[4:7], v[60:63]\n\t" \
  "s_add_u32 s50, s50, 524288\n\t" \
  "s_waitcnt lgkmcnt(4)\n\t" \
  "v_mfma_f32_16x16x32_bf16 v[48:51], a[24:27], v[8:11], v[48:51]\n\t" \
  "ds_read_b128 v[0:3], v73 offset:6144\n\t" \
  "v_mfma_f32_16x16x32_bf16 v[52:55], a[28:31], v[8:11], v[52:55]\n\t" \
  "ds_read_b128 v[4:7], v73 offset:24576\n\t" \
  "v_mfma_f32_16x16x32_bf16 v[56:59], a[24:27], v[12:15], v[56:59]\n\t" \
  "v_mfma_f32_16x16x32_bf16 v[60:63], a[28:31], v[12:15], v[60:63]\n\t" \
  "s_waitcnt lgkmcnt(4)\n\t" \
  "v_mfma_f32_16x16x32_bf16 v[48:51], a[32:35], v[16:19], v[48:51]\n\t" \
  "ds_read_b128 v[8:11], v73 offset:7168\n\t" \
  "v_mfma_f32_16x16x32_bf16 v[52:55], a[36:39], v[16:19], v[52:55]\n\t" \
  "ds_read_b128 v[12:15], v73 offset:25600\n\t" \
  "v_mfma_f32_16x16x32_bf16 v[56:59], a[32:35], v[20:23], v[56:59]\n\t" \
  "v_mfma_f32_16x16x32_bf16 v[60:63], a[36:39], v[20:23], v[60:63]\n\t" \
  "s_waitcnt lgkmcnt(4)\n\t" \
  "v_mfma_f32_16x16x32_bf16 v[48:51], a[40:43], v[24:27], v[48:51]\n\t" \
  "ds_read_b128 v[16:19], v73 offset:8192\n\t" \
  "v_mfma_f32_16x16x32_bf16 v[52:55], a[44:47], v[24:27], v[52:55]\n\t" \
  "ds_read_b128 v[20:23], v73 offset:26624\n\t" \
  "v_mfma_f32_16x16x32_bf16 v[56:59], a[40:43], v[28:31], v[56:59]\n\t" \
  "v_mfma_f32_16x16x32_bf16 v[60:63], a[44:47], v[28:31], v[60:63]\n\t" \
  "s_waitcnt lgkmcnt(4)\n\t" \
  "v_mfma_f32_16x16x32_bf16 v[48:51], a[48:51], v[0:3], v[48:51]\n\t" \
  "ds_read_b128 v[24:27], v73 offset:9216\n\t" \
  "v_mfma_f32_16x16x32_bf16 v[52:55], a[52:55], v[0:3], v[52:55]\n\t" \
  "ds_read_b128 v[28:31], v73 offset:27648\n\t" \
  "v_mfma_f32_16x16x32_bf16 v[56:59], a[48:51], v[4:7], v[56:59]\n\t" \
  "v_mfma_f32_16x16x32_bf16 v[60:63], a[52:55], v[4:7], v[60:63]\n\t" \
  "s_waitcnt lgkmcnt(4)\n\t" \
  "v_mfma_f32_16x16x32_bf16 v[48:51], a[56:59], v[8:11], v[48:51]\n\t" \
  "ds_read_b128 v[0:3], v73 offset:10240\n\t" \
  "v_mfma_f32_16x16x32_bf16 v[52:55], a[60:63], v[8:11], v[52:55]\n\t" \
  "ds_read_b128 v[4:7], v73 offset:28672\n\t" \
  "v_mfma_f32_16x16x32_bf16 v[56:59], a[56:59], v[12:15], v[56:59]\n\t" \
  "v_mfma_f32_16x16x32_bf16 v[60:63], a[60:63], v[12:15], v[60:63]\n\t" \
  "s_waitcnt lgkmcnt(4)\n\t" \
  "v_mfma_f32_16x16x32_bf16 v[48:51], a[64:67], v[16:19], v[48:51]\n\t" \
  "ds_read_b128 v[8:11], v73 offset:11264\n\t" \
  "v_mfma_f32_16x16x32_bf16 v[52:55], a[68:71], v[16:19], v[52:55]\n\t" \
  "ds_read_b128 v[12:15], v73 offset:29696\n\t" \
  "v_mfma_f32_16x16x32_bf16 v[56:59], a[64:67], v[20:23], v[56:59]\n\t" \
  "v_mfma_f32_16x16x32_bf16 v[60:63], a[68:71], v[20:23], v[60:63]\n\t" \
  "s_waitcnt vmcnt(2)\n\t" \
  "s_barrier\n\t" \
  "s_waitcnt lgkmcnt(4)\n\t" \
  "v_mfma_f32_16x16x32_bf16 v[48:51], a[72:75], v[24:27], v[48:51]\n\t" \
  "ds_read_b128 v[16:19], v73 offset:12288\n\t" \
  "v_mov_b32 v77, 1\n\t" \
  "v_mfma_f32_16x16x32_bf16 v[52:55], a[76:79], v[24:27], v[52:55]\n\t" \
  "ds_read_b128 v[20:23], v73 offset:30720\n\t" \
  "s_mov_b64 exec, s[54:55]\n\t" \
  "buffer_store_dword v77, off, s[20:23], s51 sc1\n\t" \
  "s_mov_b64 exec, -1\n\t" \
  "v_mfma_f32_16x16x32_bf16 v[56:59], a[72:75], v[28:31], v[56:59]\n\t" \
  "s_add_u32 s51, s51, 1024\n\t" \
  "v_mfma_f32_16x16x32_bf16 v[60:63], a[76:79], v[28:31], v[60:63]\n\t" \
  "s_waitcnt lgkmcnt(4)\n\t" \
  "v_mfma_f32_16x16x32_bf16 v[48:51], a[80:83], v[0:3], v[48:51]\n\t" \
  "ds_read_b128 v[24:27], v73 offset:13312\n\t" \
  "v_mfma_f32_16x16x32_bf16 v[52:55], a[84:87], v[0:3], v[52:55]\n\t" \
  "ds_read_b128 v[28:31], v73 offset:31744\n\t" \
  "v_mfma_f32_16x16x32_bf16 v[56:59], a[80:83], v[4:7], v[56:59]\n\t" \
  "v_mfma_f32_16x16x32_bf16 v[60:63], a[84:87], v[4:7], v[60:63]\n\t" \
  "s_waitcnt lgkmcnt(4)\n\t" \
  "v_mfma_f32_16x16x32_bf16 v[48:51], a[88:91], v[8:11], v[48:51]\n\t" \
  "ds_read_b128 v[0:3], v73 offset:14336\n\t" \
  "v_mfma_f32_16x16x32_bf16 v[52:55], a[92:95], v[8:11], v[52:55]\n\t" \
  "ds_read_b128 v[4:7], v73 offset:32768\n\t" \
  "v_mfma_f32_16x16x32_bf16 v[56:59], a[88:91], v[12:15], v[56:59]\n\t" \
  "v_mfma_f32_16x16x32_bf16 v[60:63], a[92:95], v[12:15], v[60:63]\n\t" \
  "s_waitcnt lgkmcnt(4)\n\t" \
  "v_mfma_f32_16x16x32_bf16 v[48:51], a[96:99], v[16:19], v[48:51]\n\t" \
  "ds_read_b128 v[8:11], v73 offset:15360\n\t" \
  "v_mfma_f32_16x16x32_bf16 v[52:55], a[100:103], v[16:19], v[52:55]\n\t" \
  "ds_read_b128 v[12:15], v73 offset:33792\n\t" \
  "v_mfma_f32_16x16x32_bf16 v[56:59], a[96:99], v[20:23], v[56:59]\n\t" \
  "v_mfma_f32_16x16x32_bf16 v[60:63], a[100:103], v[20:23], v[60:63]\n\t" \
  "s_waitcnt lgkmcnt(4)\n\t" \
  "v_mfma_f32_16x16x32_bf16 v[48:51], a[104:107], v[24:27], v[48:51]\n\t" \
  "ds_read_b128 v[16:19], v73 offset:16384\n\t" \
  "v_mfma_f32_16x16x32_bf16 v[52:55], a[108:111], v[24:27], v[52:55]\n\t" \
  "ds_read_b128 v[20:23], v73 offset:34816\n\t" \
  "v_mfma_f32_16x16x32_bf16 v[56:59], a[104:107], v[28:31], v[56:59]\n\t" \
  "v_mfma_f32_16x16x32_bf16 v[60:63], a[108:111], v[28:31], v[60:63]\n\t" \
  "s_waitcnt lgkmcnt(4)\n\t" \
  "v_mfma_f32_16x16x32_bf16 v[48:51], a[112:115], v[0:3], v[48:51]\n\t" \
  "ds_read_b128 v[24:27], v73 offset:17408\n\t" \
  "v_mfma_f32_16x16x32_bf16 v[52:55], a[116:119], v[0:3], v[52:55]\n\t" \
  "ds_read_b128 v[28:31], v73 offset:35840\n\t" \
  "v_mfma_f32_16x16x32_bf16 v[56:59], a[112:115], v[4:7], v[56:59]\n\t" \
  "v_mfma_f32_16x16x32_bf16 v[60:63], a[116:119], v[4:7], v[60:63]\n\t" \
  "s_waitcnt lgkmcnt(4)\n\t" \
  "v_mfma_f32_16x16x32_bf16 v[48:51], a[120:123], v[8:11], v[48:51]\n\t" \
  "v_mfma_f32_16x16x32_bf16 v[52:55], a[124:127], v[8:11], v[52:55]\n\t" \
  "v_mfma_f32_16x16x32_bf16 v[56:59], a[120:123], v[12:15], v[56:59]\n\t" \
  "v_mfma_f32_16x16x32_bf16 v[60:63], a[124:127], v[12:15], v[60:63]\n\t" \
  "s_waitcnt lgkmcnt(2)\n\t" \
  "v_mfma_f32_16x16x32_bf16 v[48:51], a[128:131], v[16:19], v[48:51]\n\t" \
  "v_mfma_f32_16x16x32_bf16 v[52:55], a[132:135], v[16:19], v[52:55]\n\t" \
  "v_mfma_f32_16x16x32_bf16 v[56:59], a[128:131], v[20:23], v[56:59]\n\t" \
  "v_mfma_f32_16x16x32_bf16 v[60:63], a[132:135], v[20:23], v[60:63]\n\t" \
  "s_waitcnt lgkmcnt(0)\n\t" \
  "v_mfma_f32_16x16x32_bf16 v[48:51], a[136:139], v[24:27], v[48:51]\n\t" \
  "v_mfma_f32_16x16x32_bf16 v[52:55], a[140:143], v[24:27], v[52:55]\n\t" \
  "v_mfma_f32_16x16x32_bf16 v[56:59], a[136:139], v[28:31], v[56:59]\n\t" \
  "v_mfma_f32_16x16x32_bf16 v[60:63], a[140:143], v[28:31], v[60:63]\n\t" \
  "s_nop 7\n\t" \
  "v_cvt_pk_bf16_f32 v64, v48, v49\n\t" \
  "v_cvt_pk_bf16_f32 v65, v50, v51\n\t" \
  "v_cvt_pk_bf16_f32 v66, v52, v53\n\t" \
  "v_cvt_pk_bf16_f32 v67, v54, v55\n\t" \
  "buffer_store_dwordx4 v[64:67], v72, s[20:23], s50 offen offset:0 sc1\n\t" \
  "v_cvt_pk_bf16_f32 v68, v56, v57\n\t" \
  "v_cvt_pk_bf16_f32 v69, v58, v59\n\t" \
  "v_cvt_pk_bf16_f32 v70, v60, v61\n\t" \
  "v_cvt_pk_bf16_f32 v71, v62, v63\n\t" \
  "buffer_store_dwordx4 v[68:71], v78, s[20:23], s50 offen offset:0 sc1\n\t" \
  "s_add_u32 s50, s50, 524288\n\t" \
  "s_waitcnt vmcnt(0) lgkmcnt(0)\n\t" \
  "s_barrier\n\t" \
  "v_mov_b32 v77, 1\n\t" \
  "s_mov_b64 exec, s[54:55]\n\t" \
  "buffer_store_dword v77, off, s[20:23], s51 sc1\n\t" \
  "s_mov_b64 exec, -1\n\t" \
  "s_add_u32 s51, s51, 1024\n\t" \
  "v_mov_b32 v77, 1\n\t" \
  "s_mov_b64 exec, s[54:55]\n\t" \
  "buffer_store_dword v77, off, s[20:23], s51 sc1\n\t" \
  "s_mov_b64 exec, -1\n\t" \
  "s_add_u32 s51, s51, 1024\n\t" \
  "s_barrier\n\t" \
  "s_branch Lend_%=\n\t" \
  "Ltmo_%=:\n\t" \
  "v_mov_b32 v77, 1\n\t" \
  "s_mov_b32 s40, 2157568\n\t" \
  "buffer_store_dword v77, off, s[20:23], s40 sc1\n\t" \
  "Lquit_%=:\n\t" \
  "s_endpgm\n\t" \
  "Lend_%=:\n\t" \
  "s_mov_b32 m0, s59\n\t"
#define V_ASM \
  "s_mov_b32 s59, m0\n\t" \
  "s_load_dwordx2 s[24:25], %0, 80\n\t" \
  "s_and_b32 s26, %1, 3\n\t" \
  "s_bfe_u32 s27, %1, 0x50008\n\t" \
  "s_bfe_u32 s28, %1, 0x10010\n\t" \
  "s_bfe_u32 s29, %1, 0x20014\n\t" \
  "s_lshr_b32 s52, %1, 24\n\t" \
  "s_lshl_b32 s52, s52, 7\n\t" \
  "s_add_u32 s52, s52, 2752512\n\t" \
  "s_mov_b64 exec, -1\n\t" \
  "s_waitcnt lgkmcnt(0)\n\t" \
  "s_mov_b32 s20, s24\n\t" \
  "s_and_b32 s21, s25, 0xffff\n\t" \
  "s_mov_b32 s22, 113246208\n\t" \
  "s_mov_b32 s23, 0x20000\n\t" \
  "v_mbcnt_lo_u32_b32 v76, -1, 0\n\t" \
  "v_mbcnt_hi_u32_b32 v76, -1, v76\n\t" \
  "s_lshl_b32 s40, s26, 3\n\t" \
  "s_add_u32 s40, s40, 16\n\t" \
  "s_load_dwordx2 s[46:47], %0, s40\n\t" \
  "s_cmp_eq_u32 s29, 0\n\t" \
  "s_cselect_b64 s[54:55], 1, 0\n\t" \
  "s_mov_b32 s56, 0\n\t" \
  "s_mov_b32 s57, 0x80000000\n\t" \
  "v_lshrrev_b32 v0, 4, v76\n\t" \
  "v_and_b32 v1, 15, v76\n\t" \
  "v_lshlrev_b32 v2, 1, v0\n\t" \
  "v_xor_b32 v1, v1, v2\n\t" \
  "v_lshlrev_b32 v0, 13, v0\n\t" \
  "v_lshl_add_u32 v0, v1, 4, v0\n\t" \
  "v_xor_b32 v1, 128, v0\n\t" \
  "v_add_u32 v1, 65536, v1\n\t" \
  "v_add_u32 v2, 32768, v0\n\t" \
  "v_add_u32 v3, 32768, v1\n\t" \
  "v_min_u32 v75, 62, v76\n\t" \
  "v_lshlrev_b32 v75, 2, v75\n\t" \
  "v_add_u32 v75, 2158848, v75\n\t" \
  "s_mul_i32 s40, s29, 1179648\n\t" \
  "s_lshl_b32 s41, s27, 8\n\t" \
  "s_add_u32 s40, s40, s41\n\t" \
  "s_mul_i32 s58, s29, 36864\n\t" \
  "s_waitcnt lgkmcnt(0)\n\t" \
  "s_add_u32 s46, s46, s40\n\t" \
  "s_addc_u32 s47, s47, 0\n\t" \
  "s_add_u32 m0, s58, 0\n\t" \
  "s_nop 0\n\t" \
  "global_load_lds_dwordx4 v0, s[46:47]\n\t" \
  "s_add_u32 m0, s58, 1024\n\t" \
  "s_nop 0\n\t" \
  "global_load_lds_dwordx4 v2, s[46:47]\n\t" \
  "s_add_u32 m0, s58, 2048\n\t" \
  "s_nop 0\n\t" \
  "global_load_lds_dwordx4 v1, s[46:47]\n\t" \
  "s_add_u32 m0, s58, 3072\n\t" \
  "s_nop 0\n\t" \
  "global_load_lds_dwordx4 v3, s[46:47]\n\t" \
  "s_add_u32 s46, s46, 131072\n\t" \
  "s_addc_u32 s47, s47, 0\n\t" \
  "s_add_u32 m0, s58, 4096\n\t" \
  "s_nop 0\n\t" \
  "global_load_lds_dwordx4 v0, s[46:47]\n\t" \
  "s_add_u32 m0, s58, 5120\n\t" \
  "s_nop 0\n\t" \
  "global_load_lds_dwordx4 v2, s[46:47]\n\t" \
  "s_add_u32 m0, s58, 6144\n\t" \
  "s_nop 0\n\t" \
  "global_load_lds_dwordx4 v1, s[46:47]\n\t" \
  "s_add_u32 m0, s58, 7168\n\t" \
  "s_nop 0\n\t" \
  "global_load_lds_dwordx4 v3, s[46:47]\n\t" \
  "s_add_u32 s46, s46, 131072\n\t" \
  "s_addc_u32 s47, s47, 0\n\t" \
  "s_add_u32 m0, s58, 8192\n\t" \
  "s_nop 0\n\t" \
  "global_load_lds_dwordx4 v0, s[46:47]\n\t" \
  "s_add_u32 m0, s58, 9216\n\t" \
  "s_nop 0\n\t" \
  "global_load_lds_dwordx4 v2, s[46:47]\n\t" \
  "s_add_u32 m0, s58, 10240\n\t" \
  "s_nop 0\n\t" \
  "global_load_lds_dwordx4 v1, s[46:47]\n\t" \
  "s_add_u32 m0, s58, 11264\n\t" \
  "s_nop 0\n\t" \
  "global_load_lds_dwordx4 v3, s[46:47]\n\t" \
  "s_add_u32 s46, s46, 131072\n\t" \
  "s_addc_u32 s47, s47, 0\n\t" \
  "s_add_u32 m0, s58, 12288\n\t" \
  "s_nop 0\n\t" \
  "global_load_lds_dwordx4 v0, s[46:47]\n\t" \
  "s_add_u32 m0, s58, 13312\n\t" \
  "s_nop 0\n\t" \
  "global_load_lds_dwordx4 v2, s[46:47]\n\t" \
  "s_add_u32 m0, s58, 14336\n\t" \
  "s_nop 0\n\t" \
  "global_load_lds_dwordx4 v1, s[46:47]\n\t" \
  "s_add_u32 m0, s58, 15360\n\t" \
  "s_nop 0\n\t" \
  "global_load_lds_dwordx4 v3, s[46:47]\n\t" \
  "s_add_u32 s46, s46, 131072\n\t" \
  "s_addc_u32 s47, s47, 0\n\t" \
  "s_or_b32 s40, s29, 0\n\t" \
  "s_cbranch_scc1 Lh0_%=\n\t" \
  "s_waitcnt vmcnt(16)\n\t" \
  "buffer_load_dword v84, off, s[20:23], s52 sc1\n\t" \
  "Lh0_%=:\n\t" \
  "s_add_u32 m0, s58, 16384\n\t" \
  "s_nop 0\n\t" \
  "global_load_lds_dwordx4 v0, s[46:47]\n\t" \
  "s_add_u32 m0, s58, 17408\n\t" \
  "s_nop 0\n\t" \
  "global_load_lds_dwordx4 v2, s[46:47]\n\t" \
  "s_add_u32 m0, s58, 18432\n\t" \
  "s_nop 0\n\t" \
  "global_load_lds_dwordx4 v1, s[46:47]\n\t" \
  "s_add_u32 m0, s58, 19456\n\t" \
  "s_nop 0\n\t" \
  "global_load_lds_dwordx4 v3, s[46:47]\n\t" \
  "s_add_u32 s46, s46, 131072\n\t" \
  "s_addc_u32 s47, s47, 0\n\t" \
  "s_add_u32 m0, s58, 20480\n\t" \
  "s_nop 0\n\t" \
  "global_load_lds_dwordx4 v0, s[46:47]\n\t" \
  "s_add_u32 m0, s58, 21504\n\t" \
  "s_nop 0\n\t" \
  "global_load_lds_dwordx4 v2, s[46:47]\n\t" \
  "s_add_u32 m0, s58, 22528\n\t" \
  "s_nop 0\n\t" \
  "global_load_lds_dwordx4 v1, s[46:47]\n\t" \
  "s_add_u32 m0, s58, 23552\n\t" \
  "s_nop 0\n\t" \
  "global_load_lds_dwordx4 v3, s[46:47]\n\t" \
  "s_add_u32 s46, s46, 131072\n\t" \
  "s_addc_u32 s47, s47, 0\n\t" \
  "s_add_u32 m0, s58, 24576\n\t" \
  "s_nop 0\n\t" \
  "global_load_lds_dwordx4 v0, s[46:47]\n\t" \
  "s_add_u32 m0, s58, 25600\n\t" \
  "s_nop 0\n\t" \
  "global_load_lds_dwordx4 v2, s[46:47]\n\t" \
  "s_add_u32 m0, s58, 26624\n\t" \
  "s_nop 0\n\t" \
  "global_load_lds_dwordx4 v1, s[46:47]\n\t" \
  "s_add_u32 m0, s58, 27648\n\t" \
  "s_nop 0\n\t" \
  "global_load_lds_dwordx4 v3, s[46:47]\n\t" \
  "s_add_u32 s46, s46, 131072\n\t" \
  "s_addc_u32 s47, s47, 0\n\t" \
  "s_add_u32 m0, s58, 28672\n\t" \
  "s_nop 0\n\t" \
  "global_load_lds_dwordx4 v0, s[46:47]\n\t" \
  "s_add_u32 m0, s58, 29696\n\t" \
  "s_nop 0\n\t" \
  "global_load_lds_dwordx4 v2, s[46:47]\n\t" \
  "s_add_u32 m0, s58, 30720\n\t" \
  "s_nop 0\n\t" \
  "global_load_lds_dwordx4 v1, s[46:47]\n\t" \
  "s_add_u32 m0, s58, 31744\n\t" \
  "s_nop 0\n\t" \
  "global_load_lds_dwordx4 v3, s[46:47]\n\t" \
  "s_add_u32 s46, s46, 131072\n\t" \
  "s_addc_u32 s47, s47, 0\n\t" \
  "s_add_u32 m0, s58, 32768\n\t" \
  "s_nop 0\n\t" \
  "global_load_lds_dwordx4 v0, s[46:47]\n\t" \
  "s_add_u32 m0, s58, 33792\n\t" \
  "s_nop 0\n\t" \
  "global_load_lds_dwordx4 v2, s[46:47]\n\t" \
  "s_add_u32 m0, s58, 34816\n\t" \
  "s_nop 0\n\t" \
  "global_load_lds_dwordx4 v1, s[46:47]\n\t" \
  "s_add_u32 m0, s58, 35840\n\t" \
  "s_nop 0\n\t" \
  "global_load_lds_dwordx4 v3, s[46:47]\n\t" \
  "s_add_u32 s46, s46, 131072\n\t" \
  "s_addc_u32 s47, s47, 0\n\t" \
  "s_or_b32 s40, s29, 0\n\t" \
  "s_cbranch_scc1 Lh3_%=\n\t" \
  "s_cmp_eq_u32 s26, 0\n\t" \
  "s_cbranch_scc1 Lh2_%=\n\t" \
  "s_mov_b32 s42, 0\n\t" \
  "s_waitcnt vmcnt(20)\n\t" \
  "Lhs0_%=:\n\t" \
  "v_cmp_eq_u32 vcc, 0, v84\n\t" \
  "s_nop 0\n\t" \
  "s_cbranch_vccz Lhs1_%=\n\t" \
  "s_add_u32 s42, s42, 1\n\t" \
  "s_cmp_gt_u32 s42, 524288\n\t" \
  "s_cbranch_scc1 Lquit_%=\n\t" \
  "s_sleep 2\n\t" \
  "buffer_load_dword v84, off, s[20:23], s52 sc1\n\t" \
  "s_waitcnt vmcnt(0)\n\t" \
  "s_branch Lhs0_%=\n\t" \
  "Lhs1_%=:\n\t" \
  "s_sub_u32 s52, s52, 32768\n\t" \
  "s_mov_b64 exec, 1\n\t" \
  "buffer_store_dword v84, off, s[20:23], s52 sc1\n\t" \
  "s_mov_b64 exec, -1\n\t" \
  "Lh2_%=:\n\t" \
  "s_waitcnt vmcnt(21)\n\t" \
  "buffer_load_dword v84, v75, s[20:23], 0 offen sc1\n\t" \
  "s_mov_b32 s42, 0\n\t" \
  "s_waitcnt vmcnt(0)\n\t" \
  "Lxf0_%=:\n\t" \
  "v_cmp_eq_u32 vcc, 0, v84\n\t" \
  "s_nop 0\n\t" \
  "s_cbranch_vccz Lxf1_%=\n\t" \
  "s_add_u32 s42, s42, 1\n\t" \
  "s_cmp_gt_u32 s42, 524288\n\t" \
  "s_cbranch_scc1 Ltmo_%=\n\t" \
  "s_sleep 2\n\t" \
  "buffer_load_dword v84, v75, s[20:23], 0 offen sc1\n\t" \
  "s_waitcnt vmcnt(0)\n\t" \
  "s_branch Lxf0_%=\n\t" \
  "Lxf1_%=:\n\t" \
  "Lh3_%=:\n\t" \
  "s_waitcnt vmcnt(0)\n\t" \
  "s_barrier\n\t" \
  "v_lshrrev_b32 v0, 4, v76\n\t" \
  "v_bfe_u32 v77, v76, 2, 2\n\t" \
  "v_lshl_add_u32 v78, v0, 3, v77\n\t" \
  "v_lshlrev_b32 v78, 8, v78\n\t" \
  "v_and_b32 v0, 1, v0\n\t" \
  "v_lshl_add_u32 v77, v0, 2, v77\n\t" \
  "v_lshlrev_b32 v77, 1, v77\n\t" \
  "s_lshl_b32 s40, s29, 2\n\t" \
  "v_bfe_u32 v1, v76, 1, 1\n\t" \
  "v_add_u32 v1, s40, v1\n\t" \
  "v_xor_b32 v1, v1, v77\n\t" \
  "v_lshl_add_u32 v1, v1, 4, v78\n\t" \
  "v_and_b32 v0, 1, v76\n\t" \
  "v_lshl_add_u32 v2, v0, 3, v1\n\t" \
  "v_xor_b32 v5, 32, v2\n\t" \
  "v_add_u32 v3, 65536, v2\n\t" \
  "v_add_u32 v4, 131072, v2\n\t" \
  "v_add_u32 v6, 65536, v5\n\t" \
  "v_add_u32 v7, 131072, v5\n\t" \
  "v_lshlrev_b32 v72, 4, v76\n\t" \
  "s_mul_i32 s40, s28, 36864\n\t" \
  "s_mul_i32 s58, s29, 9216\n\t" \
  "s_add_u32 s58, s58, 4096\n\t" \
  "s_add_u32 s40, s40, s58\n\t" \
  "s_add_u32 s40, s40, 3145728\n\t" \
  "s_add_u32 s46, s24, s40\n\t" \
  "s_addc_u32 s47, s25, 0\n\t" \
  "s_lshl_b32 s50, s26, 24\n\t" \
  "s_lshl_b32 s40, s27, 13\n\t" \
  "s_add_u32 s50, s50, s40\n\t" \
  "s_lshl_b32 s40, s28, 18\n\t" \
  "s_add_u32 s50, s50, s40\n\t" \
  "s_lshl_b32 s40, s29, 11\n\t" \
  "s_add_u32 s50, s50, s40\n\t" \
  "s_add_u32 s50, s50, 10485760\n\t" \
  "s_sub_u32 s51, 2, s26\n\t" \
  "s_lshl_b32 s51, s51, 7\n\t" \
  "s_lshl_b32 s40, s27, 2\n\t" \
  "s_add_u32 s51, s51, s40\n\t" \
  "s_lshl_b32 s40, s28, 9\n\t" \
  "s_add_u32 s51, s51, s40\n\t" \
  "s_add_u32 s51, s51, 2097152\n\t" \
  "v_add_u32 v75, 0, v75\n\t" \
  "s_lshl_b32 s40, s29, 2\n\t" \
  "s_add_u32 s40, s40, 147456\n\t" \
  "v_mov_b32 v80, s40\n\t" \
  "v_add_u32 v78, 4096, v72\n\t" \
  "ds_read_b64_tr_b16 a[0:1], v2 offset:0\n\t" \
  "ds_read_b64_tr_b16 a[2:3], v2 offset:1024\n\t" \
  "ds_read_b64_tr_b16 a[4:5], v5 offset:0\n\t" \
  "ds_read_b64_tr_b16 a[6:7], v5 offset:1024\n\t" \
  "ds_read_b64_tr_b16 a[8:9], v2 offset:8192\n\t" \
  "ds_read_b64_tr_b16 a[10:11], v2 offset:9216\n\t" \
  "ds_read_b64_tr_b16 a[12:13], v5 offset:8192\n\t" \
  "ds_read_b64_tr_b16 a[14:15], v5 offset:9216\n\t" \
  "ds_read_b64_tr_b16 a[16:17], v2 offset:16384\n\t" \
  "ds_read_b64_tr_b16 a[18:19], v2 offset:17408\n\t" \
  "ds_read_b64_tr_b16 a[20:21], v5 offset:16384\n\t" \
  "ds_read_b64_tr_b16 a[22:23], v5 offset:17408\n\t" \
  "ds_read_b64_tr_b16 a[24:25], v2 offset:24576\n\t" \
  "ds_read_b64_tr_b16 a[26:27], v2 offset:25600\n\t" \
  "ds_read_b64_tr_b16 a[28:29], v5 offset:24576\n\t" \
  "ds_read_b64_tr_b16 a[30:31], v5 offset:25600\n\t" \
  "ds_read_b64_tr_b16 a[32:33], v2 offset:32768\n\t" \
  "ds_read_b64_tr_b16 a[34:35], v2 offset:33792\n\t" \
  "ds_read_b64_tr_b16 a[36:37], v5 offset:32768\n\t" \
  "ds_read_b64_tr_b16 a[38:39], v5 offset:33792\n\t" \
  "s_waitcnt lgkmcnt(0)\n\t" \
  "s_barrier\n\t" \
  "s_add_u32 s48, s58, 0\n\t" \
  "s_mov_b32 m0, s48\n\t" \
  "s_nop 0\n\t" \
  "global_load_lds_dwordx4 v72, s[46:47] offset:-4096 sc1\n\t" \
  "global_load_lds_dwordx4 v72, s[46:47] offset:-3072 sc1\n\t" \
  "global_load_lds_dwordx4 v72, s[46:47] offset:-2048 sc1\n\t" \
  "global_load_lds_dwordx4 v72, s[46:47] offset:-1024 sc1\n\t" \
  "global_load_lds_dwordx4 v72, s[46:47] offset:0 sc1\n\t" \
  "global_load_lds_dwordx4 v72, s[46:47] offset:1024 sc1\n\t" \
  "global_load_lds_dwordx4 v72, s[46:47] offset:2048 sc1\n\t" \
  "global_load_lds_dwordx4 v72, s[46:47] offset:3072 sc1\n\t" \
  "s_add_u32 m0, s48, 4096\n\t" \
  "s_nop 0\n\t" \
  "global_load_lds_dwordx4 v78, s[46:47] sc1\n\t" \
  "s_add_u32 s46, s46, 73728\n\t" \
  "s_addc_u32 s47, s47, 0\n\t" \
  "ds_read_b64_tr_b16 a[40:41], v2 offset:40960\n\t" \
  "ds_read_b64_tr_b16 a[42:43], v2 offset:41984\n\t" \
  "ds_read_b64_tr_b16 a[44:45], v5 offset:40960\n\t" \
  "ds_read_b64_tr_b16 a[46:47], v5 offset:41984\n\t" \
  "ds_read_b64_tr_b16 a[48:49], v2 offset:49152\n\t" \
  "ds_read_b64_tr_b16 a[50:51], v2 offset:50176\n\t" \
  "ds_read_b64_tr_b16 a[52:53], v5 offset:49152\n\t" \
  "ds_read_b64_tr_b16 a[54:55], v5 offset:50176\n\t" \
  "ds_read_b64_tr_b16 a[56:57], v2 offset:57344\n\t" \
  "ds_read_b64_tr_b16 a[58:59], v2 offset:58368\n\t" \
  "ds_read_b64_tr_b16 a[60:61], v5 offset:57344\n\t" \
  "ds_read_b64_tr_b16 a[62:63], v5 offset:58368\n\t" \
  "ds_read_b64_tr_b16 a[64:65], v3 offset:0\n\t" \
  "ds_read_b64_tr_b16 a[66:67], v3 offset:1024\n\t" \
  "ds_read_b64_tr_b16 a[68:69], v6 offset:0\n\t" \
  "ds_read_b64_tr_b16 a[70:71], v6 offset:1024\n\t" \
  "s_waitcnt lgkmcnt(0)\n\t" \
  "s_barrier\n\t" \
  "s_add_u32 s48, s58, 36864\n\t" \
  "s_mov_b32 m0, s48\n\t" \
  "s_nop 0\n\t" \
  "global_load_lds_dwordx4 v72, s[46:47] offset:-4096 sc1\n\t" \
  "global_load_lds_dwordx4 v72, s[46:47] offset:-3072 sc1\n\t" \
  "global_load_lds_dwordx4 v72, s[46:47] offset:-2048 sc1\n\t" \
  "global_load_lds_dwordx4 v72, s[46:47] offset:-1024 sc1\n\t" \
  "global_load_lds_dwordx4 v72, s[46:47] offset:0 sc1\n\t" \
  "global_load_lds_dwordx4 v72, s[46:47] offset:1024 sc1\n\t" \
  "global_load_lds_dwordx4 v72, s[46:47] offset:2048 sc1\n\t" \
  "global_load_lds_dwordx4 v72, s[46:47] offset:3072 sc1\n\t" \
  "s_add_u32 m0, s48, 4096\n\t" \
  "s_nop 0\n\t" \
  "global_load_lds_dwordx4 v78, s[46:47] sc1\n\t" \
  "s_add_u32 s46, s46, 73728\n\t" \
  "s_addc_u32 s47, s47, 0\n\t" \
  "ds_read_b64_tr_b16 a[72:73], v3 offset:8192\n\t" \
  "ds_read_b64_tr_b16 a[74:75], v3 offset:9216\n\t" \
  "ds_read_b64_tr_b16 a[76:77], v6 offset:8192\n\t" \
  "ds_read_b64_tr_b16 a[78:79], v6 offset:9216\n\t" \
  "ds_read_b64_tr_b16 a[80:81], v3 offset:16384\n\t" \
  "ds_read_b64_tr_b16 a[82:83], v3 offset:17408\n\t" \
  "ds_read_b64_tr_b16 a[84:85], v6 offset:16384\n\t" \
  "ds_read_b64_tr_b16 a[86:87], v6 offset:17408\n\t" \
  "ds_read_b64_tr_b16 a[88:89], v3 offset:24576\n\t" \
  "ds_read_b64_tr_b16 a[90:91], v3 offset:25600\n\t" \
  "ds_read_b64_tr_b16 a[92:93], v6 offset:24576\n\t" \
  "ds_read_b64_tr_b16 a[94:95], v6 offset:25600\n\t" \
  "ds_read_b64_tr_b16 a[96:97], v3 offset:32768\n\t" \
  "ds_read_b64_tr_b16 a[98:99], v3 offset:33792\n\t" \
  "ds_read_b64_tr_b16 a[100:101], v6 offset:32768\n\t" \
  "ds_read_b64_tr_b16 a[102:103], v6 offset:33792\n\t" \
  "ds_read_b64_tr_b16 a[104:105], v3 offset:40960\n\t" \
  "ds_read_b64_tr_b16 a[106:107], v3 offset:41984\n\t" \
  "ds_read_b64_tr_b16 a[108:109], v6 offset:40960\n\t" \
  "ds_read_b64_tr_b16 a[110:111], v6 offset:41984\n\t" \
  "s_waitcnt lgkmcnt(0)\n\t" \
  "s_barrier\n\t" \
  "s_add_u32 s48, s58, 73728\n\t" \
  "s_mov_b32 m0, s48\n\t" \
  "s_nop 0\n\t" \
  "global_load_lds_dwordx4 v72, s[46:47] offset:-4096 sc1\n\t" \
  "global_load_lds_dwordx4 v72, s[46:47] offset:-3072 sc1\n\t" \
  "global_load_lds_dwordx4 v72, s[46:47] offset:-2048 sc1\n\t" \
  "global_load_lds_dwordx4 v72, s[46:47] offset:-1024 sc1\n\t" \
  "global_load_lds_dwordx4 v72, s[46:47] offset:0 sc1\n\t" \
  "global_load_lds_dwordx4 v72, s[46:47] offset:1024 sc1\n\t" \
  "global_load_lds_dwordx4 v72, s[46:47] offset:2048 sc1\n\t" \
  "global_load_lds_dwordx4 v72, s[46:47] offset:3072 sc1\n\t" \
  "s_add_u32 m0, s48, 4096\n\t" \
  "s_nop 0\n\t" \
  "global_load_lds_dwordx4 v78, s[46:47] sc1\n\t" \
  "s_add_u32 s46, s46, 73728\n\t" \
  "s_addc_u32 s47, s47, 0\n\t" \
  "ds_read_b64_tr_b16 a[112:113], v3 offset:49152\n\t" \
  "ds_read_b64_tr_b16 a[114:115], v3 offset:50176\n\t" \
  "ds_read_b64_tr_b16 a[116:117], v6 offset:49152\n\t" \
  "ds_read_b64_tr_b16 a[118:119], v6 offset:50176\n\t" \
  "ds_read_b64_tr_b16 a[120:121], v3 offset:57344\n\t" \
  "ds_read_b64_tr_b16 a[122:123], v3 offset:58368\n\t" \
  "ds_read_b64_tr_b16 a[124:125], v6 offset:57344\n\t" \
  "ds_read_b64_tr_b16 a[126:127], v6 offset:58368\n\t" \
  "ds_read_b64_tr_b16 a[128:129], v4 offset:0\n\t" \
  "ds_read_b64_tr_b16 a[130:131], v4 offset:1024\n\t" \
  "ds_read_b64_tr_b16 a[132:133], v7 offset:0\n\t" \
  "ds_read_b64_tr_b16 a[134:135], v7 offset:1024\n\t" \
  "ds_read_b64_tr_b16 a[136:137], v4 offset:8192\n\t" \
  "ds_read_b64_tr_b16 a[138:139], v4 offset:9216\n\t" \
  "ds_read_b64_tr_b16 a[140:141], v7 offset:8192\n\t" \
  "ds_read_b64_tr_b16 a[142:143], v7 offset:9216\n\t" \
  "s_waitcnt vmcnt(18) lgkmcnt(0)\n\t" \
  "s_barrier\n\t" \
  "v_mov_b32 v73, v72\n\t" \
  "ds_read_b128 v[0:3], v73 offset:0\n\t" \
  "ds_read_b128 v[4:7], v73 offset:18432\n\t" \
  "ds_read_b128 v[8:11], v73 offset:1024\n\t" \
  "ds_read_b128 v[12:15], v73 offset:19456\n\t" \
  "ds_read_b128 v[16:19], v73 offset:2048\n\t" \
  "ds_read_b128 v[20:23], v73 offset:20480\n\t" \
  "v_add_u32 v74, 36864, v72\n\t" \
  "s_waitcnt lgkmcnt(4)\n\t" \
  "v_mfma_f32_16x16x32_bf16 v[32:35], v[0:3], a[0:3], 0\n\t" \
  "ds_read_b128 v[24:27], v73 offset:3072\n\t" \
  "v_mfma_f32_16x16x32_bf16 v[36:39], v[0:3], a[4:7], 0\n\t" \
  "ds_read_b128 v[28:31], v73 offset:21504\n\t" \
  "v_mfma_f32_16x16x32_bf16 v[40:43], v[4:7], a[0:3], 0\n\t" \
  "v_mfma_f32_16x16x32_bf16 v[44:47], v[4:7], a[4:7], 0\n\t" \
  "s_waitcnt lgkmcnt(4)\n\t" \
  "v_mfma_f32_16x16x32_bf16 v[32:35], v[8:11], a[8:11], v[32:35]\n\t" \
  "ds_read_b128 v[0:3], v73 offset:4096\n\t" \
  "v_mfma_f32_16x16x32_bf16 v[36:39], v[8:11], a[12:15], v[36:39]\n\t" \
  "ds_read_b128 v[4:7], v73 offset:22528\n\t" \
  "v_mfma_f32_16x16x32_bf16 v[40:43], v[12:15], a[8:11], v[40:43]\n\t" \
  "v_mfma_f32_16x16x32_bf16 v[44:47], v[12:15], a[12:15], v[44:47]\n\t" \
  "s_waitcnt lgkmcnt(4)\n\t" \
  "v_mfma_f32_16x16x32_bf16 v[32:35], v[16:19], a[16:19], v[32:35]\n\t" \
  "ds_read_b128 v[8:11], v73 offset:5120\n\t" \
  "v_mfma_f32_16x16x32_bf16 v[36:39], v[16:19], a[20:23], v[36:39]\n\t" \
  "ds_read_b128 v[12:15], v73 offset:23552\n\t" \
  "v_mfma_f32_16x16x32_bf16 v[40:43], v[20:23], a[16:19], v[40:43]\n\t" \
  "v_mfma_f32_16x16x32_bf16 v[44:47], v[20:23], a[20:23], v[44:47]\n\t" \
  "s_waitcnt lgkmcnt(4)\n\t" \
  "v_mfma_f32_16x16x32_bf16 v[32:35], v[24:27], a[24:27], v[32:35]\n\t" \
  "ds_read_b128 v[16:19], v73 offset:6144\n\t" \
  "v_mfma_f32_16x16x32_bf16 v[36:39], v[24:27], a[28:31], v[36:39]\n\t" \
  "ds_read_b128 v[20:23], v73 offset:24576\n\t" \
  "v_mfma_f32_16x16x32_bf16 v[40:43], v[28:31], a[24:27], v[40:43]\n\t" \
  "v_mfma_f32_16x16x32_bf16 v[44:47], v[28:31], a[28:31], v[44:47]\n\t" \
  "s_waitcnt lgkmcnt(4)\n\t" \
  "v_mfma_f32_16x16x32_bf16 v[32:35], v[0:3], a[32:35], v[32:35]\n\t" \
  "ds_read_b128 v[24:27], v73 offset:7168\n\t" \
  "v_mfma_f32_16x16x32_bf16 v[36:39], v[0:3], a[36:39], v[36:39]\n\t" \
  "ds_read_b128 v[28:31], v73 offset:25600\n\t" \
  "v_mfma_f32_16x16x32_bf16 v[40:43], v[4:7], a[32:35], v[40:43]\n\t" \
  "v_mfma_f32_16x16x32_bf16 v[44:47], v[4:7], a[36:39], v[44:47]\n\t" \
  "s_waitcnt lgkmcnt(4)\n\t" \
  "v_mfma_f32_16x16x32_bf16 v[32:35], v[8:11], a[40:43], v[32:35]\n\t" \
  "ds_read_b128 v[0:3], v73 offset:8192\n\t" \
  "v_mfma_f32_16x16x32_bf16 v[36:39], v[8:11], a[44:47], v[36:39]\n\t" \
  "ds_read_b128 v[4:7], v73 offset:26624\n\t" \
  "v_mfma_f32_16x16x32_bf16 v[40:43], v[12:15], a[40:43], v[40:43]\n\t" \
  "v_mfma_f32_16x16x32_bf16 v[44:47], v[12:15], a[44:47], v[44:47]\n\t" \
  "s_waitcnt lgkmcnt(4)\n\t" \
  "v_mfma_f32_16x16x32_bf16 v[32:35], v[16:19], a[48:51], v[32:35]\n\t" \
  "ds_read_b128 v[8:11], v73 offset:9216\n\t" \
  "v_mfma_f32_16x16x32_bf16 v[36:39], v[16:19], a[52:55], v[36:39]\n\t" \
  "ds_read_b128 v[12:15], v73 offset:27648\n\t" \
  "v_mfma_f32_16x16x32_bf16 v[40:43], v[20:23], a[48:51], v[40:43]\n\t" \
  "v_mfma_f32_16x16x32_bf16 v[44:47], v[20:23], a[52:55], v[44:47]\n\t" \
  "s_waitcnt lgkmcnt(4)\n\t" \
  "v_mfma_f32_16x16x32_bf16 v[32:35], v[24:27], a[56:59], v[32:35]\n\t" \
  "ds_read_b128 v[16:19], v73 offset:10240\n\t" \
  "v_mfma_f32_16x16x32_bf16 v[36:39], v[24:27], a[60:63], v[36:39]\n\t" \
  "ds_read_b128 v[20:23], v73 offset:28672\n\t" \
  "v_mfma_f32_16x16x32_bf16 v[40:43], v[28:31], a[56:59], v[40:43]\n\t" \
  "v_mfma_f32_16x16x32_bf16 v[44:47], v[28:31], a[60:63], v[44:47]\n\t" \
  "s_waitcnt lgkmcnt(4)\n\t" \
  "v_mfma_f32_16x16x32_bf16 v[32:35], v[0:3], a[64:67], v[32:35]\n\t" \
  "ds_read_b128 v[24:27], v73 offset:11264\n\t" \
  "v_mfma_f32_16x16x32_bf16 v[36:39], v[0:3], a[68:71], v[36:39]\n\t" \
  "ds_read_b128 v[28:31], v73 offset:29696\n\t" \
  "v_mfma_f32_16x16x32_bf16 v[40:43], v[4:7], a[64:67], v[40:43]\n\t" \
  "v_mfma_f32_16x16x32_bf16 v[44:47], v[4:7], a[68:71], v[44:47]\n\t" \
  "s_waitcnt vmcnt(9)\n\t" \
  "s_barrier\n\t" \
  "s_waitcnt lgkmcnt(4)\n\t" \
  "v_mfma_f32_16x16x32_bf16 v[32:35], v[8:11], a[72:75], v[32:35]\n\t" \
  "ds_read_b128 v[0:3], v73 offset:12288\n\t" \
  "s_add_u32 s48, s58, 110592\n\t" \
  "v_mfma_f32_16x16x32_bf16 v[36:39], v[8:11], a[76:79], v[36:39]\n\t" \
  "ds_read_b128 v[4:7], v73 offset:30720\n\t" \
  "s_mov_b32 m0, s48\n\t" \
  "v_mfma_f32_16x16x32_bf16 v[40:43], v[12:15], a[72:75], v[40:43]\n\t" \
  "s_nop 0\n\t" \
  "global_load_lds_dwordx4 v72, s[46:47] offset:-4096 sc1\n\t" \
  "v_mfma_f32_16x16x32_bf16 v[44:47], v[12:15], a[76:79], v[44:47]\n\t" \
  "global_load_lds_dwordx4 v72, s[46:47] offset:-3072 sc1\n\t" \
  "global_load_lds_dwordx4 v72, s[46:47] offset:-2048 sc1\n\t" \
  "s_waitcnt lgkmcnt(4)\n\t" \
  "v_mfma_f32_16x16x32_bf16 v[32:35], v[16:19], a[80:83], v[32:35]\n\t" \
  "ds_read_b128 v[8:11], v73 offset:13312\n\t" \
  "global_load_lds_dwordx4 v72, s[46:47] offset:-1024 sc1\n\t" \
  "v_mfma_f32_16x16x32_bf16 v[36:39], v[16:19], a[84:87], v[36:39]\n\t" \
  "ds_read_b128 v[12:15], v73 offset:31744\n\t" \
  "global_load_lds_dwordx4 v72, s[46:47] offset:0 sc1\n\t" \
  "v_mfma_f32_16x16x32_bf16 v[40:43], v[20:23], a[80:83], v[40:43]\n\t" \
  "global_load_lds_dwordx4 v72, s[46:47] offset:1024 sc1\n\t" \
  "global_load_lds_dwordx4 v72, s[46:47] offset:2048 sc1\n\t" \
  "v_mfma_f32_16x16x32_bf16 v[44:47], v[20:23], a[84:87], v[44:47]\n\t" \
  "global_load_lds_dwordx4 v72, s[46:47] offset:3072 sc1\n\t" \
  "s_add_u32 m0, s48, 4096\n\t" \
  "s_waitcnt lgkmcnt(4)\n\t" \
  "v_mfma_f32_16x16x32_bf16 v[32:35], v[24:27], a[88:91], v[32:35]\n\t" \
  "ds_read_b128 v[16:19], v73 offset:14336\n\t" \
  "s_nop 0\n\t" \
  "v_mfma_f32_16x16x32_bf16 v[36:39], v[24:27], a[92:95], v[36:39]\n\t" \
  "ds_read_b128 v[20:23], v73 offset:32768\n\t" \
  "global_load_lds_dwordx4 v78, s[46:47] sc1\n\t" \
  "v_mfma_f32_16x16x32_bf16 v[40:43], v[28:31], a[88:91], v[40:43]\n\t" \
  "s_add_u32 s46, s46, 73728\n\t" \
  "s_addc_u32 s47, s47, 0\n\t" \
  "v_mfma_f32_16x16x32_bf16 v[44:47], v[28:31], a[92:95], v[44:47]\n\t" \
  "s_waitcnt lgkmcnt(4)\n\t" \
  "v_mfma_f32_16x16x32_bf16 v[32:35], v[0:3], a[96:99], v[32:35]\n\t" \
  "ds_read_b128 v[24:27], v73 offset:15360\n\t" \
  "v_mfma_f32_16x16x32_bf16 v[36:39], v[0:3], a[100:103], v[36:39]\n\t" \
  "ds_read_b128 v[28:31], v73 offset:33792\n\t" \
  "v_mfma_f32_16x16x32_bf16 v[40:43], v[4:7], a[96:99], v[40:43]\n\t" \
  "v_mfma_f32_16x16x32_bf16 v[44:47], v[4:7], a[100:103], v[44:47]\n\t" \
  "s_waitcnt lgkmcnt(4)\n\t" \
  "v_mfma_f32_16x16x32_bf16 v[32:35], v[8:11], a[104:107], v[32:35]\n\t" \
  "ds_read_b128 v[0:3], v73 offset:16384\n\t" \
  "v_mfma_f32_16x16x32_bf16 v[36:39], v[8:11], a[108:111], v[36:39]\n\t" \
  "ds_read_b128 v[4:7], v73 offset:34816\n\t" \
  "v_mfma_f32_16x16x32_bf16 v[40:43], v[12:15], a[104:107], v[40:43]\n\t" \
  "v_mfma_f32_16x16x32_bf16 v[44:47], v[12:15], a[108:111], v[44:47]\n\t" \
  "s_waitcnt lgkmcnt(4)\n\t" \
  "v_mfma_f32_16x16x32_bf16 v[32:35], v[16:19], a[112:115], v[32:35]\n\t" \
  "ds_read_b128 v[8:11], v73 offset:17408\n\t" \
  "v_mfma_f32_16x16x32_bf16 v[36:39], v[16:19], a[116:119], v[36:39]\n\t" \
  "ds_read_b128 v[12:15], v73 offset:35840\n\t" \
  "v_mfma_f32_16x16x32_bf16 v[40:43], v[20:23], a[112:115], v[40:43]\n\t" \
  "v_mfma_f32_16x16x32_bf16 v[44:47], v[20:23], a[116:119], v[44:47]\n\t" \
  "s_waitcnt lgkmcnt(4)\n\t" \
  "v_mfma_f32_16x16x32_bf16 v[32:35], v[24:27], a[120:123], v[32:35]\n\t" \
  "ds_read_b128 v[16:19], v74 offset:0\n\t" \
  "v_mfma_f32_16x16x32_bf16 v[36:39], v[24:27], a[124:127], v[36:39]\n\t" \
  "ds_read_b128 v[20:23], v74 offset:18432\n\t" \
  "v_mfma_f32_16x16x32_bf16 v[40:43], v[28:31], a[120:123], v[40:43]\n\t" \
  "v_mfma_f32_16x16x32_bf16 v[44:47], v[28:31], a[124:127], v[44:47]\n\t" \
  "s_waitcnt lgkmcnt(4)\n\t" \
  "v_mfma_f32_16x16x32_bf16 v[32:35], v[0:3], a[128:131], v[32:35]\n\t" \
  "ds_read_b128 v[24:27], v74 offset:1024\n\t" \
  "v_mfma_f32_16x16x32_bf16 v[36:39], v[0:3], a[132:135], v[36:39]\n\t" \
  "ds_read_b128 v[28:31], v74 offset:19456\n\t" \
  "v_mfma_f32_16x16x32_bf16 v[40:43], v[4:7], a[128:131], v[40:43]\n\t" \
  "v_mfma_f32_16x16x32_bf16 v[44:47], v[4:7], a[132:135], v[44:47]\n\t" \
  "s_waitcnt lgkmcnt(4)\n\t" \
  "v_mfma_f32_16x16x32_bf16 v[32:35], v[8:11], a[136:139], v[32:35]\n\t" \
  "ds_read_b128 v[0:3], v74 offset:2048\n\t" \
  "v_mfma_f32_16x16x32_bf16 v[36:39], v[8:11], a[140:143], v[36:39]\n\t" \
  "ds_read_b128 v[4:7], v74 offset:20480\n\t" \
  "v_mfma_f32_16x16x32_bf16 v[40:43], v[12:15], a[136:139], v[40:43]\n\t" \
  "v_mfma_f32_16x16x32_bf16 v[44:47], v[12:15], a[140:143], v[44:47]\n\t" \
  "v_mov_b32 v73, v74\n\t" \
  "s_nop 7\n\t" \
  "v_cvt_pk_bf16_f32 v64, v32, v33\n\t" \
  "v_cvt_pk_bf16_f32 v65, v34, v35\n\t" \
  "v_cvt_pk_bf16_f32 v66, v40, v41\n\t" \
  "v_cvt_pk_bf16_f32 v67, v42, v43\n\t" \
  "buffer_store_dwordx4 v[64:67], v72, s[20:23], s50 offen offset:0 sc1\n\t" \
  "v_cvt_pk_bf16_f32 v68, v36, v37\n\t" \
  "v_cvt_pk_bf16_f32 v69, v38, v39\n\t" \
  "v_cvt_pk_bf16_f32 v70, v44, v45\n\t" \
  "v_cvt_pk_bf16_f32 v71, v46, v47\n\t" \
  "buffer_store_dwordx4 v[68:71], v72, s[20:23], s50 offen offset:1024 sc1\n\t" \
  "s_add_u32 s50, s50, 524288\n\t" \
  "v_max3_f32 v79, |v32|, |v33|, |v34|\n\t" \
  "v_max3_f32 v79, v79, |v35|, |v36|\n\t" \
  "v_max3_f32 v79, v79, |v37|, |v38|\n\t" \
  "v_max3_f32 v79, v79, |v39|, |v40|\n\t" \
  "v_max3_f32 v79, v79, |v41|, |v42|\n\t" \
  "v_max3_f32 v79, v79, |v43|, |v44|\n\t" \
  "v_max3_f32 v79, v79, |v45|, |v46|\n\t" \
  "v_max_f32 v79, v79, |v47|\n\t" \
  "s_nop 1\n\t" \
  "v_max_f32_dpp v79, v79, v79 row_ror:1 row_mask:0xf bank_mask:0xf\n\t" \
  "s_nop 1\n\t" \
  "v_max_f32_dpp v79, v79, v79 row_ror:2 row_mask:0xf bank_mask:0xf\n\t" \
  "s_nop 1\n\t" \
  "v_max_f32_dpp v79, v79, v79 row_ror:4 row_mask:0xf bank_mask:0xf\n\t" \
  "s_nop 1\n\t" \
  "v_max_f32_dpp v79, v79, v79 row_ror:8 row_mask:0xf bank_mask:0xf\n\t" \
  "s_nop 1\n\t" \
  "v_max_f32_dpp v79, v79, v79 row_bcast:15 row_mask:0xa bank_mask:0xf\n\t" \
  "s_nop 1\n\t" \
  "v_max_f32_dpp v79, v79, v79 row_bcast:31 row_mask:0xc bank_mask:0xf\n\t" \
  "v_mul_f32 v79, 0x3f810000, v79\n\t" \
  "s_mov_b64 exec, s[56:57]\n\t" \
  "ds_write_b32 v80, v79 offset:0\n\t" \
  "s_mov_b64 exec, -1\n\t" \
  "v_add_u32 v74, 73728, v72\n\t" \
  "s_waitcnt lgkmcnt(4)\n\t" \
  "v_mfma_f32_16x16x32_bf16 v[48:51], v[16:19], a[0:3], 0\n\t" \
  "ds_read_b128 v[8:11], v73 offset:3072\n\t" \
  "v_mfma_f32_16x16x32_bf16 v[52:55], v[16:19], a[4:7], 0\n\t" \
  "ds_read_b128 v[12:15], v73 offset:21504\n\t" \
  "v_mfma_f32_16x16x32_bf16 v[56:59], v[20:23], a[0:3], 0\n\t" \
  "v_mfma_f32_16x16x32_bf16 v[60:63], v[20:23], a[4:7], 0\n\t" \
  "s_waitcnt lgkmcnt(4)\n\t" \
  "v_mfma_f32_16x16x32_bf16 v[48:51], v[24:27], a[8:11], v[48:51]\n\t" \
  "ds_read_b128 v[16:19], v73 offset:4096\n\t" \
  "v_mfma_f32_16x16x32_bf16 v[52:55], v[24:27], a[12:15], v[52:55]\n\t" \
  "ds_read_b128 v[20:23], v73 offset:22528\n\t" \
  "v_mfma_f32_16x16x32_bf16 v[56:59], v[28:31], a[8:11], v[56:59]\n\t" \
  "v_mfma_f32_16x16x32_bf16 v[60:63], v[28:31], a[12:15], v[60:63]\n\t" \
  "s_waitcnt lgkmcnt(4)\n\t" \
  "v_mfma_f32_16x16x32_bf16 v[48:51], v[0:3], a[16:19], v[48:51]\n\t" \
  "ds_read_b128 v[24:27], v73 offset:5120\n\t" \
  "v_mfma_f32_16x16x32_bf16 v[52:55], v[0:3], a[20:23], v[52:55]\n\t" \
  "ds_read_b128 v[28:31], v73 offset:23552\n\t" \
  "v_mfma_f32_16x16x32_bf16 v[56:59], v[4:7], a[16:19], v[56:59]\n\t" \
  "v_mfma_f32_16x16x32_bf16 v[60:63], v[4:7], a[20:23], v[60:63]\n\t" \
  "s_waitcnt lgkmcnt(4)\n\t" \
  "v_mfma_f32_16x16x32_bf16 v[48:51], v[8:11], a[24:27], v[48:51]\n\t" \
  "ds_read_b128 v[0:3], v73 offset:6144\n\t" \
  "v_mfma_f32_16x16x32_bf16 v[52:55], v[8:11], a[28:31], v[52:55]\n\t" \
  "ds_read_b128 v[4:7], v73 offset:24576\n\t" \
  "v_mfma_f32_16x16x32_bf16 v[56:59], v[12:15], a[24:27], v[56:59]\n\t" \
  "v_mfma_f32_16x16x32_bf16 v[60:63], v[12:15], a[28:31], v[60:63]\n\t" \
  "s_waitcnt lgkmcnt(4)\n\t" \
  "v_mfma_f32_16x16x32_bf16 v[48:51], v[16:19], a[32:35], v[48:51]\n\t" \
  "ds_read_b128 v[8:11], v73 offset:7168\n\t" \
  "v_mfma_f32_16x16x32_bf16 v[52:55], v[16:19], a[36:39], v[52:55]\n\t" \
  "ds_read_b128 v[12:15], v73 offset:25600\n\t" \
  "v_mfma_f32_16x16x32_bf16 v[56:59], v[20:23], a[32:35], v[56:59]\n\t" \
  "v_mfma_f32_16x16x32_bf16 v[60:63], v[20:23], a[36:39], v[60:63]\n\t" \
  "s_waitcnt lgkmcnt(4)\n\t" \
  "v_mfma_f32_16x16x32_bf16 v[48:51], v[24:27], a[40:43], v[48:51]\n\t" \
  "ds_read_b128 v[16:19], v73 offset:8192\n\t" \
  "v_mfma_f32_16x16x32_bf16 v[52:55], v[24:27], a[44:47], v[52:55]\n\t" \
  "ds_read_b128 v[20:23], v73 offset:26624\n\t" \
  "v_mfma_f32_16x16x32_bf16 v[56:59], v[28:31], a[40:43], v[56:59]\n\t" \
  "v_mfma_f32_16x16x32_bf16 v[60:63], v[28:31], a[44:47], v[60:63]\n\t" \
  "s_waitcnt lgkmcnt(4)\n\t" \
  "v_mfma_f32_16x16x32_bf16 v[48:51], v[0:3], a[48:51], v[48:51]\n\t" \
  "ds_read_b128 v[24:27], v73 offset:9216\n\t" \
  "v_mfma_f32_16x16x32_bf16 v[52:55], v[0:3], a[52:55], v[52:55]\n\t" \
  "ds_read_b128 v[28:31], v73 offset:27648\n\t" \
  "v_mfma_f32_16x16x32_bf16 v[56:59], v[4:7], a[48:51], v[56:59]\n\t" \
  "v_mfma_f32_16x16x32_bf16 v[60:63], v[4:7], a[52:55], v[60:63]\n\t" \
  "s_waitcnt lgkmcnt(4)\n\t" \
  "v_mfma_f32_16x16x32_bf16 v[48:51], v[8:11], a[56:59], v[48:51]\n\t" \
  "ds_read_b128 v[0:3], v73 offset:10240\n\t" \
  "v_mfma_f32_16x16x32_bf16 v[52:55], v[8:11], a[60:63], v[52:55]\n\t" \
  "ds_read_b128 v[4:7], v73 offset:28672\n\t" \
  "v_mfma_f32_16x16x32_bf16 v[56:59], v[12:15], a[56:59], v[56:59]\n\t" \
  "v_mfma_f32_16x16x32_bf16 v[60:63], v[12:15], a[60:63], v[60:63]\n\t" \
  "s_waitcnt lgkmcnt(4)\n\t" \
  "v_mfma_f32_16x16x32_bf16 v[48:51], v[16:19], a[64:67], v[48:51]\n\t" \
  "ds_read_b128 v[8:11], v73 offset:11264\n\t" \
  "v_mfma_f32_16x16x32_bf16 v[52:55], v[16:19], a[68:71], v[52:55]\n\t" \
  "ds_read_b128 v[12:15], v73 offset:29696\n\t" \
  "v_mfma_f32_16x16x32_bf16 v[56:59], v[20:23], a[64:67], v[56:59]\n\t" \
  "v_mfma_f32_16x16x32_bf16 v[60:63], v[20:23], a[68:71], v[60:63]\n\t" \
  "s_waitcnt vmcnt(0) lgkmcnt(0)\n\t" \
  "s_barrier\n\t" \
  "s_waitcnt lgkmcnt(4)\n\t" \
  "v_mfma_f32_16x16x32_bf16 v[48:51], v[24:27], a[72:75], v[48:51]\n\t" \
  "ds_read_b128 v[16:19], v73 offset:12288\n\t" \
  "v_mov_b32 v77, 147456\n\t" \
  "v_mfma_f32_16x16x32_bf16 v[52:55], v[24:27], a[76:79], v[52:55]\n\t" \
  "ds_read_b128 v[20:23], v73 offset:30720\n\t" \
  "ds_read_b128 v[84:87], v77\n\t" \
  "v_mfma_f32_16x16x32_bf16 v[56:59], v[28:31], a[72:75], v[56:59]\n\t" \
  "s_waitcnt lgkmcnt(0)\n\t" \
  "v_max3_f32 v84, v84, v85, v86\n\t" \
  "v_mfma_f32_16x16x32_bf16 v[60:63], v[28:31], a[76:79], v[60:63]\n\t" \
  "v_max_f32 v84, v84, v87\n\t" \
  "v_or_b32 v77, 1, v84\n\t" \
  "s_waitcnt lgkmcnt(4)\n\t" \
  "v_mfma_f32_16x16x32_bf16 v[48:51], v[0:3], a[80:83], v[48:51]\n\t" \
  "ds_read_b128 v[24:27], v73 offset:13312\n\t" \
  "s_mov_b64 exec, s[54:55]\n\t" \
  "buffer_store_dword v77, off, s[20:23], s51 sc1\n\t" \
  "s_mov_b64 exec, -1\n\t" \
  "v_mfma_f32_16x16x32_bf16 v[52:55], v[0:3], a[84:87], v[52:55]\n\t" \
  "ds_read_b128 v[28:31], v73 offset:31744\n\t" \
  "s_add_u32 s51, s51, 1024\n\t" \
  "v_mfma_f32_16x16x32_bf16 v[56:59], v[4:7], a[80:83], v[56:59]\n\t" \
  "s_add_u32 s48, s58, 0\n\t" \
  "s_mov_b32 m0, s48\n\t" \
  "v_mfma_f32_16x16x32_bf16 v[60:63], v[4:7], a[84:87], v[60:63]\n\t" \
  "s_nop 0\n\t" \
  "global_load_lds_dwordx4 v72, s[46:47] offset:-4096 sc1\n\t" \
  "s_waitcnt lgkmcnt(4)\n\t" \
  "v_mfma_f32_16x16x32_bf16 v[48:51], v[8:11], a[88:91], v[48:51]\n\t" \
  "ds_read_b128 v[0:3], v73 offset:14336\n\t" \
  "global_load_lds_dwordx4 v72, s[46:47] offset:-3072 sc1\n\t" \
  "v_mfma_f32_16x16x32_bf16 v[52:55], v[8:11], a[92:95], v[52:55]\n\t" \
  "ds_read_b128 v[4:7], v73 offset:32768\n\t" \
  "global_load_lds_dwordx4 v72, s[46:47] offset:-2048 sc1\n\t" \
  "v_mfma_f32_16x16x32_bf16 v[56:59], v[12:15], a[88:91], v[56:59]\n\t" \
  "global_load_lds_dwordx4 v72, s[46:47] offset:-1024 sc1\n\t" \
  "global_load_lds_dwordx4 v72, s[46:47] offset:0 sc1\n\t" \
  "v_mfma_f32_16x16x32_bf16 v[60:63], v[12:15], a[92:95], v[60:63]\n\t" \
  "global_load_lds_dwordx4 v72, s[46:47] offset:1024 sc1\n\t" \
  "global_load_lds_dwordx4 v72, s[46:47] offset:2048 sc1\n\t" \
  "s_waitcnt lgkmcnt(4)\n\t" \
  "v_mfma_f32_16x16x32_bf16 v[48:51], v[16:19], a[96:99], v[48:51]\n\t" \
  "ds_read_b128 v[8:11], v73 offset:15360\n\t" \
  "global_load_lds_dwordx4 v72, s[46:47] offset:3072 sc1\n\t" \
  "v_mfma_f32_16x16x32_bf16 v[52:55], v[16:19], a[100:103], v[52:55]\n\t" \
  "ds_read_b128 v[12:15], v73 offset:33792\n\t" \
  "s_add_u32 m0, s48, 4096\n\t" \
  "v_mfma_f32_16x16x32_bf16 v[56:59], v[20:23], a[96:99], v[56:59]\n\t" \
  "s_nop 0\n\t" \
  "global_load_lds_dwordx4 v78, s[46:47] sc1\n\t" \
  "v_mfma_f32_16x16x32_bf16 v[60:63], v[20:23], a[100:103], v[60:63]\n\t" \
  "s_add_u32 s46, s46, 73728\n\t" \
  "s_addc_u32 s47, s47, 0\n\t" \
  "s_waitcnt lgkmcnt(4)\n\t" \
  "v_mfma_f32_16x16x32_bf16 v[48:51], v[24:27], a[104:107], v[48:51]\n\t" \
  "ds_read_b128 v[16:19], v73 offset:16384\n\t" \
  "v_mfma_f32_16x16x32_bf16 v[52:55], v[24:27], a[108:111], v[52:55]\n\t" \
  "ds_read_b128 v[20:23], v73 offset:34816\n\t" \
  "v_mfma_f32_16x16x32_bf16 v[56:59], v[28:31], a[104:107], v[56:59]\n\t" \
  "v_mfma_f32_16x16x32_bf16 v[60:63], v[28:31], a[108:111], v[60:63]\n\t" \
  "s_waitcnt lgkmcnt(4)\n\t" \
  "v_mfma_f32_16x16x32_bf16 v[48:51], v[0:3], a[112:115], v[48:51]\n\t" \
  "ds_read_b128 v[24:27], v73 offset:17408\n\t" \
  "v_mfma_f32_16x16x32_bf16 v[52:55], v[0:3], a[116:119], v[52:55]\n\t" \
  "ds_read_b128 v[28:31], v73 offset:35840\n\t" \
  "v_mfma_f32_16x16x32_bf16 v[56:59], v[4:7], a[112:115], v[56:59]\n\t" \
  "v_mfma_f32_16x16x32_bf16 v[60:63], v[4:7], a[116:119], v[60:63]\n\t" \
  "s_waitcnt lgkmcnt(4)\n\t" \
  "v_mfma_f32_16x16x32_bf16 v[48:51], v[8:11], a[120:123], v[48:51]\n\t" \
  "ds_read_b128 v[0:3], v74 offset:0\n\t" \
  "v_mfma_f32_16x16x32_bf16 v[52:55], v[8:11], a[124:127], v[52:55]\n\t" \
  "ds_read_b128 v[4:7], v74 offset:18432\n\t" \
  "v_mfma_f32_16x16x32_bf16 v[56:59], v[12:15], a[120:123], v[56:59]\n\t" \
  "v_mfma_f32_16x16x32_bf16 v[60:63], v[12:15], a[124:127], v[60:63]\n\t" \
  "s_waitcnt lgkmcnt(4)\n\t" \
  "v_mfma_f32_16x16x32_bf16 v[48:51], v[16:19], a[128:131], v[48:51]\n\t" \
  "ds_read_b128 v[8:11], v74 offset:1024\n\t" \
  "v_mfma_f32_16x16x32_bf16 v[52:55], v[16:19], a[132:135], v[52:55]\n\t" \
  "ds_read_b128 v[12:15], v74 offset:19456\n\t" \
  "v_mfma_f32_16x16x32_bf16 v[56:59], v[20:23], a[128:131], v[56:59]\n\t" \
  "v_mfma_f32_16x16x32_bf16 v[60:63], v[20:23], a[132:135], v[60:63]\n\t" \
  "s_waitcnt lgkmcnt(4)\n\t" \
  "v_mfma_f32_16x16x32_bf16 v[48:51], v[24:27], a[136:139], v[48:51]\n\t" \
  "ds_read_b128 v[16:19], v74 offset:2048\n\t" \
  "v_mfma_f32_16x16x32_bf16 v[52:55], v[24:27], a[140:143], v[52:55]\n\t" \
  "ds_read_b128 v[20:23], v74 offset:20480\n\t" \
  "v_mfma_f32_16x16x32_bf16 v[56:59], v[28:31], a[136:139], v[56:59]\n\t" \
  "v_mfma_f32_16x16x32_bf16 v[60:63], v[28:31], a[140:143], v[60:63]\n\t" \
  "v_mov_b32 v73, v74\n\t" \
  "s_cmp_eq_u32 s28, 0\n\t" \
  "s_cbranch_scc0 Lng_%=\n\t" \
  "v_mfma_f32_16x16x32_bf16 v[64:67], a[0:3], a[0:3], 0\n\t" \
  "v_mfma_f32_16x16x32_bf16 v[68:71], a[4:7], a[4:7], 0\n\t" \
  "v_mfma_f32_16x16x32_bf16 v[64:67], a[8:11], a[8:11], v[64:67]\n\t" \
  "v_mfma_f32_16x16x32_bf16 v[68:71], a[12:15], a[12:15], v[68:71]\n\t" \
  "v_mfma_f32_16x16x32_bf16 v[64:67], a[16:19], a[16:19], v[64:67]\n\t" \
  "v_mfma_f32_16x16x32_bf16 v[68:71], a[20:23], a[20:23], v[68:71]\n\t" \
  "v_mfma_f32_16x16x32_bf16 v[64:67], a[24:27], a[24:27], v[64:67]\n\t" \
  "v_mfma_f32_16x16x32_bf16 v[68:71], a[28:31], a[28:31], v[68:71]\n\t" \
  "v_mfma_f32_16x16x32_bf16 v[64:67], a[32:35], a[32:35], v[64:67]\n\t" \
  "v_mfma_f32_16x16x32_bf16 v[68:71], a[36:39], a[36:39], v[68:71]\n\t" \
  "v_mfma_f32_16x16x32_bf16 v[64:67], a[40:43], a[40:43], v[64:67]\n\t" \
  "v_mfma_f32_16x16x32_bf16 v[68:71], a[44:47], a[44:47], v[68:71]\n\t" \
  "v_mfma_f32_16x16x32_bf16 v[64:67], a[48:51], a[48:51], v[64:67]\n\t" \
  "v_mfma_f32_16x16x32_bf16 v[68:71], a[52:55], a[52:55], v[68:71]\n\t" \
  "v_mfma_f32_16x16x32_bf16 v[64:67], a[56:59], a[56:59], v[64:67]\n\t" \
  "v_mfma_f32_16x16x32_bf16 v[68:71], a[60:63], a[60:63], v[68:71]\n\t" \
  "v_mfma_f32_16x16x32_bf16 v[64:67], a[64:67], a[64:67], v[64:67]\n\t" \
  "v_mfma_f32_16x16x32_bf16 v[68:71], a[68:71], a[68:71], v[68:71]\n\t" \
  "v_mfma_f32_16x16x32_bf16 v[64:67], a[72:75], a[72:75], v[64:67]\n\t" \
  "v_mfma_f32_16x16x32_bf16 v[68:71], a[76:79], a[76:79], v[68:71]\n\t" \
  "v_mfma_f32_16x16x32_bf16 v[64:67], a[80:83], a[80:83], v[64:67]\n\t" \
  "v_mfma_f32_16x16x32_bf16 v[68:71], a[84:87], a[84:87], v[68:71]\n\t" \
  "v_mfma_f32_16x16x32_bf16 v[64:67], a[88:91], a[88:91], v[64:67]\n\t" \
  "v_mfma_f32_16x16x32_bf16 v[68:71], a[92:95], a[92:95], v[68:71]\n\t" \
  "v_mfma_f32_16x16x32_bf16 v[64:67], a[96:99], a[96:99], v[64:67]\n\t" \
  "v_mfma_f32_16x16x32_bf16 v[68:71], a[100:103], a[100:103], v[68:71]\n\t" \
  "v_mfma_f32_16x16x32_bf16 v[64:67], a[104:107], a[104:107], v[64:67]\n\t" \
  "v_mfma_f32_16x16x32_bf16 v[68:71], a[108:111], a[108:111], v[68:71]\n\t" \
  "v_mfma_f32_16x16x32_bf16 v[64:67], a[112:115], a[112:115], v[64:67]\n\t" \
  "v_mfma_f32_16x16x32_bf16 v[68:71], a[116:119], a[116:119], v[68:71]\n\t" \
  "v_mfma_f32_16x16x32_bf16 v[64:67], a[120:123], a[120:123], v[64:67]\n\t" \
  "v_mfma_f32_16x16x32_bf16 v[68:71], a[124:127], a[124:127], v[68:71]\n\t" \
  "v_mfma_f32_16x16x32_bf16 v[64:67], a[128:131], a[128:131], v[64:67]\n\t" \
  "v_mfma_f32_16x16x32_bf16 v[68:71], a[132:135], a[132:135], v[68:71]\n\t" \
  "v_mfma_f32_16x16x32_bf16 v[64:67], a[136:139], a[136:139], v[64:67]\n\t" \
  "v_mfma_f32_16x16x32_bf16 v[68:71], a[140:143], a[140:143], v[68:71]\n\t" \
  "s_lshl_b32 s40, s27, 2\n\t" \
  "s_add_u32 s40, s40, s29\n\t" \
  "s_lshl_b32 s40, s40, 2\n\t" \
  "s_add_u32 s40, s40, 2160640\n\t" \
  "s_nop 5\n\t" \
  "v_max3_f32 v79, v64, v65, v66\n\t" \
  "v_max3_f32 v79, v79, v67, v68\n\t" \
  "v_max3_f32 v79, v79, v69, v70\n\t" \
  "v_max_f32 v79, v79, v71\n\t" \
  "s_nop 1\n\t" \
  "v_max_f32_dpp v79, v79, v79 row_ror:1 row_mask:0xf bank_mask:0xf\n\t" \
  "s_nop 1\n\t" \
  "v_max_f32_dpp v79, v79, v79 row_ror:2 row_mask:0xf bank_mask:0xf\n\t" \
  "s_nop 1\n\t" \
  "v_max_f32_dpp v79, v79, v79 row_ror:4 row_mask:0xf bank_mask:0xf\n\t" \
  "s_nop 1\n\t" \
  "v_max_f32_dpp v79, v79, v79 row_ror:8 row_mask:0xf bank_mask:0xf\n\t" \
  "s_nop 1\n\t" \
  "v_max_f32_dpp v79, v79, v79 row_bcast:15 row_mask:0xa bank_mask:0xf\n\t" \
  "s_nop 1\n\t" \
  "v_max_f32_dpp v79, v79, v79 row_bcast:31 row_mask:0xc bank_mask:0xf\n\t" \
  "v_or_b32 v79, 1, v79\n\t" \
  "s_mov_b64 exec, s[56:57]\n\t" \
  "buffer_store_dword v79, off, s[20:23], s40 sc1\n\t" \
  "s_mov_b64 exec, -1\n\t" \
  "Lng_%=:\n\t" \
  "v_add_u32 v74, 110592, v72\n\t" \
  "s_waitcnt lgkmcnt(4)\n\t" \
  "v_mfma_f32_16x16x32_bf16 v[32:35], v[0:3], a[0:3], 0\n\t" \
  "ds_read_b128 v[24:27], v73 offset:3072\n\t" \
  "v_mfma_f32_16x16x32_bf16 v[36:39], v[0:3], a[4:7], 0\n\t" \
  "ds_read_b128 v[28:31], v73 offset:21504\n\t" \
  "v_mfma_f32_16x16x32_bf16 v[40:43], v[4:7], a[0:3], 0\n\t" \
  "v_mfma_f32_16x16x32_bf16 v[44:47], v[4:7], a[4:7], 0\n\t" \
  "s_waitcnt lgkmcnt(4)\n\t" \
  "v_mfma_f32_16x16x32_bf16 v[32:35], v[8:11], a[8:11], v[32:35]\n\t" \
  "ds_read_b128 v[0:3], v73 offset:4096\n\t" \
  "v_cvt_pk_bf16_f32 v64, v48, v49\n\t" \
  "v_mfma_f32_16x16x32_bf16 v[36:39], v[8:11], a[12:15], v[36:39]\n\t" \
  "ds_read_b128 v[4:7], v73 offset:22528\n\t" \
  "v_cvt_pk_bf16_f32 v65, v50, v51\n\t" \
  "v_mfma_f32_16x16x32_bf16 v[40:43], v[12:15], a[8:11], v[40:43]\n\t" \
  "v_cvt_pk_bf16_f32 v66, v56, v57\n\t" \
  "v_cvt_pk_bf16_f32 v67, v58, v59\n\t" \
  "v_mfma_f32_16x16x32_bf16 v[44:47], v[12:15], a[12:15], v[44:47]\n\t" \
  "buffer_store_dwordx4 v[64:67], v72, s[20:23], s50 offen offset:0 sc1\n\t" \
  "v_cvt_pk_bf16_f32 v68, v52, v53\n\t" \
  "s_waitcnt lgkmcnt(4)\n\t" \
  "v_mfma_f32_16x16x32_bf16 v[32:35], v[16:19], a[16:19], v[32:35]\n\t" \
  "ds_read_b128 v[8:11], v73 offset:5120\n\t" \
  "v_cvt_pk_bf16_f32 v69, v54, v55\n\t" \
  "v_mfma_f32_16x16x32_bf16 v[36:39], v[16:19], a[20:23], v[36:39]\n\t" \
  "ds_read_b128 v[12:15], v73 offset:23552\n\t" \
  "v_cvt_pk_bf16_f32 v70, v60, v61\n\t" \
  "v_mfma_f32_16x16x32_bf16 v[40:43], v[20:23], a[16:19], v[40:43]\n\t" \
  "v_cvt_pk_bf16_f32 v71, v62, v63\n\t" \
  "buffer_store_dwordx4 v[68:71], v72, s[20:23], s50 offen offset:1024 sc1\n\t" \
  "v_mfma_f32_16x16x32_bf16 v[44:47], v[20:23], a[20:23], v[44:47]\n\t" \
  "s_add_u32 s50, s50, 524288\n\t" \
  "v_max3_f32 v79, |v48|, |v49|, |v50|\n\t" \
  "s_waitcnt lgkmcnt(4)\n\t" \
  "v_mfma_f32_16x16x32_bf16 v[32:35], v[24:27], a[24:27], v[32:35]\n\t" \
  "ds_read_b128 v[16:19], v73 offset:6144\n\t" \
  "v_max3_f32 v79, v79, |v51|, |v52|\n\t" \
  "v_mfma_f32_16x16x32_bf16 v[36:39], v[24:27], a[28:31], v[36:39]\n\t" \
  "ds_read_b128 v[20:23], v73 offset:24576\n\t" \
  "v_max3_f32 v79, v79, |v53|, |v54|\n\t" \
  "v_mfma_f32_16x16x32_bf16 v[40:43], v[28:31], a[24:27], v[40:43]\n\t" \
  "v_max3_f32 v79, v79, |v55|, |v56|\n\t" \
  "v_max3_f32 v79, v79, |v57|, |v58|\n\t" \
  "v_mfma_f32_16x16x32_bf16 v[44:47], v[28:31], a[28:31], v[44:47]\n\t" \
  "v_max3_f32 v79, v79, |v59|, |v60|\n\t" \
  "v_max3_f32 v79, v79, |v61|, |v62|\n\t" \
  "s_waitcnt lgkmcnt(4)\n\t" \
  "v_mfma_f32_16x16x32_bf16 v[32:35], v[0:3], a[32:35], v[32:35]\n\t" \
  "ds_read_b128 v[24:27], v73 offset:7168\n\t" \
  "v_max_f32 v79, v79, |v63|\n\t" \
  "v_mfma_f32_16x16x32_bf16 v[36:39], v[0:3], a[36:39], v[36:39]\n\t" \
  "ds_read_b128 v[28:31], v73 offset:25600\n\t" \
  "v_max_f32_dpp v79, v79, v79 row_ror:1 row_mask:0xf bank_mask:0xf\n\t" \
  "v_mfma_f32_16x16x32_bf16 v[40:43], v[4:7], a[32:35], v[40:43]\n\t" \
  "s_nop 0\n\t" \
  "v_max_f32_dpp v79, v79, v79 row_ror:2 row_mask:0xf bank_mask:0xf\n\t" \
  "s_nop 1\n\t" \
  "v_max_f32_dpp v79, v79, v79 row_ror:4 row_mask:0xf bank_mask:0xf\n\t" \
  "v_mfma_f32_16x16x32_bf16 v[44:47], v[4:7], a[36:39], v[44:47]\n\t" \
  "s_nop 0\n\t" \
  "v_max_f32_dpp v79, v79, v79 row_ror:8 row_mask:0xf bank_mask:0xf\n\t" \
  "s_nop 1\n\t" \
  "v_max_f32_dpp v79, v79, v79 row_bcast:15 row_mask:0xa bank_mask:0xf\n\t" \
  "s_waitcnt lgkmcnt(4)\n\t" \
  "v_mfma_f32_16x16x32_bf16 v[32:35], v[8:11], a[40:43], v[32:35]\n\t" \
  "ds_read_b128 v[0:3], v73 offset:8192\n\t" \
  "v_max_f32_dpp v79, v79, v79 row_bcast:31 row_mask:0xc bank_mask:0xf\n\t" \
  "v_mfma_f32_16x16x32_bf16 v[36:39], v[8:11], a[44:47], v[36:39]\n\t" \
  "ds_read_b128 v[4:7], v73 offset:26624\n\t" \
  "v_mul_f32 v79, 0x3f810000, v79\n\t" \
  "v_mfma_f32_16x16x32_bf16 v[40:43], v[12:15], a[40:43], v[40:43]\n\t" \
  "s_mov_b64 exec, s[56:57]\n\t" \
  "ds_write_b32 v80, v79 offset:16\n\t" \
  "s_mov_b64 exec, -1\n\t" \
  "v_mfma_f32_16x16x32_bf16 v[44:47], v[12:15], a[44:47], v[44:47]\n\t" \
  "s_waitcnt lgkmcnt(4)\n\t" \
  "v_mfma_f32_16x16x32_bf16 v[32:35], v[16:19], a[48:51], v[32:35]\n\t" \
  "ds_read_b128 v[8:11], v73 offset:9216\n\t" \
  "v_mfma_f32_16x16x32_bf16 v[36:39], v[16:19], a[52:55], v[36:39]\n\t" \
  "ds_read_b128 v[12:15], v73 offset:27648\n\t" \
  "v_mfma_f32_16x16x32_bf16 v[40:43], v[20:23], a[48:51], v[40:43]\n\t" \
  "v_mfma_f32_16x16x32_bf16 v[44:47], v[20:23], a[52:55], v[44:47]\n\t" \
  "s_waitcnt lgkmcnt(4)\n\t" \
  "v_mfma_f32_16x16x32_bf16 v[32:35], v[24:27], a[56:59], v[32:35]\n\t" \
  "ds_read_b128 v[16:19], v73 offset:10240\n\t" \
  "v_mfma_f32_16x16x32_bf16 v[36:39], v[24:27], a[60:63], v[36:39]\n\t" \
  "ds_read_b128 v[20:23], v73 offset:28672\n\t" \
  "v_mfma_f32_16x16x32_bf16 v[40:43], v[28:31], a[56:59], v[40:43]\n\t" \
  "v_mfma_f32_16x16x32_bf16 v[44:47], v[28:31], a[60:63], v[44:47]\n\t" \
  "s_waitcnt lgkmcnt(4)\n\t" \
  "v_mfma_f32_16x16x32_bf16 v[32:35], v[0:3], a[64:67], v[32:35]\n\t" \
  "ds_read_b128 v[24:27], v73 offset:11264\n\t" \
  "v_mfma_f32_16x16x32_bf16 v[36:39], v[0:3], a[68:71], v[36:39]\n\t" \
  "ds_read_b128 v[28:31], v73 offset:29696\n\t" \
  "v_mfma_f32_16x16x32_bf16 v[40:43], v[4:7], a[64:67], v[40:43]\n\t" \
  "v_mfma_f32_16x16x32_bf16 v[44:47], v[4:7], a[68:71], v[44:47]\n\t" \
  "s_waitcnt vmcnt(11)\n\t" \
  "s_barrier\n\t" \
  "s_waitcnt lgkmcnt(4)\n\t" \
  "v_mfma_f32_16x16x32_bf16 v[32:35], v[8:11], a[72:75], v[32:35]\n\t" \
  "ds_read_b128 v[0:3], v73 offset:12288\n\t" \
  "s_add_u32 s48, s58, 36864\n\t" \
  "v_mfma_f32_16x16x32_bf16 v[36:39], v[8:11], a[76:79], v[36:39]\n\t" \
  "ds_read_b128 v[4:7], v73 offset:30720\n\t" \
  "s_mov_b32 m0, s48\n\t" \
  "v_mfma_f32_16x16x32_bf16 v[40:43], v[12:15], a[72:75], v[40:43]\n\t" \
  "s_nop 0\n\t" \
  "global_load_lds_dwordx4 v72, s[46:47] offset:-4096 sc1\n\t" \
  "v_mfma_f32_16x16x32_bf16 v[44:47], v[12:15], a[76:79], v[44:47]\n\t" \
  "global_load_lds_dwordx4 v72, s[46:47] offset:-3072 sc1\n\t" \
  "global_load_lds_dwordx4 v72, s[46:47] offset:-2048 sc1\n\t" \
  "s_waitcnt lgkmcnt(4)\n\t" \
  "v_mfma_f32_16x16x32_bf16 v[32:35], v[16:19], a[80:83], v[32:35]\n\t" \
  "ds_read_b128 v[8:11], v73 offset:13312\n\t" \
  "global_load_lds_dwordx4 v72, s[46:47] offset:-1024 sc1\n\t" \
  "v_mfma_f32_16x16x32_bf16 v[36:39], v[16:19], a[84:87], v[36:39]\n\t" \
  "ds_read_b128 v[12:15], v73 offset:31744\n\t" \
  "global_load_lds_dwordx4 v72, s[46:47] offset:0 sc1\n\t" \
  "v_mfma_f32_16x16x32_bf16 v[40:43], v[20:23], a[80:83], v[40:43]\n\t" \
  "global_load_lds_dwordx4 v72, s[46:47] offset:1024 sc1\n\t" \
  "global_load_lds_dwordx4 v72, s[46:47] offset:2048 sc1\n\t" \
  "v_mfma_f32_16x16x32_bf16 v[44:47], v[20:23], a[84:87], v[44:47]\n\t" \
  "global_load_lds_dwordx4 v72, s[46:47] offset:3072 sc1\n\t" \
  "s_add_u32 m0, s48, 4096\n\t" \
  "s_waitcnt lgkmcnt(4)\n\t" \
  "v_mfma_f32_16x16x32_bf16 v[32:35], v[24:27], a[88:91], v[32:35]\n\t" \
  "ds_read_b128 v[16:19], v73 offset:14336\n\t" \
  "s_nop 0\n\t" \
  "v_mfma_f32_16x16x32_bf16 v[36:39], v[24:27], a[92:95], v[36:39]\n\t" \
  "ds_read_b128 v[20:23], v73 offset:32768\n\t" \
  "global_load_lds_dwordx4 v78, s[46:47] sc1\n\t" \
  "v_mfma_f32_16x16x32_bf16 v[40:43], v[28:31], a[88:91], v[40:43]\n\t" \
  "s_add_u32 s46, s46, 73728\n\t" \
  "s_addc_u32 s47, s47, 0\n\t" \
  "v_mfma_f32_16x16x32_bf16 v[44:47], v[28:31], a[92:95], v[44:47]\n\t" \
  "s_waitcnt lgkmcnt(4)\n\t" \
  "v_mfma_f32_16x16x32_bf16 v[32:35], v[0:3], a[96:99], v[32:35]\n\t" \
  "ds_read_b128 v[24:27], v73 offset:15360\n\t" \
  "v_mfma_f32_16x16x32_bf16 v[36:39], v[0:3], a[100:103], v[36:39]\n\t" \
  "ds_read_b128 v[28:31], v73 offset:33792\n\t" \
  "v_mfma_f32_16x16x32_bf16 v[40:43], v[4:7], a[96:99], v[40:43]\n\t" \
  "v_mfma_f32_16x16x32_bf16 v[44:47], v[4:7], a[100:103], v[44:47]\n\t" \
  "s_waitcnt lgkmcnt(4)\n\t" \
  "v_mfma_f32_16x16x32_bf16 v[32:35], v[8:11], a[104:107], v[32:35]\n\t" \
  "ds_read_b128 v[0:3], v73 offset:16384\n\t" \
  "v_mfma_f32_16x16x32_bf16 v[36:39], v[8:11], a[108:111], v[36:39]\n\t" \
  "ds_read_b128 v[4:7], v73 offset:34816\n\t" \
  "v_mfma_f32_16x16x32_bf16 v[40:43], v[12:15], a[104:107], v[40:43]\n\t" \
  "v_mfma_f32_16x16x32_bf16 v[44:47], v[12:15], a[108:111], v[44:47]\n\t" \
  "s_waitcnt lgkmcnt(4)\n\t" \
  "v_mfma_f32_16x16x32_bf16 v[32:35], v[16:19], a[112:115], v[32:35]\n\t" \
  "ds_read_b128 v[8:11], v73 offset:17408\n\t" \
  "v_mfma_f32_16x16x32_bf16 v[36:39], v[16:19], a[116:119], v[36:39]\n\t" \
  "ds_read_b128 v[12:15], v73 offset:35840\n\t" \
  "v_mfma_f32_16x16x32_bf16 v[40:43], v[20:23], a[112:115], v[40:43]\n\t" \
  "v_mfma_f32_16x16x32_bf16 v[44:47], v[20:23], a[116:119], v[44:47]\n\t" \
  "s_waitcnt lgkmcnt(4)\n\t" \
  "v_mfma_f32_16x16x32_bf16 v[32:35], v[24:27], a[120:123], v[32:35]\n\t" \
  "ds_read_b128 v[16:19], v74 offset:0\n\t" \
  "v_mfma_f32_16x16x32_bf16 v[36:39], v[24:27], a[124:127], v[36:39]\n\t" \
  "ds_read_b128 v[20:23], v74 offset:18432\n\t" \
  "v_mfma_f32_16x16x32_bf16 v[40:43], v[28:31], a[120:123], v[40:43]\n\t" \
  "v_mfma_f32_16x16x32_bf16 v[44:47], v[28:31], a[124:127], v[44:47]\n\t" \
  "s_waitcnt lgkmcnt(4)\n\t" \
  "v_mfma_f32_16x16x32_bf16 v[32:35], v[0:3], a[128:131], v[32:35]\n\t" \
  "ds_read_b128 v[24:27], v74 offset:1024\n\t" \
  "v_mfma_f32_16x16x32_bf16 v[36:39], v[0:3], a[132:135], v[36:39]\n\t" \
  "ds_read_b128 v[28:31], v74 offset:19456\n\t" \
  "v_mfma_f32_16x16x32_bf16 v[40:43], v[4:7], a[128:131], v[40:43]\n\t" \
  "v_mfma_f32_16x16x32_bf16 v[44:47], v[4:7], a[132:135], v[44:47]\n\t" \
  "s_waitcnt lgkmcnt(4)\n\t" \
  "v_mfma_f32_16x16x32_bf16 v[32:35], v[8:11], a[136:139], v[32:35]\n\t" \
  "ds_read_b128 v[0:3], v74 offset:2048\n\t" \
  "v_mfma_f32_16x16x32_bf16 v[36:39], v[8:11], a[140:143], v[36:39]\n\t" \
  "ds_read_b128 v[4:7], v74 offset:20480\n\t" \
  "v_mfma_f32_16x16x32_bf16 v[40:43], v[12:15], a[136:139], v[40:43]\n\t" \
  "v_mfma_f32_16x16x32_bf16 v[44:47], v[12:15], a[140:143], v[44:47]\n\t" \
  "v_mov_b32 v73, v74\n\t" \
  "v_add_u32 v74, 0, v72\n\t" \
  "s_waitcnt lgkmcnt(4)\n\t" \
  "v_mfma_f32_16x16x32_bf16 v[48:51], v[16:19], a[0:3], 0\n\t" \
  "ds_read_b128 v[8:11], v73 offset:3072\n\t" \
  "v_mfma_f32_16x16x32_bf16 v[52:55], v[16:19], a[4:7], 0\n\t" \
  "ds_read_b128 v[12:15], v73 offset:21504\n\t" \
  "v_mfma_f32_16x16x32_bf16 v[56:59], v[20:23], a[0:3], 0\n\t" \
  "v_mfma_f32_16x16x32_bf16 v[60:63], v[20:23], a[4:7], 0\n\t" \
  "s_waitcnt lgkmcnt(4)\n\t" \
  "v_mfma_f32_16x16x32_bf16 v[48:51], v[24:27], a[8:11], v[48:51]\n\t" \
  "ds_read_b128 v[16:19], v73 offset:4096\n\t" \
  "v_cvt_pk_bf16_f32 v64, v32, v33\n\t" \
  "v_mfma_f32_16x16x32_bf16 v[52:55], v[24:27], a[12:15], v[52:55]\n\t" \
  "ds_read_b128 v[20:23], v73 offset:22528\n\t" \
  "v_cvt_pk_bf16_f32 v65, v34, v35\n\t" \
  "v_mfma_f32_16x16x32_bf16 v[56:59], v[28:31], a[8:11], v[56:59]\n\t" \
  "v_cvt_pk_bf16_f32 v66, v40, v41\n\t" \
  "v_cvt_pk_bf16_f32 v67, v42, v43\n\t" \
  "v_mfma_f32_16x16x32_bf16 v[60:63], v[28:31], a[12:15], v[60:63]\n\t" \
  "buffer_store_dwordx4 v[64:67], v72, s[20:23], s50 offen offset:0 sc1\n\t" \
  "v_cvt_pk_bf16_f32 v68, v36, v37\n\t" \
  "s_waitcnt lgkmcnt(4)\n\t" \
  "v_mfma_f32_16x16x32_bf16 v[48:51], v[0:3], a[16:19], v[48:51]\n\t" \
  "ds_read_b128 v[24:27], v73 offset:5120\n\t" \
  "v_cvt_pk_bf16_f32 v69, v38, v39\n\t" \
  "v_mfma_f32_16x16x32_bf16 v[52:55], v[0:3], a[20:23], v[52:55]\n\t" \
  "ds_read_b128 v[28:31], v73 offset:23552\n\t" \
  "v_cvt_pk_bf16_f32 v70, v44, v45\n\t" \
  "v_mfma_f32_16x16x32_bf16 v[56:59], v[4:7], a[16:19], v[56:59]\n\t" \
  "v_cvt_pk_bf16_f32 v71, v46, v47\n\t" \
  "buffer_store_dwordx4 v[68:71], v72, s[20:23], s50 offen offset:1024 sc1\n\t" \
  "v_mfma_f32_16x16x32_bf16 v[60:63], v[4:7], a[20:23], v[60:63]\n\t" \
  "s_add_u32 s50, s50, 524288\n\t" \
  "v_max3_f32 v79, |v32|, |v33|, |v34|\n\t" \
  "s_waitcnt lgkmcnt(4)\n\t" \
  "v_mfma_f32_16x16x32_bf16 v[48:51], v[8:11], a[24:27], v[48:51]\n\t" \
  "ds_read_b128 v[0:3], v73 offset:6144\n\t" \
  "v_max3_f32 v79, v79, |v35|, |v36|\n\t" \
  "v_mfma_f32_16x16x32_bf16 v[52:55], v[8:11], a[28:31], v[52:55]\n\t" \
  "ds_read_b128 v[4:7], v73 offset:24576\n\t" \
  "v_max3_f32 v79, v79, |v37|, |v38|\n\t" \
  "v_mfma_f32_16x16x32_bf16 v[56:59], v[12:15], a[24:27], v[56:59]\n\t" \
  "v_max3_f32 v79, v79, |v39|, |v40|\n\t" \
  "v_max3_f32 v79, v79, |v41|, |v42|\n\t" \
  "v_mfma_f32_16x16x32_bf16 v[60:63], v[12:15], a[28:31], v[60:63]\n\t" \
  "v_max3_f32 v79, v79, |v43|, |v44|\n\t" \
  "v_max3_f32 v79, v79, |v45|, |v46|\n\t" \
  "s_waitcnt lgkmcnt(4)\n\t" \
  "v_mfma_f32_16x16x32_bf16 v[48:51], v[16:19], a[32:35], v[48:51]\n\t" \
  "ds_read_b128 v[8:11], v73 offset:7168\n\t" \
  "v_max_f32 v79, v79, |v47|\n\t" \
  "v_mfma_f32_16x16x32_bf16 v[52:55], v[16:19], a[36:39], v[52:55]\n\t" \
  "ds_read_b128 v[12:15], v73 offset:25600\n\t" \
  "v_max_f32_dpp v79, v79, v79 row_ror:1 row_mask:0xf bank_mask:0xf\n\t" \
  "v_mfma_f32_16x16x32_bf16 v[56:59], v[20:23], a[32:35], v[56:59]\n\t" \
  "s_nop 0\n\t" \
  "v_max_f32_dpp v79, v79, v79 row_ror:2 row_mask:0xf bank_mask:0xf\n\t" \
  "s_nop 1\n\t" \
  "v_max_f32_dpp v79, v79, v79 row_ror:4 row_mask:0xf bank_mask:0xf\n\t" \
  "v_mfma_f32_16x16x32_bf16 v[60:63], v[20:23], a[36:39], v[60:63]\n\t" \
  "s_nop 0\n\t" \
  "v_max_f32_dpp v79, v79, v79 row_ror:8 row_mask:0xf bank_mask:0xf\n\t" \
  "s_nop 1\n\t" \
  "v_max_f32_dpp v79, v79, v79 row_bcast:15 row_mask:0xa bank_mask:0xf\n\t" \
  "s_waitcnt lgkmcnt(4)\n\t" \
  "v_mfma_f32_16x16x32_bf16 v[48:51], v[24:27], a[40:43], v[48:51]\n\t" \
  "ds_read_b128 v[16:19], v73 offset:8192\n\t" \
  "v_max_f32_dpp v79, v79, v79 row_bcast:31 row_mask:0xc bank_mask:0xf\n\t" \
  "v_mfma_f32_16x16x32_bf16 v[52:55], v[24:27], a[44:47], v[52:55]\n\t" \
  "ds_read_b128 v[20:23], v73 offset:26624\n\t" \
  "v_mul_f32 v79, 0x3f810000, v79\n\t" \
  "v_mfma_f32_16x16x32_bf16 v[56:59], v[28:31], a[40:43], v[56:59]\n\t" \
  "s_mov_b64 exec, s[56:57]\n\t" \
  "ds_write_b32 v80, v79 offset:32\n\t" \
  "s_mov_b64 exec, -1\n\t" \
  "v_mfma_f32_16x16x32_bf16 v[60:63], v[28:31], a[44:47], v[60:63]\n\t" \
  "s_waitcnt lgkmcnt(4)\n\t" \
  "v_mfma_f32_16x16x32_bf16 v[48:51], v[0:3], a[48:51], v[48:51]\n\t" \
  "ds_read_b128 v[24:27], v73 offset:9216\n\t" \
  "v_mfma_f32_16x16x32_bf16 v[52:55], v[0:3], a[52:55], v[52:55]\n\t" \
  "ds_read_b128 v[28:31], v73 offset:27648\n\t" \
  "v_mfma_f32_16x16x32_bf16 v[56:59], v[4:7], a[48:51], v[56:59]\n\t" \
  "v_mfma_f32_16x16x32_bf16 v[60:63], v[4:7], a[52:55], v[60:63]\n\t" \
  "s_waitcnt lgkmcnt(4)\n\t" \
  "v_mfma_f32_16x16x32_bf16 v[48:51], v[8:11], a[56:59], v[48:51]\n\t" \
  "ds_read_b128 v[0:3], v73 offset:10240\n\t" \
  "v_mfma_f32_16x16x32_bf16 v[52:55], v[8:11], a[60:63], v[52:55]\n\t" \
  "ds_read_b128 v[4:7], v73 offset:28672\n\t" \
  "v_mfma_f32_16x16x32_bf16 v[56:59], v[12:15], a[56:59], v[56:59]\n\t" \
  "v_mfma_f32_16x16x32_bf16 v[60:63], v[12:15], a[60:63], v[60:63]\n\t" \
  "s_waitcnt lgkmcnt(4)\n\t" \
  "v_mfma_f32_16x16x32_bf16 v[48:51], v[16:19], a[64:67], v[48:51]\n\t" \
  "ds_read_b128 v[8:11], v73 offset:11264\n\t" \
  "v_mfma_f32_16x16x32_bf16 v[52:55], v[16:19], a[68:71], v[52:55]\n\t" \
  "ds_read_b128 v[12:15], v73 offset:29696\n\t" \
  "v_mfma_f32_16x16x32_bf16 v[56:59], v[20:23], a[64:67], v[56:59]\n\t" \
  "v_mfma_f32_16x16x32_bf16 v[60:63], v[20:23], a[68:71], v[60:63]\n\t" \
  "s_waitcnt vmcnt(11)\n\t" \
  "s_barrier\n\t" \
  "s_waitcnt lgkmcnt(4)\n\t" \
  "v_mfma_f32_16x16x32_bf16 v[48:51], v[24:27], a[72:75], v[48:51]\n\t" \
  "ds_read_b128 v[16:19], v73 offset:12288\n\t" \
  "v_mov_b32 v77, 147472\n\t" \
  "v_mfma_f32_16x16x32_bf16 v[52:55], v[24:27], a[76:79], v[52:55]\n\t" \
  "ds_read_b128 v[20:23], v73 offset:30720\n\t" \
  "ds_read_b128 v[84:87], v77\n\t" \
  "v_mfma_f32_16x16x32_bf16 v[56:59], v[28:31], a[72:75], v[56:59]\n\t" \
  "s_waitcnt lgkmcnt(0)\n\t" \
  "v_max3_f32 v84, v84, v85, v86\n\t" \
  "v_mfma_f32_16x16x32_bf16 v[60:63], v[28:31], a[76:79], v[60:63]\n\t" \
  "v_max_f32 v84, v84, v87\n\t" \
  "v_or_b32 v77, 1, v84\n\t" \
  "s_waitcnt lgkmcnt(4)\n\t" \
  "v_mfma_f32_16x16x32_bf16 v[48:51], v[0:3], a[80:83], v[48:51]\n\t" \
  "ds_read_b128 v[24:27], v73 offset:13312\n\t" \
  "s_mov_b64 exec, s[54:55]\n\t" \
  "buffer_store_dword v77, off, s[20:23], s51 sc1\n\t" \
  "s_mov_b64 exec, -1\n\t" \
  "v_mfma_f32_16x16x32_bf16 v[52:55], v[0:3], a[84:87], v[52:55]\n\t" \
  "ds_read_b128 v[28:31], v73 offset:31744\n\t" \
  "s_add_u32 s51, s51, 1024\n\t" \
  "v_mfma_f32_16x16x32_bf16 v[56:59], v[4:7], a[80:83], v[56:59]\n\t" \
  "s_add_u32 s48, s58, 73728\n\t" \
  "s_mov_b32 m0, s48\n\t" \
  "v_mfma_f32_16x16x32_bf16 v[60:63], v[4:7], a[84:87], v[60:63]\n\t" \
  "s_nop 0\n\t" \
  "global_load_lds_dwordx4 v72, s[46:47] offset:-4096 sc1\n\t" \
  "s_waitcnt lgkmcnt(4)\n\t" \
  "v_mfma_f32_16x16x32_bf16 v[48:51], v[8:11], a[88:91], v[48:51]\n\t" \
  "ds_read_b128 v[0:3], v73 offset:14336\n\t" \
  "global_load_lds_dwordx4 v72, s[46:47] offset:-3072 sc1\n\t" \
  "v_mfma_f32_16x16x32_bf16 v[52:55], v[8:11], a[92:95], v[52:55]\n\t" \
  "ds_read_b128 v[4:7], v73 offset:32768\n\t" \
  "global_load_lds_dwordx4 v72, s[46:47] offset:-2048 sc1\n\t" \
  "v_mfma_f32_16x16x32_bf16 v[56:59], v[12:15], a[88:91], v[56:59]\n\t" \
  "global_load_lds_dwordx4 v72, s[46:47] offset:-1024 sc1\n\t" \
  "global_load_lds_dwordx4 v72, s[46:47] offset:0 sc1\n\t" \
  "v_mfma_f32_16x16x32_bf16 v[60:63], v[12:15], a[92:95], v[60:63]\n\t" \
  "global_load_lds_dwordx4 v72, s[46:47] offset:1024 sc1\n\t" \
  "global_load_lds_dwordx4 v72, s[46:47] offset:2048 sc1\n\t" \
  "s_waitcnt lgkmcnt(4)\n\t" \
  "v_mfma_f32_16x16x32_bf16 v[48:51], v[16:19], a[96:99], v[48:51]\n\t" \
  "ds_read_b128 v[8:11], v73 offset:15360\n\t" \
  "global_load_lds_dwordx4 v72, s[46:47] offset:3072 sc1\n\t" \
  "v_mfma_f32_16x16x32_bf16 v[52:55], v[16:19], a[100:103], v[52:55]\n\t" \
  "ds_read_b128 v[12:15], v73 offset:33792\n\t" \
  "s_add_u32 m0, s48, 4096\n\t" \
  "v_mfma_f32_16x16x32_bf16 v[56:59], v[20:23], a[96:99], v[56:59]\n\t" \
  "s_nop 0\n\t" \
  "global_load_lds_dwordx4 v78, s[46:47] sc1\n\t" \
  "v_mfma_f32_16x16x32_bf16 v[60:63], v[20:23], a[100:103], v[60:63]\n\t" \
  "s_add_u32 s46, s46, 73728\n\t" \
  "s_addc_u32 s47, s47, 0\n\t" \
  "s_waitcnt lgkmcnt(4)\n\t" \
  "v_mfma_f32_16x16x32_bf16 v[48:51], v[24:27], a[104:107], v[48:51]\n\t" \
  "ds_read_b128 v[16:19], v73 offset:16384\n\t" \
  "v_mfma_f32_16x16x32_bf16 v[52:55], v[24:27], a[108:111], v[52:55]\n\t" \
  "ds_read_b128 v[20:23], v73 offset:34816\n\t" \
  "v_mfma_f32_16x16x32_bf16 v[56:59], v[28:31], a[104:107], v[56:59]\n\t" \
  "v_mfma_f32_16x16x32_bf16 v[60:63], v[28:31], a[108:111], v[60:63]\n\t" \
  "s_waitcnt lgkmcnt(4)\n\t" \
  "v_mfma_f32_16x16x32_bf16 v[48:51], v[0:3], a[112:115], v[48:51]\n\t" \
  "ds_read_b128 v[24:27], v73 offset:17408\n\t" \
  "v_mfma_f32_16x16x32_bf16 v[52:55], v[0:3], a[116:119], v[52:55]\n\t" \
  "ds_read_b128 v[28:31], v73 offset:35840\n\t" \
  "v_mfma_f32_16x16x32_bf16 v[56:59], v[4:7], a[112:115], v[56:59]\n\t" \
  "v_mfma_f32_16x16x32_bf16 v[60:63], v[4:7], a[116:119], v[60:63]\n\t" \
  "s_waitcnt lgkmcnt(4)\n\t" \
  "v_mfma_f32_16x16x32_bf16 v[48:51], v[8:11], a[120:123], v[48:51]\n\t" \
  "ds_read_b128 v[0:3], v74 offset:0\n\t" \
  "v_mfma_f32_16x16x32_bf16 v[52:55], v[8:11], a[124:127], v[52:55]\n\t" \
  "ds_read_b128 v[4:7], v74 offset:18432\n\t" \
  "v_mfma_f32_16x16x32_bf16 v[56:59], v[12:15], a[120:123], v[56:59]\n\t" \
  "v_mfma_f32_16x16x32_bf16 v[60:63], v[12:15], a[124:127], v[60:63]\n\t" \
  "s_waitcnt lgkmcnt(4)\n\t" \
  "v_mfma_f32_16x16x32_bf16 v[48:51], v[16:19], a[128:131], v[48:51]\n\t" \
  "ds_read_b128 v[8:11], v74 offset:1024\n\t" \
  "v_mfma_f32_16x16x32_bf16 v[52:55], v[16:19], a[132:135], v[52:55]\n\t" \
  "ds_read_b128 v[12:15], v74 offset:19456\n\t" \
  "v_mfma_f32_16x16x32_bf16 v[56:59], v[20:23], a[128:131], v[56:59]\n\t" \
  "v_mfma_f32_16x16x32_bf16 v[60:63], v[20:23], a[132:135], v[60:63]\n\t" \
  "s_waitcnt lgkmcnt(4)\n\t" \
  "v_mfma_f32_16x16x32_bf16 v[48:51], v[24:27], a[136:139], v[48:51]\n\t" \
  "ds_read_b128 v[16:19], v74 offset:2048\n\t" \
  "v_mfma_f32_16x16x32_bf16 v[52:55], v[24:27], a[140:143], v[52:55]\n\t" \
  "ds_read_b128 v[20:23], v74 offset:20480\n\t" \
  "v_mfma_f32_16x16x32_bf16 v[56:59], v[28:31], a[136:139], v[56:59]\n\t" \
  "v_mfma_f32_16x16x32_bf16 v[60:63], v[28:31], a[140:143], v[60:63]\n\t" \
  "v_mov_b32 v73, v74\n\t" \
  "s_mov_b32 s45, 6\n\t" \
  "Lq_%=:\n\t" \
  "v_add_u32 v74, 36864, v72\n\t" \
  "s_waitcnt lgkmcnt(4)\n\t" \
  "v_mfma_f32_16x16x32_bf16 v[32:35], v[0:3], a[0:3], 0\n\t" \
  "ds_read_b128 v[24:27], v73 offset:3072\n\t" \
  "v_mfma_f32_16x16x32_bf16 v[36:39], v[0:3], a[4:7], 0\n\t" \
  "ds_read_b128 v[28:31], v73 offset:21504\n\t" \
  "v_mfma_f32_16x16x32_bf16 v[40:43], v[4:7], a[0:3], 0\n\t" \
  "v_mfma_f32_16x16x32_bf16 v[44:47], v[4:7], a[4:7], 0\n\t" \
  "s_waitcnt lgkmcnt(4)\n\t" \
  "v_mfma_f32_16x16x32_bf16 v[32:35], v[8:11], a[8:11], v[32:35]\n\t" \
  "ds_read_b128 v[0:3], v73 offset:4096\n\t" \
  "v_cvt_pk_bf16_f32 v64, v48, v49\n\t" \
  "v_mfma_f32_16x16x32_bf16 v[36:39], v[8:11], a[12:15], v[36:39]\n\t" \
  "ds_read_b128 v[4:7], v73 offset:22528\n\t" \
  "v_cvt_pk_bf16_f32 v65, v50, v51\n\t" \
  "v_mfma_f32_16x16x32_bf16 v[40:43], v[12:15], a[8:11], v[40:43]\n\t" \
  "v_cvt_pk_bf16_f32 v66, v56, v57\n\t" \
  "v_cvt_pk_bf16_f32 v67, v58, v59\n\t" \
  "v_mfma_f32_16x16x32_bf16 v[44:47], v[12:15], a[12:15], v[44:47]\n\t" \
  "buffer_store_dwordx4 v[64:67], v72, s[20:23], s50 offen offset:0 sc1\n\t" \
  "v_cvt_pk_bf16_f32 v68, v52, v53\n\t" \
  "s_waitcnt lgkmcnt(4)\n\t" \
  "v_mfma_f32_16x16x32_bf16 v[32:35], v[16:19], a[16:19], v[32:35]\n\t" \
  "ds_read_b128 v[8:11], v73 offset:5120\n\t" \
  "v_cvt_pk_bf16_f32 v69, v54, v55\n\t" \
  "v_mfma_f32_16x16x32_bf16 v[36:39], v[16:19], a[20:23], v[36:39]\n\t" \
  "ds_read_b128 v[12:15], v73 offset:23552\n\t" \
  "v_cvt_pk_bf16_f32 v70, v60, v61\n\t" \
  "v_mfma_f32_16x16x32_bf16 v[40:43], v[20:23], a[16:19], v[40:43]\n\t" \
  "v_cvt_pk_bf16_f32 v71, v62, v63\n\t" \
  "buffer_store_dwordx4 v[68:71], v72, s[20:23], s50 offen offset:1024 sc1\n\t" \
  "v_mfma_f32_16x16x32_bf16 v[44:47], v[20:23], a[20:23], v[44:47]\n\t" \
  "s_add_u32 s50, s50, 524288\n\t" \
  "v_max3_f32 v79, |v48|, |v49|, |v50|\n\t" \
  "s_waitcnt lgkmcnt(4)\n\t" \
  "v_mfma_f32_16x16x32_bf16 v[32:35], v[24:27], a[24:27], v[32:35]\n\t" \
  "ds_read_b128 v[16:19], v73 offset:6144\n\t" \
  "v_max3_f32 v79, v79, |v51|, |v52|\n\t" \
  "v_mfma_f32_16x16x32_bf16 v[36:39], v[24:27], a[28:31], v[36:39]\n\t" \
  "ds_read_b128 v[20:23], v73 offset:24576\n\t" \
  "v_max3_f32 v79, v79, |v53|, |v54|\n\t" \
  "v_mfma_f32_16x16x32_bf16 v[40:43], v[28:31], a[24:27], v[40:43]\n\t" \
  "v_max3_f32 v79, v79, |v55|, |v56|\n\t" \
  "v_max3_f32 v79, v79, |v57|, |v58|\n\t" \
  "v_mfma_f32_16x16x32_bf16 v[44:47], v[28:31], a[28:31], v[44:47]\n\t" \
  "v_max3_f32 v79, v79, |v59|, |v60|\n\t" \
  "v_max3_f32 v79, v79, |v61|, |v62|\n\t" \
  "s_waitcnt lgkmcnt(4)\n\t" \
  "v_mfma_f32_16x16x32_bf16 v[32:35], v[0:3], a[32:35], v[32:35]\n\t" \
  "ds_read_b128 v[24:27], v73 offset:7168\n\t" \
  "v_max_f32 v79, v79, |v63|\n\t" \
  "v_mfma_f32_16x16x32_bf16 v[36:39], v[0:3], a[36:39], v[36:39]\n\t" \
  "ds_read_b128 v[28:31], v73 offset:25600\n\t" \
  "v_max_f32_dpp v79, v79, v79 row_ror:1 row_mask:0xf bank_mask:0xf\n\t" \
  "v_mfma_f32_16x16x32_bf16 v[40:43], v[4:7], a[32:35], v[40:43]\n\t" \
  "s_nop 0\n\t" \
  "v_max_f32_dpp v79, v79, v79 row_ror:2 row_mask:0xf bank_mask:0xf\n\t" \
  "s_nop 1\n\t" \
  "v_max_f32_dpp v79, v79, v79 row_ror:4 row_mask:0xf bank_mask:0xf\n\t" \
  "v_mfma_f32_16x16x32_bf16 v[44:47], v[4:7], a[36:39], v[44:47]\n\t" \
  "s_nop 0\n\t" \
  "v_max_f32_dpp v79, v79, v79 row_ror:8 row_mask:0xf bank_mask:0xf\n\t" \
  "s_nop 1\n\t" \
  "v_max_f32_dpp v79, v79, v79 row_bcast:15 row_mask:0xa bank_mask:0xf\n\t" \
  "s_waitcnt lgkmcnt(4)\n\t" \
  "v_mfma_f32_16x16x32_bf16 v[32:35], v[8:11], a[40:43], v[32:35]\n\t" \
  "ds_read_b128 v[0:3], v73 offset:8192\n\t" \
  "v_max_f32_dpp v79, v79, v79 row_bcast:31 row_mask:0xc bank_mask:0xf\n\t" \
  "v_mfma_f32_16x16x32_bf16 v[36:39], v[8:11], a[44:47], v[36:39]\n\t" \
  "ds_read_b128 v[4:7], v73 offset:26624\n\t" \
  "v_mul_f32 v79, 0x3f810000, v79\n\t" \
  "v_mfma_f32_16x16x32_bf16 v[40:43], v[12:15], a[40:43], v[40:43]\n\t" \
  "s_mov_b64 exec, s[56:57]\n\t" \
  "ds_write_b32 v80, v79 offset:48\n\t" \
  "s_mov_b64 exec, -1\n\t" \
  "v_mfma_f32_16x16x32_bf16 v[44:47], v[12:15], a[44:47], v[44:47]\n\t" \
  "s_waitcnt lgkmcnt(4)\n\t" \
  "v_mfma_f32_16x16x32_bf16 v[32:35], v[16:19], a[48:51], v[32:35]\n\t" \
  "ds_read_b128 v[8:11], v73 offset:9216\n\t" \
  "v_mfma_f32_16x16x32_bf16 v[36:39], v[16:19], a[52:55], v[36:39]\n\t" \
  "ds_read_b128 v[12:15], v73 offset:27648\n\t" \
  "v_mfma_f32_16x16x32_bf16 v[40:43], v[20:23], a[48:51], v[40:43]\n\t" \
  "v_mfma_f32_16x16x32_bf16 v[44:47], v[20:23], a[52:55], v[44:47]\n\t" \
  "s_waitcnt lgkmcnt(4)\n\t" \
  "v_mfma_f32_16x16x32_bf16 v[32:35], v[24:27], a[56:59], v[32:35]\n\t" \
  "ds_read_b128 v[16:19], v73 offset:10240\n\t" \
  "v_mfma_f32_16x16x32_bf16 v[36:39], v[24:27], a[60:63], v[36:39]\n\t" \
  "ds_read_b128 v[20:23], v73 offset:28672\n\t" \
  "v_mfma_f32_16x16x32_bf16 v[40:43], v[28:31], a[56:59], v[40:43]\n\t" \
  "v_mfma_f32_16x16x32_bf16 v[44:47], v[28:31], a[60:63], v[44:47]\n\t" \
  "s_waitcnt lgkmcnt(4)\n\t" \
  "v_mfma_f32_16x16x32_bf16 v[32:35], v[0:3], a[64:67], v[32:35]\n\t" \
  "ds_read_b128 v[24:27], v73 offset:11264\n\t" \
  "v_mfma_f32_16x16x32_bf16 v[36:39], v[0:3], a[68:71], v[36:39]\n\t" \
  "ds_read_b128 v[28:31], v73 offset:29696\n\t" \
  "v_mfma_f32_16x16x32_bf16 v[40:43], v[4:7], a[64:67], v[40:43]\n\t" \
  "v_mfma_f32_16x16x32_bf16 v[44:47], v[4:7], a[68:71], v[44:47]\n\t" \
  "s_waitcnt vmcnt(11)\n\t" \
  "s_barrier\n\t" \
  "s_waitcnt lgkmcnt(4)\n\t" \
  "v_mfma_f32_16x16x32_bf16 v[32:35], v[8:11], a[72:75], v[32:35]\n\t" \
  "ds_read_b128 v[0:3], v73 offset:12288\n\t" \
  "v_mov_b32 v77, 147488\n\t" \
  "v_mfma_f32_16x16x32_bf16 v[36:39], v[8:11], a[76:79], v[36:39]\n\t" \
  "ds_read_b128 v[4:7], v73 offset:30720\n\t" \
  "ds_read_b128 v[84:87], v77\n\t" \
  "v_mfma_f32_16x16x32_bf16 v[40:43], v[12:15], a[72:75], v[40:43]\n\t" \
  "s_waitcnt lgkmcnt(0)\n\t" \
  "v_max3_f32 v84, v84, v85, v86\n\t" \
  "v_mfma_f32_16x16x32_bf16 v[44:47], v[12:15], a[76:79], v[44:47]\n\t" \
  "v_max_f32 v84, v84, v87\n\t" \
  "v_or_b32 v77, 1, v84\n\t" \
  "s_waitcnt lgkmcnt(4)\n\t" \
  "v_mfma_f32_16x16x32_bf16 v[32:35], v[16:19], a[80:83], v[32:35]\n\t" \
  "ds_read_b128 v[8:11], v73 offset:13312\n\t" \
  "s_mov_b64 exec, s[54:55]\n\t" \
  "buffer_store_dword v77, off, s[20:23], s51 sc1\n\t" \
  "s_mov_b64 exec, -1\n\t" \
  "v_mfma_f32_16x16x32_bf16 v[36:39], v[16:19], a[84:87], v[36:39]\n\t" \
  "ds_read_b128 v[12:15], v73 offset:31744\n\t" \
  "s_add_u32 s51, s51, 1024\n\t" \
  "v_mfma_f32_16x16x32_bf16 v[40:43], v[20:23], a[80:83], v[40:43]\n\t" \
  "s_add_u32 s48, s58, 110592\n\t" \
  "s_mov_b32 m0, s48\n\t" \
  "v_mfma_f32_16x16x32_bf16 v[44:47], v[20:23], a[84:87], v[44:47]\n\t" \
  "s_nop 0\n\t" \
  "global_load_lds_dwordx4 v72, s[46:47] offset:-4096 sc1\n\t" \
  "s_waitcnt lgkmcnt(4)\n\t" \
  "v_mfma_f32_16x16x32_bf16 v[32:35], v[24:27], a[88:91], v[32:35]\n\t" \
  "ds_read_b128 v[16:19], v73 offset:14336\n\t" \
  "global_load_lds_dwordx4 v72, s[46:47] offset:-3072 sc1\n\t" \
  "v_mfma_f32_16x16x32_bf16 v[36:39], v[24:27], a[92:95], v[36:39]\n\t" \
  "ds_read_b128 v[20:23], v73 offset:32768\n\t" \
  "global_load_lds_dwordx4 v72, s[46:47] offset:-2048 sc1\n\t" \
  "v_mfma_f32_16x16x32_bf16 v[40:43], v[28:31], a[88:91], v[40:43]\n\t" \
  "global_load_lds_dwordx4 v72, s[46:47] offset:-1024 sc1\n\t" \
  "global_load_lds_dwordx4 v72, s[46:47] offset:0 sc1\n\t" \
  "v_mfma_f32_16x16x32_bf16 v[44:47], v[28:31], a[92:95], v[44:47]\n\t" \
  "global_load_lds_dwordx4 v72, s[46:47] offset:1024 sc1\n\t" \
  "global_load_lds_dwordx4 v72, s[46:47] offset:2048 sc1\n\t" \
  "s_waitcnt lgkmcnt(4)\n\t" \
  "v_mfma_f32_16x16x32_bf16 v[32:35], v[0:3], a[96:99], v[32:35]\n\t" \
  "ds_read_b128 v[24:27], v73 offset:15360\n\t" \
  "global_load_lds_dwordx4 v72, s[46:47] offset:3072 sc1\n\t" \
  "v_mfma_f32_16x16x32_bf16 v[36:39], v[0:3], a[100:103], v[36:39]\n\t" \
  "ds_read_b128 v[28:31], v73 offset:33792\n\t" \
  "s_add_u32 m0, s48, 4096\n\t" \
  "v_mfma_f32_16x16x32_bf16 v[40:43], v[4:7], a[96:99], v[40:43]\n\t" \
  "s_nop 0\n\t" \
  "global_load_lds_dwordx4 v78, s[46:47] sc1\n\t" \
  "v_mfma_f32_16x16x32_bf16 v[44:47], v[4:7], a[100:103], v[44:47]\n\t" \
  "s_add_u32 s46, s46, 73728\n\t" \
  "s_addc_u32 s47, s47, 0\n\t" \
  "s_waitcnt lgkmcnt(4)\n\t" \
  "v_mfma_f32_16x16x32_bf16 v[32:35], v[8:11], a[104:107], v[32:35]\n\t" \
  "ds_read_b128 v[0:3], v73 offset:16384\n\t" \
  "v_mfma_f32_16x16x32_bf16 v[36:39], v[8:11], a[108:111], v[36:39]\n\t" \
  "ds_read_b128 v[4:7], v73 offset:34816\n\t" \
  "v_mfma_f32_16x16x32_bf16 v[40:43], v[12:15], a[104:107], v[40:43]\n\t" \
  "v_mfma_f32_16x16x32_bf16 v[44:47], v[12:15], a[108:111], v[44:47]\n\t" \
  "s_waitcnt lgkmcnt(4)\n\t" \
  "v_mfma_f32_16x16x32_bf16 v[32:35], v[16:19], a[112:115], v[32:35]\n\t" \
  "ds_read_b128 v[8:11], v73 offset:17408\n\t" \
  "v_mfma_f32_16x16x32_bf16 v[36:39], v[16:19], a[116:119], v[36:39]\n\t" \
  "ds_read_b128 v[12:15], v73 offset:35840\n\t" \
  "v_mfma_f32_16x16x32_bf16 v[40:43], v[20:23], a[112:115], v[40:43]\n\t" \
  "v_mfma_f32_16x16x32_bf16 v[44:47], v[20:23], a[116:119], v[44:47]\n\t" \
  "s_waitcnt lgkmcnt(4)\n\t" \
  "v_mfma_f32_16x16x32_bf16 v[32:35], v[24:27], a[120:123], v[32:35]\n\t" \
  "ds_read_b128 v[16:19], v74 offset:0\n\t" \
  "v_mfma_f32_16x16x32_bf16 v[36:39], v[24:27], a[124:127], v[36:39]\n\t" \
  "ds_read_b128 v[20:23], v74 offset:18432\n\t" \
  "v_mfma_f32_16x16x32_bf16 v[40:43], v[28:31], a[120:123], v[40:43]\n\t" \
  "v_mfma_f32_16x16x32_bf16 v[44:47], v[28:31], a[124:127], v[44:47]\n\t" \
  "s_waitcnt lgkmcnt(4)\n\t" \
  "v_mfma_f32_16x16x32_bf16 v[32:35], v[0:3], a[128:131], v[32:35]\n\t" \
  "ds_read_b128 v[24:27], v74 offset:1024\n\t" \
  "v_mfma_f32_16x16x32_bf16 v[36:39], v[0:3], a[132:135], v[36:39]\n\t" \
  "ds_read_b128 v[28:31], v74 offset:19456\n\t" \
  "v_mfma_f32_16x16x32_bf16 v[40:43], v[4:7], a[128:131], v[40:43]\n\t" \
  "v_mfma_f32_16x16x32_bf16 v[44:47], v[4:7], a[132:135], v[44:47]\n\t" \
  "s_waitcnt lgkmcnt(4)\n\t" \
  "v_mfma_f32_16x16x32_bf16 v[32:35], v[8:11], a[136:139], v[32:35]\n\t" \
  "ds_read_b128 v[0:3], v74 offset:2048\n\t" \
  "v_mfma_f32_16x16x32_bf16 v[36:39], v[8:11], a[140:143], v[36:39]\n\t" \
  "ds_read_b128 v[4:7], v74 offset:20480\n\t" \
  "v_mfma_f32_16x16x32_bf16 v[40:43], v[12:15], a[136:139], v[40:43]\n\t" \
  "v_mfma_f32_16x16x32_bf16 v[44:47], v[12:15], a[140:143], v[44:47]\n\t" \
  "v_mov_b32 v73, v74\n\t" \
  "v_add_u32 v74, 73728, v72\n\t" \
  "s_waitcnt lgkmcnt(4)\n\t" \
  "v_mfma_f32_16x16x32_bf16 v[48:51], v[16:19], a[0:3], 0\n\t" \
  "ds_read_b128 v[8:11], v73 offset:3072\n\t" \
  "v_mfma_f32_16x16x32_bf16 v[52:55], v[16:19], a[4:7], 0\n\t" \
  "ds_read_b128 v[12:15], v73 offset:21504\n\t" \
  "v_mfma_f32_16x16x32_bf16 v[56:59], v[20:23], a[0:3], 0\n\t" \
  "v_mfma_f32_16x16x32_bf16 v[60:63], v[20:23], a[4:7], 0\n\t" \
  "s_waitcnt lgkmcnt(4)\n\t" \
  "v_mfma_f32_16x16x32_bf16 v[48:51], v[24:27], a[8:11], v[48:51]\n\t" \
  "ds_read_b128 v[16:19], v73 offset:4096\n\t" \
  "v_cvt_pk_bf16_f32 v64, v32, v33\n\t" \
  "v_mfma_f32_16x16x32_bf16 v[52:55], v[24:27], a[12:15], v[52:55]\n\t" \
  "ds_read_b128 v[20:23], v73 offset:22528\n\t" \
  "v_cvt_pk_bf16_f32 v65, v34, v35\n\t" \
  "v_mfma_f32_16x16x32_bf16 v[56:59], v[28:31], a[8:11], v[56:59]\n\t" \
  "v_cvt_pk_bf16_f32 v66, v40, v41\n\t" \
  "v_cvt_pk_bf16_f32 v67, v42, v43\n\t" \
  "v_mfma_f32_16x16x32_bf16 v[60:63], v[28:31], a[12:15], v[60:63]\n\t" \
  "buffer_store_dwordx4 v[64:67], v72, s[20:23], s50 offen offset:0 sc1\n\t" \
  "v_cvt_pk_bf16_f32 v68, v36, v37\n\t" \
  "s_waitcnt lgkmcnt(4)\n\t" \
  "v_mfma_f32_16x16x32_bf16 v[48:51], v[0:3], a[16:19], v[48:51]\n\t" \
  "ds_read_b128 v[24:27], v73 offset:5120\n\t" \
  "v_cvt_pk_bf16_f32 v69, v38, v39\n\t" \
  "v_mfma_f32_16x16x32_bf16 v[52:55], v[0:3], a[20:23], v[52:55]\n\t" \
  "ds_read_b128 v[28:31], v73 offset:23552\n\t" \
  "v_cvt_pk_bf16_f32 v70, v44, v45\n\t" \
  "v_mfma_f32_16x16x32_bf16 v[56:59], v[4:7], a[16:19], v[56:59]\n\t" \
  "v_cvt_pk_bf16_f32 v71, v46, v47\n\t" \
  "buffer_store_dwordx4 v[68:71], v72, s[20:23], s50 offen offset:1024 sc1\n\t" \
  "v_mfma_f32_16x16x32_bf16 v[60:63], v[4:7], a[20:23], v[60:63]\n\t" \
  "s_add_u32 s50, s50, 524288\n\t" \
  "v_max3_f32 v79, |v32|, |v33|, |v34|\n\t" \
  "s_waitcnt lgkmcnt(4)\n\t" \
  "v_mfma_f32_16x16x32_bf16 v[48:51], v[8:11], a[24:27], v[48:51]\n\t" \
  "ds_read_b128 v[0:3], v73 offset:6144\n\t" \
  "v_max3_f32 v79, v79, |v35|, |v36|\n\t" \
  "v_mfma_f32_16x16x32_bf16 v[52:55], v[8:11], a[28:31], v[52:55]\n\t" \
  "ds_read_b128 v[4:7], v73 offset:24576\n\t" \
  "v_max3_f32 v79, v79, |v37|, |v38|\n\t" \
  "v_mfma_f32_16x16x32_bf16 v[56:59], v[12:15], a[24:27], v[56:59]\n\t" \
  "v_max3_f32 v79, v79, |v39|, |v40|\n\t" \
  "v_max3_f32 v79, v79, |v41|, |v42|\n\t" \
  "v_mfma_f32_16x16x32_bf16 v[60:63], v[12:15], a[28:31], v[60:63]\n\t" \
  "v_max3_f32 v79, v79, |v43|, |v44|\n\t" \
  "v_max3_f32 v79, v79, |v45|, |v46|\n\t" \
  "s_waitcnt lgkmcnt(4)\n\t" \
  "v_mfma_f32_16x16x32_bf16 v[48:51], v[16:19], a[32:35], v[48:51]\n\t" \
  "ds_read_b128 v[8:11], v73 offset:7168\n\t" \
  "v_max_f32 v79, v79, |v47|\n\t" \
  "v_mfma_f32_16x16x32_bf16 v[52:55], v[16:19], a[36:39], v[52:55]\n\t" \
  "ds_read_b128 v[12:15], v73 offset:25600\n\t" \
  "v_max_f32_dpp v79, v79, v79 row_ror:1 row_mask:0xf bank_mask:0xf\n\t" \
  "v_mfma_f32_16x16x32_bf16 v[56:59], v[20:23], a[32:35], v[56:59]\n\t" \
  "s_nop 0\n\t" \
  "v_max_f32_dpp v79, v79, v79 row_ror:2 row_mask:0xf bank_mask:0xf\n\t" \
  "s_nop 1\n\t" \
  "v_max_f32_dpp v79, v79, v79 row_ror:4 row_mask:0xf bank_mask:0xf\n\t" \
  "v_mfma_f32_16x16x32_bf16 v[60:63], v[20:23], a[36:39], v[60:63]\n\t" \
  "s_nop 0\n\t" \
  "v_max_f32_dpp v79, v79, v79 row_ror:8 row_mask:0xf bank_mask:0xf\n\t" \
  "s_nop 1\n\t" \
  "v_max_f32_dpp v79, v79, v79 row_bcast:15 row_mask:0xa bank_mask:0xf\n\t" \
  "s_waitcnt lgkmcnt(4)\n\t" \
  "v_mfma_f32_16x16x32_bf16 v[48:51], v[24:27], a[40:43], v[48:51]\n\t" \
  "ds_read_b128 v[16:19], v73 offset:8192\n\t" \
  "v_max_f32_dpp v79, v79, v79 row_bcast:31 row_mask:0xc bank_mask:0xf\n\t" \
  "v_mfma_f32_16x16x32_bf16 v[52:55], v[24:27], a[44:47], v[52:55]\n\t" \
  "ds_read_b128 v[20:23], v73 offset:26624\n\t" \
  "v_mul_f32 v79, 0x3f810000, v79\n\t" \
  "v_mfma_f32_16x16x32_bf16 v[56:59], v[28:31], a[40:43], v[56:59]\n\t" \
  "s_mov_b64 exec, s[56:57]\n\t" \
  "ds_write_b32 v80, v79 offset:0\n\t" \
  "s_mov_b64 exec, -1\n\t" \
  "v_mfma_f32_16x16x32_bf16 v[60:63], v[28:31], a[44:47], v[60:63]\n\t" \
  "s_waitcnt lgkmcnt(4)\n\t" \
  "v_mfma_f32_16x16x32_bf16 v[48:51], v[0:3], a[48:51], v[48:51]\n\t" \
  "ds_read_b128 v[24:27], v73 offset:9216\n\t" \
  "v_mfma_f32_16x16x32_bf16 v[52:55], v[0:3], a[52:55], v[52:55]\n\t" \
  "ds_read_b128 v[28:31], v73 offset:27648\n\t" \
  "v_mfma_f32_16x16x32_bf16 v[56:59], v[4:7], a[48:51], v[56:59]\n\t" \
  "v_mfma_f32_16x16x32_bf16 v[60:63], v[4:7], a[52:55], v[60:63]\n\t" \
  "s_waitcnt lgkmcnt(4)\n\t" \
  "v_mfma_f32_16x16x32_bf16 v[48:51], v[8:11], a[56:59], v[48:51]\n\t" \
  "ds_read_b128 v[0:3], v73 offset:10240\n\t" \
  "v_mfma_f32_16x16x32_bf16 v[52:55], v[8:11], a[60:63], v[52:55]\n\t" \
  "ds_read_b128 v[4:7], v73 offset:28672\n\t" \
  "v_mfma_f32_16x16x32_bf16 v[56:59], v[12:15], a[56:59], v[56:59]\n\t" \
  "v_mfma_f32_16x16x32_bf16 v[60:63], v[12:15], a[60:63], v[60:63]\n\t" \
  "s_waitcnt lgkmcnt(4)\n\t" \
  "v_mfma_f32_16x16x32_bf16 v[48:51], v[16:19], a[64:67], v[48:51]\n\t" \
  "ds_read_b128 v[8:11], v73 offset:11264\n\t" \
  "v_mfma_f32_16x16x32_bf16 v[52:55], v[16:19], a[68:71], v[52:55]\n\t" \
  "ds_read_b128 v[12:15], v73 offset:29696\n\t" \
  "v_mfma_f32_16x16x32_bf16 v[56:59], v[20:23], a[64:67], v[56:59]\n\t" \
  "v_mfma_f32_16x16x32_bf16 v[60:63], v[20:23], a[68:71], v[60:63]\n\t" \
  "s_waitcnt vmcnt(11)\n\t" \
  "s_barrier\n\t" \
  "s_waitcnt lgkmcnt(4)\n\t" \
  "v_mfma_f32_16x16x32_bf16 v[48:51], v[24:27], a[72:75], v[48:51]\n\t" \
  "ds_read_b128 v[16:19], v73 offset:12288\n\t" \
  "v_mov_b32 v77, 147504\n\t" \
  "v_mfma_f32_16x16x32_bf16 v[52:55], v[24:27], a[76:79], v[52:55]\n\t" \
  "ds_read_b128 v[20:23], v73 offset:30720\n\t" \
  "ds_read_b128 v[84:87], v77\n\t" \
  "v_mfma_f32_16x16x32_bf16 v[56:59], v[28:31], a[72:75], v[56:59]\n\t" \
  "s_waitcnt lgkmcnt(0)\n\t" \
  "v_max3_f32 v84, v84, v85, v86\n\t" \
  "v_mfma_f32_16x16x32_bf16 v[60:63], v[28:31], a[76:79], v[60:63]\n\t" \
  "v_max_f32 v84, v84, v87\n\t" \
  "v_or_b32 v77, 1, v84\n\t" \
  "s_waitcnt lgkmcnt(4)\n\t" \
  "v_mfma_f32_16x16x32_bf16 v[48:51], v[0:3], a[80:83], v[48:51]\n\t" \
  "ds_read_b128 v[24:27], v73 offset:13312\n\t" \
  "s_mov_b64 exec, s[54:55]\n\t" \
  "buffer_store_dword v77, off, s[20:23], s51 sc1\n\t" \
  "s_mov_b64 exec, -1\n\t" \
  "v_mfma_f32_16x16x32_bf16 v[52:55], v[0:3], a[84:87], v[52:55]\n\t" \
  "ds_read_b128 v[28:31], v73 offset:31744\n\t" \
  "s_add_u32 s51, s51, 1024\n\t" \
  "v_mfma_f32_16x16x32_bf16 v[56:59], v[4:7], a[80:83], v[56:59]\n\t" \
  "s_add_u32 s48, s58, 0\n\t" \
  "s_mov_b32 m0, s48\n\t" \
  "v_mfma_f32_16x16x32_bf16 v[60:63], v[4:7], a[84:87], v[60:63]\n\t" \
  "s_nop 0\n\t" \
  "global_load_lds_dwordx4 v72, s[46:47] offset:-4096 sc1\n\t" \
  "s_waitcnt lgkmcnt(4)\n\t" \
  "v_mfma_f32_16x16x32_bf16 v[48:51], v[8:11], a[88:91], v[48:51]\n\t" \
  "ds_read_b128 v[0:3], v73 offset:14336\n\t" \
  "global_load_lds_dwordx4 v72, s[46:47] offset:-3072 sc1\n\t" \
  "v_mfma_f32_16x16x32_bf16 v[52:55], v[8:11], a[92:95], v[52:55]\n\t" \
  "ds_read_b128 v[4:7], v73 offset:32768\n\t" \
  "global_load_lds_dwordx4 v72, s[46:47] offset:-2048 sc1\n\t" \
  "v_mfma_f32_16x16x32_bf16 v[56:59], v[12:15], a[88:91], v[56:59]\n\t" \
  "global_load_lds_dwordx4 v72, s[46:47] offset:-1024 sc1\n\t" \
  "global_load_lds_dwordx4 v72, s[46:47] offset:0 sc1\n\t" \
  "v_mfma_f32_16x16x32_bf16 v[60:63], v[12:15], a[92:95], v[60:63]\n\t" \
  "global_load_lds_dwordx4 v72, s[46:47] offset:1024 sc1\n\t" \
  "global_load_lds_dwordx4 v72, s[46:47] offset:2048 sc1\n\t" \
  "s_waitcnt lgkmcnt(4)\n\t" \
  "v_mfma_f32_16x16x32_bf16 v[48:51], v[16:19], a[96:99], v[48:51]\n\t" \
  "ds_read_b128 v[8:11], v73 offset:15360\n\t" \
  "global_load_lds_dwordx4 v72, s[46:47] offset:3072 sc1\n\t" \
  "v_mfma_f32_16x16x32_bf16 v[52:55], v[16:19], a[100:103], v[52:55]\n\t" \
  "ds_read_b128 v[12:15], v73 offset:33792\n\t" \
  "s_add_u32 m0, s48, 4096\n\t" \
  "v_mfma_f32_16x16x32_bf16 v[56:59], v[20:23], a[96:99], v[56:59]\n\t" \
  "s_nop 0\n\t" \
  "global_load_lds_dwordx4 v78, s[46:47] sc1\n\t" \
  "v_mfma_f32_16x16x32_bf16 v[60:63], v[20:23], a[100:103], v[60:63]\n\t" \
  "s_add_u32 s46, s46, 73728\n\t" \
  "s_addc_u32 s47, s47, 0\n\t" \
  "s_waitcnt lgkmcnt(4)\n\t" \
  "v_mfma_f32_16x16x32_bf16 v[48:51], v[24:27], a[104:107], v[48:51]\n\t" \
  "ds_read_b128 v[16:19], v73 offset:16384\n\t" \
  "v_mfma_f32_16x16x32_bf16 v[52:55], v[24:27], a[108:111], v[52:55]\n\t" \
  "ds_read_b128 v[20:23], v73 offset:34816\n\t" \
  "v_mfma_f32_16x16x32_bf16 v[56:59], v[28:31], a[104:107], v[56:59]\n\t" \
  "v_mfma_f32_16x16x32_bf16 v[60:63], v[28:31], a[108:111], v[60:63]\n\t" \
  "s_waitcnt lgkmcnt(4)\n\t" \
  "v_mfma_f32_16x16x32_bf16 v[48:51], v[0:3], a[112:115], v[48:51]\n\t" \
  "ds_read_b128 v[24:27], v73 offset:17408\n\t" \
  "v_mfma_f32_16x16x32_bf16 v[52:55], v[0:3], a[116:119], v[52:55]\n\t" \
  "ds_read_b128 v[28:31], v73 offset:35840\n\t" \
  "v_mfma_f32_16x16x32_bf16 v[56:59], v[4:7], a[112:115], v[56:59]\n\t" \
  "v_mfma_f32_16x16x32_bf16 v[60:63], v[4:7], a[116:119], v[60:63]\n\t" \
  "s_waitcnt lgkmcnt(4)\n\t" \
  "v_mfma_f32_16x16x32_bf16 v[48:51], v[8:11], a[120:123], v[48:51]\n\t" \
  "ds_read_b128 v[0:3], v74 offset:0\n\t" \
  "v_mfma_f32_16x16x32_bf16 v[52:55], v[8:11], a[124:127], v[52:55]\n\t" \
  "ds_read_b128 v[4:7], v74 offset:18432\n\t" \
  "v_mfma_f32_16x16x32_bf16 v[56:59], v[12:15], a[120:123], v[56:59]\n\t" \
  "v_mfma_f32_16x16x32_bf16 v[60:63], v[12:15], a[124:127], v[60:63]\n\t" \
  "s_waitcnt lgkmcnt(4)\n\t" \
  "v_mfma_f32_16x16x32_bf16 v[48:51], v[16:19], a[128:131], v[48:51]\n\t" \
  "ds_read_b128 v[8:11], v74 offset:1024\n\t" \
  "v_mfma_f32_16x16x32_bf16 v[52:55], v[16:19], a[132:135], v[52:55]\n\t" \
  "ds_read_b128 v[12:15], v74 offset:19456\n\t" \
  "v_mfma_f32_16x16x32_bf16 v[56:59], v[20:23], a[128:131], v[56:59]\n\t" \
  "v_mfma_f32_16x16x32_bf16 v[60:63], v[20:23], a[132:135], v[60:63]\n\t" \
  "s_waitcnt lgkmcnt(4)\n\t" \
  "v_mfma_f32_16x16x32_bf16 v[48:51], v[24:27], a[136:139], v[48:51]\n\t" \
  "ds_read_b128 v[16:19], v74 offset:2048\n\t" \
  "v_mfma_f32_16x16x32_bf16 v[52:55], v[24:27], a[140:143], v[52:55]\n\t" \
  "ds_read_b128 v[20:23], v74 offset:20480\n\t" \
  "v_mfma_f32_16x16x32_bf16 v[56:59], v[28:31], a[136:139], v[56:59]\n\t" \
  "v_mfma_f32_16x16x32_bf16 v[60:63], v[28:31], a[140:143], v[60:63]\n\t" \
  "v_mov_b32 v73, v74\n\t" \
  "v_add_u32 v74, 110592, v72\n\t" \
  "s_waitcnt lgkmcnt(4)\n\t" \
  "v_mfma_f32_16x16x32_bf16 v[32:35], v[0:3], a[0:3], 0\n\t" \
  "ds_read_b128 v[24:27], v73 offset:3072\n\t" \
  "v_mfma_f32_16x16x32_bf16 v[36:39], v[0:3], a[4:7], 0\n\t" \
  "ds_read_b128 v[28:31], v73 offset:21504\n\t" \
  "v_mfma_f32_16x16x32_bf16 v[40:43], v[4:7], a[0:3], 0\n\t" \
  "v_mfma_f32_16x16x32_bf16 v[44:47], v[4:7], a[4:7], 0\n\t" \
  "s_waitcnt lgkmcnt(4)\n\t" \
  "v_mfma_f32_16x16x32_bf16 v[32:35], v[8:11], a[8:11], v[32:35]\n\t" \
  "ds_read_b128 v[0:3], v73 offset:4096\n\t" \
  "v_cvt_pk_bf16_f32 v64, v48, v49\n\t" \
  "v_mfma_f32_16x16x32_bf16 v[36:39], v[8:11], a[12:15], v[36:39]\n\t" \
  "ds_read_b128 v[4:7], v73 offset:22528\n\t" \
  "v_cvt_pk_bf16_f32 v65, v50, v51\n\t" \
  "v_mfma_f32_16x16x32_bf16 v[40:43], v[12:15], a[8:11], v[40:43]\n\t" \
  "v_cvt_pk_bf16_f32 v66, v56, v57\n\t" \
  "v_cvt_pk_bf16_f32 v67, v58, v59\n\t" \
  "v_mfma_f32_16x16x32_bf16 v[44:47], v[12:15], a[12:15], v[44:47]\n\t" \
  "buffer_store_dwordx4 v[64:67], v72, s[20:23], s50 offen offset:0 sc1\n\t" \
  "v_cvt_pk_bf16_f32 v68, v52, v53\n\t" \
  "s_waitcnt lgkmcnt(4)\n\t" \
  "v_mfma_f32_16x16x32_bf16 v[32:35], v[16:19], a[16:19], v[32:35]\n\t" \
  "ds_read_b128 v[8:11], v73 offset:5120\n\t" \
  "v_cvt_pk_bf16_f32 v69, v54, v55\n\t" \
  "v_mfma_f32_16x16x32_bf16 v[36:39], v[16:19], a[20:23], v[36:39]\n\t" \
  "ds_read_b128 v[12:15], v73 offset:23552\n\t" \
  "v_cvt_pk_bf16_f32 v70, v60, v61\n\t" \
  "v_mfma_f32_16x16x32_bf16 v[40:43], v[20:23], a[16:19], v[40:43]\n\t" \
  "v_cvt_pk_bf16_f32 v71, v62, v63\n\t" \
  "buffer_store_dwordx4 v[68:71], v72, s[20:23], s50 offen offset:1024 sc1\n\t" \
  "v_mfma_f32_16x16x32_bf16 v[44:47], v[20:23], a[20:23], v[44:47]\n\t" \
  "s_add_u32 s50, s50, 524288\n\t" \
  "v_max3_f32 v79, |v48|, |v49|, |v50|\n\t" \
  "s_waitcnt lgkmcnt(4)\n\t" \
  "v_mfma_f32_16x16x32_bf16 v[32:35], v[24:27], a[24:27], v[32:35]\n\t" \
  "ds_read_b128 v[16:19], v73 offset:6144\n\t" \
  "v_max3_f32 v79, v79, |v51|, |v52|\n\t" \
  "v_mfma_f32_16x16x32_bf16 v[36:39], v[24:27], a[28:31], v[36:39]\n\t" \
  "ds_read_b128 v[20:23], v73 offset:24576\n\t" \
  "v_max3_f32 v79, v79, |v53|, |v54|\n\t" \
  "v_mfma_f32_16x16x32_bf16 v[40:43], v[28:31], a[24:27], v[40:43]\n\t" \
  "v_max3_f32 v79, v79, |v55|, |v56|\n\t" \
  "v_max3_f32 v79, v79, |v57|, |v58|\n\t" \
  "v_mfma_f32_16x16x32_bf16 v[44:47], v[28:31], a[28:31], v[44:47]\n\t" \
  "v_max3_f32 v79, v79, |v59|, |v60|\n\t" \
  "v_max3_f32 v79, v79, |v61|, |v62|\n\t" \
  "s_waitcnt lgkmcnt(4)\n\t" \
  "v_mfma_f32_16x16x32_bf16 v[32:35], v[0:3], a[32:35], v[32:35]\n\t" \
  "ds_read_b128 v[24:27], v73 offset:7168\n\t" \
  "v_max_f32 v79, v79, |v63|\n\t" \
  "v_mfma_f32_16x16x32_bf16 v[36:39], v[0:3], a[36:39], v[36:39]\n\t" \
  "ds_read_b128 v[28:31], v73 offset:25600\n\t" \
  "v_max_f32_dpp v79, v79, v79 row_ror:1 row_mask:0xf bank_mask:0xf\n\t" \
  "v_mfma_f32_16x16x32_bf16 v[40:43], v[4:7], a[32:35], v[40:43]\n\t" \
  "s_nop 0\n\t" \
  "v_max_f32_dpp v79, v79, v79 row_ror:2 row_mask:0xf bank_mask:0xf\n\t" \
  "s_nop 1\n\t" \
  "v_max_f32_dpp v79, v79, v79 row_ror:4 row_mask:0xf bank_mask:0xf\n\t" \
  "v_mfma_f32_16x16x32_bf16 v[44:47], v[4:7], a[36:39], v[44:47]\n\t" \
  "s_nop 0\n\t" \
  "v_max_f32_dpp v79, v79, v79 row_ror:8 row_mask:0xf bank_mask:0xf\n\t" \
  "s_nop 1\n\t" \
  "v_max_f32_dpp v79, v79, v79 row_bcast:15 row_mask:0xa bank_mask:0xf\n\t" \
  "s_waitcnt lgkmcnt(4)\n\t" \
  "v_mfma_f32_16x16x32_bf16 v[32:35], v[8:11], a[40:43], v[32:35]\n\t" \
  "ds_read_b128 v[0:3], v73 offset:8192\n\t" \
  "v_max_f32_dpp v79, v79, v79 row_bcast:31 row_mask:0xc bank_mask:0xf\n\t" \
  "v_mfma_f32_16x16x32_bf16 v[36:39], v[8:11], a[44:47], v[36:39]\n\t" \
  "ds_read_b128 v[4:7], v73 offset:26624\n\t" \
  "v_mul_f32 v79, 0x3f810000, v79\n\t" \
  "v_mfma_f32_16x16x32_bf16 v[40:43], v[12:15], a[40:43], v[40:43]\n\t" \
  "s_mov_b64 exec, s[56:57]\n\t" \
  "ds_write_b32 v80, v79 offset:16\n\t" \
  "s_mov_b64 exec, -1\n\t" \
  "v_mfma_f32_16x16x32_bf16 v[44:47], v[12:15], a[44:47], v[44:47]\n\t" \
  "s_waitcnt lgkmcnt(4)\n\t" \
  "v_mfma_f32_16x16x32_bf16 v[32:35], v[16:19], a[48:51], v[32:35]\n\t" \
  "ds_read_b128 v[8:11], v73 offset:9216\n\t" \
  "v_mfma_f32_16x16x32_bf16 v[36:39], v[16:19], a[52:55], v[36:39]\n\t" \
  "ds_read_b128 v[12:15], v73 offset:27648\n\t" \
  "v_mfma_f32_16x16x32_bf16 v[40:43], v[20:23], a[48:51], v[40:43]\n\t" \
  "v_mfma_f32_16x16x32_bf16 v[44:47], v[20:23], a[52:55], v[44:47]\n\t" \
  "s_waitcnt lgkmcnt(4)\n\t" \
  "v_mfma_f32_16x16x32_bf16 v[32:35], v[24:27], a[56:59], v[32:35]\n\t" \
  "ds_read_b128 v[16:19], v73 offset:10240\n\t" \
  "v_mfma_f32_16x16x32_bf16 v[36:39], v[24:27], a[60:63], v[36:39]\n\t" \
  "ds_read_b128 v[20:23], v73 offset:28672\n\t" \
  "v_mfma_f32_16x16x32_bf16 v[40:43], v[28:31], a[56:59], v[40:43]\n\t" \
  "v_mfma_f32_16x16x32_bf16 v[44:47], v[28:31], a[60:63], v[44:47]\n\t" \
  "s_waitcnt lgkmcnt(4)\n\t" \
  "v_mfma_f32_16x16x32_bf16 v[32:35], v[0:3], a[64:67], v[32:35]\n\t" \
  "ds_read_b128 v[24:27], v73 offset:11264\n\t" \
  "v_mfma_f32_16x16x32_bf16 v[36:39], v[0:3], a[68:71], v[36:39]\n\t" \
  "ds_read_b128 v[28:31], v73 offset:29696\n\t" \
  "v_mfma_f32_16x16x32_bf16 v[40:43], v[4:7], a[64:67], v[40:43]\n\t" \
  "v_mfma_f32_16x16x32_bf16 v[44:47], v[4:7], a[68:71], v[44:47]\n\t" \
  "s_waitcnt vmcnt(11)\n\t" \
  "s_barrier\n\t" \
  "s_waitcnt lgkmcnt(4)\n\t" \
  "v_mfma_f32_16x16x32_bf16 v[32:35], v[8:11], a[72:75], v[32:35]\n\t" \
  "ds_read_b128 v[0:3], v73 offset:12288\n\t" \
  "v_mov_b32 v77, 147456\n\t" \
  "v_mfma_f32_16x16x32_bf16 v[36:39], v[8:11], a[76:79], v[36:39]\n\t" \
  "ds_read_b128 v[4:7], v73 offset:30720\n\t" \
  "ds_read_b128 v[84:87], v77\n\t" \
  "v_mfma_f32_16x16x32_bf16 v[40:43], v[12:15], a[72:75], v[40:43]\n\t" \
  "s_waitcnt lgkmcnt(0)\n\t" \
  "v_max3_f32 v84, v84, v85, v86\n\t" \
  "v_mfma_f32_16x16x32_bf16 v[44:47], v[12:15], a[76:79], v[44:47]\n\t" \
  "v_max_f32 v84, v84, v87\n\t" \
  "v_or_b32 v77, 1, v84\n\t" \
  "s_waitcnt lgkmcnt(4)\n\t" \
  "v_mfma_f32_16x16x32_bf16 v[32:35], v[16:19], a[80:83], v[32:35]\n\t" \
  "ds_read_b128 v[8:11], v73 offset:13312\n\t" \
  "s_mov_b64 exec, s[54:55]\n\t" \
  "buffer_store_dword v77, off, s[20:23], s51 sc1\n\t" \
  "s_mov_b64 exec, -1\n\t" \
  "v_mfma_f32_16x16x32_bf16 v[36:39], v[16:19], a[84:87], v[36:39]\n\t" \
  "ds_read_b128 v[12:15], v73 offset:31744\n\t" \
  "s_add_u32 s51, s51, 1024\n\t" \
  "v_mfma_f32_16x16x32_bf16 v[40:43], v[20:23], a[80:83], v[40:43]\n\t" \
  "s_add_u32 s48, s58, 36864\n\t" \
  "s_mov_b32 m0, s48\n\t" \
  "v_mfma_f32_16x16x32_bf16 v[44:47], v[20:23], a[84:87], v[44:47]\n\t" \
  "s_nop 0\n\t" \
  "global_load_lds_dwordx4 v72, s[46:47] offset:-4096 sc1\n\t" \
  "s_waitcnt lgkmcnt(4)\n\t" \
  "v_mfma_f32_16x16x32_bf16 v[32:35], v[24:27], a[88:91], v[32:35]\n\t" \
  "ds_read_b128 v[16:19], v73 offset:14336\n\t" \
  "global_load_lds_dwordx4 v72, s[46:47] offset:-3072 sc1\n\t" \
  "v_mfma_f32_16x16x32_bf16 v[36:39], v[24:27], a[92:95], v[36:39]\n\t" \
  "ds_read_b128 v[20:23], v73 offset:32768\n\t" \
  "global_load_lds_dwordx4 v72, s[46:47] offset:-2048 sc1\n\t" \
  "v_mfma_f32_16x16x32_bf16 v[40:43], v[28:31], a[88:91], v[40:43]\n\t" \
  "global_load_lds_dwordx4 v72, s[46:47] offset:-1024 sc1\n\t" \
  "global_load_lds_dwordx4 v72, s[46:47] offset:0 sc1\n\t" \
  "v_mfma_f32_16x16x32_bf16 v[44:47], v[28:31], a[92:95], v[44:47]\n\t" \
  "global_load_lds_dwordx4 v72, s[46:47] offset:1024 sc1\n\t" \
  "global_load_lds_dwordx4 v72, s[46:47] offset:2048 sc1\n\t" \
  "s_waitcnt lgkmcnt(4)\n\t" \
  "v_mfma_f32_16x16x32_bf16 v[32:35], v[0:3], a[96:99], v[32:35]\n\t" \
  "ds_read_b128 v[24:27], v73 offset:15360\n\t" \
  "global_load_lds_dwordx4 v72, s[46:47] offset:3072 sc1\n\t" \
  "v_mfma_f32_16x16x32_bf16 v[36:39], v[0:3], a[100:103], v[36:39]\n\t" \
  "ds_read_b128 v[28:31], v73 offset:33792\n\t" \
  "s_add_u32 m0, s48, 4096\n\t" \
  "v_mfma_f32_16x16x32_bf16 v[40:43], v[4:7], a[96:99], v[40:43]\n\t" \
  "s_nop 0\n\t" \
  "global_load_lds_dwordx4 v78, s[46:47] sc1\n\t" \
  "v_mfma_f32_16x16x32_bf16 v[44:47], v[4:7], a[100:103], v[44:47]\n\t" \
  "s_add_u32 s46, s46, 73728\n\t" \
  "s_addc_u32 s47, s47, 0\n\t" \
  "s_waitcnt lgkmcnt(4)\n\t" \
  "v_mfma_f32_16x16x32_bf16 v[32:35], v[8:11], a[104:107], v[32:35]\n\t" \
  "ds_read_b128 v[0:3], v73 offset:16384\n\t" \
  "v_mfma_f32_16x16x32_bf16 v[36:39], v[8:11], a[108:111], v[36:39]\n\t" \
  "ds_read_b128 v[4:7], v73 offset:34816\n\t" \
  "v_mfma_f32_16x16x32_bf16 v[40:43], v[12:15], a[104:107], v[40:43]\n\t" \
  "v_mfma_f32_16x16x32_bf16 v[44:47], v[12:15], a[108:111], v[44:47]\n\t" \
  "s_waitcnt lgkmcnt(4)\n\t" \
  "v_mfma_f32_16x16x32_bf16 v[32:35], v[16:19], a[112:115], v[32:35]\n\t" \
  "ds_read_b128 v[8:11], v73 offset:17408\n\t" \
  "v_mfma_f32_16x16x32_bf16 v[36:39], v[16:19], a[116:119], v[36:39]\n\t" \
  "ds_read_b128 v[12:15], v73 offset:35840\n\t" \
  "v_mfma_f32_16x16x32_bf16 v[40:43], v[20:23], a[112:115], v[40:43]\n\t" \
  "v_mfma_f32_16x16x32_bf16 v[44:47], v[20:23], a[116:119], v[44:47]\n\t" \
  "s_waitcnt lgkmcnt(4)\n\t" \
  "v_mfma_f32_16x16x32_bf16 v[32:35], v[24:27], a[120:123], v[32:35]\n\t" \
  "ds_read_b128 v[16:19], v74 offset:0\n\t" \
  "v_mfma_f32_16x16x32_bf16 v[36:39], v[24:27], a[124:127], v[36:39]\n\t" \
  "ds_read_b128 v[20:23], v74 offset:18432\n\t" \
  "v_mfma_f32_16x16x32_bf16 v[40:43], v[28:31], a[120:123], v[40:43]\n\t" \
  "v_mfma_f32_16x16x32_bf16 v[44:47], v[28:31], a[124:127], v[44:47]\n\t" \
  "s_waitcnt lgkmcnt(4)\n\t" \
  "v_mfma_f32_16x16x32_bf16 v[32:35], v[0:3], a[128:131], v[32:35]\n\t" \
  "ds_read_b128 v[24:27], v74 offset:1024\n\t" \
  "v_mfma_f32_16x16x32_bf16 v[36:39], v[0:3], a[132:135], v[36:39]\n\t" \
  "ds_read_b128 v[28:31], v74 offset:19456\n\t" \
  "v_mfma_f32_16x16x32_bf16 v[40:43], v[4:7], a[128:131], v[40:43]\n\t" \
  "v_mfma_f32_16x16x32_bf16 v[44:47], v[4:7], a[132:135], v[44:47]\n\t" \
  "s_waitcnt lgkmcnt(4)\n\t" \
  "v_mfma_f32_16x16x32_bf16 v[32:35], v[8:11], a[136:139], v[32:35]\n\t" \
  "ds_read_b128 v[0:3], v74 offset:2048\n\t" \
  "v_mfma_f32_16x16x32_bf16 v[36:39], v[8:11], a[140:143], v[36:39]\n\t" \
  "ds_read_b128 v[4:7], v74 offset:20480\n\t" \
  "v_mfma_f32_16x16x32_bf16 v[40:43], v[12:15], a[136:139], v[40:43]\n\t" \
  "v_mfma_f32_16x16x32_bf16 v[44:47], v[12:15], a[140:143], v[44:47]\n\t" \
  "v_mov_b32 v73, v74\n\t" \
  "v_add_u32 v74, 0, v72\n\t" \
  "s_waitcnt lgkmcnt(4)\n\t" \
  "v_mfma_f32_16x16x32_bf16 v[48:51], v[16:19], a[0:3], 0\n\t" \
  "ds_read_b128 v[8:11], v73 offset:3072\n\t" \
  "v_mfma_f32_16x16x32_bf16 v[52:55], v[16:19], a[4:7], 0\n\t" \
  "ds_read_b128 v[12:15], v73 offset:21504\n\t" \
  "v_mfma_f32_16x16x32_bf16 v[56:59], v[20:23], a[0:3], 0\n\t" \
  "v_mfma_f32_16x16x32_bf16 v[60:63], v[20:23], a[4:7], 0\n\t" \
  "s_waitcnt lgkmcnt(4)\n\t" \
  "v_mfma_f32_16x16x32_bf16 v[48:51], v[24:27], a[8:11], v[48:51]\n\t" \
  "ds_read_b128 v[16:19], v73 offset:4096\n\t" \
  "v_cvt_pk_bf16_f32 v64, v32, v33\n\t" \
  "v_mfma_f32_16x16x32_bf16 v[52:55], v[24:27], a[12:15], v[52:55]\n\t" \
  "ds_read_b128 v[20:23], v73 offset:22528\n\t" \
  "v_cvt_pk_bf16_f32 v65, v34, v35\n\t" \
  "v_mfma_f32_16x16x32_bf16 v[56:59], v[28:31], a[8:11], v[56:59]\n\t" \
  "v_cvt_pk_bf16_f32 v66, v40, v41\n\t" \
  "v_cvt_pk_bf16_f32 v67, v42, v43\n\t" \
  "v_mfma_f32_16x16x32_bf16 v[60:63], v[28:31], a[12:15], v[60:63]\n\t" \
  "buffer_store_dwordx4 v[64:67], v72, s[20:23], s50 offen offset:0 sc1\n\t" \
  "v_cvt_pk_bf16_f32 v68, v36, v37\n\t" \
  "s_waitcnt lgkmcnt(4)\n\t" \
  "v_mfma_f32_16x16x32_bf16 v[48:51], v[0:3], a[16:19], v[48:51]\n\t" \
  "ds_read_b128 v[24:27], v73 offset:5120\n\t" \
  "v_cvt_pk_bf16_f32 v69, v38, v39\n\t" \
  "v_mfma_f32_16x16x32_bf16 v[52:55], v[0:3], a[20:23], v[52:55]\n\t" \
  "ds_read_b128 v[28:31], v73 offset:23552\n\t" \
  "v_cvt_pk_bf16_f32 v70, v44, v45\n\t" \
  "v_mfma_f32_16x16x32_bf16 v[56:59], v[4:7], a[16:19], v[56:59]\n\t" \
  "v_cvt_pk_bf16_f32 v71, v46, v47\n\t" \
  "buffer_store_dwordx4 v[68:71], v72, s[20:23], s50 offen offset:1024 sc1\n\t" \
  "v_mfma_f32_16x16x32_bf16 v[60:63], v[4:7], a[20:23], v[60:63]\n\t" \
  "s_add_u32 s50, s50, 524288\n\t" \
  "v_max3_f32 v79, |v32|, |v33|, |v34|\n\t" \
  "s_waitcnt lgkmcnt(4)\n\t" \
  "v_mfma_f32_16x16x32_bf16 v[48:51], v[8:11], a[24:27], v[48:51]\n\t" \
  "ds_read_b128 v[0:3], v73 offset:6144\n\t" \
  "v_max3_f32 v79, v79, |v35|, |v36|\n\t" \
  "v_mfma_f32_16x16x32_bf16 v[52:55], v[8:11], a[28:31], v[52:55]\n\t" \
  "ds_read_b128 v[4:7], v73 offset:24576\n\t" \
  "v_max3_f32 v79, v79, |v37|, |v38|\n\t" \
  "v_mfma_f32_16x16x32_bf16 v[56:59], v[12:15], a[24:27], v[56:59]\n\t" \
  "v_max3_f32 v79, v79, |v39|, |v40|\n\t" \
  "v_max3_f32 v79, v79, |v41|, |v42|\n\t" \
  "v_mfma_f32_16x16x32_bf16 v[60:63], v[12:15], a[28:31], v[60:63]\n\t" \
  "v_max3_f32 v79, v79, |v43|, |v44|\n\t" \
  "v_max3_f32 v79, v79, |v45|, |v46|\n\t" \
  "s_waitcnt lgkmcnt(4)\n\t" \
  "v_mfma_f32_16x16x32_bf16 v[48:51], v[16:19], a[32:35], v[48:51]\n\t" \
  "ds_read_b128 v[8:11], v73 offset:7168\n\t" \
  "v_max_f32 v79, v79, |v47|\n\t" \
  "v_mfma_f32_16x16x32_bf16 v[52:55], v[16:19], a[36:39], v[52:55]\n\t" \
  "ds_read_b128 v[12:15], v73 offset:25600\n\t" \
  "v_max_f32_dpp v79, v79, v79 row_ror:1 row_mask:0xf bank_mask:0xf\n\t" \
  "v_mfma_f32_16x16x32_bf16 v[56:59], v[20:23], a[32:35], v[56:59]\n\t" \
  "s_nop 0\n\t" \
  "v_max_f32_dpp v79, v79, v79 row_ror:2 row_mask:0xf bank_mask:0xf\n\t" \
  "s_nop 1\n\t" \
  "v_max_f32_dpp v79, v79, v79 row_ror:4 row_mask:0xf bank_mask:0xf\n\t" \
  "v_mfma_f32_16x16x32_bf16 v[60:63], v[20:23], a[36:39], v[60:63]\n\t" \
  "s_nop 0\n\t" \
  "v_max_f32_dpp v79, v79, v79 row_ror:8 row_mask:0xf bank_mask:0xf\n\t" \
  "s_nop 1\n\t" \
  "v_max_f32_dpp v79, v79, v79 row_bcast:15 row_mask:0xa bank_mask:0xf\n\t" \
  "s_waitcnt lgkmcnt(4)\n\t" \
  "v_mfma_f32_16x16x32_bf16 v[48:51], v[24:27], a[40:43], v[48:51]\n\t" \
  "ds_read_b128 v[16:19], v73 offset:8192\n\t" \
  "v_max_f32_dpp v79, v79, v79 row_bcast:31 row_mask:0xc bank_mask:0xf\n\t" \
  "v_mfma_f32_16x16x32_bf16 v[52:55], v[24:27], a[44:47], v[52:55]\n\t" \
  "ds_read_b128 v[20:23], v73 offset:26624\n\t" \
  "v_mul_f32 v79, 0x3f810000, v79\n\t" \
  "v_mfma_f32_16x16x32_bf16 v[56:59], v[28:31], a[40:43], v[56:59]\n\t" \
  "s_mov_b64 exec, s[56:57]\n\t" \
  "ds_write_b32 v80, v79 offset:32\n\t" \
  "s_mov_b64 exec, -1\n\t" \
  "v_mfma_f32_16x16x32_bf16 v[60:63], v[28:31], a[44:47], v[60:63]\n\t" \
  "s_waitcnt lgkmcnt(4)\n\t" \
  "v_mfma_f32_16x16x32_bf16 v[48:51], v[0:3], a[48:51], v[48:51]\n\t" \
  "ds_read_b128 v[24:27], v73 offset:9216\n\t" \
  "v_mfma_f32_16x16x32_bf16 v[52:55], v[0:3], a[52:55], v[52:55]\n\t" \
  "ds_read_b128 v[28:31], v73 offset:27648\n\t" \
  "v_mfma_f32_16x16x32_bf16 v[56:59], v[4:7], a[48:51], v[56:59]\n\t" \
  "v_mfma_f32_16x16x32_bf16 v[60:63], v[4:7], a[52:55], v[60:63]\n\t" \
  "s_waitcnt lgkmcnt(4)\n\t" \
  "v_mfma_f32_16x16x32_bf16 v[48:51], v[8:11], a[56:59], v[48:51]\n\t" \
  "ds_read_b128 v[0:3], v73 offset:10240\n\t" \
  "v_mfma_f32_16x16x32_bf16 v[52:55], v[8:11], a[60:63], v[52:55]\n\t" \
  "ds_read_b128 v[4:7], v73 offset:28672\n\t" \
  "v_mfma_f32_16x16x32_bf16 v[56:59], v[12:15], a[56:59], v[56:59]\n\t" \
  "v_mfma_f32_16x16x32_bf16 v[60:63], v[12:15], a[60:63], v[60:63]\n\t" \
  "s_waitcnt lgkmcnt(4)\n\t" \
  "v_mfma_f32_16x16x32_bf16 v[48:51], v[16:19], a[64:67], v[48:51]\n\t" \
  "ds_read_b128 v[8:11], v73 offset:11264\n\t" \
  "v_mfma_f32_16x16x32_bf16 v[52:55], v[16:19], a[68:71], v[52:55]\n\t" \
  "ds_read_b128 v[12:15], v73 offset:29696\n\t" \
  "v_mfma_f32_16x16x32_bf16 v[56:59], v[20:23], a[64:67], v[56:59]\n\t" \
  "v_mfma_f32_16x16x32_bf16 v[60:63], v[20:23], a[68:71], v[60:63]\n\t" \
  "s_waitcnt vmcnt(11)\n\t" \
  "s_barrier\n\t" \
  "s_waitcnt lgkmcnt(4)\n\t" \
  "v_mfma_f32_16x16x32_bf16 v[48:51], v[24:27], a[72:75], v[48:51]\n\t" \
  "ds_read_b128 v[16:19], v73 offset:12288\n\t" \
  "v_mov_b32 v77, 147472\n\t" \
  "v_mfma_f32_16x16x32_bf16 v[52:55], v[24:27], a[76:79], v[52:55]\n\t" \
  "ds_read_b128 v[20:23], v73 offset:30720\n\t" \
  "ds_read_b128 v[84:87], v77\n\t" \
  "v_mfma_f32_16x16x32_bf16 v[56:59], v[28:31], a[72:75], v[56:59]\n\t" \
  "s_waitcnt lgkmcnt(0)\n\t" \
  "v_max3_f32 v84, v84, v85, v86\n\t" \
  "v_mfma_f32_16x16x32_bf16 v[60:63], v[28:31], a[76:79], v[60:63]\n\t" \
  "v_max_f32 v84, v84, v87\n\t" \
  "v_or_b32 v77, 1, v84\n\t" \
  "s_waitcnt lgkmcnt(4)\n\t" \
  "v_mfma_f32_16x16x32_bf16 v[48:51], v[0:3], a[80:83], v[48:51]\n\t" \
  "ds_read_b128 v[24:27], v73 offset:13312\n\t" \
  "s_mov_b64 exec, s[54:55]\n\t" \
  "buffer_store_dword v77, off, s[20:23], s51 sc1\n\t" \
  "s_mov_b64 exec, -1\n\t" \
  "v_mfma_f32_16x16x32_bf16 v[52:55], v[0:3], a[84:87], v[52:55]\n\t" \
  "ds_read_b128 v[28:31], v73 offset:31744\n\t" \
  "s_add_u32 s51, s51, 1024\n\t" \
  "v_mfma_f32_16x16x32_bf16 v[56:59], v[4:7], a[80:83], v[56:59]\n\t" \
  "s_add_u32 s48, s58, 73728\n\t" \
  "s_mov_b32 m0, s48\n\t" \
  "v_mfma_f32_16x16x32_bf16 v[60:63], v[4:7], a[84:87], v[60:63]\n\t" \
  "s_nop 0\n\t" \
  "global_load_lds_dwordx4 v72, s[46:47] offset:-4096 sc1\n\t" \
  "s_waitcnt lgkmcnt(4)\n\t" \
  "v_mfma_f32_16x16x32_bf16 v[48:51], v[8:11], a[88:91], v[48:51]\n\t" \
  "ds_read_b128 v[0:3], v73 offset:14336\n\t" \
  "global_load_lds_dwordx4 v72, s[46:47] offset:-3072 sc1\n\t" \
  "v_mfma_f32_16x16x32_bf16 v[52:55], v[8:11], a[92:95], v[52:55]\n\t" \
  "ds_read_b128 v[4:7], v73 offset:32768\n\t" \
  "global_load_lds_dwordx4 v72, s[46:47] offset:-2048 sc1\n\t" \
  "v_mfma_f32_16x16x32_bf16 v[56:59], v[12:15], a[88:91], v[56:59]\n\t" \
  "global_load_lds_dwordx4 v72, s[46:47] offset:-1024 sc1\n\t" \
  "global_load_lds_dwordx4 v72, s[46:47] offset:0 sc1\n\t" \
  "v_mfma_f32_16x16x32_bf16 v[60:63], v[12:15], a[92:95], v[60:63]\n\t" \
  "global_load_lds_dwordx4 v72, s[46:47] offset:1024 sc1\n\t" \
  "global_load_lds_dwordx4 v72, s[46:47] offset:2048 sc1\n\t" \
  "s_waitcnt lgkmcnt(4)\n\t" \
  "v_mfma_f32_16x16x32_bf16 v[48:51], v[16:19], a[96:99], v[48:51]\n\t" \
  "ds_read_b128 v[8:11], v73 offset:15360\n\t" \
  "global_load_lds_dwordx4 v72, s[46:47] offset:3072 sc1\n\t" \
  "v_mfma_f32_16x16x32_bf16 v[52:55], v[16:19], a[100:103], v[52:55]\n\t" \
  "ds_read_b128 v[12:15], v73 offset:33792\n\t" \
  "s_add_u32 m0, s48, 4096\n\t" \
  "v_mfma_f32_16x16x32_bf16 v[56:59], v[20:23], a[96:99], v[56:59]\n\t" \
  "s_nop 0\n\t" \
  "global_load_lds_dwordx4 v78, s[46:47] sc1\n\t" \
  "v_mfma_f32_16x16x32_bf16 v[60:63], v[20:23], a[100:103], v[60:63]\n\t" \
  "s_add_u32 s46, s46, 73728\n\t" \
  "s_addc_u32 s47, s47, 0\n\t" \
  "s_waitcnt lgkmcnt(4)\n\t" \
  "v_mfma_f32_16x16x32_bf16 v[48:51], v[24:27], a[104:107], v[48:51]\n\t" \
  "ds_read_b128 v[16:19], v73 offset:16384\n\t" \
  "v_mfma_f32_16x16x32_bf16 v[52:55], v[24:27], a[108:111], v[52:55]\n\t" \
  "ds_read_b128 v[20:23], v73 offset:34816\n\t" \
  "v_mfma_f32_16x16x32_bf16 v[56:59], v[28:31], a[104:107], v[56:59]\n\t" \
  "v_mfma_f32_16x16x32_bf16 v[60:63], v[28:31], a[108:111], v[60:63]\n\t" \
  "s_waitcnt lgkmcnt(4)\n\t" \
  "v_mfma_f32_16x16x32_bf16 v[48:51], v[0:3], a[112:115], v[48:51]\n\t" \
  "ds_read_b128 v[24:27], v73 offset:17408\n\t" \
  "v_mfma_f32_16x16x32_bf16 v[52:55], v[0:3], a[116:119], v[52:55]\n\t" \
  "ds_read_b128 v[28:31], v73 offset:35840\n\t" \
  "v_mfma_f32_16x16x32_bf16 v[56:59], v[4:7], a[112:115], v[56:59]\n\t" \
  "v_mfma_f32_16x16x32_bf16 v[60:63], v[4:7], a[116:119], v[60:63]\n\t" \
  "s_waitcnt lgkmcnt(4)\n\t" \
  "v_mfma_f32_16x16x32_bf16 v[48:51], v[8:11], a[120:123], v[48:51]\n\t" \
  "ds_read_b128 v[0:3], v74 offset:0\n\t" \
  "v_mfma_f32_16x16x32_bf16 v[52:55], v[8:11], a[124:127], v[52:55]\n\t" \
  "ds_read_b128 v[4:7], v74 offset:18432\n\t" \
  "v_mfma_f32_16x16x32_bf16 v[56:59], v[12:15], a[120:123], v[56:59]\n\t" \
  "v_mfma_f32_16x16x32_bf16 v[60:63], v[12:15], a[124:127], v[60:63]\n\t" \
  "s_waitcnt lgkmcnt(4)\n\t" \
  "v_mfma_f32_16x16x32_bf16 v[48:51], v[16:19], a[128:131], v[48:51]\n\t" \
  "ds_read_b128 v[8:11], v74 offset:1024\n\t" \
  "v_mfma_f32_16x16x32_bf16 v[52:55], v[16:19], a[132:135], v[52:55]\n\t" \
  "ds_read_b128 v[12:15], v74 offset:19456\n\t" \
  "v_mfma_f32_16x16x32_bf16 v[56:59], v[20:23], a[128:131], v[56:59]\n\t" \
  "v_mfma_f32_16x16x32_bf16 v[60:63], v[20:23], a[132:135], v[60:63]\n\t" \
  "s_waitcnt lgkmcnt(4)\n\t" \
  "v_mfma_f32_16x16x32_bf16 v[48:51], v[24:27], a[136:139], v[48:51]\n\t" \
  "ds_read_b128 v[16:19], v74 offset:2048\n\t" \
  "v_mfma_f32_16x16x32_bf16 v[52:55], v[24:27], a[140:143], v[52:55]\n\t" \
  "ds_read_b128 v[20:23], v74 offset:20480\n\t" \
  "v_mfma_f32_16x16x32_bf16 v[56:59], v[28:31], a[136:139], v[56:59]\n\t" \
  "v_mfma_f32_16x16x32_bf16 v[60:63], v[28:31], a[140:143], v[60:63]\n\t" \
  "v_mov_b32 v73, v74\n\t" \
  "s_sub_u32 s45, s45, 1\n\t" \
  "s_cmp_lg_u32 s45, 0\n\t" \
  "s_cbranch_scc1 Lq_%=\n\t" \
  "s_cmp_eq_u32 s28, 0\n\t" \
  "s_cbranch_scc1 Lx3_%=\n\t" \
  "v_mov_b32 v75, 2159100\n\t" \
  "buffer_load_dword v84, v75, s[20:23], 0 offen sc1\n\t" \
  "s_mov_b32 s42, 0\n\t" \
  "s_waitcnt vmcnt(0)\n\t" \
  "Lxl0_%=:\n\t" \
  "v_cmp_eq_u32 vcc, 0, v84\n\t" \
  "s_nop 0\n\t" \
  "s_cbranch_vccz Lxl1_%=\n\t" \
  "s_add_u32 s42, s42, 1\n\t" \
  "s_cmp_gt_u32 s42, 524288\n\t" \
  "s_cbranch_scc1 Ltmo_%=\n\t" \
  "s_sleep 2\n\t" \
  "buffer_load_dword v84, v75, s[20:23], 0 offen sc1\n\t" \
  "s_waitcnt vmcnt(0)\n\t" \
  "s_branch Lxl0_%=\n\t" \
  "Lxl1_%=:\n\t" \
  "Lx3_%=:\n\t" \
  "v_add_u32 v74, 36864, v72\n\t" \
  "s_waitcnt lgkmcnt(4)\n\t" \
  "v_mfma_f32_16x16x32_bf16 v[32:35], v[0:3], a[0:3], 0\n\t" \
  "ds_read_b128 v[24:27], v73 offset:3072\n\t" \
  "v_mfma_f32_16x16x32_bf16 v[36:39], v[0:3], a[4:7], 0\n\t" \
  "ds_read_b128 v[28:31], v73 offset:21504\n\t" \
  "v_mfma_f32_16x16x32_bf16 v[40:43], v[4:7], a[0:3], 0\n\t" \
  "v_mfma_f32_16x16x32_bf16 v[44:47], v[4:7], a[4:7], 0\n\t" \
  "s_waitcnt lgkmcnt(4)\n\t" \
  "v_mfma_f32_16x16x32_bf16 v[32:35], v[8:11], a[8:11], v[32:35]\n\t" \
  "ds_read_b128 v[0:3], v73 offset:4096\n\t" \
  "v_cvt_pk_bf16_f32 v64, v48, v49\n\t" \
  "v_mfma_f32_16x16x32_bf16 v[36:39], v[8:11], a[12:15], v[36:39]\n\t" \
  "ds_read_b128 v[4:7], v73 offset:22528\n\t" \
  "v_cvt_pk_bf16_f32 v65, v50, v51\n\t" \
  "v_mfma_f32_16x16x32_bf16 v[40:43], v[12:15], a[8:11], v[40:43]\n\t" \
  "v_cvt_pk_bf16_f32 v66, v56, v57\n\t" \
  "v_cvt_pk_bf16_f32 v67, v58, v59\n\t" \
  "v_mfma_f32_16x16x32_bf16 v[44:47], v[12:15], a[12:15], v[44:47]\n\t" \
  "buffer_store_dwordx4 v[64:67], v72, s[20:23], s50 offen offset:0 sc1\n\t" \
  "v_cvt_pk_bf16_f32 v68, v52, v53\n\t" \
  "s_waitcnt lgkmcnt(4)\n\t" \
  "v_mfma_f32_16x16x32_bf16 v[32:35], v[16:19], a[16:19], v[32:35]\n\t" \
  "ds_read_b128 v[8:11], v73 offset:5120\n\t" \
  "v_cvt_pk_bf16_f32 v69, v54, v55\n\t" \
  "v_mfma_f32_16x16x32_bf16 v[36:39], v[16:19], a[20:23], v[36:39]\n\t" \
  "ds_read_b128 v[12:15], v73 offset:23552\n\t" \
  "v_cvt_pk_bf16_f32 v70, v60, v61\n\t" \
  "v_mfma_f32_16x16x32_bf16 v[40:43], v[20:23], a[16:19], v[40:43]\n\t" \
  "v_cvt_pk_bf16_f32 v71, v62, v63\n\t" \
  "buffer_store_dwordx4 v[68:71], v72, s[20:23], s50 offen offset:1024 sc1\n\t" \
  "v_mfma_f32_16x16x32_bf16 v[44:47], v[20:23], a[20:23], v[44:47]\n\t" \
  "s_add_u32 s50, s50, 524288\n\t" \
  "v_max3_f32 v79, |v48|, |v49|, |v50|\n\t" \
  "s_waitcnt lgkmcnt(4)\n\t" \
  "v_mfma_f32_16x16x32_bf16 v[32:35], v[24:27], a[24:27], v[32:35]\n\t" \
  "ds_read_b128 v[16:19], v73 offset:6144\n\t" \
  "v_max3_f32 v79, v79, |v51|, |v52|\n\t" \
  "v_mfma_f32_16x16x32_bf16 v[36:39], v[24:27], a[28:31], v[36:39]\n\t" \
  "ds_read_b128 v[20:23], v73 offset:24576\n\t" \
  "v_max3_f32 v79, v79, |v53|, |v54|\n\t" \
  "v_mfma_f32_16x16x32_bf16 v[40:43], v[28:31], a[24:27], v[40:43]\n\t" \
  "v_max3_f32 v79, v79, |v55|, |v56|\n\t" \
  "v_max3_f32 v79, v79, |v57|, |v58|\n\t" \
  "v_mfma_f32_16x16x32_bf16 v[44:47], v[28:31], a[28:31], v[44:47]\n\t" \
  "v_max3_f32 v79, v79, |v59|, |v60|\n\t" \
  "v_max3_f32 v79, v79, |v61|, |v62|\n\t" \
  "s_waitcnt lgkmcnt(4)\n\t" \
  "v_mfma_f32_16x16x32_bf16 v[32:35], v[0:3], a[32:35], v[32:35]\n\t" \
  "ds_read_b128 v[24:27], v73 offset:7168\n\t" \
  "v_max_f32 v79, v79, |v63|\n\t" \
  "v_mfma_f32_16x16x32_bf16 v[36:39], v[0:3], a[36:39], v[36:39]\n\t" \
  "ds_read_b128 v[28:31], v73 offset:25600\n\t" \
  "v_max_f32_dpp v79, v79, v79 row_ror:1 row_mask:0xf bank_mask:0xf\n\t" \
  "v_mfma_f32_16x16x32_bf16 v[40:43], v[4:7], a[32:35], v[40:43]\n\t" \
  "s_nop 0\n\t" \
  "v_max_f32_dpp v79, v79, v79 row_ror:2 row_mask:0xf bank_mask:0xf\n\t" \
  "s_nop 1\n\t" \
  "v_max_f32_dpp v79, v79, v79 row_ror:4 row_mask:0xf bank_mask:0xf\n\t" \
  "v_mfma_f32_16x16x32_bf16 v[44:47], v[4:7], a[36:39], v[44:47]\n\t" \
  "s_nop 0\n\t" \
  "v_max_f32_dpp v79, v79, v79 row_ror:8 row_mask:0xf bank_mask:0xf\n\t" \
  "s_nop 1\n\t" \
  "v_max_f32_dpp v79, v79, v79 row_bcast:15 row_mask:0xa bank_mask:0xf\n\t" \
  "s_waitcnt lgkmcnt(4)\n\t" \
  "v_mfma_f32_16x16x32_bf16 v[32:35], v[8:11], a[40:43], v[32:35]\n\t" \
  "ds_read_b128 v[0:3], v73 offset:8192\n\t" \
  "v_max_f32_dpp v79, v79, v79 row_bcast:31 row_mask:0xc bank_mask:0xf\n\t" \
  "v_mfma_f32_16x16x32_bf16 v[36:39], v[8:11], a[44:47], v[36:39]\n\t" \
  "ds_read_b128 v[4:7], v73 offset:26624\n\t" \
  "v_mul_f32 v79, 0x3f810000, v79\n\t" \
  "v_mfma_f32_16x16x32_bf16 v[40:43], v[12:15], a[40:43], v[40:43]\n\t" \
  "s_mov_b64 exec, s[56:57]\n\t" \
  "ds_write_b32 v80, v79 offset:48\n\t" \
  "s_mov_b64 exec, -1\n\t" \
  "v_mfma_f32_16x16x32_bf16 v[44:47], v[12:15], a[44:47], v[44:47]\n\t" \
  "s_waitcnt lgkmcnt(4)\n\t" \
  "v_mfma_f32_16x16x32_bf16 v[32:35], v[16:19], a[48:51], v[32:35]\n\t" \
  "ds_read_b128 v[8:11], v73 offset:9216\n\t" \
  "v_mfma_f32_16x16x32_bf16 v[36:39], v[16:19], a[52:55], v[36:39]\n\t" \
  "ds_read_b128 v[12:15], v73 offset:27648\n\t" \
  "v_mfma_f32_16x16x32_bf16 v[40:43], v[20:23], a[48:51], v[40:43]\n\t" \
  "v_mfma_f32_16x16x32_bf16 v[44:47], v[20:23], a[52:55], v[44:47]\n\t" \
  "s_waitcnt lgkmcnt(4)\n\t" \
  "v_mfma_f32_16x16x32_bf16 v[32:35], v[24:27], a[56:59], v[32:35]\n\t" \
  "ds_read_b128 v[16:19], v73 offset:10240\n\t" \
  "v_mfma_f32_16x16x32_bf16 v[36:39], v[24:27], a[60:63], v[36:39]\n\t" \
  "ds_read_b128 v[20:23], v73 offset:28672\n\t" \
  "v_mfma_f32_16x16x32_bf16 v[40:43], v[28:31], a[56:59], v[40:43]\n\t" \
  "v_mfma_f32_16x16x32_bf16 v[44:47], v[28:31], a[60:63], v[44:47]\n\t" \
  "s_waitcnt lgkmcnt(4)\n\t" \
  "v_mfma_f32_16x16x32_bf16 v[32:35], v[0:3], a[64:67], v[32:35]\n\t" \
  "ds_read_b128 v[24:27], v73 offset:11264\n\t" \
  "v_mfma_f32_16x16x32_bf16 v[36:39], v[0:3], a[68:71], v[36:39]\n\t" \
  "ds_read_b128 v[28:31], v73 offset:29696\n\t" \
  "v_mfma_f32_16x16x32_bf16 v[40:43], v[4:7], a[64:67], v[40:43]\n\t" \
  "v_mfma_f32_16x16x32_bf16 v[44:47], v[4:7], a[68:71], v[44:47]\n\t" \
  "s_waitcnt vmcnt(11)\n\t" \
  "s_barrier\n\t" \
  "s_waitcnt lgkmcnt(4)\n\t" \
  "v_mfma_f32_16x16x32_bf16 v[32:35], v[8:11], a[72:75], v[32:35]\n\t" \
  "ds_read_b128 v[0:3], v73 offset:12288\n\t" \
  "v_mov_b32 v77, 147488\n\t" \
  "v_mfma_f32_16x16x32_bf16 v[36:39], v[8:11], a[76:79], v[36:39]\n\t" \
  "ds_read_b128 v[4:7], v73 offset:30720\n\t" \
  "ds_read_b128 v[84:87], v77\n\t" \
  "v_mfma_f32_16x16x32_bf16 v[40:43], v[12:15], a[72:75], v[40:43]\n\t" \
  "s_waitcnt lgkmcnt(0)\n\t" \
  "v_max3_f32 v84, v84, v85, v86\n\t" \
  "v_mfma_f32_16x16x32_bf16 v[44:47], v[12:15], a[76:79], v[44:47]\n\t" \
  "v_max_f32 v84, v84, v87\n\t" \
  "v_or_b32 v77, 1, v84\n\t" \
  "s_waitcnt lgkmcnt(4)\n\t" \
  "v_mfma_f32_16x16x32_bf16 v[32:35], v[16:19], a[80:83], v[32:35]\n\t" \
  "ds_read_b128 v[8:11], v73 offset:13312\n\t" \
  "s_mov_b64 exec, s[54:55]\n\t" \
  "buffer_store_dword v77, off, s[20:23], s51 sc1\n\t" \
  "s_mov_b64 exec, -1\n\t" \
  "v_mfma_f32_16x16x32_bf16 v[36:39], v[16:19], a[84:87], v[36:39]\n\t" \
  "ds_read_b128 v[12:15], v73 offset:31744\n\t" \
  "s_add_u32 s51, s51, 1024\n\t" \
  "v_mfma_f32_16x16x32_bf16 v[40:43], v[20:23], a[80:83], v[40:43]\n\t" \
  "s_add_u32 s48, s58, 110592\n\t" \
  "s_mov_b32 m0, s48\n\t" \
  "v_mfma_f32_16x16x32_bf16 v[44:47], v[20:23], a[84:87], v[44:47]\n\t" \
  "s_nop 0\n\t" \
  "global_load_lds_dwordx4 v72, s[46:47] offset:-4096 sc1\n\t" \
  "s_waitcnt lgkmcnt(4)\n\t" \
  "v_mfma_f32_16x16x32_bf16 v[32:35], v[24:27], a[88:91], v[32:35]\n\t" \
  "ds_read_b128 v[16:19], v73 offset:14336\n\t" \
  "global_load_lds_dwordx4 v72, s[46:47] offset:-3072 sc1\n\t" \
  "v_mfma_f32_16x16x32_bf16 v[36:39], v[24:27], a[92:95], v[36:39]\n\t" \
  "ds_read_b128 v[20:23], v73 offset:32768\n\t" \
  "global_load_lds_dwordx4 v72, s[46:47] offset:-2048 sc1\n\t" \
  "v_mfma_f32_16x16x32_bf16 v[40:43], v[28:31], a[88:91], v[40:43]\n\t" \
  "global_load_lds_dwordx4 v72, s[46:47] offset:-1024 sc1\n\t" \
  "global_load_lds_dwordx4 v72, s[46:47] offset:0 sc1\n\t" \
  "v_mfma_f32_16x16x32_bf16 v[44:47], v[28:31], a[92:95], v[44:47]\n\t" \
  "global_load_lds_dwordx4 v72, s[46:47] offset:1024 sc1\n\t" \
  "global_load_lds_dwordx4 v72, s[46:47] offset:2048 sc1\n\t" \
  "s_waitcnt lgkmcnt(4)\n\t" \
  "v_mfma_f32_16x16x32_bf16 v[32:35], v[0:3], a[96:99], v[32:35]\n\t" \
  "ds_read_b128 v[24:27], v73 offset:15360\n\t" \
  "global_load_lds_dwordx4 v72, s[46:47] offset:3072 sc1\n\t" \
  "v_mfma_f32_16x16x32_bf16 v[36:39], v[0:3], a[100:103], v[36:39]\n\t" \
  "ds_read_b128 v[28:31], v73 offset:33792\n\t" \
  "s_add_u32 m0, s48, 4096\n\t" \
  "v_mfma_f32_16x16x32_bf16 v[40:43], v[4:7], a[96:99], v[40:43]\n\t" \
  "s_nop 0\n\t" \
  "global_load_lds_dwordx4 v78, s[46:47] sc1\n\t" \
  "v_mfma_f32_16x16x32_bf16 v[44:47], v[4:7], a[100:103], v[44:47]\n\t" \
  "s_add_u32 s46, s46, 73728\n\t" \
  "s_addc_u32 s47, s47, 0\n\t" \
  "s_waitcnt lgkmcnt(4)\n\t" \
  "v_mfma_f32_16x16x32_bf16 v[32:35], v[8:11], a[104:107], v[32:35]\n\t" \
  "ds_read_b128 v[0:3], v73 offset:16384\n\t" \
  "v_mfma_f32_16x16x32_bf16 v[36:39], v[8:11], a[108:111], v[36:39]\n\t" \
  "ds_read_b128 v[4:7], v73 offset:34816\n\t" \
  "v_mfma_f32_16x16x32_bf16 v[40:43], v[12:15], a[104:107], v[40:43]\n\t" \
  "v_mfma_f32_16x16x32_bf16 v[44:47], v[12:15], a[108:111], v[44:47]\n\t" \
  "s_waitcnt lgkmcnt(4)\n\t" \
  "v_mfma_f32_16x16x32_bf16 v[32:35], v[16:19], a[112:115], v[32:35]\n\t" \
  "ds_read_b128 v[8:11], v73 offset:17408\n\t" \
  "v_mfma_f32_16x16x32_bf16 v[36:39], v[16:19], a[116:119], v[36:39]\n\t" \
  "ds_read_b128 v[12:15], v73 offset:35840\n\t" \
  "v_mfma_f32_16x16x32_bf16 v[40:43], v[20:23], a[112:115], v[40:43]\n\t" \
  "v_mfma_f32_16x16x32_bf16 v[44:47], v[20:23], a[116:119], v[44:47]\n\t" \
  "s_waitcnt lgkmcnt(4)\n\t" \
  "v_mfma_f32_16x16x32_bf16 v[32:35], v[24:27], a[120:123], v[32:35]\n\t" \
  "ds_read_b128 v[16:19], v74 offset:0\n\t" \
  "v_mfma_f32_16x16x32_bf16 v[36:39], v[24:27], a[124:127], v[36:39]\n\t" \
  "ds_read_b128 v[20:23], v74 offset:18432\n\t" \
  "v_mfma_f32_16x16x32_bf16 v[40:43], v[28:31], a[120:123], v[40:43]\n\t" \
  "v_mfma_f32_16x16x32_bf16 v[44:47], v[28:31], a[124:127], v[44:47]\n\t" \
  "s_waitcnt lgkmcnt(4)\n\t" \
  "v_mfma_f32_16x16x32_bf16 v[32:35], v[0:3], a[128:131], v[32:35]\n\t" \
  "ds_read_b128 v[24:27], v74 offset:1024\n\t" \
  "v_mfma_f32_16x16x32_bf16 v[36:39], v[0:3], a[132:135], v[36:39]\n\t" \
  "ds_read_b128 v[28:31], v74 offset:19456\n\t" \
  "v_mfma_f32_16x16x32_bf16 v[40:43], v[4:7], a[128:131], v[40:43]\n\t" \
  "v_mfma_f32_16x16x32_bf16 v[44:47], v[4:7], a[132:135], v[44:47]\n\t" \
  "s_waitcnt lgkmcnt(4)\n\t" \
  "v_mfma_f32_16x16x32_bf16 v[32:35], v[8:11], a[136:139], v[32:35]\n\t" \
  "ds_read_b128 v[0:3], v74 offset:2048\n\t" \
  "v_mfma_f32_16x16x32_bf16 v[36:39], v[8:11], a[140:143], v[36:39]\n\t" \
  "ds_read_b128 v[4:7], v74 offset:20480\n\t" \
  "v_mfma_f32_16x16x32_bf16 v[40:43], v[12:15], a[136:139], v[40:43]\n\t" \
  "v_mfma_f32_16x16x32_bf16 v[44:47], v[12:15], a[140:143], v[44:47]\n\t" \
  "v_mov_b32 v73, v74\n\t" \
  "v_add_u32 v74, 73728, v72\n\t" \
  "s_waitcnt lgkmcnt(4)\n\t" \
  "v_mfma_f32_16x16x32_bf16 v[48:51], v[16:19], a[0:3], 0\n\t" \
  "ds_read_b128 v[8:11], v73 offset:3072\n\t" \
  "v_mfma_f32_16x16x32_bf16 v[52:55], v[16:19], a[4:7], 0\n\t" \
  "ds_read_b128 v[12:15], v73 offset:21504\n\t" \
  "v_mfma_f32_16x16x32_bf16 v[56:59], v[20:23], a[0:3], 0\n\t" \
  "v_mfma_f32_16x16x32_bf16 v[60:63], v[20:23], a[4:7], 0\n\t" \
  "s_waitcnt lgkmcnt(4)\n\t" \
  "v_mfma_f32_16x16x32_bf16 v[48:51], v[24:27], a[8:11], v[48:51]\n\t" \
  "ds_read_b128 v[16:19], v73 offset:4096\n\t" \
  "v_cvt_pk_bf16_f32 v64, v32, v33\n\t" \
  "v_mfma_f32_16x16x32_bf16 v[52:55], v[24:27], a[12:15], v[52:55]\n\t" \
  "ds_read_b128 v[20:23], v73 offset:22528\n\t" \
  "v_cvt_pk_bf16_f32 v65, v34, v35\n\t" \
  "v_mfma_f32_16x16x32_bf16 v[56:59], v[28:31], a[8:11], v[56:59]\n\t" \
  "v_cvt_pk_bf16_f32 v66, v40, v41\n\t" \
  "v_cvt_pk_bf16_f32 v67, v42, v43\n\t" \
  "v_mfma_f32_16x16x32_bf16 v[60:63], v[28:31], a[12:15], v[60:63]\n\t" \
  "buffer_store_dwordx4 v[64:67], v72, s[20:23], s50 offen offset:0 sc1\n\t" \
  "v_cvt_pk_bf16_f32 v68, v36, v37\n\t" \
  "s_waitcnt lgkmcnt(4)\n\t" \
  "v_mfma_f32_16x16x32_bf16 v[48:51], v[0:3], a[16:19], v[48:51]\n\t" \
  "ds_read_b128 v[24:27], v73 offset:5120\n\t" \
  "v_cvt_pk_bf16_f32 v69, v38, v39\n\t" \
  "v_mfma_f32_16x16x32_bf16 v[52:55], v[0:3], a[20:23], v[52:55]\n\t" \
  "ds_read_b128 v[28:31], v73 offset:23552\n\t" \
  "v_cvt_pk_bf16_f32 v70, v44, v45\n\t" \
  "v_mfma_f32_16x16x32_bf16 v[56:59], v[4:7], a[16:19], v[56:59]\n\t" \
  "v_cvt_pk_bf16_f32 v71, v46, v47\n\t" \
  "buffer_store_dwordx4 v[68:71], v72, s[20:23], s50 offen offset:1024 sc1\n\t" \
  "v_mfma_f32_16x16x32_bf16 v[60:63], v[4:7], a[20:23], v[60:63]\n\t" \
  "s_add_u32 s50, s50, 524288\n\t" \
  "v_max3_f32 v79, |v32|, |v33|, |v34|\n\t" \
  "s_waitcnt lgkmcnt(4)\n\t" \
  "v_mfma_f32_16x16x32_bf16 v[48:51], v[8:11], a[24:27], v[48:51]\n\t" \
  "ds_read_b128 v[0:3], v73 offset:6144\n\t" \
  "v_max3_f32 v79, v79, |v35|, |v36|\n\t" \
  "v_mfma_f32_16x16x32_bf16 v[52:55], v[8:11], a[28:31], v[52:55]\n\t" \
  "ds_read_b128 v[4:7], v73 offset:24576\n\t" \
  "v_max3_f32 v79, v79, |v37|, |v38|\n\t" \
  "v_mfma_f32_16x16x32_bf16 v[56:59], v[12:15], a[24:27], v[56:59]\n\t" \
  "v_max3_f32 v79, v79, |v39|, |v40|\n\t" \
  "v_max3_f32 v79, v79, |v41|, |v42|\n\t" \
  "v_mfma_f32_16x16x32_bf16 v[60:63], v[12:15], a[28:31], v[60:63]\n\t" \
  "v_max3_f32 v79, v79, |v43|, |v44|\n\t" \
  "v_max3_f32 v79, v79, |v45|, |v46|\n\t" \
  "s_waitcnt lgkmcnt(4)\n\t" \
  "v_mfma_f32_16x16x32_bf16 v[48:51], v[16:19], a[32:35], v[48:51]\n\t" \
  "ds_read_b128 v[8:11], v73 offset:7168\n\t" \
  "v_max_f32 v79, v79, |v47|\n\t" \
  "v_mfma_f32_16x16x32_bf16 v[52:55], v[16:19], a[36:39], v[52:55]\n\t" \
  "ds_read_b128 v[12:15], v73 offset:25600\n\t" \
  "v_max_f32_dpp v79, v79, v79 row_ror:1 row_mask:0xf bank_mask:0xf\n\t" \
  "v_mfma_f32_16x16x32_bf16 v[56:59], v[20:23], a[32:35], v[56:59]\n\t" \
  "s_nop 0\n\t" \
  "v_max_f32_dpp v79, v79, v79 row_ror:2 row_mask:0xf bank_mask:0xf\n\t" \
  "s_nop 1\n\t" \
  "v_max_f32_dpp v79, v79, v79 row_ror:4 row_mask:0xf bank_mask:0xf\n\t" \
  "v_mfma_f32_16x16x32_bf16 v[60:63], v[20:23], a[36:39], v[60:63]\n\t" \
  "s_nop 0\n\t" \
  "v_max_f32_dpp v79, v79, v79 row_ror:8 row_mask:0xf bank_mask:0xf\n\t" \
  "s_nop 1\n\t" \
  "v_max_f32_dpp v79, v79, v79 row_bcast:15 row_mask:0xa bank_mask:0xf\n\t" \
  "s_waitcnt lgkmcnt(4)\n\t" \
  "v_mfma_f32_16x16x32_bf16 v[48:51], v[24:27], a[40:43], v[48:51]\n\t" \
  "ds_read_b128 v[16:19], v73 offset:8192\n\t" \
  "v_max_f32_dpp v79, v79, v79 row_bcast:31 row_mask:0xc bank_mask:0xf\n\t" \
  "v_mfma_f32_16x16x32_bf16 v[52:55], v[24:27], a[44:47], v[52:55]\n\t" \
  "ds_read_b128 v[20:23], v73 offset:26624\n\t" \
  "v_mul_f32 v79, 0x3f810000, v79\n\t" \
  "v_mfma_f32_16x16x32_bf16 v[56:59], v[28:31], a[40:43], v[56:59]\n\t" \
  "s_mov_b64 exec, s[56:57]\n\t" \
  "ds_write_b32 v80, v79 offset:0\n\t" \
  "s_mov_b64 exec, -1\n\t" \
  "v_mfma_f32_16x16x32_bf16 v[60:63], v[28:31], a[44:47], v[60:63]\n\t" \
  "s_waitcnt lgkmcnt(4)\n\t" \
  "v_mfma_f32_16x16x32_bf16 v[48:51], v[0:3], a[48:51], v[48:51]\n\t" \
  "ds_read_b128 v[24:27], v73 offset:9216\n\t" \
  "v_mfma_f32_16x16x32_bf16 v[52:55], v[0:3], a[52:55], v[52:55]\n\t" \
  "ds_read_b128 v[28:31], v73 offset:27648\n\t" \
  "v_mfma_f32_16x16x32_bf16 v[56:59], v[4:7], a[48:51], v[56:59]\n\t" \
  "v_mfma_f32_16x16x32_bf16 v[60:63], v[4:7], a[52:55], v[60:63]\n\t" \
  "s_waitcnt lgkmcnt(4)\n\t" \
  "v_mfma_f32_16x16x32_bf16 v[48:51], v[8:11], a[56:59], v[48:51]\n\t" \
  "ds_read_b128 v[0:3], v73 offset:10240\n\t" \
  "v_mfma_f32_16x16x32_bf16 v[52:55], v[8:11], a[60:63], v[52:55]\n\t" \
  "ds_read_b128 v[4:7], v73 offset:28672\n\t" \
  "v_mfma_f32_16x16x32_bf16 v[56:59], v[12:15], a[56:59], v[56:59]\n\t" \
  "v_mfma_f32_16x16x32_bf16 v[60:63], v[12:15], a[60:63], v[60:63]\n\t" \
  "s_waitcnt lgkmcnt(4)\n\t" \
  "v_mfma_f32_16x16x32_bf16 v[48:51], v[16:19], a[64:67], v[48:51]\n\t" \
  "ds_read_b128 v[8:11], v73 offset:11264\n\t" \
  "v_mfma_f32_16x16x32_bf16 v[52:55], v[16:19], a[68:71], v[52:55]\n\t" \
  "ds_read_b128 v[12:15], v73 offset:29696\n\t" \
  "v_mfma_f32_16x16x32_bf16 v[56:59], v[20:23], a[64:67], v[56:59]\n\t" \
  "v_mfma_f32_16x16x32_bf16 v[60:63], v[20:23], a[68:71], v[60:63]\n\t" \
  "s_waitcnt vmcnt(11)\n\t" \
  "s_barrier\n\t" \
  "s_waitcnt lgkmcnt(4)\n\t" \
  "v_mfma_f32_16x16x32_bf16 v[48:51], v[24:27], a[72:75], v[48:51]\n\t" \
  "ds_read_b128 v[16:19], v73 offset:12288\n\t" \
  "v_mov_b32 v77, 147504\n\t" \
  "v_mfma_f32_16x16x32_bf16 v[52:55], v[24:27], a[76:79], v[52:55]\n\t" \
  "ds_read_b128 v[20:23], v73 offset:30720\n\t" \
  "ds_read_b128 v[84:87], v77\n\t" \
  "v_mfma_f32_16x16x32_bf16 v[56:59], v[28:31], a[72:75], v[56:59]\n\t" \
  "s_waitcnt lgkmcnt(0)\n\t" \
  "v_max3_f32 v84, v84, v85, v86\n\t" \
  "v_mfma_f32_16x16x32_bf16 v[60:63], v[28:31], a[76:79], v[60:63]\n\t" \
  "v_max_f32 v84, v84, v87\n\t" \
  "v_or_b32 v77, 1, v84\n\t" \
  "s_waitcnt lgkmcnt(4)\n\t" \
  "v_mfma_f32_16x16x32_bf16 v[48:51], v[0:3], a[80:83], v[48:51]\n\t" \
  "ds_read_b128 v[24:27], v73 offset:13312\n\t" \
  "s_mov_b64 exec, s[54:55]\n\t" \
  "buffer_store_dword v77, off, s[20:23], s51 sc1\n\t" \
  "s_mov_b64 exec, -1\n\t" \
  "v_mfma_f32_16x16x32_bf16 v[52:55], v[0:3], a[84:87], v[52:55]\n\t" \
  "ds_read_b128 v[28:31], v73 offset:31744\n\t" \
  "s_add_u32 s51, s51, 1024\n\t" \
  "v_mfma_f32_16x16x32_bf16 v[56:59], v[4:7], a[80:83], v[56:59]\n\t" \
  "v_mfma_f32_16x16x32_bf16 v[60:63], v[4:7], a[84:87], v[60:63]\n\t" \
  "s_waitcnt lgkmcnt(4)\n\t" \
  "v_mfma_f32_16x16x32_bf16 v[48:51], v[8:11], a[88:91], v[48:51]\n\t" \
  "ds_read_b128 v[0:3], v73 offset:14336\n\t" \
  "v_mfma_f32_16x16x32_bf16 v[52:55], v[8:11], a[92:95], v[52:55]\n\t" \
  "ds_read_b128 v[4:7], v73 offset:32768\n\t" \
  "v_mfma_f32_16x16x32_bf16 v[56:59], v[12:15], a[88:91], v[56:59]\n\t" \
  "v_mfma_f32_16x16x32_bf16 v[60:63], v[12:15], a[92:95], v[60:63]\n\t" \
  "s_waitcnt lgkmcnt(4)\n\t" \
  "v_mfma_f32_16x16x32_bf16 v[48:51], v[16:19], a[96:99], v[48:51]\n\t" \
  "ds_read_b128 v[8:11], v73 offset:15360\n\t" \
  "v_mfma_f32_16x16x32_bf16 v[52:55], v[16:19], a[100:103], v[52:55]\n\t" \
  "ds_read_b128 v[12:15], v73 offset:33792\n\t" \
  "v_mfma_f32_16x16x32_bf16 v[56:59], v[20:23], a[96:99], v[56:59]\n\t" \
  "v_mfma_f32_16x16x32_bf16 v[60:63], v[20:23], a[100:103], v[60:63]\n\t" \
  "s_waitcnt lgkmcnt(4)\n\t" \
  "v_mfma_f32_16x16x32_bf16 v[48:51], v[24:27], a[104:107], v[48:51]\n\t" \
  "ds_read_b128 v[16:19], v73 offset:16384\n\t" \
  "v_mfma_f32_16x16x32_bf16 v[52:55], v[24:27], a[108:111], v[52:55]\n\t" \
  "ds_read_b128 v[20:23], v73 offset:34816\n\t" \
  "v_mfma_f32_16x16x32_bf16 v[56:59], v[28:31], a[104:107], v[56:59]\n\t" \
  "v_mfma_f32_16x16x32_bf16 v[60:63], v[28:31], a[108:111], v[60:63]\n\t" \
  "s_waitcnt lgkmcnt(4)\n\t" \
  "v_mfma_f32_16x16x32_bf16 v[48:51], v[0:3], a[112:115], v[48:51]\n\t" \
  "ds_read_b128 v[24:27], v73 offset:17408\n\t" \
  "v_mfma_f32_16x16x32_bf16 v[52:55], v[0:3], a[116:119], v[52:55]\n\t" \
  "ds_read_b128 v[28:31], v73 offset:35840\n\t" \
  "v_mfma_f32_16x16x32_bf16 v[56:59], v[4:7], a[112:115], v[56:59]\n\t" \
  "v_mfma_f32_16x16x32_bf16 v[60:63], v[4:7], a[116:119], v[60:63]\n\t" \
  "s_waitcnt lgkmcnt(4)\n\t" \
  "v_mfma_f32_16x16x32_bf16 v[48:51], v[8:11], a[120:123], v[48:51]\n\t" \
  "ds_read_b128 v[0:3], v74 offset:0\n\t" \
  "v_mfma_f32_16x16x32_bf16 v[52:55], v[8:11], a[124:127], v[52:55]\n\t" \
  "ds_read_b128 v[4:7], v74 offset:18432\n\t" \
  "v_mfma_f32_16x16x32_bf16 v[56:59], v[12:15], a[120:123], v[56:59]\n\t" \
  "v_mfma_f32_16x16x32_bf16 v[60:63], v[12:15], a[124:127], v[60:63]\n\t" \
  "s_waitcnt lgkmcnt(4)\n\t" \
  "v_mfma_f32_16x16x32_bf16 v[48:51], v[16:19], a[128:131], v[48:51]\n\t" \
  "ds_read_b128 v[8:11], v74 offset:1024\n\t" \
  "v_mfma_f32_16x16x32_bf16 v[52:55], v[16:19], a[132:135], v[52:55]\n\t" \
  "ds_read_b128 v[12:15], v74 offset:19456\n\t" \
  "v_mfma_f32_16x16x32_bf16 v[56:59], v[20:23], a[128:131], v[56:59]\n\t" \
  "v_mfma_f32_16x16x32_bf16 v[60:63], v[20:23], a[132:135], v[60:63]\n\t" \
  "s_waitcnt lgkmcnt(4)\n\t" \
  "v_mfma_f32_16x16x32_bf16 v[48:51], v[24:27], a[136:139], v[48:51]\n\t" \
  "ds_read_b128 v[16:19], v74 offset:2048\n\t" \
  "v_mfma_f32_16x16x32_bf16 v[52:55], v[24:27], a[140:143], v[52:55]\n\t" \
  "ds_read_b128 v[20:23], v74 offset:20480\n\t" \
  "v_mfma_f32_16x16x32_bf16 v[56:59], v[28:31], a[136:139], v[56:59]\n\t" \
  "v_mfma_f32_16x16x32_bf16 v[60:63], v[28:31], a[140:143], v[60:63]\n\t" \
  "v_mov_b32 v73, v74\n\t" \
  "v_add_u32 v74, 110592, v72\n\t" \
  "s_waitcnt lgkmcnt(4)\n\t" \
  "v_mfma_f32_16x16x32_bf16 v[32:35], v[0:3], a[0:3], 0\n\t" \
  "ds_read_b128 v[24:27], v73 offset:3072\n\t" \
  "v_mfma_f32_16x16x32_bf16 v[36:39], v[0:3], a[4:7], 0\n\t" \
  "ds_read_b128 v[28:31], v73 offset:21504\n\t" \
  "v_mfma_f32_16x16x32_bf16 v[40:43], v[4:7], a[0:3], 0\n\t" \
  "v_mfma_f32_16x16x32_bf16 v[44:47], v[4:7], a[4:7], 0\n\t" \
  "s_waitcnt lgkmcnt(4)\n\t" \
  "v_mfma_f32_16x16x32_bf16 v[32:35], v[8:11], a[8:11], v[32:35]\n\t" \
  "ds_read_b128 v[0:3], v73 offset:4096\n\t" \
  "v_cvt_pk_bf16_f32 v64, v48, v49\n\t" \
  "v_mfma_f32_16x16x32_bf16 v[36:39], v[8:11], a[12:15], v[36:39]\n\t" \
  "ds_read_b128 v[4:7], v73 offset:22528\n\t" \
  "v_cvt_pk_bf16_f32 v65, v50, v51\n\t" \
  "v_mfma_f32_16x16x32_bf16 v[40:43], v[12:15], a[8:11], v[40:43]\n\t" \
  "v_cvt_pk_bf16_f32 v66, v56, v57\n\t" \
  "v_cvt_pk_bf16_f32 v67, v58, v59\n\t" \
  "v_mfma_f32_16x16x32_bf16 v[44:47], v[12:15], a[12:15], v[44:47]\n\t" \
  "buffer_store_dwordx4 v[64:67], v72, s[20:23], s50 offen offset:0 sc1\n\t" \
  "v_cvt_pk_bf16_f32 v68, v52, v53\n\t" \
  "s_waitcnt lgkmcnt(4)\n\t" \
  "v_mfma_f32_16x16x32_bf16 v[32:35], v[16:19], a[16:19], v[32:35]\n\t" \
  "ds_read_b128 v[8:11], v73 offset:5120\n\t" \
  "v_cvt_pk_bf16_f32 v69, v54, v55\n\t" \
  "v_mfma_f32_16x16x32_bf16 v[36:39], v[16:19], a[20:23], v[36:39]\n\t" \
  "ds_read_b128 v[12:15], v73 offset:23552\n\t" \
  "v_cvt_pk_bf16_f32 v70, v60, v61\n\t" \
  "v_mfma_f32_16x16x32_bf16 v[40:43], v[20:23], a[16:19], v[40:43]\n\t" \
  "v_cvt_pk_bf16_f32 v71, v62, v63\n\t" \
  "buffer_store_dwordx4 v[68:71], v72, s[20:23], s50 offen offset:1024 sc1\n\t" \
  "v_mfma_f32_16x16x32_bf16 v[44:47], v[20:23], a[20:23], v[44:47]\n\t" \
  "s_add_u32 s50, s50, 524288\n\t" \
  "v_max3_f32 v79, |v48|, |v49|, |v50|\n\t" \
  "s_waitcnt lgkmcnt(4)\n\t" \
  "v_mfma_f32_16x16x32_bf16 v[32:35], v[24:27], a[24:27], v[32:35]\n\t" \
  "ds_read_b128 v[16:19], v73 offset:6144\n\t" \
  "v_max3_f32 v79, v79, |v51|, |v52|\n\t" \
  "v_mfma_f32_16x16x32_bf16 v[36:39], v[24:27], a[28:31], v[36:39]\n\t" \
  "ds_read_b128 v[20:23], v73 offset:24576\n\t" \
  "v_max3_f32 v79, v79, |v53|, |v54|\n\t" \
  "v_mfma_f32_16x16x32_bf16 v[40:43], v[28:31], a[24:27], v[40:43]\n\t" \
  "v_max3_f32 v79, v79, |v55|, |v56|\n\t" \
  "v_max3_f32 v79, v79, |v57|, |v58|\n\t" \
  "v_mfma_f32_16x16x32_bf16 v[44:47], v[28:31], a[28:31], v[44:47]\n\t" \
  "v_max3_f32 v79, v79, |v59|, |v60|\n\t" \
  "v_max3_f32 v79, v79, |v61|, |v62|\n\t" \
  "s_waitcnt lgkmcnt(4)\n\t" \
  "v_mfma_f32_16x16x32_bf16 v[32:35], v[0:3], a[32:35], v[32:35]\n\t" \
  "ds_read_b128 v[24:27], v73 offset:7168\n\t" \
  "v_max_f32 v79, v79, |v63|\n\t" \
  "v_mfma_f32_16x16x32_bf16 v[36:39], v[0:3], a[36:39], v[36:39]\n\t" \
  "ds_read_b128 v[28:31], v73 offset:25600\n\t" \
  "v_max_f32_dpp v79, v79, v79 row_ror:1 row_mask:0xf bank_mask:0xf\n\t" \
  "v_mfma_f32_16x16x32_bf16 v[40:43], v[4:7], a[32:35], v[40:43]\n\t" \
  "s_nop 0\n\t" \
  "v_max_f32_dpp v79, v79, v79 row_ror:2 row_mask:0xf bank_mask:0xf\n\t" \
  "s_nop 1\n\t" \
  "v_max_f32_dpp v79, v79, v79 row_ror:4 row_mask:0xf bank_mask:0xf\n\t" \
  "v_mfma_f32_16x16x32_bf16 v[44:47], v[4:7], a[36:39], v[44:47]\n\t" \
  "s_nop 0\n\t" \
  "v_max_f32_dpp v79, v79, v79 row_ror:8 row_mask:0xf bank_mask:0xf\n\t" \
  "s_nop 1\n\t" \
  "v_max_f32_dpp v79, v79, v79 row_bcast:15 row_mask:0xa bank_mask:0xf\n\t" \
  "s_waitcnt lgkmcnt(4)\n\t" \
  "v_mfma_f32_16x16x32_bf16 v[32:35], v[8:11], a[40:43], v[32:35]\n\t" \
  "ds_read_b128 v[0:3], v73 offset:8192\n\t" \
  "v_max_f32_dpp v79, v79, v79 row_bcast:31 row_mask:0xc bank_mask:0xf\n\t" \
  "v_mfma_f32_16x16x32_bf16 v[36:39], v[8:11], a[44:47], v[36:39]\n\t" \
  "ds_read_b128 v[4:7], v73 offset:26624\n\t" \
  "v_mul_f32 v79, 0x3f810000, v79\n\t" \
  "v_mfma_f32_16x16x32_bf16 v[40:43], v[12:15], a[40:43], v[40:43]\n\t" \
  "s_mov_b64 exec, s[56:57]\n\t" \
  "ds_write_b32 v80, v79 offset:16\n\t" \
  "s_mov_b64 exec, -1\n\t" \
  "v_mfma_f32_16x16x32_bf16 v[44:47], v[12:15], a[44:47], v[44:47]\n\t" \
  "s_waitcnt lgkmcnt(4)\n\t" \
  "v_mfma_f32_16x16x32_bf16 v[32:35], v[16:19], a[48:51], v[32:35]\n\t" \
  "ds_read_b128 v[8:11], v73 offset:9216\n\t" \
  "v_mfma_f32_16x16x32_bf16 v[36:39], v[16:19], a[52:55], v[36:39]\n\t" \
  "ds_read_b128 v[12:15], v73 offset:27648\n\t" \
  "v_mfma_f32_16x16x32_bf16 v[40:43], v[20:23], a[48:51], v[40:43]\n\t" \
  "v_mfma_f32_16x16x32_bf16 v[44:47], v[20:23], a[52:55], v[44:47]\n\t" \
  "s_waitcnt lgkmcnt(4)\n\t" \
  "v_mfma_f32_16x16x32_bf16 v[32:35], v[24:27], a[56:59], v[32:35]\n\t" \
  "ds_read_b128 v[16:19], v73 offset:10240\n\t" \
  "v_mfma_f32_16x16x32_bf16 v[36:39], v[24:27], a[60:63], v[36:39]\n\t" \
  "ds_read_b128 v[20:23], v73 offset:28672\n\t" \
  "v_mfma_f32_16x16x32_bf16 v[40:43], v[28:31], a[56:59], v[40:43]\n\t" \
  "v_mfma_f32_16x16x32_bf16 v[44:47], v[28:31], a[60:63], v[44:47]\n\t" \
  "s_waitcnt lgkmcnt(4)\n\t" \
  "v_mfma_f32_16x16x32_bf16 v[32:35], v[0:3], a[64:67], v[32:35]\n\t" \
  "ds_read_b128 v[24:27], v73 offset:11264\n\t" \
  "v_mfma_f32_16x16x32_bf16 v[36:39], v[0:3], a[68:71], v[36:39]\n\t" \
  "ds_read_b128 v[28:31], v73 offset:29696\n\t" \
  "v_mfma_f32_16x16x32_bf16 v[40:43], v[4:7], a[64:67], v[40:43]\n\t" \
  "v_mfma_f32_16x16x32_bf16 v[44:47], v[4:7], a[68:71], v[44:47]\n\t" \
  "s_waitcnt vmcnt(2)\n\t" \
  "s_barrier\n\t" \
  "s_waitcnt lgkmcnt(4)\n\t" \
  "v_mfma_f32_16x16x32_bf16 v[32:35], v[8:11], a[72:75], v[32:35]\n\t" \
  "ds_read_b128 v[0:3], v73 offset:12288\n\t" \
  "v_mov_b32 v77, 147456\n\t" \
  "v_mfma_f32_16x16x32_bf16 v[36:39], v[8:11], a[76:79], v[36:39]\n\t" \
  "ds_read_b128 v[4:7], v73 offset:30720\n\t" \
  "ds_read_b128 v[84:87], v77\n\t" \
  "v_mfma_f32_16x16x32_bf16 v[40:43], v[12:15], a[72:75], v[40:43]\n\t" \
  "s_waitcnt lgkmcnt(0)\n\t" \
  "v_max3_f32 v84, v84, v85, v86\n\t" \
  "v_mfma_f32_16x16x32_bf16 v[44:47], v[12:15], a[76:79], v[44:47]\n\t" \
  "v_max_f32 v84, v84, v87\n\t" \
  "v_or_b32 v77, 1, v84\n\t" \
  "s_waitcnt lgkmcnt(4)\n\t" \
  "v_mfma_f32_16x16x32_bf16 v[32:35], v[16:19], a[80:83], v[32:35]\n\t" \
  "ds_read_b128 v[8:11], v73 offset:13312\n\t" \
  "s_mov_b64 exec, s[54:55]\n\t" \
  "buffer_store_dword v77, off, s[20:23], s51 sc1\n\t" \
  "s_mov_b64 exec, -1\n\t" \
  "v_mfma_f32_16x16x32_bf16 v[36:39], v[16:19], a[84:87], v[36:39]\n\t" \
  "ds_read_b128 v[12:15], v73 offset:31744\n\t" \
  "s_add_u32 s51, s51, 1024\n\t" \
  "v_mfma_f32_16x16x32_bf16 v[40:43], v[20:23], a[80:83], v[40:43]\n\t" \
  "v_mfma_f32_16x16x32_bf16 v[44:47], v[20:23], a[84:87], v[44:47]\n\t" \
  "s_waitcnt lgkmcnt(4)\n\t" \
  "v_mfma_f32_16x16x32_bf16 v[32:35], v[24:27], a[88:91], v[32:35]\n\t" \
  "ds_read_b128 v[16:19], v73 offset:14336\n\t" \
  "v_mfma_f32_16x16x32_bf16 v[36:39], v[24:27], a[92:95], v[36:39]\n\t" \
  "ds_read_b128 v[20:23], v73 offset:32768\n\t" \
  "v_mfma_f32_16x16x32_bf16 v[40:43], v[28:31], a[88:91], v[40:43]\n\t" \
  "v_mfma_f32_16x16x32_bf16 v[44:47], v[28:31], a[92:95], v[44:47]\n\t" \
  "s_waitcnt lgkmcnt(4)\n\t" \
  "v_mfma_f32_16x16x32_bf16 v[32:35], v[0:3], a[96:99], v[32:35]\n\t" \
  "ds_read_b128 v[24:27], v73 offset:15360\n\t" \
  "v_mfma_f32_16x16x32_bf16 v[36:39], v[0:3], a[100:103], v[36:39]\n\t" \
  "ds_read_b128 v[28:31], v73 offset:33792\n\t" \
  "v_mfma_f32_16x16x32_bf16 v[40:43], v[4:7], a[96:99], v[40:43]\n\t" \
  "v_mfma_f32_16x16x32_bf16 v[44:47], v[4:7], a[100:103], v[44:47]\n\t" \
  "s_waitcnt lgkmcnt(4)\n\t" \
  "v_mfma_f32_16x16x32_bf16 v[32:35], v[8:11], a[104:107], v[32:35]\n\t" \
  "ds_read_b128 v[0:3], v73 offset:16384\n\t" \
  "v_mfma_f32_16x16x32_bf16 v[36:39], v[8:11], a[108:111], v[36:39]\n\t" \
  "ds_read_b128 v[4:7], v73 offset:34816\n\t" \
  "v_mfma_f32_16x16x32_bf16 v[40:43], v[12:15], a[104:107], v[40:43]\n\t" \
  "v_mfma_f32_16x16x32_bf16 v[44:47], v[12:15], a[108:111], v[44:47]\n\t" \
  "s_waitcnt lgkmcnt(4)\n\t" \
  "v_mfma_f32_16x16x32_bf16 v[32:35], v[16:19], a[112:115], v[32:35]\n\t" \
  "ds_read_b128 v[8:11], v73 offset:17408\n\t" \
  "v_mfma_f32_16x16x32_bf16 v[36:39], v[16:19], a[116:119], v[36:39]\n\t" \
  "ds_read_b128 v[12:15], v73 offset:35840\n\t" \
  "v_mfma_f32_16x16x32_bf16 v[40:43], v[20:23], a[112:115], v[40:43]\n\t" \
  "v_mfma_f32_16x16x32_bf16 v[44:47], v[20:23], a[116:119], v[44:47]\n\t" \
  "s_waitcnt lgkmcnt(4)\n\t" \
  "v_mfma_f32_16x16x32_bf16 v[32:35], v[24:27], a[120:123], v[32:35]\n\t" \
  "ds_read_b128 v[16:19], v74 offset:0\n\t" \
  "v_mfma_f32_16x16x32_bf16 v[36:39], v[24:27], a[124:127], v[36:39]\n\t" \
  "ds_read_b128 v[20:23], v74 offset:18432\n\t" \
  "v_mfma_f32_16x16x32_bf16 v[40:43], v[28:31], a[120:123], v[40:43]\n\t" \
  "v_mfma_f32_16x16x32_bf16 v[44:47], v[28:31], a[124:127], v[44:47]\n\t" \
  "s_waitcnt lgkmcnt(4)\n\t" \
  "v_mfma_f32_16x16x32_bf16 v[32:35], v[0:3], a[128:131], v[32:35]\n\t" \
  "ds_read_b128 v[24:27], v74 offset:1024\n\t" \
  "v_mfma_f32_16x16x32_bf16 v[36:39], v[0:3], a[132:135], v[36:39]\n\t" \
  "ds_read_b128 v[28:31], v74 offset:19456\n\t" \
  "v_mfma_f32_16x16x32_bf16 v[40:43], v[4:7], a[128:131], v[40:43]\n\t" \
  "v_mfma_f32_16x16x32_bf16 v[44:47], v[4:7], a[132:135], v[44:47]\n\t" \
  "s_waitcnt lgkmcnt(4)\n\t" \
  "v_mfma_f32_16x16x32_bf16 v[32:35], v[8:11], a[136:139], v[32:35]\n\t" \
  "ds_read_b128 v[0:3], v74 offset:2048\n\t" \
  "v_mfma_f32_16x16x32_bf16 v[36:39], v[8:11], a[140:143], v[36:39]\n\t" \
  "ds_read_b128 v[4:7], v74 offset:20480\n\t" \
  "v_mfma_f32_16x16x32_bf16 v[40:43], v[12:15], a[136:139], v[40:43]\n\t" \
  "v_mfma_f32_16x16x32_bf16 v[44:47], v[12:15], a[140:143], v[44:47]\n\t" \
  "v_mov_b32 v73, v74\n\t" \
  "s_waitcnt lgkmcnt(4)\n\t" \
  "v_mfma_f32_16x16x32_bf16 v[48:51], v[16:19], a[0:3], 0\n\t" \
  "ds_read_b128 v[8:11], v73 offset:3072\n\t" \
  "v_mfma_f32_16x16x32_bf16 v[52:55], v[16:19], a[4:7], 0\n\t" \
  "ds_read_b128 v[12:15], v73 offset:21504\n\t" \
  "v_mfma_f32_16x16x32_bf16 v[56:59], v[20:23], a[0:3], 0\n\t" \
  "v_mfma_f32_16x16x32_bf16 v[60:63], v[20:23], a[4:7], 0\n\t" \
  "s_waitcnt lgkmcnt(4)\n\t" \
  "v_mfma_f32_16x16x32_bf16 v[48:51], v[24:27], a[8:11], v[48:51]\n\t" \
  "ds_read_b128 v[16:19], v73 offset:4096\n\t" \
  "v_cvt_pk_bf16_f32 v64, v32, v33\n\t" \
  "v_mfma_f32_16x16x32_bf16 v[52:55], v[24:27], a[12:15], v[52:55]\n\t" \
  "ds_read_b128 v[20:23], v73 offset:22528\n\t" \
  "v_cvt_pk_bf16_f32 v65, v34, v35\n\t" \
  "v_mfma_f32_16x16x32_bf16 v[56:59], v[28:31], a[8:11], v[56:59]\n\t" \
  "v_cvt_pk_bf16_f32 v66, v40, v41\n\t" \
  "v_cvt_pk_bf16_f32 v67, v42, v43\n\t" \
  "v_mfma_f32_16x16x32_bf16 v[60:63], v[28:31], a[12:15], v[60:63]\n\t" \
  "buffer_store_dwordx4 v[64:67], v72, s[20:23], s50 offen offset:0 sc1\n\t" \
  "v_cvt_pk_bf16_f32 v68, v36, v37\n\t" \
  "s_waitcnt lgkmcnt(4)\n\t" \
  "v_mfma_f32_16x16x32_bf16 v[48:51], v[0:3], a[16:19], v[48:51]\n\t" \
  "ds_read_b128 v[24:27], v73 offset:5120\n\t" \
  "v_cvt_pk_bf16_f32 v69, v38, v39\n\t" \
  "v_mfma_f32_16x16x32_bf16 v[52:55], v[0:3], a[20:23], v[52:55]\n\t" \
  "ds_read_b128 v[28:31], v73 offset:23552\n\t" \
  "v_cvt_pk_bf16_f32 v70, v44, v45\n\t" \
  "v_mfma_f32_16x16x32_bf16 v[56:59], v[4:7], a[16:19], v[56:59]\n\t" \
  "v_cvt_pk_bf16_f32 v71, v46, v47\n\t" \
  "buffer_store_dwordx4 v[68:71], v72, s[20:23], s50 offen offset:1024 sc1\n\t" \
  "v_mfma_f32_16x16x32_bf16 v[60:63], v[4:7], a[20:23], v[60:63]\n\t" \
  "s_add_u32 s50, s50, 524288\n\t" \
  "v_max3_f32 v79, |v32|, |v33|, |v34|\n\t" \
  "s_waitcnt lgkmcnt(4)\n\t" \
  "v_mfma_f32_16x16x32_bf16 v[48:51], v[8:11], a[24:27], v[48:51]\n\t" \
  "ds_read_b128 v[0:3], v73 offset:6144\n\t" \
  "v_max3_f32 v79, v79, |v35|, |v36|\n\t" \
  "v_mfma_f32_16x16x32_bf16 v[52:55], v[8:11], a[28:31], v[52:55]\n\t" \
  "ds_read_b128 v[4:7], v73 offset:24576\n\t" \
  "v_max3_f32 v79, v79, |v37|, |v38|\n\t" \
  "v_mfma_f32_16x16x32_bf16 v[56:59], v[12:15], a[24:27], v[56:59]\n\t" \
  "v_max3_f32 v79, v79, |v39|, |v40|\n\t" \
  "v_max3_f32 v79, v79, |v41|, |v42|\n\t" \
  "v_mfma_f32_16x16x32_bf16 v[60:63], v[12:15], a[28:31], v[60:63]\n\t" \
  "v_max3_f32 v79, v79, |v43|, |v44|\n\t" \
  "v_max3_f32 v79, v79, |v45|, |v46|\n\t" \
  "s_waitcnt lgkmcnt(4)\n\t" \
  "v_mfma_f32_16x16x32_bf16 v[48:51], v[16:19], a[32:35], v[48:51]\n\t" \
  "ds_read_b128 v[8:11], v73 offset:7168\n\t" \
  "v_max_f32 v79, v79, |v47|\n\t" \
  "v_mfma_f32_16x16x32_bf16 v[52:55], v[16:19], a[36:39], v[52:55]\n\t" \
  "ds_read_b128 v[12:15], v73 offset:25600\n\t" \
  "v_max_f32_dpp v79, v79, v79 row_ror:1 row_mask:0xf bank_mask:0xf\n\t" \
  "v_mfma_f32_16x16x32_bf16 v[56:59], v[20:23], a[32:35], v[56:59]\n\t" \
  "s_nop 0\n\t" \
  "v_max_f32_dpp v79, v79, v79 row_ror:2 row_mask:0xf bank_mask:0xf\n\t" \
  "s_nop 1\n\t" \
  "v_max_f32_dpp v79, v79, v79 row_ror:4 row_mask:0xf bank_mask:0xf\n\t" \
  "v_mfma_f32_16x16x32_bf16 v[60:63], v[20:23], a[36:39], v[60:63]\n\t" \
  "s_nop 0\n\t" \
  "v_max_f32_dpp v79, v79, v79 row_ror:8 row_mask:0xf bank_mask:0xf\n\t" \
  "s_nop 1\n\t" \
  "v_max_f32_dpp v79, v79, v79 row_bcast:15 row_mask:0xa bank_mask:0xf\n\t" \
  "s_waitcnt lgkmcnt(4)\n\t" \
  "v_mfma_f32_16x16x32_bf16 v[48:51], v[24:27], a[40:43], v[48:51]\n\t" \
  "ds_read_b128 v[16:19], v73 offset:8192\n\t" \
  "v_max_f32_dpp v79, v79, v79 row_bcast:31 row_mask:0xc bank_mask:0xf\n\t" \
  "v_mfma_f32_16x16x32_bf16 v[52:55], v[24:27], a[44:47], v[52:55]\n\t" \
  "ds_read_b128 v[20:23], v73 offset:26624\n\t" \
  "v_mul_f32 v79, 0x3f810000, v79\n\t" \
  "v_mfma_f32_16x16x32_bf16 v[56:59], v[28:31], a[40:43], v[56:59]\n\t" \
  "s_mov_b64 exec, s[56:57]\n\t" \
  "ds_write_b32 v80, v79 offset:32\n\t" \
  "s_mov_b64 exec, -1\n\t" \
  "v_mfma_f32_16x16x32_bf16 v[60:63], v[28:31], a[44:47], v[60:63]\n\t" \
  "s_waitcnt lgkmcnt(4)\n\t" \
  "v_mfma_f32_16x16x32_bf16 v[48:51], v[0:3], a[48:51], v[48:51]\n\t" \
  "ds_read_b128 v[24:27], v73 offset:9216\n\t" \
  "v_mfma_f32_16x16x32_bf16 v[52:55], v[0:3], a[52:55], v[52:55]\n\t" \
  "ds_read_b128 v[28:31], v73 offset:27648\n\t" \
  "v_mfma_f32_16x16x32_bf16 v[56:59], v[4:7], a[48:51], v[56:59]\n\t" \
  "v_mfma_f32_16x16x32_bf16 v[60:63], v[4:7], a[52:55], v[60:63]\n\t" \
  "s_waitcnt lgkmcnt(4)\n\t" \
  "v_mfma_f32_16x16x32_bf16 v[48:51], v[8:11], a[56:59], v[48:51]\n\t" \
  "ds_read_b128 v[0:3], v73 offset:10240\n\t" \
  "v_mfma_f32_16x16x32_bf16 v[52:55], v[8:11], a[60:63], v[52:55]\n\t" \
  "ds_read_b128 v[4:7], v73 offset:28672\n\t" \
  "v_mfma_f32_16x16x32_bf16 v[56:59], v[12:15], a[56:59], v[56:59]\n\t" \
  "v_mfma_f32_16x16x32_bf16 v[60:63], v[12:15], a[60:63], v[60:63]\n\t" \
  "s_waitcnt lgkmcnt(4)\n\t" \
  "v_mfma_f32_16x16x32_bf16 v[48:51], v[16:19], a[64:67], v[48:51]\n\t" \
  "ds_read_b128 v[8:11], v73 offset:11264\n\t" \
  "v_mfma_f32_16x16x32_bf16 v[52:55], v[16:19], a[68:71], v[52:55]\n\t" \
  "ds_read_b128 v[12:15], v73 offset:29696\n\t" \
  "v_mfma_f32_16x16x32_bf16 v[56:59], v[20:23], a[64:67], v[56:59]\n\t" \
  "v_mfma_f32_16x16x32_bf16 v[60:63], v[20:23], a[68:71], v[60:63]\n\t" \
  "s_waitcnt vmcnt(2)\n\t" \
  "s_barrier\n\t" \
  "s_waitcnt lgkmcnt(4)\n\t" \
  "v_mfma_f32_16x16x32_bf16 v[48:51], v[24:27], a[72:75], v[48:51]\n\t" \
  "ds_read_b128 v[16:19], v73 offset:12288\n\t" \
  "v_mov_b32 v77, 147472\n\t" \
  "v_mfma_f32_16x16x32_bf16 v[52:55], v[24:27], a[76:79], v[52:55]\n\t" \
  "ds_read_b128 v[20:23], v73 offset:30720\n\t" \
  "ds_read_b128 v[84:87], v77\n\t" \
  "v_mfma_f32_16x16x32_bf16 v[56:59], v[28:31], a[72:75], v[56:59]\n\t" \
  "s_waitcnt lgkmcnt(0)\n\t" \
  "v_max3_f32 v84, v84, v85, v86\n\t" \
  "v_mfma_f32_16x16x32_bf16 v[60:63], v[28:31], a[76:79], v[60:63]\n\t" \
  "v_max_f32 v84, v84, v87\n\t" \
  "v_or_b32 v77, 1, v84\n\t" \
  "s_waitcnt lgkmcnt(4)\n\t" \
  "v_mfma_f32_16x16x32_bf16 v[48:51], v[0:3], a[80:83], v[48:51]\n\t" \
  "ds_read_b128 v[24:27], v73 offset:13312\n\t" \
  "s_mov_b64 exec, s[54:55]\n\t" \
  "buffer_store_dword v77, off, s[20:23], s51 sc1\n\t" \
  "s_mov_b64 exec, -1\n\t" \
  "v_mfma_f32_16x16x32_bf16 v[52:55], v[0:3], a[84:87], v[52:55]\n\t" \
  "ds_read_b128 v[28:31], v73 offset:31744\n\t" \
  "s_add_u32 s51, s51, 1024\n\t" \
  "v_mfma_f32_16x16x32_bf16 v[56:59], v[4:7], a[80:83], v[56:59]\n\t" \
  "v_mfma_f32_16x16x32_bf16 v[60:63], v[4:7], a[84:87], v[60:63]\n\t" \
  "s_waitcnt lgkmcnt(4)\n\t" \
  "v_mfma_f32_16x16x32_bf16 v[48:51], v[8:11], a[88:91], v[48:51]\n\t" \
  "ds_read_b128 v[0:3], v73 offset:14336\n\t" \
  "v_mfma_f32_16x16x32_bf16 v[52:55], v[8:11], a[92:95], v[52:55]\n\t" \
  "ds_read_b128 v[4:7], v73 offset:32768\n\t" \
  "v_mfma_f32_16x16x32_bf16 v[56:59], v[12:15], a[88:91], v[56:59]\n\t" \
  "v_mfma_f32_16x16x32_bf16 v[60:63], v[12:15], a[92:95], v[60:63]\n\t" \
  "s_waitcnt lgkmcnt(4)\n\t" \
  "v_mfma_f32_16x16x32_bf16 v[48:51], v[16:19], a[96:99], v[48:51]\n\t" \
  "ds_read_b128 v[8:11], v73 offset:15360\n\t" \
  "v_mfma_f32_16x16x32_bf16 v[52:55], v[16:19], a[100:103], v[52:55]\n\t" \
  "ds_read_b128 v[12:15], v73 offset:33792\n\t" \
  "v_mfma_f32_16x16x32_bf16 v[56:59], v[20:23], a[96:99], v[56:59]\n\t" \
  "v_mfma_f32_16x16x32_bf16 v[60:63], v[20:23], a[100:103], v[60:63]\n\t" \
  "s_waitcnt lgkmcnt(4)\n\t" \
  "v_mfma_f32_16x16x32_bf16 v[48:51], v[24:27], a[104:107], v[48:51]\n\t" \
  "ds_read_b128 v[16:19], v73 offset:16384\n\t" \
  "v_mfma_f32_16x16x32_bf16 v[52:55], v[24:27], a[108:111], v[52:55]\n\t" \
  "ds_read_b128 v[20:23], v73 offset:34816\n\t" \
  "v_mfma_f32_16x16x32_bf16 v[56:59], v[28:31], a[104:107], v[56:59]\n\t" \
  "v_mfma_f32_16x16x32_bf16 v[60:63], v[28:31], a[108:111], v[60:63]\n\t" \
  "s_waitcnt lgkmcnt(4)\n\t" \
  "v_mfma_f32_16x16x32_bf16 v[48:51], v[0:3], a[112:115], v[48:51]\n\t" \
  "ds_read_b128 v[24:27], v73 offset:17408\n\t" \
  "v_mfma_f32_16x16x32_bf16 v[52:55], v[0:3], a[116:119], v[52:55]\n\t" \
  "ds_read_b128 v[28:31], v73 offset:35840\n\t" \
  "v_mfma_f32_16x16x32_bf16 v[56:59], v[4:7], a[112:115], v[56:59]\n\t" \
  "v_mfma_f32_16x16x32_bf16 v[60:63], v[4:7], a[116:119], v[60:63]\n\t" \
  "s_waitcnt lgkmcnt(4)\n\t" \
  "v_mfma_f32_16x16x32_bf16 v[48:51], v[8:11], a[120:123], v[48:51]\n\t" \
  "v_mfma_f32_16x16x32_bf16 v[52:55], v[8:11], a[124:127], v[52:55]\n\t" \
  "v_mfma_f32_16x16x32_bf16 v[56:59], v[12:15], a[120:123], v[56:59]\n\t" \
  "v_mfma_f32_16x16x32_bf16 v[60:63], v[12:15], a[124:127], v[60:63]\n\t" \
  "s_waitcnt lgkmcnt(2)\n\t" \
  "v_mfma_f32_16x16x32_bf16 v[48:51], v[16:19], a[128:131], v[48:51]\n\t" \
  "v_mfma_f32_16x16x32_bf16 v[52:55], v[16:19], a[132:135], v[52:55]\n\t" \
  "v_mfma_f32_16x16x32_bf16 v[56:59], v[20:23], a[128:131], v[56:59]\n\t" \
  "v_mfma_f32_16x16x32_bf16 v[60:63], v[20:23], a[132:135], v[60:63]\n\t" \
  "s_waitcnt lgkmcnt(0)\n\t" \
  "v_mfma_f32_16x16x32_bf16 v[48:51], v[24:27], a[136:139], v[48:51]\n\t" \
  "v_mfma_f32_16x16x32_bf16 v[52:55], v[24:27], a[140:143], v[52:55]\n\t" \
  "v_mfma_f32_16x16x32_bf16 v[56:59], v[28:31], a[136:139], v[56:59]\n\t" \
  "v_mfma_f32_16x16x32_bf16 v[60:63], v[28:31], a[140:143], v[60:63]\n\t" \
  "s_nop 7\n\t" \
  "v_cvt_pk_bf16_f32 v64, v48, v49\n\t" \
  "v_cvt_pk_bf16_f32 v65, v50, v51\n\t" \
  "v_cvt_pk_bf16_f32 v66, v56, v57\n\t" \
  "v_cvt_pk_bf16_f32 v67, v58, v59\n\t" \
  "buffer_store_dwordx4 v[64:67], v72, s[20:23], s50 offen offset:0 sc1\n\t" \
  "v_cvt_pk_bf16_f32 v68, v52, v53\n\t" \
  "v_cvt_pk_bf16_f32 v69, v54, v55\n\t" \
  "v_cvt_pk_bf16_f32 v70, v60, v61\n\t" \
  "v_cvt_pk_bf16_f32 v71, v62, v63\n\t" \
  "buffer_store_dwordx4 v[68:71], v72, s[20:23], s50 offen offset:1024 sc1\n\t" \
  "s_add_u32 s50, s50, 524288\n\t" \
  "v_max3_f32 v79, |v48|, |v49|, |v50|\n\t" \
  "v_max3_f32 v79, v79, |v51|, |v52|\n\t" \
  "v_max3_f32 v79, v79, |v53|, |v54|\n\t" \
  "v_max3_f32 v79, v79, |v55|, |v56|\n\t" \
  "v_max3_f32 v79, v79, |v57|, |v58|\n\t" \
  "v_max3_f32 v79, v79, |v59|, |v60|\n\t" \
  "v_max3_f32 v79, v79, |v61|, |v62|\n\t" \
  "v_max_f32 v79, v79, |v63|\n\t" \
  "s_nop 1\n\t" \
  "v_max_f32_dpp v79, v79, v79 row_ror:1 row_mask:0xf bank_mask:0xf\n\t" \
  "s_nop 1\n\t" \
  "v_max_f32_dpp v79, v79, v79 row_ror:2 row_mask:0xf bank_mask:0xf\n\t" \
  "s_nop 1\n\t" \
  "v_max_f32_dpp v79, v79, v79 row_ror:4 row_mask:0xf bank_mask:0xf\n\t" \
  "s_nop 1\n\t" \
  "v_max_f32_dpp v79, v79, v79 row_ror:8 row_mask:0xf bank_mask:0xf\n\t" \
  "s_nop 1\n\t" \
  "v_max_f32_dpp v79, v79, v79 row_bcast:15 row_mask:0xa bank_mask:0xf\n\t" \
  "s_nop 1\n\t" \
  "v_max_f32_dpp v79, v79, v79 row_bcast:31 row_mask:0xc bank_mask:0xf\n\t" \
  "v_mul_f32 v79, 0x3f810000, v79\n\t" \
  "s_mov_b64 exec, s[56:57]\n\t" \
  "ds_write_b32 v80, v79 offset:48\n\t" \
  "s_mov_b64 exec, -1\n\t" \
  "s_waitcnt vmcnt(0) lgkmcnt(0)\n\t" \
  "s_barrier\n\t" \
  "v_mov_b32 v77, 147488\n\t" \
  "ds_read_b128 v[84:87], v77\n\t" \
  "s_waitcnt lgkmcnt(0)\n\t" \
  "v_max3_f32 v84, v84, v85, v86\n\t" \
  "v_max_f32 v84, v84, v87\n\t" \
  "v_or_b32 v77, 1, v84\n\t" \
  "s_mov_b64 exec, s[54:55]\n\t" \
  "buffer_store_dword v77, off, s[20:23], s51 sc1\n\t" \
  "s_mov_b64 exec, -1\n\t" \
  "s_add_u32 s51, s51, 1024\n\t" \
  "v_mov_b32 v77, 147504\n\t" \
  "ds_read_b128 v[84:87], v77\n\t" \
  "s_waitcnt lgkmcnt(0)\n\t" \
  "v_max3_f32 v84, v84, v85, v86\n\t" \
  "v_max_f32 v84, v84, v87\n\t" \
  "v_or_b32 v77, 1, v84\n\t" \
  "s_mov_b64 exec, s[54:55]\n\t" \
  "buffer_store_dword v77, off, s[20:23], s51 sc1\n\t" \
  "s_mov_b64 exec, -1\n\t" \
  "s_add_u32 s51, s51, 1024\n\t" \
  "s_barrier\n\t" \
  "s_branch Lend_%=\n\t" \
  "Ltmo_%=:\n\t" \
  "v_mov_b32 v77, 1\n\t" \
  "s_mov_b32 s40, 2157568\n\t" \
  "buffer_store_dword v77, off, s[20:23], s40 sc1\n\t" \
  "Lquit_%=:\n\t" \
  "s_endpgm\n\t" \
  "Lend_%=:\n\t" \
  "s_mov_b32 m0, s59\n\t"

#define GI0 0
#define PLACE 1
__global__ void __launch_bounds__(256, 1) k_main(Params) {
  extern __shared__ __attribute__((aligned(16))) char smem[];
  const int b = blockIdx.x, xc = b & 7, n = b >> 3;
  const int w = rfl(threadIdx.x >> 6);
  int rec, gi, role, ri;
  if (PLACE == 0) {
    rec = n < 8 ? xc * 8 + n : -1; const int n2 = n - 8; gi = (n2 >> 3) * 64 + xc * 8 + (n2 & 7);
    role = n2 < 9 ? (xc < 4 ? 1 : 3) : (n2 < 18 ? 2 : (n2 < 22 ? 0 : 4)); ri = n2 < 9 ? (xc & 3) * 9 + n2 : (n2 < 18 ? xc * 9 + n2 - 9 : (n2 - 18) * 8 + xc);
  } else if (PLACE == 1) {
    rec = (xc < 4 && n < 16) ? xc * 16 + n : -1;
    if (xc < 4) { const int n2 = n - 16; gi = GI0 + xc * 16 + n2;
      role = n2 < 6 ? 0 : (n2 < 10 ? 2 : (xc < 2 ? 1 : 3));
      ri = n2 < 6 ? (n2 >> 1) * 8 + xc * 2 + (n2 & 1) : (n2 < 10 ? xc * 4 + n2 - 6 : 36 + (xc & 1) * 6 + n2 - 10); }
    else { gi = (xc - 4) * 32 + n; if (gi >= GI0) gi += 64; role = n < 18 ? (xc < 6 ? 1 : 3) : 2; ri = n < 18 ? (xc & 1) * 18 + n : 16 + (xc - 4) * 14 + n - 18; }
  } else {
    rec = xc < 2 ? xc * 32 + n : -1; gi = (xc - 2) * 32 + n;
    role = n < 12 ? (xc < 5 ? 1 : 3) : (n < 24 ? 2 : 0); ri = n < 12 ? ((xc - 2) % 3) * 12 + n : (n < 24 ? (xc - 2) * 12 + n - 12 : (xc - 2) * 8 + n - 24);
    if (role == 0 && ri >= 32) role = 4;
  }
  if (rec >= 0) {
    asm volatile(REC_ASM ::"s"(kargs()), "s"(rec | w << 8 | b << 16) : REC_CLOB);
    __builtin_amdgcn_endpgm();
  }
  lptr lds = (lptr)smem;
  {
  const kargs_t p = kargs();
  const int l = lane_id();
  const rsrc_t ws = __builtin_amdgcn_make_buffer_rsrc(p->ws, 0, WS_END, 0x00020000);
  TS(0);
  if (gi == 63) {
    hs_open(ws, 64 * w + l);
    bound_part(p, ws, lds, 63, w, l);
    for (unsigned k = 0; k < SPIN_CAP && !hs_round(ws, 64 * w + l); ++k) __builtin_amdgcn_s_sleep(4);
    ln_rows(p, ws, 63, w, l);
    if (w == 0 && l == 0) st4(1u, ws, OFF_CP + 256 + 63 * 4, 0);
  } else {
    hs_begin(ws, w, l);
    if (gi < 64) {
      ln_rows(p, ws, gi, w, l);
      if (!hs_wait(ws, w, l)) return;
      if (w == 0 && l == 0) st4(1u, ws, OFF_CP + 256 + gi * 4, 0);
      bound_part(p, ws, lds, gi, w, l);
    }
  }
  }
  { const int code = gi >> 6 | ((gi >> 1) & 31) << 8 | (gi & 1) << 16 | w << 20 | b << 24;
    if (gi >= 128) asm volatile(V_ASM ::"s"(kargs()), "s"(code) : QKV_CLOB); else asm volatile(QK_ASM ::"s"(kargs()), "s"(code) : QKV_CLOB); }
  if (role == 3) for (int i = 0; i < 6; ++i) __builtin_amdgcn_s_sleep(32);
  if ((role & 1) && ri >= 36) fill_part(__builtin_amdgcn_make_buffer_rsrc(kargs()->ws, 0, WS_END, 0x00020000), (role >> 1) * 12 + ri - 36, w);
  if (role == 1) {
    if (ROLES & 2) { asm volatile(OPJ_ASM ::"s"(kargs()), "s"(ri % 12 | ri / 12 << 8 | w << 16) : REC_CLOB); __builtin_amdgcn_endpgm(); }
  } else if (role == 3) {
    if (ROLES & 8) { asm volatile(DOWN_ASM ::"s"(kargs()), "s"(ri % 12 | ri / 12 << 8 | w << 16) : REC_CLOB); __builtin_amdgcn_endpgm(); }
  } else if (role == 2) {
    if (ROLES & 4) {
      const kargs_t p = kargs();
      stage_slab((const char*)p->Wg + ri * 128, (const char*)p->Wu + ri * 128, F * 2, w, lane_id());
      waitv<0>(); BAR();
      asm volatile(GU_ASM ::"s"(kargs()), "s"(ri | w << 16) : REC_CLOB);
      __builtin_amdgcn_endpgm();
    }
  }
  else if (role == 0) { if (ROLES & 1) att_role<3>(__builtin_amdgcn_make_buffer_rsrc(kargs()->ws, 0, WS_END, 0x00020000), (ri & 7) * 4 + w, ri >> 3); }
}

extern "C" void kernel_launch(void* const* d_in, const int* in_sizes, int n_in, void* d_out, int out_size, void* d_ws, size_t ws_size, hipStream_t stream) {
  Params p;
  p.x = (cw16)d_in[0]; p.mem0 = (cw16)d_in[1]; p.Wq = (cw16)d_in[2]; p.Wk = (cw16)d_in[3]; p.Wv = (cw16)d_in[4]; p.Wo = (cw16)d_in[5];
  p.Wg = (cw16)d_in[8]; p.Wu = (cw16)d_in[9]; p.Wd = (cw16)d_in[10]; p.out = (unsigned short*)d_out; p.ws = (char*)d_ws;
  (void)hipFuncSetAttribute((const void*)k_main, hipFuncAttributeMaxDynamicSharedMemorySize, LDS_BYTES);
  hipLaunchKernelGGL(k_main, dim3(256), dim3(256), LDS_BYTES, stream, p);
}
```
